```python
import torch

def _softmax(x, axis=-1):
    return torch.softmax(x, dim=axis)
def _gelu(x):
    return torch.nn.functional.gelu(x, approximate="tanh")
def _silu(x):
    return torch.nn.functional.silu(x)
def _sigmoid(x):
    return torch.sigmoid(x)
def _layernorm(x, axis=-1, eps=1e-5):
    m = x.mean(dim=axis, keepdim=True)
    v = x.var(dim=axis, keepdim=True, unbiased=False)
    return (x - m) / torch.sqrt(v + eps)
def _scatter_add(tgt, idx, src, axis=0):
    return tgt.clone().index_add_(axis, idx, src)
def _segment_sum(src, seg, n):
    out = torch.zeros((n,) + src.shape[1:], dtype=src.dtype)
    return out.index_add_(0, seg, src)
def _pad(x, pw):
    flat = tuple(v for pair in reversed(pw) for v in pair)
    return torch.nn.functional.pad(x, flat)

kernel_name = 'parallel_sum__ssm_conv_s5__mixer_channel_mlp__prompt'

def setup_inputs():

    _w = (lambda a: (lambda t: t.to(torch.bfloat16) if t.is_floating_point() else t)(torch.as_tensor(a)))
    def _A_setup():
        B, S, D = 1, 2048, 576
        P = D
        Lambda_re = -0.5 * (lambda _x: torch.exp(torch.as_tensor(_x)))((lambda *s: torch.randn(*s, dtype=torch.bfloat16) * 0.2)(P))
        Lambda_im = (lambda *s: torch.randn(*s, dtype=torch.bfloat16) * 0.8)(P) * 1.0
        B_re = (lambda *s: torch.randn(*s, dtype=torch.bfloat16) * 0.2)(P, D) * (D ** -0.5)
        B_im = (lambda *s: torch.randn(*s, dtype=torch.bfloat16) * 0.2)(P, D) * (D ** -0.5)
        C_re = (lambda *s: torch.randn(*s, dtype=torch.bfloat16) * 0.2)(D, P) * (P ** -0.5)
        C_im = (lambda *s: torch.randn(*s, dtype=torch.bfloat16) * 0.2)(D, P) * (P ** -0.5)
        log_dt = (lambda *s: torch.randn(*s, dtype=torch.bfloat16) * 0.2)(P) * 0.3 - 2.0
        Dskip = (lambda *s: torch.randn(*s, dtype=torch.bfloat16) * 0.05)(D)
        return {
            "x": _w((lambda *s: torch.randn(*s, dtype=torch.bfloat16) * 0.05)(B, S, D)),
            "Lambda_re": _w(Lambda_re),
            "Lambda_im": _w(Lambda_im),
            "B_re": _w(B_re),
            "B_im": _w(B_im),
            "C_re": _w(C_re),
            "C_im": _w(C_im),
            "log_dt": _w(log_dt),
            "Dskip": _w(Dskip),
        }
    _A_in = _A_setup()

    def _B_setup():
        B, S, D, F = 1, 2048, 576, 4608
        return {
            "x": _w((lambda *s: torch.randn(*s, dtype=torch.bfloat16) * 0.05)(B, S, D)),
            "w1": _w((lambda *s: torch.randn(*s, dtype=torch.bfloat16) * 1.6)(D, F) * (D ** -0.5)),
            "b1": _w((lambda *s: torch.randn(*s, dtype=torch.bfloat16) * 0.8)(F)),
            "w2": _w((lambda *s: torch.randn(*s, dtype=torch.bfloat16) * 0.05)(F, D) * (F ** -0.5)),
            "b2": _w((lambda *s: torch.randn(*s, dtype=torch.bfloat16) * 0.05)(D)),
        }
    _B_in = _B_setup()

    return {
        "A_x": _A_in["x"],
        "A_Lambda_re": _A_in["Lambda_re"],
        "A_Lambda_im": _A_in["Lambda_im"],
        "A_B_re": _A_in["B_re"],
        "A_B_im": _A_in["B_im"],
        "A_C_re": _A_in["C_re"],
        "A_C_im": _A_in["C_im"],
        "A_log_dt": _A_in["log_dt"],
        "A_Dskip": _A_in["Dskip"],
        "B_x": _B_in["x"],
        "B_w1": _B_in["w1"],
        "B_b1": _B_in["b1"],
        "B_w2": _B_in["w2"],
        "B_b2": _B_in["b2"],
    }

def reference(A_x, A_Lambda_re, A_Lambda_im, A_B_re, A_B_im, A_C_re, A_C_im, A_log_dt, A_Dskip, B_x, B_w1, B_b1, B_w2, B_b2):
    def _A_ref(x, Lambda_re, Lambda_im, B_re, B_im, C_re, C_im, log_dt, Dskip):
        Bsz, L, H = x.shape
        Lam = Lambda_re + 1j * Lambda_im
        Bmat = B_re + 1j * B_im
        Cmat = C_re + 1j * C_im
        dt = torch.exp(log_dt)
        Lam_bar = torch.exp(dt * Lam)
        B_bar = ((Lam_bar - 1.0) / Lam)[:, None] * Bmat
        Bu = torch.einsum("bld,pd->blp", x + 0j, B_bar)
        h = torch.zeros_like(Bu[:, 0, :])
        hs = []
        for t in range(L):
            h = Lam_bar[None, :] * h + Bu[:, t, :]
            hs.append(h)
        hseq = torch.stack(hs, 1)
        y = 2.0 * torch.einsum("blp,dp->bld", hseq, Cmat).real + x * Dskip[None, None, :]
        return y

    def _B_ref(x, w1, b1, w2, b2):
        y = _layernorm(x, axis=-1)
        h = _gelu(torch.einsum("bsd,df->bsf", y, w1) + b1)
        return torch.einsum("bsf,fd->bsd", h, w2) + b2

    x = A_x
    xn = _layernorm(x, axis=-1)
    return x + _A_ref(xn, A_Lambda_re, A_Lambda_im, A_B_re, A_B_im, A_C_re, A_C_im, A_log_dt, A_Dskip) + _B_ref(xn, B_w1, B_b1, B_w2, B_b2)
```

```cpp
#include <hip/hip_runtime.h>
#include <hip/hip_bf16.h>
#include <stdint.h>

constexpr int L = 2048, D = 576, F = 4608, P = 576;
typedef unsigned short u16;
__device__ __forceinline__ float bf2f(u16 v) { return __uint_as_float(((unsigned)v) << 16); }

__global__ void k_ln(const u16* x, float* xn, float* y) {
  int t = blockIdx.x, l = threadIdx.x;
  float v[9]; float s = 0;
  for (int i = 0; i < 9; i++) { v[i] = bf2f(x[t * D + i * 64 + l]); s += v[i]; }
  for (int o = 32; o; o >>= 1) s += __shfl_xor(s, o);
  float m = s / D, q = 0;
  for (int i = 0; i < 9; i++) { v[i] -= m; q += v[i] * v[i]; }
  for (int o = 32; o; o >>= 1) q += __shfl_xor(q, o);
  float r = 1.0f / sqrtf(q / D + 1e-5f);
  s = 0;
  for (int i = 0; i < 9; i++) { v[i] *= r; xn[t * D + i * 64 + l] = v[i]; s += v[i]; }
  for (int o = 32; o; o >>= 1) s += __shfl_xor(s, o);
  m = s / D; q = 0;
  for (int i = 0; i < 9; i++) { v[i] -= m; q += v[i] * v[i]; }
  for (int o = 32; o; o >>= 1) q += __shfl_xor(q, o);
  r = 1.0f / sqrtf(q / D + 1e-5f);
  for (int i = 0; i < 9; i++) y[t * D + i * 64 + l] = v[i] * r;
}
__global__ void k_g1(const float* y, const u16* w1, const u16* b1, float* H) {
  int f = blockIdx.x * 256 + threadIdx.x; int t0 = blockIdx.y * 8;
  float a[8] = {0,0,0,0,0,0,0,0};
  for (int d = 0; d < D; d++) { float w = bf2f(w1[d * F + f]);
#pragma unroll
    for (int i = 0; i < 8; i++) a[i] += y[(t0 + i) * D + d] * w; }
#pragma unroll
  for (int i = 0; i < 8; i++) { float z = a[i] + bf2f(b1[f]);
    float u = 0.7978845608028654f * (z + 0.044715f * z * z * z);
    H[(size_t)(t0 + i) * F + f] = 0.5f * z * (1.0f + tanhf(u)); }
}
__global__ void k_bu(const float* xn, const u16* Bre, const u16* Bim, float* Bu) {
  int p = blockIdx.x * 64 + threadIdx.x; int t = blockIdx.y;
  float a = 0, b = 0;
  for (int d = 0; d < D; d++) { float xv = xn[t * D + d]; a += xv * bf2f(Bre[p * D + d]); b += xv * bf2f(Bim[p * D + d]); }
  Bu[(t * P + p) * 2] = a; Bu[(t * P + p) * 2 + 1] = b;
}
__global__ void k_scan(const u16* Lre, const u16* Lim, const u16* ldt, float* Bu) {
  int p = blockIdx.x * 64 + threadIdx.x;
  float lr = bf2f(Lre[p]), li = bf2f(Lim[p]), dt = expf(bf2f(ldt[p]));
  float e = expf(dt * lr), ar = e * cosf(dt * li), ai = e * sinf(dt * li);
  float den = lr * lr + li * li; float nr = ar - 1.0f, ni = ai;
  float cr = (nr * lr + ni * li) / den, ci = (ni * lr - nr * li) / den;
  float hr = 0, hi = 0;
  for (int t = 0; t < L; t++) { float a = Bu[(t * P + p) * 2], b = Bu[(t * P + p) * 2 + 1];
    float ur = cr * a - ci * b, ui = cr * b + ci * a;
    float nhr = ar * hr - ai * hi + ur, nhi = ar * hi + ai * hr + ui; hr = nhr; hi = nhi;
    Bu[(t * P + p) * 2] = hr; Bu[(t * P + p) * 2 + 1] = hi; }
}
__global__ void k_out(const u16* x, const float* xn, const float* H, const float* h, const u16* w2, const u16* b2,
                      const u16* Cre, const u16* Cim, const u16* Dsk, float* out) {
  int d = blockIdx.x * 64 + threadIdx.x; int t0 = blockIdx.y * 8;
  float a[8] = {0,0,0,0,0,0,0,0}, c[8] = {0,0,0,0,0,0,0,0};
  for (int f = 0; f < F; f++) { float w = bf2f(w2[f * D + d]);
#pragma unroll
    for (int i = 0; i < 8; i++) a[i] += H[(size_t)(t0 + i) * F + f] * w; }
  for (int p = 0; p < P; p++) { float cr = bf2f(Cre[d * P + p]), ci = bf2f(Cim[d * P + p]);
#pragma unroll
    for (int i = 0; i < 8; i++) c[i] += h[((t0 + i) * P + p) * 2] * cr - h[((t0 + i) * P + p) * 2 + 1] * ci; }
#pragma unroll
  for (int i = 0; i < 8; i++) { int t = t0 + i;
    out[t * D + d] = bf2f(x[t * D + d]) + 2.0f * c[i] + xn[t * D + d] * bf2f(Dsk[d]) + a[i] + bf2f(b2[d]); }
}
extern "C" void kernel_launch(void* const* d_in, const int* in_sizes, int n_in, void* d_out, int out_size, void* d_ws, size_t ws_size, hipStream_t stream) {
  const u16* x = (const u16*)d_in[0];
  float* ws = (float*)d_ws;
  float* xn = ws; float* y = xn + L * D; float* Bu = y + L * D; float* H = Bu + L * P * 2;
  k_ln<<<L, 64, 0, stream>>>(x, xn, y);
  k_g1<<<dim3(F / 256, L / 8), 256, 0, stream>>>(y, (const u16*)d_in[10], (const u16*)d_in[11], H);
  k_bu<<<dim3(P / 64, L), 64, 0, stream>>>(xn, (const u16*)d_in[3], (const u16*)d_in[4], Bu);
  k_scan<<<P / 64, 64, 0, stream>>>((const u16*)d_in[1], (const u16*)d_in[2], (const u16*)d_in[7], Bu);
  k_out<<<dim3(D / 64, L / 8), 64, 0, stream>>>(x, xn, H, Bu, (const u16*)d_in[12], (const u16*)d_in[13], (const u16*)d_in[5], (const u16*)d_in[6], (const u16*)d_in[8], (float*)d_out);
}
```

```cpp
#include <hip/hip_runtime.h>
#include <stdint.h>

typedef unsigned short u16;

__global__ __launch_bounds__(512, 2) __attribute__((aligned(4096))) void fused_main(const u16* x, const u16* lre, const u16* lim, const u16* bre, const u16* bim,
                                                     const u16* cre, const u16* cim, const u16* ldt, const u16* dsk, const u16* w1,
                                                     const u16* b1, const u16* w2, const u16* b2, float* out, char* ws) {
  __shared__ __attribute__((aligned(16))) char smem[163840];
  unsigned tid = threadIdx.x;
  asm volatile(
"s_mov_b64 s[8:9],%[ka]\n"
"s_mov_b32 s10,%[bid]\n"
"s_load_dwordx16 s[36:51],s[8:9],0\n"
"s_load_dwordx8 s[52:59],s[8:9],64\n"
"s_load_dwordx4 s[60:63],s[8:9],96\n"
"s_load_dwordx2 s[64:65],s[8:9],112\n"
"v_mov_b32 v255,v0\n"
"v_lshrrev_b32 v240,6,v0\n"
"v_and_b32 v254,63,v0\n"
"v_readfirstlane_b32 s11,v240\n"
"v_lshlrev_b32 v252,4,v254\n"
"s_getpc_b64 s[96:97]\n"
".Lk_pf0:\n"
"s_and_b32 s97,s97,0xffff\n"
"s_mov_b32 s98,.Lk_ssm_end-.Lk_pf0\n"
"s_mov_b32 s99,0x20000\n"
"s_lshr_b32 s75,s10,3\n"
"s_and_b32 s75,s75,15\n"
"s_lshl_b32 s75,s75,7\n"
"v_mul_u32_u24 v246,2048,v0\n"
"s_cmp_ge_u32 s11,1\n"
"s_cbranch_scc1 .Lk_pf1\n"
"s_mov_b32 m0,161280\n"
"s_nop 0\n"
"buffer_load_dword v246,s[96:99],s75 offen lds\n"
".Lk_pf1:\n"
"s_and_b32 s71,s11,3\n"
"s_lshr_b32 s72,s11,2\n"
"s_bfe_u32 s69,s10,0x20001\n"
"s_and_b32 s75,s10,1\n"
"s_lshr_b32 s76,s10,3\n"
"s_mul_i32 s77,s76,43\n"
"s_lshr_b32 s77,s77,7\n"
"s_mul_i32 s70,s77,3\n"
"s_sub_u32 s70,s76,s70\n"
"s_sub_u32 s78,s76,24\n"
"s_cmp_ge_u32 s76,24\n"
"s_cselect_b32 s77,s78,s77\n"
"s_lshl_b32 s68,s75,3\n"
"s_add_u32 s68,s68,s77\n"
"s_lshl_b32 s73,s71,10\n"
"s_cmp_ge_u32 s10,192\n"
"s_cselect_b32 s89,1,s70\n"
"s_and_b32 s89,s89,1\n"
"s_xor_b32 s89,s89,s72\n"
"v_bfe_u32 v240,v254,3,2\n"
"v_lshrrev_b32 v241,5,v254\n"
"v_lshl_add_u32 v242,v241,3,v240\n"
"v_mul_u32_u24 v240,1152,v242\n"
"v_lshrrev_b32 v241,4,v254\n"
"v_and_b32 v243,7,v254\n"
"v_xor_b32 v241,v241,v243\n"
"s_lshl_b32 s75,s89,2\n"
"v_xor_b32 v241,s75,v241\n"
"v_lshl_add_u32 v240,v241,4,v240\n"
"s_lshl_b32 s75,s68,3\n"
"s_lshl_b32 s77,s71,1\n"
"s_add_u32 s75,s75,s77\n"
"s_add_u32 s75,s75,s89\n"
"s_mul_i32 s75,s75,18432\n"
"s_add_u32 s76,s75,4608\n"
"s_waitcnt lgkmcnt(0)\n"
"s_mov_b32 s12,s36\n"
"s_and_b32 s13,s37,0xffff\n"
"s_mov_b32 s14,2359296\n"
"s_mov_b32 s15,0x20000\n"
"s_cmp_eq_u32 s72,0\n"
"s_cbranch_scc1 .Lk_hop_e\n"
"s_cmp_ge_u32 s10,192\n"
"s_cbranch_scc1 .Lk_hop_es\n"
"s_getpc_b64 s[96:97]\n"
".Lk_hop_pcem:\n"
"s_add_u32 s96,s96,.Lk_hop_backem-.Lk_hop_pcem\n"
"s_addc_u32 s97,s97,0\n"
"s_mov_b64 exec,0\n"
"s_cmp_eq_u32 s71,0\n"
"s_cbranch_scc1 .Lk_padm8\n"
"s_cmp_eq_u32 s71,1\n"
"s_cbranch_scc1 .Lk_padm9\n"
"s_cmp_eq_u32 s71,2\n"
"s_cbranch_scc1 .Lk_padm10\n"
"s_branch .Lk_padm11\n"
".Lk_hop_backem:\n"
"s_branch .Lk_hop_e\n"
".Lk_hop_es:\n"
"s_getpc_b64 s[96:97]\n"
".Lk_hop_pces:\n"
"s_add_u32 s96,s96,.Lk_hop_backes-.Lk_hop_pces\n"
"s_addc_u32 s97,s97,0\n"
"s_mov_b64 exec,0\n"
"s_cmp_eq_u32 s71,0\n"
"s_cbranch_scc1 .Lk_pads8\n"
"s_cmp_eq_u32 s71,1\n"
"s_cbranch_scc1 .Lk_pads9\n"
"s_cmp_eq_u32 s71,2\n"
"s_cbranch_scc1 .Lk_pads10\n"
"s_branch .Lk_pads11\n"
".Lk_hop_backes:\n"
".Lk_hop_e:\n"
"buffer_load_dwordx4 v[0:3],v240,s[12:15],s75 offen\n"
"buffer_load_dwordx4 v[4:7],v240,s[12:15],s75 offen offset:128\n"
"buffer_load_dwordx4 v[8:11],v240,s[12:15],s75 offen offset:256\n"
"buffer_load_dwordx4 v[12:15],v240,s[12:15],s75 offen offset:384\n"
"buffer_load_dwordx4 v[16:19],v240,s[12:15],s75 offen offset:512\n"
"buffer_load_dwordx4 v[20:23],v240,s[12:15],s75 offen offset:640\n"
"buffer_load_dwordx4 v[24:27],v240,s[12:15],s75 offen offset:768\n"
"buffer_load_dwordx4 v[28:31],v240,s[12:15],s75 offen offset:896\n"
"buffer_load_dwordx4 v[32:35],v240,s[12:15],s75 offen offset:1024\n"
"s_mov_b32 s32,s64\n"
"s_and_b32 s33,s65,0xffff\n"
"s_mov_b32 s34,39845888\n"
"s_mov_b32 s35,0x20000\n"
"s_lshl_b32 s77,s68,2\n"
"s_add_u32 s77,s77,s69\n"
"s_mul_i32 s78,s77,3\n"
"s_add_u32 s78,s78,s70\n"
"s_add_u32 s77,s77,192\n"
"s_cmp_ge_u32 s10,192\n"
"s_cselect_b32 s78,s77,s78\n"
"s_lshl_b32 s88,s78,10\n"
"v_mov_b32 v177,2146435072\n"
"s_cmp_lg_u32 s11,4\n"
"s_cbranch_scc1 .Lk_he0\n"
"v_mov_b32 v180,0\n"
"v_mov_b32 v181,0\n"
"v_mov_b32 v182,0\n"
"v_mov_b32 v183,0\n"
"s_lshl_b32 s77,s68,7\n"
"s_add_u32 s77,s77,262144\n"
"s_lshl_b32 s78,s69,7\n"
"s_add_u32 s78,s78,264064\n"
"s_cmp_lt_u32 s10,192\n"
"s_cselect_b32 s78,2146435072,s78\n"
"v_mov_b32 v177,s77\n"
"v_mov_b32 v178,s78\n"
"v_cmp_lt_u32 vcc,31,v254\n"
"s_nop 1\n"
"v_cndmask_b32 v177,v177,v178,vcc\n"
"v_lshl_add_u32 v177,v254,2,v177\n"
"buffer_load_dword v176,v177,s[32:35],0 offen sc1\n"
"buffer_store_dwordx4 v[180:183],v252,s[32:35],s88 offen sc1\n"
"s_cbranch_scc1 .Lk_he0\n"
"s_mul_i32 s78,s88,3\n"
"s_add_u32 s78,s78,4294647808\n"
"buffer_store_dwordx4 v[180:183],v252,s[32:35],s78 offen sc1\n"
"buffer_store_dwordx4 v[180:183],v252,s[32:35],s78 offen offset:1024 sc1\n"
"buffer_store_dwordx4 v[180:183],v252,s[32:35],s78 offen offset:2048 sc1\n"
".Lk_he0:\n"
"v_mov_b32 v247,v240\n"
"s_mov_b32 s95,s76\n"
"s_cmp_ge_u32 s10,192\n"
"s_cbranch_scc1 .Lk_ssm\n"
"s_mov_b32 s16,s54\n"
"s_and_b32 s17,s55,0xffff\n"
"s_mov_b32 s18,5308416\n"
"s_mov_b32 s19,0x20000\n"
"s_mov_b32 s20,s58\n"
"s_and_b32 s21,s59,0xffff\n"
"s_mov_b32 s22,5308416\n"
"s_mov_b32 s23,0x20000\n"
"s_mov_b32 s24,s56\n"
"s_and_b32 s25,s57,0xffff\n"
"s_mov_b32 s26,9216\n"
"s_mov_b32 s27,0x20000\n"
"s_mov_b32 s28,s62\n"
"s_and_b32 s29,s63,0xffff\n"
"s_mov_b32 s30,4718592\n"
"s_mov_b32 s31,0x20000\n"
"s_mov_b64 s[4:5],s[52:53]\n"
"s_mov_b64 s[6:7],s[60:61]\n"
"s_and_b32 s75,s71,1\n"
"s_lshl_b32 s75,s75,2\n"
"s_lshr_b32 s76,s71,1\n"
"s_lshl_b32 s76,s76,5\n"
"s_add_u32 s75,s75,s76\n"
"v_bfe_u32 v241,v254,3,2\n"
"v_lshrrev_b32 v242,5,v254\n"
"v_lshl_add_u32 v241,v242,3,v241\n"
"v_add_u32 v241,s75,v241\n"
"v_mul_u32_u24 v250,9216,v241\n"
"v_lshrrev_b32 v241,3,v254\n"
"v_xor_b32 v241,v241,v254\n"
"v_and_b32 v241,6,v241\n"
"v_and_b32 v242,1,v254\n"
"v_or_b32 v241,v241,v242\n"
"v_lshl_add_u32 v250,v241,4,v250\n"
"v_add_u32 v191,146432,v250\n"
"s_mul_i32 s75,s69,3\n"
"s_add_u32 s76,s75,s70\n"
"s_mul_i32 s80,s76,768\n"
"v_lshlrev_b32 v193,1,v255\n"
"s_lshl_b32 s77,s71,3\n"
"v_lshrrev_b32 v241,3,v254\n"
"v_add_u32 v241,s77,v241\n"
"v_mul_u32_u24 v251,1152,v241\n"
"v_lshrrev_b32 v241,3,v254\n"
"v_xor_b32 v241,v241,v254\n"
"v_and_b32 v241,6,v241\n"
"v_and_b32 v242,1,v254\n"
"v_or_b32 v241,v241,v242\n"
"v_lshl_add_u32 v251,v241,4,v251\n"
"s_lshl_b32 s77,s68,2\n"
"s_add_u32 s77,s77,s69\n"
"s_mul_i32 s77,s77,3\n"
"s_add_u32 s76,s70,0\n"
"s_sub_u32 s78,s76,3\n"
"s_cmp_ge_u32 s76,3\n"
"s_cselect_b32 s76,s78,s76\n"
"s_add_u32 s78,s75,s76\n"
"s_mul_i32 s81,s78,442368\n"
"s_mul_i32 s78,s70,384\n"
"s_add_u32 s81,s81,s78\n"
"s_add_u32 s78,s77,s76\n"
"s_lshl_b32 s88,s78,10\n"
"s_lshl_b32 s78,s78,3\n"
"s_add_u32 s78,s78,s11\n"
"s_mul_i32 s84,s78,12288\n"
"s_add_u32 s84,s84,2097152\n"
"s_add_u32 s76,s70,1\n"
"s_sub_u32 s78,s76,3\n"
"s_cmp_ge_u32 s76,3\n"
"s_cselect_b32 s76,s78,s76\n"
"s_add_u32 s78,s75,s76\n"
"s_mul_i32 s82,s78,442368\n"
"s_mul_i32 s78,s70,384\n"
"s_add_u32 s82,s82,s78\n"
"s_add_u32 s78,s77,s76\n"
"s_lshl_b32 s90,s78,10\n"
"s_lshl_b32 s78,s78,3\n"
"s_add_u32 s78,s78,s11\n"
"s_mul_i32 s85,s78,12288\n"
"s_add_u32 s85,s85,2097152\n"
"s_add_u32 s76,s70,2\n"
"s_sub_u32 s78,s76,3\n"
"s_cmp_ge_u32 s76,3\n"
"s_cselect_b32 s76,s78,s76\n"
"s_add_u32 s78,s75,s76\n"
"s_mul_i32 s83,s78,442368\n"
"s_mul_i32 s78,s70,384\n"
"s_add_u32 s83,s83,s78\n"
"s_add_u32 s78,s77,s76\n"
"s_lshl_b32 s92,s78,10\n"
"s_lshl_b32 s78,s78,3\n"
"s_add_u32 s78,s78,s11\n"
"s_mul_i32 s86,s78,12288\n"
"s_add_u32 s86,s86,2097152\n"
"s_lshl_b32 s78,s11,6\n"
"s_add_u32 s88,s88,s78\n"
"s_add_u32 s90,s90,s78\n"
"s_add_u32 s92,s92,s78\n"
"s_lshl_b32 s76,s69,4\n"
"s_add_u32 s76,s76,s68\n"
"s_lshl_b32 s76,s76,2\n"
"s_add_u32 s76,s76,s71\n"
"s_mul_i32 s87,s76,36864\n"
"s_mul_i32 s76,s70,6144\n"
"s_add_u32 s87,s87,s76\n"
"s_add_u32 s87,s87,20971520\n"
"s_cmp_eq_u32 s71,s69\n"
"s_cselect_b32 s9,2146435072,s87\n"
"s_mov_b32 s79,0xc09d30fc\n"
"s_mov_b32 s93,589824\n"
"s_lshl_b32 s91,s71,11\n"
"s_lshl_b32 s75,s72,1\n"
"v_bfe_u32 v241,v254,3,2\n"
"s_add_u32 s78,s75,0\n"
"v_xor_b32 v241,s78,v241\n"
"v_lshrrev_b32 v248,2,v254\n"
"v_lshl_add_u32 v248,v248,2,v241\n"
"v_and_b32 v241,3,v254\n"
"v_lshl_add_u32 v248,v248,2,v241\n"
"v_lshlrev_b32 v248,3,v248\n"
"v_bfe_u32 v241,v254,3,2\n"
"s_add_u32 s78,s75,1\n"
"v_xor_b32 v241,s78,v241\n"
"v_lshrrev_b32 v249,2,v254\n"
"v_lshl_add_u32 v249,v249,2,v241\n"
"v_and_b32 v241,3,v254\n"
"v_lshl_add_u32 v249,v249,2,v241\n"
"v_lshlrev_b32 v249,3,v249\n"
"v_lshlrev_b32 v194,2,v255\n"
"v_add_u32 v194,161536,v194\n"
"s_mul_i32 s75,s72,147456\n"
"v_add_u32 v195,s75,v250\n"
"v_and_b32 v253,48,v254\n"
"s_lshl_b32 s75,s72,7\n"
"s_add_u32 s75,s75,161536\n"
"v_add_u32 v253,s75,v253\n"
"v_and_b32 v241,1,v254\n"
"s_sub_u32 s76,s92,s90\n"
"v_mul_lo_u32 v241,s76,v241\n"
"v_add_u32 v255,s90,v241\n"
"v_mov_b32 v208,0x3f803f80\n"
"s_mul_i32 s75,s71,18432\n"
"s_add_u32 s75,s75,73728\n"
"s_lshl_b32 s76,s89,10\n"
"s_add_u32 s76,s76,s75\n"
"v_add_u32 v220,s76,v252\n"
"v_and_b32 v240,15,v254\n"
"v_lshrrev_b32 v241,4,v254\n"
"v_bfe_u32 v242,v254,1,3\n"
"v_xor_b32 v241,v241,v242\n"
"v_lshl_add_u32 v240,v240,3,v241\n"
"v_lshlrev_b32 v240,4,v240\n"
"v_xor_b32 v241,64,v240\n"
"v_add_u32 v174,s75,v240\n"
"v_add_u32 v175,s75,v241\n"
"v_bfe_u32 v240,v254,3,2\n"
"v_lshrrev_b32 v241,5,v254\n"
"v_lshl_add_u32 v240,v241,3,v240\n"
"s_lshl_b32 s75,s71,1\n"
"s_add_u32 s75,s75,s89\n"
"s_lshl_b32 s75,s75,7\n"
"s_add_u32 s75,s75,147456\n"
"v_lshl_add_u32 v221,v240,3,s75\n"
"s_cmp_eq_u32 s72,1\n"
"s_cbranch_scc1 .Lk_hop_skipm\n"
"s_getpc_b64 s[96:97]\n"
".Lk_hop_pcam:\n"
"s_add_u32 s96,s96,.Lk_hop_backam-.Lk_hop_pcam\n"
"s_addc_u32 s97,s97,0\n"
"s_mov_b64 exec,0\n"
"s_cmp_eq_u32 s71,0\n"
"s_cbranch_scc1 .Lk_padm0\n"
"s_cmp_eq_u32 s71,1\n"
"s_cbranch_scc1 .Lk_padm1\n"
"s_cmp_eq_u32 s71,2\n"
"s_cbranch_scc1 .Lk_padm2\n"
"s_branch .Lk_padm3\n"
".Lk_hop_retm:\n"
"s_mov_b64 exec,-1\n"
"s_setpc_b64 s[96:97]\n"
".Lk_hop_backam:\n"
".Lk_hop_skipm:\n"
"s_setprio 2\n"
"v_mov_b32 v210,0\n"
"v_mov_b32 v211,0\n"
"v_mov_b32 v212,0\n"
"v_mov_b32 v213,0\n"
"s_waitcnt vmcnt(8)\n"
"v_dot2c_f32_bf16 v210,v0,v208\n"
"v_dot2c_f32_bf16 v212,v0,v0\n"
"v_dot2c_f32_bf16 v211,v1,v208\n"
"v_dot2c_f32_bf16 v213,v1,v1\n"
"v_dot2c_f32_bf16 v210,v2,v208\n"
"v_dot2c_f32_bf16 v212,v2,v2\n"
"v_dot2c_f32_bf16 v211,v3,v208\n"
"v_dot2c_f32_bf16 v213,v3,v3\n"
"s_waitcnt vmcnt(7)\n"
"v_dot2c_f32_bf16 v210,v4,v208\n"
"v_dot2c_f32_bf16 v212,v4,v4\n"
"v_dot2c_f32_bf16 v211,v5,v208\n"
"v_dot2c_f32_bf16 v213,v5,v5\n"
"v_dot2c_f32_bf16 v210,v6,v208\n"
"v_dot2c_f32_bf16 v212,v6,v6\n"
"v_dot2c_f32_bf16 v211,v7,v208\n"
"v_dot2c_f32_bf16 v213,v7,v7\n"
"s_waitcnt vmcnt(6)\n"
"v_dot2c_f32_bf16 v210,v8,v208\n"
"v_dot2c_f32_bf16 v212,v8,v8\n"
"v_dot2c_f32_bf16 v211,v9,v208\n"
"v_dot2c_f32_bf16 v213,v9,v9\n"
"v_dot2c_f32_bf16 v210,v10,v208\n"
"v_dot2c_f32_bf16 v212,v10,v10\n"
"v_dot2c_f32_bf16 v211,v11,v208\n"
"v_dot2c_f32_bf16 v213,v11,v11\n"
"s_waitcnt vmcnt(5)\n"
"v_dot2c_f32_bf16 v210,v12,v208\n"
"v_dot2c_f32_bf16 v212,v12,v12\n"
"v_dot2c_f32_bf16 v211,v13,v208\n"
"v_dot2c_f32_bf16 v213,v13,v13\n"
"v_dot2c_f32_bf16 v210,v14,v208\n"
"v_dot2c_f32_bf16 v212,v14,v14\n"
"v_dot2c_f32_bf16 v211,v15,v208\n"
"v_dot2c_f32_bf16 v213,v15,v15\n"
"s_waitcnt vmcnt(4)\n"
"v_dot2c_f32_bf16 v210,v16,v208\n"
"v_dot2c_f32_bf16 v212,v16,v16\n"
"v_dot2c_f32_bf16 v211,v17,v208\n"
"v_dot2c_f32_bf16 v213,v17,v17\n"
"v_dot2c_f32_bf16 v210,v18,v208\n"
"v_dot2c_f32_bf16 v212,v18,v18\n"
"v_dot2c_f32_bf16 v211,v19,v208\n"
"v_dot2c_f32_bf16 v213,v19,v19\n"
"s_waitcnt vmcnt(3)\n"
"v_dot2c_f32_bf16 v210,v20,v208\n"
"v_dot2c_f32_bf16 v212,v20,v20\n"
"v_dot2c_f32_bf16 v211,v21,v208\n"
"v_dot2c_f32_bf16 v213,v21,v21\n"
"v_dot2c_f32_bf16 v210,v22,v208\n"
"v_dot2c_f32_bf16 v212,v22,v22\n"
"v_dot2c_f32_bf16 v211,v23,v208\n"
"v_dot2c_f32_bf16 v213,v23,v23\n"
"s_waitcnt vmcnt(2)\n"
"v_dot2c_f32_bf16 v210,v24,v208\n"
"v_dot2c_f32_bf16 v212,v24,v24\n"
"v_dot2c_f32_bf16 v211,v25,v208\n"
"v_dot2c_f32_bf16 v213,v25,v25\n"
"v_dot2c_f32_bf16 v210,v26,v208\n"
"v_dot2c_f32_bf16 v212,v26,v26\n"
"v_dot2c_f32_bf16 v211,v27,v208\n"
"v_dot2c_f32_bf16 v213,v27,v27\n"
"s_waitcnt vmcnt(1)\n"
"buffer_load_dwordx4 v[72:75],v247,s[12:15],s95 offen\n"
"v_dot2c_f32_bf16 v210,v28,v208\n"
"v_dot2c_f32_bf16 v212,v28,v28\n"
"v_dot2c_f32_bf16 v211,v29,v208\n"
"v_dot2c_f32_bf16 v213,v29,v29\n"
"v_dot2c_f32_bf16 v210,v30,v208\n"
"v_dot2c_f32_bf16 v212,v30,v30\n"
"v_dot2c_f32_bf16 v211,v31,v208\n"
"v_dot2c_f32_bf16 v213,v31,v31\n"
"s_waitcnt vmcnt(1)\n"
"buffer_load_dwordx4 v[76:79],v247,s[12:15],s95 offen offset:128\n"
"v_dot2c_f32_bf16 v210,v32,v208\n"
"v_dot2c_f32_bf16 v212,v32,v32\n"
"v_dot2c_f32_bf16 v211,v33,v208\n"
"v_dot2c_f32_bf16 v213,v33,v33\n"
"v_dot2c_f32_bf16 v210,v34,v208\n"
"v_dot2c_f32_bf16 v212,v34,v34\n"
"v_dot2c_f32_bf16 v211,v35,v208\n"
"v_dot2c_f32_bf16 v213,v35,v35\n"
"buffer_load_dwordx4 v[80:83],v247,s[12:15],s95 offen offset:256\n"
"buffer_load_dwordx4 v[84:87],v247,s[12:15],s95 offen offset:384\n"
"buffer_load_dwordx4 v[88:91],v247,s[12:15],s95 offen offset:512\n"
"buffer_load_dwordx4 v[92:95],v247,s[12:15],s95 offen offset:640\n"
"buffer_load_dwordx4 v[96:99],v247,s[12:15],s95 offen offset:768\n"
"buffer_load_dwordx4 v[100:103],v247,s[12:15],s95 offen offset:896\n"
"buffer_load_dwordx4 v[104:107],v247,s[12:15],s95 offen offset:1024\n"
"v_add_f32 v210,v210,v211\n"
"v_add_f32 v212,v212,v213\n"
"s_nop 0\n"
"v_add_f32_dpp v210,v210,v210 quad_perm:[1,0,3,2] row_mask:0xf bank_mask:0xf\n"
"v_add_f32_dpp v212,v212,v212 quad_perm:[1,0,3,2] row_mask:0xf bank_mask:0xf\n"
"s_nop 0\n"
"v_add_f32_dpp v210,v210,v210 quad_perm:[2,3,0,1] row_mask:0xf bank_mask:0xf\n"
"v_add_f32_dpp v212,v212,v212 quad_perm:[2,3,0,1] row_mask:0xf bank_mask:0xf\n"
"s_nop 0\n"
"v_add_f32_dpp v210,v210,v210 row_half_mirror row_mask:0xf bank_mask:0xf\n"
"v_add_f32_dpp v212,v212,v212 row_half_mirror row_mask:0xf bank_mask:0xf\n"
"v_mul_f32 v218,0x3ae38e39,v210\n"
"v_mul_f32 v212,0x3ae38e39,v212\n"
"v_fma_f32 v212,-v218,v218,v212\n"
"v_max_f32 v212,0,v212\n"
"v_add_f32 v219,0x3727c5ac,v212\n"
"v_rsq_f32 v219,v219\n"
"s_nop 0\n"
"v_mul_f32 v240,v219,v219\n"
"v_fmaak_f32 v240,v240,v212,0x3727c5ac\n"
"v_rsq_f32 v240,v240\n"
"v_mul_f32 v212,0x3eeff558,v219\n"
"v_mul_f32 v240,v212,v240\n"
"ds_write_b64 v221,v[218:219]\n"
"v_cvt_pk_bf16_f32 v214,v240,0\n"
"v_lshlrev_b32 v215,16,v214\n"
"v_mul_f32_e64 v216,-v218,v215\n"
"v_dot2_f32_bf16 v240,v0,v214,v216\n"
"v_dot2_f32_bf16 v241,v0,v215,v216\n"
"v_dot2_f32_bf16 v242,v1,v214,v216\n"
"v_dot2_f32_bf16 v243,v1,v215,v216\n"
"v_dot2_f32_bf16 v244,v2,v214,v216\n"
"v_dot2_f32_bf16 v245,v2,v215,v216\n"
"v_cvt_pk_bf16_f32 v0,v240,v241\n"
"v_dot2_f32_bf16 v240,v3,v214,v216\n"
"v_dot2_f32_bf16 v241,v3,v215,v216\n"
"v_cvt_pk_bf16_f32 v1,v242,v243\n"
"v_dot2_f32_bf16 v242,v4,v214,v216\n"
"v_dot2_f32_bf16 v243,v4,v215,v216\n"
"v_cvt_pk_bf16_f32 v2,v244,v245\n"
"v_dot2_f32_bf16 v244,v5,v214,v216\n"
"v_dot2_f32_bf16 v245,v5,v215,v216\n"
"v_cvt_pk_bf16_f32 v3,v240,v241\n"
"ds_write_b128 v220,v[0:3]\n"
"v_dot2_f32_bf16 v240,v6,v214,v216\n"
"v_dot2_f32_bf16 v241,v6,v215,v216\n"
"v_cvt_pk_bf16_f32 v4,v242,v243\n"
"v_dot2_f32_bf16 v242,v7,v214,v216\n"
"v_dot2_f32_bf16 v243,v7,v215,v216\n"
"v_cvt_pk_bf16_f32 v5,v244,v245\n"
"v_dot2_f32_bf16 v244,v8,v214,v216\n"
"v_dot2_f32_bf16 v245,v8,v215,v216\n"
"v_cvt_pk_bf16_f32 v6,v240,v241\n"
"v_dot2_f32_bf16 v240,v9,v214,v216\n"
"v_dot2_f32_bf16 v241,v9,v215,v216\n"
"v_cvt_pk_bf16_f32 v7,v242,v243\n"
"ds_write_b128 v220,v[4:7] offset:2048\n"
"v_dot2_f32_bf16 v242,v10,v214,v216\n"
"v_dot2_f32_bf16 v243,v10,v215,v216\n"
"v_cvt_pk_bf16_f32 v8,v244,v245\n"
"v_dot2_f32_bf16 v244,v11,v214,v216\n"
"v_dot2_f32_bf16 v245,v11,v215,v216\n"
"v_cvt_pk_bf16_f32 v9,v240,v241\n"
"v_dot2_f32_bf16 v240,v12,v214,v216\n"
"v_dot2_f32_bf16 v241,v12,v215,v216\n"
"v_cvt_pk_bf16_f32 v10,v242,v243\n"
"v_dot2_f32_bf16 v242,v13,v214,v216\n"
"v_dot2_f32_bf16 v243,v13,v215,v216\n"
"v_cvt_pk_bf16_f32 v11,v244,v245\n"
"ds_write_b128 v220,v[8:11] offset:4096\n"
"v_dot2_f32_bf16 v244,v14,v214,v216\n"
"v_dot2_f32_bf16 v245,v14,v215,v216\n"
"v_cvt_pk_bf16_f32 v12,v240,v241\n"
"v_dot2_f32_bf16 v240,v15,v214,v216\n"
"v_dot2_f32_bf16 v241,v15,v215,v216\n"
"v_cvt_pk_bf16_f32 v13,v242,v243\n"
"v_dot2_f32_bf16 v242,v16,v214,v216\n"
"v_dot2_f32_bf16 v243,v16,v215,v216\n"
"v_cvt_pk_bf16_f32 v14,v244,v245\n"
"v_dot2_f32_bf16 v244,v17,v214,v216\n"
"v_dot2_f32_bf16 v245,v17,v215,v216\n"
"v_cvt_pk_bf16_f32 v15,v240,v241\n"
"ds_write_b128 v220,v[12:15] offset:6144\n"
"v_dot2_f32_bf16 v240,v18,v214,v216\n"
"v_dot2_f32_bf16 v241,v18,v215,v216\n"
"v_cvt_pk_bf16_f32 v16,v242,v243\n"
"v_dot2_f32_bf16 v242,v19,v214,v216\n"
"v_dot2_f32_bf16 v243,v19,v215,v216\n"
"v_cvt_pk_bf16_f32 v17,v244,v245\n"
"v_dot2_f32_bf16 v244,v20,v214,v216\n"
"v_dot2_f32_bf16 v245,v20,v215,v216\n"
"v_cvt_pk_bf16_f32 v18,v240,v241\n"
"v_dot2_f32_bf16 v240,v21,v214,v216\n"
"v_dot2_f32_bf16 v241,v21,v215,v216\n"
"v_cvt_pk_bf16_f32 v19,v242,v243\n"
"ds_write_b128 v220,v[16:19] offset:8192\n"
"v_dot2_f32_bf16 v242,v22,v214,v216\n"
"v_dot2_f32_bf16 v243,v22,v215,v216\n"
"v_cvt_pk_bf16_f32 v20,v244,v245\n"
"v_dot2_f32_bf16 v244,v23,v214,v216\n"
"v_dot2_f32_bf16 v245,v23,v215,v216\n"
"v_cvt_pk_bf16_f32 v21,v240,v241\n"
"v_dot2_f32_bf16 v240,v24,v214,v216\n"
"v_dot2_f32_bf16 v241,v24,v215,v216\n"
"v_cvt_pk_bf16_f32 v22,v242,v243\n"
"v_dot2_f32_bf16 v242,v25,v214,v216\n"
"v_dot2_f32_bf16 v243,v25,v215,v216\n"
"v_cvt_pk_bf16_f32 v23,v244,v245\n"
"ds_write_b128 v220,v[20:23] offset:10240\n"
"v_dot2_f32_bf16 v244,v26,v214,v216\n"
"v_dot2_f32_bf16 v245,v26,v215,v216\n"
"v_cvt_pk_bf16_f32 v24,v240,v241\n"
"v_dot2_f32_bf16 v240,v27,v214,v216\n"
"v_dot2_f32_bf16 v241,v27,v215,v216\n"
"v_cvt_pk_bf16_f32 v25,v242,v243\n"
"v_dot2_f32_bf16 v242,v28,v214,v216\n"
"v_dot2_f32_bf16 v243,v28,v215,v216\n"
"v_cvt_pk_bf16_f32 v26,v244,v245\n"
"v_dot2_f32_bf16 v244,v29,v214,v216\n"
"v_dot2_f32_bf16 v245,v29,v215,v216\n"
"v_cvt_pk_bf16_f32 v27,v240,v241\n"
"ds_write_b128 v220,v[24:27] offset:12288\n"
"v_dot2_f32_bf16 v240,v30,v214,v216\n"
"v_dot2_f32_bf16 v241,v30,v215,v216\n"
"v_cvt_pk_bf16_f32 v28,v242,v243\n"
"v_dot2_f32_bf16 v242,v31,v214,v216\n"
"v_dot2_f32_bf16 v243,v31,v215,v216\n"
"v_cvt_pk_bf16_f32 v29,v244,v245\n"
"v_dot2_f32_bf16 v244,v32,v214,v216\n"
"v_dot2_f32_bf16 v245,v32,v215,v216\n"
"v_cvt_pk_bf16_f32 v30,v240,v241\n"
"v_dot2_f32_bf16 v240,v33,v214,v216\n"
"v_dot2_f32_bf16 v241,v33,v215,v216\n"
"v_cvt_pk_bf16_f32 v31,v242,v243\n"
"ds_write_b128 v220,v[28:31] offset:14336\n"
"v_dot2_f32_bf16 v242,v34,v214,v216\n"
"v_dot2_f32_bf16 v243,v34,v215,v216\n"
"v_cvt_pk_bf16_f32 v32,v244,v245\n"
"v_dot2_f32_bf16 v244,v35,v214,v216\n"
"v_dot2_f32_bf16 v245,v35,v215,v216\n"
"v_cvt_pk_bf16_f32 v33,v240,v241\n"
"v_cvt_pk_bf16_f32 v34,v242,v243\n"
"s_nop 0\n"
"v_cvt_pk_bf16_f32 v35,v244,v245\n"
"ds_write_b128 v220,v[32:35] offset:16384\n"
"s_cmp_eq_u32 s72,1\n"
"s_cbranch_scc1 .Lk_mlp_g1\n"
".Lk_mlp_code:\n"
"s_setprio 0\n"
"buffer_load_ushort v192,v193,s[24:27],s80 offen\n"
"v_mov_b32 v210,0\n"
"v_mov_b32 v211,0\n"
"v_mov_b32 v212,0\n"
"v_mov_b32 v213,0\n"
"s_waitcnt vmcnt(9)\n"
"s_add_u32 m0,s91,0\n"
"s_add_u32 s74,s80,0\n"
"buffer_load_dwordx4 v195,s[16:19],s74 offen lds\n"
"v_dot2c_f32_bf16 v210,v72,v208\n"
"v_dot2c_f32_bf16 v212,v72,v72\n"
"v_dot2c_f32_bf16 v211,v73,v208\n"
"v_dot2c_f32_bf16 v213,v73,v73\n"
"v_dot2c_f32_bf16 v210,v74,v208\n"
"v_dot2c_f32_bf16 v212,v74,v74\n"
"v_dot2c_f32_bf16 v211,v75,v208\n"
"v_dot2c_f32_bf16 v213,v75,v75\n"
"s_waitcnt vmcnt(9)\n"
"s_addk_i32 m0,8192\n"
"s_add_u32 s74,s74,s93\n"
"buffer_load_dwordx4 v195,s[16:19],s74 offen lds\n"
"v_dot2c_f32_bf16 v210,v76,v208\n"
"v_dot2c_f32_bf16 v212,v76,v76\n"
"v_dot2c_f32_bf16 v211,v77,v208\n"
"v_dot2c_f32_bf16 v213,v77,v77\n"
"v_dot2c_f32_bf16 v210,v78,v208\n"
"v_dot2c_f32_bf16 v212,v78,v78\n"
"v_dot2c_f32_bf16 v211,v79,v208\n"
"v_dot2c_f32_bf16 v213,v79,v79\n"
"s_waitcnt vmcnt(9)\n"
"s_addk_i32 m0,8192\n"
"s_add_u32 s74,s74,s93\n"
"buffer_load_dwordx4 v195,s[16:19],s74 offen lds\n"
"v_dot2c_f32_bf16 v210,v80,v208\n"
"v_dot2c_f32_bf16 v212,v80,v80\n"
"v_dot2c_f32_bf16 v211,v81,v208\n"
"v_dot2c_f32_bf16 v213,v81,v81\n"
"v_dot2c_f32_bf16 v210,v82,v208\n"
"v_dot2c_f32_bf16 v212,v82,v82\n"
"v_dot2c_f32_bf16 v211,v83,v208\n"
"v_dot2c_f32_bf16 v213,v83,v83\n"
"s_waitcnt vmcnt(9)\n"
"s_addk_i32 m0,8192\n"
"s_add_u32 s74,s74,s93\n"
"buffer_load_dwordx4 v195,s[16:19],s74 offen lds\n"
"v_dot2c_f32_bf16 v210,v84,v208\n"
"v_dot2c_f32_bf16 v212,v84,v84\n"
"v_dot2c_f32_bf16 v211,v85,v208\n"
"v_dot2c_f32_bf16 v213,v85,v85\n"
"v_dot2c_f32_bf16 v210,v86,v208\n"
"v_dot2c_f32_bf16 v212,v86,v86\n"
"v_dot2c_f32_bf16 v211,v87,v208\n"
"v_dot2c_f32_bf16 v213,v87,v87\n"
"s_waitcnt vmcnt(9)\n"
"s_addk_i32 m0,8192\n"
"s_add_u32 s74,s74,s93\n"
"buffer_load_dwordx4 v195,s[16:19],s74 offen lds\n"
"v_dot2c_f32_bf16 v210,v88,v208\n"
"v_dot2c_f32_bf16 v212,v88,v88\n"
"v_dot2c_f32_bf16 v211,v89,v208\n"
"v_dot2c_f32_bf16 v213,v89,v89\n"
"v_dot2c_f32_bf16 v210,v90,v208\n"
"v_dot2c_f32_bf16 v212,v90,v90\n"
"v_dot2c_f32_bf16 v211,v91,v208\n"
"v_dot2c_f32_bf16 v213,v91,v91\n"
"s_waitcnt vmcnt(9)\n"
"s_addk_i32 m0,8192\n"
"s_add_u32 s74,s74,s93\n"
"buffer_load_dwordx4 v195,s[16:19],s74 offen lds\n"
"v_dot2c_f32_bf16 v210,v92,v208\n"
"v_dot2c_f32_bf16 v212,v92,v92\n"
"v_dot2c_f32_bf16 v211,v93,v208\n"
"v_dot2c_f32_bf16 v213,v93,v93\n"
"v_dot2c_f32_bf16 v210,v94,v208\n"
"v_dot2c_f32_bf16 v212,v94,v94\n"
"v_dot2c_f32_bf16 v211,v95,v208\n"
"v_dot2c_f32_bf16 v213,v95,v95\n"
"s_waitcnt vmcnt(9)\n"
"s_addk_i32 m0,8192\n"
"s_add_u32 s74,s74,s93\n"
"buffer_load_dwordx4 v195,s[16:19],s74 offen lds\n"
"v_dot2c_f32_bf16 v210,v96,v208\n"
"v_dot2c_f32_bf16 v212,v96,v96\n"
"v_dot2c_f32_bf16 v211,v97,v208\n"
"v_dot2c_f32_bf16 v213,v97,v97\n"
"v_dot2c_f32_bf16 v210,v98,v208\n"
"v_dot2c_f32_bf16 v212,v98,v98\n"
"v_dot2c_f32_bf16 v211,v99,v208\n"
"v_dot2c_f32_bf16 v213,v99,v99\n"
"s_waitcnt vmcnt(9)\n"
"s_addk_i32 m0,8192\n"
"s_add_u32 s74,s74,s93\n"
"buffer_load_dwordx4 v195,s[16:19],s74 offen lds\n"
"v_dot2c_f32_bf16 v210,v100,v208\n"
"v_dot2c_f32_bf16 v212,v100,v100\n"
"v_dot2c_f32_bf16 v211,v101,v208\n"
"v_dot2c_f32_bf16 v213,v101,v101\n"
"v_dot2c_f32_bf16 v210,v102,v208\n"
"v_dot2c_f32_bf16 v212,v102,v102\n"
"v_dot2c_f32_bf16 v211,v103,v208\n"
"v_dot2c_f32_bf16 v213,v103,v103\n"
"s_waitcnt vmcnt(9)\n"
"s_addk_i32 m0,8192\n"
"s_add_u32 s74,s74,s93\n"
"buffer_load_dwordx4 v195,s[16:19],s74 offen lds\n"
"v_dot2c_f32_bf16 v210,v104,v208\n"
"v_dot2c_f32_bf16 v212,v104,v104\n"
"v_dot2c_f32_bf16 v211,v105,v208\n"
"v_dot2c_f32_bf16 v213,v105,v105\n"
"v_dot2c_f32_bf16 v210,v106,v208\n"
"v_dot2c_f32_bf16 v212,v106,v106\n"
"v_dot2c_f32_bf16 v211,v107,v208\n"
"v_dot2c_f32_bf16 v213,v107,v107\n"
"s_nop 1\n"
"v_add_f32 v210,v210,v211\n"
"v_add_f32 v212,v212,v213\n"
"s_nop 0\n"
"v_add_f32_dpp v210,v210,v210 quad_perm:[1,0,3,2] row_mask:0xf bank_mask:0xf\n"
"v_add_f32_dpp v212,v212,v212 quad_perm:[1,0,3,2] row_mask:0xf bank_mask:0xf\n"
"s_nop 0\n"
"v_add_f32_dpp v210,v210,v210 quad_perm:[2,3,0,1] row_mask:0xf bank_mask:0xf\n"
"v_add_f32_dpp v212,v212,v212 quad_perm:[2,3,0,1] row_mask:0xf bank_mask:0xf\n"
"s_nop 0\n"
"v_add_f32_dpp v210,v210,v210 row_half_mirror row_mask:0xf bank_mask:0xf\n"
"v_add_f32_dpp v212,v212,v212 row_half_mirror row_mask:0xf bank_mask:0xf\n"
"v_mul_f32 v218,0x3ae38e39,v210\n"
"v_mul_f32 v212,0x3ae38e39,v212\n"
"v_fma_f32 v212,-v218,v218,v212\n"
"v_max_f32 v212,0,v212\n"
"v_add_f32 v219,0x3727c5ac,v212\n"
"v_rsq_f32 v219,v219\n"
"s_nop 0\n"
"v_mul_f32 v240,v219,v219\n"
"v_fmaak_f32 v240,v240,v212,0x3727c5ac\n"
"v_rsq_f32 v240,v240\n"
"v_mul_f32 v212,0x3eeff558,v219\n"
"v_mul_f32 v240,v212,v240\n"
"ds_write_b64 v221,v[218:219] offset:32\n"
"v_cvt_pk_bf16_f32 v214,v240,0\n"
"v_lshlrev_b32 v215,16,v214\n"
"v_mul_f32_e64 v216,-v218,v215\n"
"s_waitcnt lgkmcnt(0)\n"
"s_barrier\n"
"s_waitcnt vmcnt(9)\n"
"v_lshlrev_b32 v192,16,v192\n"
"v_mul_f32 v192,0x3eeff558,v192\n"
"ds_write_b32 v194,v192\n"
"v_dot2_f32_bf16 v240,v72,v214,v216\n"
"v_dot2_f32_bf16 v241,v72,v215,v216\n"
"v_dot2_f32_bf16 v242,v73,v214,v216\n"
"v_dot2_f32_bf16 v243,v73,v215,v216\n"
"v_dot2_f32_bf16 v244,v74,v214,v216\n"
"v_dot2_f32_bf16 v245,v74,v215,v216\n"
"v_cvt_pk_bf16_f32 v72,v240,v241\n"
"v_dot2_f32_bf16 v240,v75,v214,v216\n"
"v_dot2_f32_bf16 v241,v75,v215,v216\n"
"v_cvt_pk_bf16_f32 v73,v242,v243\n"
"v_dot2_f32_bf16 v242,v76,v214,v216\n"
"v_dot2_f32_bf16 v243,v76,v215,v216\n"
"v_cvt_pk_bf16_f32 v74,v244,v245\n"
"v_dot2_f32_bf16 v244,v77,v214,v216\n"
"v_dot2_f32_bf16 v245,v77,v215,v216\n"
"v_cvt_pk_bf16_f32 v75,v240,v241\n"
"ds_read_b128 v[0:3],v174\n"
"ds_read_b128 v[4:7],v175\n"
".Lk_padm1:\n"
"s_cbranch_execz .Lk_padm5\n"
"v_dot2_f32_bf16 v240,v78,v214,v216\n"
"v_dot2_f32_bf16 v241,v78,v215,v216\n"
"v_cvt_pk_bf16_f32 v76,v242,v243\n"
"v_dot2_f32_bf16 v242,v79,v214,v216\n"
"v_dot2_f32_bf16 v243,v79,v215,v216\n"
"v_cvt_pk_bf16_f32 v77,v244,v245\n"
"v_dot2_f32_bf16 v244,v80,v214,v216\n"
"v_dot2_f32_bf16 v245,v80,v215,v216\n"
"v_cvt_pk_bf16_f32 v78,v240,v241\n"
"v_dot2_f32_bf16 v240,v81,v214,v216\n"
"v_dot2_f32_bf16 v241,v81,v215,v216\n"
"v_cvt_pk_bf16_f32 v79,v242,v243\n"
"ds_read_b128 v[8:11],v174 offset:2048\n"
"ds_read_b128 v[12:15],v175 offset:2048\n"
"v_dot2_f32_bf16 v242,v82,v214,v216\n"
"v_dot2_f32_bf16 v243,v82,v215,v216\n"
"v_cvt_pk_bf16_f32 v80,v244,v245\n"
"v_dot2_f32_bf16 v244,v83,v214,v216\n"
"v_dot2_f32_bf16 v245,v83,v215,v216\n"
"v_cvt_pk_bf16_f32 v81,v240,v241\n"
"v_dot2_f32_bf16 v240,v84,v214,v216\n"
"v_dot2_f32_bf16 v241,v84,v215,v216\n"
"v_cvt_pk_bf16_f32 v82,v242,v243\n"
"v_dot2_f32_bf16 v242,v85,v214,v216\n"
"v_dot2_f32_bf16 v243,v85,v215,v216\n"
"v_cvt_pk_bf16_f32 v83,v244,v245\n"
"ds_read_b128 v[16:19],v174 offset:4096\n"
"ds_read_b128 v[20:23],v175 offset:4096\n"
"v_dot2_f32_bf16 v244,v86,v214,v216\n"
"v_dot2_f32_bf16 v245,v86,v215,v216\n"
"v_cvt_pk_bf16_f32 v84,v240,v241\n"
"v_dot2_f32_bf16 v240,v87,v214,v216\n"
"v_dot2_f32_bf16 v241,v87,v215,v216\n"
"v_cvt_pk_bf16_f32 v85,v242,v243\n"
"v_dot2_f32_bf16 v242,v88,v214,v216\n"
"v_dot2_f32_bf16 v243,v88,v215,v216\n"
"v_cvt_pk_bf16_f32 v86,v244,v245\n"
"v_dot2_f32_bf16 v244,v89,v214,v216\n"
"v_dot2_f32_bf16 v245,v89,v215,v216\n"
"v_cvt_pk_bf16_f32 v87,v240,v241\n"
"ds_read_b128 v[24:27],v174 offset:6144\n"
"ds_read_b128 v[28:31],v175 offset:6144\n"
"v_dot2_f32_bf16 v240,v90,v214,v216\n"
"v_dot2_f32_bf16 v241,v90,v215,v216\n"
"v_cvt_pk_bf16_f32 v88,v242,v243\n"
"v_dot2_f32_bf16 v242,v91,v214,v216\n"
"v_dot2_f32_bf16 v243,v91,v215,v216\n"
"v_cvt_pk_bf16_f32 v89,v244,v245\n"
"v_dot2_f32_bf16 v244,v92,v214,v216\n"
"v_dot2_f32_bf16 v245,v92,v215,v216\n"
"v_cvt_pk_bf16_f32 v90,v240,v241\n"
"v_dot2_f32_bf16 v240,v93,v214,v216\n"
"v_dot2_f32_bf16 v241,v93,v215,v216\n"
"v_cvt_pk_bf16_f32 v91,v242,v243\n"
"ds_read_b128 v[32:35],v174 offset:8192\n"
"ds_read_b128 v[36:39],v175 offset:8192\n"
"v_dot2_f32_bf16 v242,v94,v214,v216\n"
"v_dot2_f32_bf16 v243,v94,v215,v216\n"
"v_cvt_pk_bf16_f32 v92,v244,v245\n"
"v_dot2_f32_bf16 v244,v95,v214,v216\n"
"v_dot2_f32_bf16 v245,v95,v215,v216\n"
"v_cvt_pk_bf16_f32 v93,v240,v241\n"
"v_dot2_f32_bf16 v240,v96,v214,v216\n"
"v_dot2_f32_bf16 v241,v96,v215,v216\n"
"v_cvt_pk_bf16_f32 v94,v242,v243\n"
"v_dot2_f32_bf16 v242,v97,v214,v216\n"
"v_dot2_f32_bf16 v243,v97,v215,v216\n"
"v_cvt_pk_bf16_f32 v95,v244,v245\n"
"ds_read_b128 v[40:43],v174 offset:10240\n"
"ds_read_b128 v[44:47],v175 offset:10240\n"
"v_dot2_f32_bf16 v244,v98,v214,v216\n"
"v_dot2_f32_bf16 v245,v98,v215,v216\n"
"v_cvt_pk_bf16_f32 v96,v240,v241\n"
"v_dot2_f32_bf16 v240,v99,v214,v216\n"
"v_dot2_f32_bf16 v241,v99,v215,v216\n"
"v_cvt_pk_bf16_f32 v97,v242,v243\n"
"v_dot2_f32_bf16 v242,v100,v214,v216\n"
"v_dot2_f32_bf16 v243,v100,v215,v216\n"
"v_cvt_pk_bf16_f32 v98,v244,v245\n"
"v_dot2_f32_bf16 v244,v101,v214,v216\n"
"v_dot2_f32_bf16 v245,v101,v215,v216\n"
"v_cvt_pk_bf16_f32 v99,v240,v241\n"
"ds_read_b128 v[48:51],v174 offset:12288\n"
"ds_read_b128 v[52:55],v175 offset:12288\n"
"v_dot2_f32_bf16 v240,v102,v214,v216\n"
"v_dot2_f32_bf16 v241,v102,v215,v216\n"
"v_cvt_pk_bf16_f32 v100,v242,v243\n"
"v_dot2_f32_bf16 v242,v103,v214,v216\n"
"v_dot2_f32_bf16 v243,v103,v215,v216\n"
"v_cvt_pk_bf16_f32 v101,v244,v245\n"
"v_dot2_f32_bf16 v244,v104,v214,v216\n"
"v_dot2_f32_bf16 v245,v104,v215,v216\n"
"v_cvt_pk_bf16_f32 v102,v240,v241\n"
"v_dot2_f32_bf16 v240,v105,v214,v216\n"
"v_dot2_f32_bf16 v241,v105,v215,v216\n"
"v_cvt_pk_bf16_f32 v103,v242,v243\n"
"ds_read_b128 v[56:59],v174 offset:14336\n"
"ds_read_b128 v[60:63],v175 offset:14336\n"
"v_dot2_f32_bf16 v242,v106,v214,v216\n"
"v_dot2_f32_bf16 v243,v106,v215,v216\n"
"v_cvt_pk_bf16_f32 v104,v244,v245\n"
"v_dot2_f32_bf16 v244,v107,v214,v216\n"
"v_dot2_f32_bf16 v245,v107,v215,v216\n"
"v_cvt_pk_bf16_f32 v105,v240,v241\n"
"v_cvt_pk_bf16_f32 v106,v242,v243\n"
"s_nop 0\n"
"v_cvt_pk_bf16_f32 v107,v244,v245\n"
"ds_read_b128 v[64:67],v174 offset:16384\n"
"ds_read_b128 v[68:71],v175 offset:16384\n"
"s_waitcnt lgkmcnt(0)\n"
"s_barrier\n"
"s_and_b32 s75,s10,1\n"
"s_mul_i32 s75,s75,24\n"
"s_lshr_b32 s76,s10,3\n"
"s_add_u32 s75,s75,s76\n"
"s_lshl_b32 s75,s75,2\n"
"s_add_u32 s75,s75,s71\n"
"s_mul_i32 s76,s75,27648\n"
"s_mul_i32 s77,s69,2304\n"
"s_add_u32 s76,s76,s77\n"
"s_add_u32 s76,s76,128\n"
"v_mul_u32_u24 v246,205,v254\n"
"v_lshrrev_b32 v246,10,v246\n"
"v_mul_u32_u24 v247,43,v246\n"
"v_lshrrev_b32 v247,7,v247\n"
"v_mul_u32_u24 v246,128,v246\n"
"v_mul_u32_u24 v247,6912,v247\n"
"v_lshl_add_u32 v246,v254,7,v246\n"
"v_add_u32 v246,v246,v247\n"
"s_mov_b32 m0,161280\n"
"s_bfm_b64 exec,45,0\n"
"buffer_load_dword v246,s[16:19],s76 offen lds\n"
"s_mov_b64 exec,-1\n"
"s_and_b32 s75,s10,1\n"
"s_mul_i32 s75,s75,24\n"
"s_lshr_b32 s76,s10,3\n"
"s_add_u32 s75,s75,s76\n"
"s_lshl_b32 s75,s75,2\n"
"s_add_u32 s75,s75,s71\n"
"s_mul_i32 s95,s75,6912\n"
"s_mul_i32 s77,s69,1327104\n"
"s_add_u32 s95,s95,s77\n"
"v_lshlrev_b32 v246,7,v254\n"
"s_add_u32 s95,s95,3072\n"
"s_mov_b32 m0,161280\n"
"s_bfm_b64 exec,30,0\n"
"buffer_load_dword v246,s[20:23],s95 offen lds\n"
"s_mov_b64 exec,-1\n"
"ds_write_b128 v220,v[72:75]\n"
"ds_write_b128 v220,v[76:79] offset:2048\n"
"ds_write_b128 v220,v[80:83] offset:4096\n"
"ds_write_b128 v220,v[84:87] offset:6144\n"
"ds_write_b128 v220,v[88:91] offset:8192\n"
"ds_write_b128 v220,v[92:95] offset:10240\n"
"ds_write_b128 v220,v[96:99] offset:12288\n"
"ds_write_b128 v220,v[100:103] offset:14336\n"
"ds_write_b128 v220,v[104:107] offset:16384\n"
"s_waitcnt lgkmcnt(0)\n"
"s_waitcnt vmcnt(2)\n"
"s_barrier\n"
"s_setprio 3\n"
"ds_read_b128 v[72:75],v174\n"
"ds_read_b128 v[76:79],v175\n"
"ds_read_b128 v[80:83],v174 offset:2048\n"
"ds_read_b128 v[84:87],v175 offset:2048\n"
"ds_read_b128 v[88:91],v174 offset:4096\n"
"ds_read_b128 v[92:95],v175 offset:4096\n"
"ds_read_b128 v[96:99],v174 offset:6144\n"
"ds_read_b128 v[100:103],v175 offset:6144\n"
"ds_read_b128 v[104:107],v174 offset:8192\n"
"ds_read_b128 v[108:111],v175 offset:8192\n"
"ds_read_b128 v[112:115],v174 offset:10240\n"
"ds_read_b128 v[116:119],v175 offset:10240\n"
"ds_read_b128 v[120:123],v174 offset:12288\n"
"ds_read_b128 v[124:127],v175 offset:12288\n"
"ds_read_b128 v[128:131],v174 offset:14336\n"
"ds_read_b128 v[132:135],v175 offset:14336\n"
"ds_read_b128 v[136:139],v174 offset:16384\n"
"ds_read_b128 v[140:143],v175 offset:16384\n"
"ds_read_b128 v[240:243],v253\n"
"ds_read_b128 v[244:247],v253 offset:64\n"
"v_lshrrev_b32 v209,4,v254\n"
"v_lshlrev_b32 v254,7,v254\n"
"v_lshl_or_b32 v254,v209,29,v254\n"
"ds_read_b64_tr_b16 v[208:209],v248\n"
"ds_read_b64_tr_b16 v[210:211],v248 offset:2048\n"
"ds_read_b64_tr_b16 v[212:213],v248 offset:4096\n"
"ds_read_b64_tr_b16 v[214:215],v248 offset:6144\n"
"ds_read_b64_tr_b16 v[216:217],v248 offset:8192\n"
"ds_read_b64_tr_b16 v[218:219],v248 offset:10240\n"
"ds_read_b64_tr_b16 v[220:221],v248 offset:12288\n"
"ds_read_b64_tr_b16 v[222:223],v248 offset:14336\n"
"ds_read_b64_tr_b16 v[224:225],v248 offset:16384\n"
"ds_read_b64_tr_b16 v[226:227],v248 offset:18432\n"
"ds_read_b64_tr_b16 v[228:229],v248 offset:20480\n"
"ds_read_b64_tr_b16 v[230:231],v248 offset:22528\n"
"s_waitcnt lgkmcnt(14)\n"
"s_setprio 1\n"
"s_waitcnt lgkmcnt(13)\n"
"s_waitcnt lgkmcnt(8)\n"
"v_mfma_f32_16x16x32_bf16 v[192:195],v[208:211],v[0:3],v[240:243]\n"
"ds_read_b64_tr_b16 v[232:233],v248 offset:24576\n"
"v_mfma_f32_16x16x32_bf16 v[192:195],v[212:215],v[4:7],v[192:195]\n"
"ds_read_b64_tr_b16 v[234:235],v248 offset:26624\n"
"v_mfma_f32_16x16x32_bf16 v[196:199],v[208:211],v[72:75],v[240:243]\n"
"ds_read_b64_tr_b16 v[236:237],v248 offset:28672\n"
"v_mfma_f32_16x16x32_bf16 v[196:199],v[212:215],v[76:79],v[196:199]\n"
"ds_read_b64_tr_b16 v[238:239],v248 offset:30720\n"
"s_waitcnt lgkmcnt(8)\n"
"v_mfma_f32_16x16x32_bf16 v[196:199],v[216:219],v[80:83],v[196:199]\n"
"ds_read_b64_tr_b16 v[208:209],v248 offset:32768\n"
"v_mfma_f32_16x16x32_bf16 v[196:199],v[220:223],v[84:87],v[196:199]\n"
"ds_read_b64_tr_b16 v[210:211],v248 offset:34816\n"
"v_mfma_f32_16x16x32_bf16 v[192:195],v[216:219],v[8:11],v[192:195]\n"
"ds_read_b64_tr_b16 v[212:213],v248 offset:36864\n"
"v_mfma_f32_16x16x32_bf16 v[192:195],v[220:223],v[12:15],v[192:195]\n"
"ds_read_b64_tr_b16 v[214:215],v248 offset:38912\n"
"s_waitcnt lgkmcnt(8)\n"
"v_mfma_f32_16x16x32_bf16 v[192:195],v[224:227],v[16:19],v[192:195]\n"
"ds_read_b64_tr_b16 v[216:217],v249\n"
"v_mfma_f32_16x16x32_bf16 v[192:195],v[228:231],v[20:23],v[192:195]\n"
"ds_read_b64_tr_b16 v[218:219],v249 offset:2048\n"
"v_mfma_f32_16x16x32_bf16 v[196:199],v[224:227],v[88:91],v[196:199]\n"
"ds_read_b64_tr_b16 v[220:221],v249 offset:4096\n"
"v_mfma_f32_16x16x32_bf16 v[196:199],v[228:231],v[92:95],v[196:199]\n"
"ds_read_b64_tr_b16 v[222:223],v249 offset:6144\n"
"s_waitcnt lgkmcnt(8)\n"
"v_mfma_f32_16x16x32_bf16 v[196:199],v[232:235],v[96:99],v[196:199]\n"
"ds_read_b64_tr_b16 v[224:225],v249 offset:8192\n"
"v_mfma_f32_16x16x32_bf16 v[196:199],v[236:239],v[100:103],v[196:199]\n"
"ds_read_b64_tr_b16 v[226:227],v249 offset:10240\n"
"v_mfma_f32_16x16x32_bf16 v[192:195],v[232:235],v[24:27],v[192:195]\n"
"ds_read_b64_tr_b16 v[228:229],v249 offset:12288\n"
"v_mfma_f32_16x16x32_bf16 v[192:195],v[236:239],v[28:31],v[192:195]\n"
"ds_read_b64_tr_b16 v[230:231],v249 offset:14336\n"
"s_waitcnt lgkmcnt(8)\n"
"v_mfma_f32_16x16x32_bf16 v[192:195],v[208:211],v[32:35],v[192:195]\n"
"ds_read_b64_tr_b16 v[232:233],v249 offset:16384\n"
"v_mfma_f32_16x16x32_bf16 v[192:195],v[212:215],v[36:39],v[192:195]\n"
"ds_read_b64_tr_b16 v[234:235],v249 offset:18432\n"
"v_mfma_f32_16x16x32_bf16 v[196:199],v[208:211],v[104:107],v[196:199]\n"
"ds_read_b64_tr_b16 v[236:237],v249 offset:20480\n"
"v_mfma_f32_16x16x32_bf16 v[196:199],v[212:215],v[108:111],v[196:199]\n"
"ds_read_b64_tr_b16 v[238:239],v249 offset:22528\n"
"s_waitcnt lgkmcnt(8)\n"
"v_mfma_f32_16x16x32_bf16 v[200:203],v[216:219],v[0:3],v[244:247]\n"
"ds_read_b64_tr_b16 v[208:209],v249 offset:24576\n"
"v_mfma_f32_16x16x32_bf16 v[200:203],v[220:223],v[4:7],v[200:203]\n"
"ds_read_b64_tr_b16 v[210:211],v249 offset:26624\n"
"s_barrier\n"
"v_mfma_f32_16x16x32_bf16 v[204:207],v[216:219],v[72:75],v[244:247]\n"
"ds_read_b64_tr_b16 v[212:213],v249 offset:28672\n"
"v_mfma_f32_16x16x32_bf16 v[204:207],v[220:223],v[76:79],v[204:207]\n"
"ds_read_b64_tr_b16 v[214:215],v249 offset:30720\n"
"s_waitcnt lgkmcnt(8)\n"
"v_mfma_f32_16x16x32_bf16 v[204:207],v[224:227],v[80:83],v[204:207]\n"
"ds_read_b64_tr_b16 v[216:217],v249 offset:32768\n"
"v_mfma_f32_16x16x32_bf16 v[204:207],v[228:231],v[84:87],v[204:207]\n"
"ds_read_b64_tr_b16 v[218:219],v249 offset:34816\n"
"v_mfma_f32_16x16x32_bf16 v[200:203],v[224:227],v[8:11],v[200:203]\n"
"ds_read_b64_tr_b16 v[220:221],v249 offset:36864\n"
"v_mfma_f32_16x16x32_bf16 v[200:203],v[228:231],v[12:15],v[200:203]\n"
"ds_read_b64_tr_b16 v[222:223],v249 offset:38912\n"
"s_waitcnt lgkmcnt(8)\n"
"v_mfma_f32_16x16x32_bf16 v[200:203],v[232:235],v[16:19],v[200:203]\n"
"ds_read_b64_tr_b16 v[224:225],v249 offset:40960\n"
"v_mfma_f32_16x16x32_bf16 v[200:203],v[236:239],v[20:23],v[200:203]\n"
"ds_read_b64_tr_b16 v[226:227],v249 offset:43008\n"
"v_mfma_f32_16x16x32_bf16 v[204:207],v[232:235],v[88:91],v[204:207]\n"
"ds_read_b64_tr_b16 v[228:229],v249 offset:45056\n"
"v_mfma_f32_16x16x32_bf16 v[204:207],v[236:239],v[92:95],v[204:207]\n"
"ds_read_b64_tr_b16 v[230:231],v249 offset:47104\n"
"s_waitcnt lgkmcnt(8)\n"
"v_mfma_f32_16x16x32_bf16 v[204:207],v[208:211],v[96:99],v[204:207]\n"
"ds_read_b64_tr_b16 v[232:233],v249 offset:49152\n"
"v_mfma_f32_16x16x32_bf16 v[204:207],v[212:215],v[100:103],v[204:207]\n"
"ds_read_b64_tr_b16 v[234:235],v249 offset:51200\n"
"v_mfma_f32_16x16x32_bf16 v[200:203],v[208:211],v[24:27],v[200:203]\n"
"ds_read_b64_tr_b16 v[236:237],v249 offset:53248\n"
"v_mfma_f32_16x16x32_bf16 v[200:203],v[212:215],v[28:31],v[200:203]\n"
"ds_read_b64_tr_b16 v[238:239],v249 offset:55296\n"
"s_waitcnt lgkmcnt(8)\n"
"v_mfma_f32_16x16x32_bf16 v[200:203],v[216:219],v[32:35],v[200:203]\n"
"ds_read_b64_tr_b16 v[208:209],v249 offset:57344\n"
"v_mfma_f32_16x16x32_bf16 v[200:203],v[220:223],v[36:39],v[200:203]\n"
"ds_read_b64_tr_b16 v[210:211],v249 offset:59392\n"
"v_mfma_f32_16x16x32_bf16 v[204:207],v[216:219],v[104:107],v[204:207]\n"
"ds_read_b64_tr_b16 v[212:213],v249 offset:61440\n"
"v_mfma_f32_16x16x32_bf16 v[204:207],v[220:223],v[108:111],v[204:207]\n"
"ds_read_b64_tr_b16 v[214:215],v249 offset:63488\n"
"s_waitcnt lgkmcnt(8)\n"
"v_mfma_f32_16x16x32_bf16 v[204:207],v[224:227],v[112:115],v[204:207]\n"
"v_add_u32 v249,0x10000,v249\n"
"ds_read_b64_tr_b16 v[216:217],v249\n"
"v_mfma_f32_16x16x32_bf16 v[204:207],v[228:231],v[116:119],v[204:207]\n"
"ds_read_b64_tr_b16 v[218:219],v249 offset:2048\n"
"s_setprio 2\n"
"v_mfma_f32_16x16x32_bf16 v[200:203],v[224:227],v[40:43],v[200:203]\n"
"ds_read_b64_tr_b16 v[220:221],v249 offset:4096\n"
"v_mfma_f32_16x16x32_bf16 v[200:203],v[228:231],v[44:47],v[200:203]\n"
"ds_read_b64_tr_b16 v[222:223],v249 offset:6144\n"
"s_waitcnt lgkmcnt(8)\n"
"v_mfma_f32_16x16x32_bf16 v[200:203],v[232:235],v[48:51],v[200:203]\n"
"ds_read_b64_tr_b16 v[224:225],v248 offset:40960\n"
"v_mfma_f32_16x16x32_bf16 v[200:203],v[236:239],v[52:55],v[200:203]\n"
"ds_read_b64_tr_b16 v[226:227],v248 offset:43008\n"
"v_mfma_f32_16x16x32_bf16 v[204:207],v[232:235],v[120:123],v[204:207]\n"
"ds_read_b64_tr_b16 v[228:229],v248 offset:45056\n"
"v_mfma_f32_16x16x32_bf16 v[204:207],v[236:239],v[124:127],v[204:207]\n"
"ds_read_b64_tr_b16 v[230:231],v248 offset:47104\n"
"s_waitcnt lgkmcnt(8)\n"
"v_mfma_f32_16x16x32_bf16 v[204:207],v[208:211],v[128:131],v[204:207]\n"
"ds_read_b64_tr_b16 v[232:233],v248 offset:49152\n"
"v_mfma_f32_16x16x32_bf16 v[204:207],v[212:215],v[132:135],v[204:207]\n"
"ds_read_b64_tr_b16 v[234:235],v248 offset:51200\n"
"v_mfma_f32_16x16x32_bf16 v[200:203],v[208:211],v[56:59],v[200:203]\n"
"ds_read_b64_tr_b16 v[236:237],v248 offset:53248\n"
"v_mfma_f32_16x16x32_bf16 v[200:203],v[212:215],v[60:63],v[200:203]\n"
"ds_read_b64_tr_b16 v[238:239],v248 offset:55296\n"
"s_waitcnt lgkmcnt(8)\n"
"v_mfma_f32_16x16x32_bf16 v[200:203],v[216:219],v[64:67],v[200:203]\n"
"ds_read_b64_tr_b16 v[208:209],v248 offset:57344\n"
"v_mfma_f32_16x16x32_bf16 v[200:203],v[220:223],v[68:71],v[200:203]\n"
"ds_read_b64_tr_b16 v[210:211],v248 offset:59392\n"
"v_mfma_f32_16x16x32_bf16 v[204:207],v[216:219],v[136:139],v[204:207]\n"
"ds_read_b64_tr_b16 v[212:213],v248 offset:61440\n"
"v_mfma_f32_16x16x32_bf16 v[204:207],v[220:223],v[140:143],v[204:207]\n"
"ds_read_b64_tr_b16 v[214:215],v248 offset:63488\n"
"s_waitcnt lgkmcnt(8)\n"
"v_mfma_f32_16x16x32_bf16 v[192:195],v[224:227],v[40:43],v[192:195]\n"
"v_add_u32 v248,0x10000,v248\n"
"ds_read_b64_tr_b16 v[216:217],v248\n"
"v_mfma_f32_16x16x32_bf16 v[192:195],v[228:231],v[44:47],v[192:195]\n"
"ds_read_b64_tr_b16 v[218:219],v248 offset:2048\n"
"v_mfma_f32_16x16x32_bf16 v[196:199],v[224:227],v[112:115],v[196:199]\n"
"ds_read_b64_tr_b16 v[220:221],v248 offset:4096\n"
"v_mfma_f32_16x16x32_bf16 v[196:199],v[228:231],v[116:119],v[196:199]\n"
"ds_read_b64_tr_b16 v[222:223],v248 offset:6144\n"
"s_waitcnt lgkmcnt(8)\n"
"v_mfma_f32_16x16x32_bf16 v[196:199],v[232:235],v[120:123],v[196:199]\n"
"v_mfma_f32_16x16x32_bf16 v[196:199],v[236:239],v[124:127],v[196:199]\n"
"v_mfma_f32_16x16x32_bf16 v[192:195],v[232:235],v[48:51],v[192:195]\n"
"v_mfma_f32_16x16x32_bf16 v[192:195],v[236:239],v[52:55],v[192:195]\n"
"s_waitcnt lgkmcnt(4)\n"
"v_mfma_f32_16x16x32_bf16 v[192:195],v[208:211],v[56:59],v[192:195]\n"
"v_mfma_f32_16x16x32_bf16 v[192:195],v[212:215],v[60:63],v[192:195]\n"
"v_add_u32 v248,8192,v248\n"
"v_mfma_f32_16x16x32_bf16 v[196:199],v[208:211],v[128:131],v[196:199]\n"
"v_mfma_f32_16x16x32_bf16 v[196:199],v[212:215],v[132:135],v[196:199]\n"
"v_add_u32 v249,8192,v249\n"
"s_waitcnt lgkmcnt(0)\n"
"v_mfma_f32_16x16x32_bf16 v[196:199],v[216:219],v[136:139],v[196:199]\n"
"v_mfma_f32_16x16x32_bf16 v[196:199],v[220:223],v[140:143],v[196:199]\n"
"v_mfma_f32_16x16x32_bf16 v[192:195],v[216:219],v[64:67],v[192:195]\n"
"v_mfma_f32_16x16x32_bf16 v[192:195],v[220:223],v[68:71],v[192:195]\n"
"s_setprio 0\n"
"v_fma_f32 v244,-v200,v200,s79\n"
"v_fma_f32 v245,-v201,v201,s79\n"
"v_fma_f32 v246,-v202,v202,s79\n"
"v_fma_f32 v247,-v203,v203,s79\n"
"s_add_u32 s92,s80,2949248\n"
"s_add_u32 m0,s91,114688\n"
"v_mul_f32 v244,v244,v200\n"
"v_mul_f32 v245,v245,v201\n"
"buffer_load_dwordx4 v250,s[16:19],s92 offen lds\n"
"v_mul_f32 v246,v246,v202\n"
"v_mul_f32 v247,v247,v203\n"
"v_exp_f32 v244,v244\n"
"v_exp_f32 v245,v245\n"
"v_exp_f32 v246,v246\n"
"v_exp_f32 v247,v247\n"
"v_add_f32 v244,1.0,v244\n"
"v_add_f32 v245,1.0,v245\n"
"v_add_f32 v246,1.0,v246\n"
"v_add_f32 v247,1.0,v247\n"
"v_rcp_f32 v244,v244\n"
"v_rcp_f32 v245,v245\n"
"v_rcp_f32 v246,v246\n"
"s_add_u32 s90,s92,s93\n"
"v_rcp_f32 v247,v247\n"
"buffer_load_dwordx4 v191,s[16:19],s92 offen offset:1024 lds\n"
"v_mul_f32 v244,v244,v200\n"
"v_mul_f32 v245,v245,v201\n"
"v_mul_f32 v246,v246,v202\n"
"v_mul_f32 v247,v247,v203\n"
"v_cvt_pk_bf16_f32 v146,v244,v245\n"
"v_cvt_pk_bf16_f32 v147,v246,v247\n"
"v_fma_f32 v240,-v192,v192,s79\n"
"v_fma_f32 v241,-v193,v193,s79\n"
"v_fma_f32 v242,-v194,v194,s79\n"
"v_fma_f32 v243,-v195,v195,s79\n"
"v_mul_f32 v240,v240,v192\n"
"v_mul_f32 v241,v241,v193\n"
"v_mul_f32 v242,v242,v194\n"
"v_mul_f32 v243,v243,v195\n"
"v_exp_f32 v240,v240\n"
"v_exp_f32 v241,v241\n"
"v_exp_f32 v242,v242\n"
"s_addk_i32 m0,8192\n"
"v_exp_f32 v243,v243\n"
"buffer_load_dwordx4 v250,s[16:19],s90 offen lds\n"
"v_add_f32 v240,1.0,v240\n"
"v_add_f32 v241,1.0,v241\n"
"v_add_f32 v242,1.0,v242\n"
"v_add_f32 v243,1.0,v243\n"
"v_rcp_f32 v240,v240\n"
"v_rcp_f32 v241,v241\n"
"v_rcp_f32 v242,v242\n"
"v_rcp_f32 v243,v243\n"
"v_mul_f32 v240,v240,v192\n"
"v_mul_f32 v241,v241,v193\n"
"v_mul_f32 v242,v242,v194\n"
"v_mul_f32 v243,v243,v195\n"
"v_cvt_pk_bf16_f32 v144,v240,v241\n"
"v_cvt_pk_bf16_f32 v145,v242,v243\n"
"v_fma_f32 v240,-v196,v196,s79\n"
"v_fma_f32 v241,-v197,v197,s79\n"
"v_fma_f32 v242,-v198,v198,s79\n"
"v_fma_f32 v243,-v199,v199,s79\n"
"buffer_load_dwordx4 v191,s[16:19],s90 offen offset:1024 lds\n"
"v_mul_f32 v240,v240,v196\n"
"v_mul_f32 v241,v241,v197\n"
"v_mul_f32 v242,v242,v198\n"
"v_mul_f32 v243,v243,v199\n"
"v_exp_f32 v240,v240\n"
"v_exp_f32 v241,v241\n"
"v_exp_f32 v242,v242\n"
"v_exp_f32 v243,v243\n"
"v_add_f32 v240,1.0,v240\n"
"v_add_f32 v241,1.0,v241\n"
"v_add_f32 v242,1.0,v242\n"
"v_add_f32 v243,1.0,v243\n"
"v_rcp_f32 v240,v240\n"
"v_rcp_f32 v241,v241\n"
"v_rcp_f32 v242,v242\n"
"v_rcp_f32 v243,v243\n"
"v_mul_f32 v240,v240,v196\n"
"v_mul_f32 v241,v241,v197\n"
"v_mul_f32 v242,v242,v198\n"
"v_mul_f32 v243,v243,v199\n"
"s_barrier\n"
"v_cvt_pk_bf16_f32 v148,v240,v241\n"
"v_cvt_pk_bf16_f32 v149,v242,v243\n"
"v_fma_f32 v244,-v204,v204,s79\n"
"v_fma_f32 v245,-v205,v205,s79\n"
"v_fma_f32 v246,-v206,v206,s79\n"
"v_fma_f32 v247,-v207,v207,s79\n"
"v_mul_f32 v244,v244,v204\n"
"v_mul_f32 v245,v245,v205\n"
"v_mul_f32 v246,v246,v206\n"
"v_mul_f32 v247,v247,v207\n"
"v_exp_f32 v244,v244\n"
"v_exp_f32 v245,v245\n"
"v_exp_f32 v246,v246\n"
"v_exp_f32 v247,v247\n"
"v_add_f32 v244,1.0,v244\n"
"v_add_f32 v245,1.0,v245\n"
"v_add_f32 v246,1.0,v246\n"
"v_add_f32 v247,1.0,v247\n"
"v_rcp_f32 v244,v244\n"
"s_setprio 3\n"
"v_rcp_f32 v245,v245\n"
"ds_read_b128 v[240:243],v253 offset:256\n"
"ds_read_b64_tr_b16 v[208:209],v248\n"
"v_rcp_f32 v246,v246\n"
"ds_read_b64_tr_b16 v[210:211],v248 offset:2048\n"
"ds_read_b64_tr_b16 v[212:213],v248 offset:4096\n"
"v_rcp_f32 v247,v247\n"
"ds_read_b64_tr_b16 v[214:215],v248 offset:6144\n"
"ds_read_b64_tr_b16 v[216:217],v248 offset:8192\n"
"v_mul_f32 v244,v244,v204\n"
"v_mul_f32 v245,v245,v205\n"
"ds_read_b64_tr_b16 v[218:219],v248 offset:10240\n"
"ds_read_b64_tr_b16 v[220:221],v248 offset:12288\n"
"v_mul_f32 v246,v246,v206\n"
"v_mul_f32 v247,v247,v207\n"
"ds_read_b64_tr_b16 v[222:223],v248 offset:14336\n"
"ds_read_b64_tr_b16 v[224:225],v248 offset:16384\n"
"v_cvt_pk_bf16_f32 v150,v244,v245\n"
"v_cvt_pk_bf16_f32 v151,v246,v247\n"
"ds_read_b64_tr_b16 v[226:227],v248 offset:18432\n"
"ds_read_b64_tr_b16 v[228:229],v248 offset:20480\n"
"ds_read_b64_tr_b16 v[230:231],v248 offset:22528\n"
"s_setprio 1\n"
"s_waitcnt lgkmcnt(12)\n"
"s_waitcnt lgkmcnt(8)\n"
"v_mfma_f32_16x16x32_bf16 v[192:195],v[208:211],v[0:3],v[240:243]\n"
"ds_read_b64_tr_b16 v[232:233],v248 offset:24576\n"
"ds_read_b128 v[244:247],v253 offset:320\n"
"v_mfma_f32_16x16x32_bf16 v[192:195],v[212:215],v[4:7],v[192:195]\n"
"ds_read_b64_tr_b16 v[234:235],v248 offset:26624\n"
"s_add_u32 s78,s84,0\n"
"v_mfma_f32_16x16x32_bf16 v[196:199],v[208:211],v[72:75],v[240:243]\n"
"ds_read_b64_tr_b16 v[236:237],v248 offset:28672\n"
"v_mfma_f32_16x16x32_bf16 v[196:199],v[212:215],v[76:79],v[196:199]\n"
"ds_read_b64_tr_b16 v[238:239],v248 offset:30720\n"
"s_waitcnt lgkmcnt(9)\n"
"v_mfma_f32_16x16x32_bf16 v[196:199],v[216:219],v[80:83],v[196:199]\n"
"ds_read_b64_tr_b16 v[208:209],v248 offset:32768\n"
"v_mfma_f32_16x16x32_bf16 v[196:199],v[220:223],v[84:87],v[196:199]\n"
"ds_read_b64_tr_b16 v[210:211],v248 offset:34816\n"
"v_mfma_f32_16x16x32_bf16 v[192:195],v[216:219],v[8:11],v[192:195]\n"
"ds_read_b64_tr_b16 v[212:213],v248 offset:36864\n"
"buffer_store_dwordx4 v[144:147],v252,s[32:35],s78 offen sc1\n"
"v_mfma_f32_16x16x32_bf16 v[192:195],v[220:223],v[12:15],v[192:195]\n"
"ds_read_b64_tr_b16 v[214:215],v248 offset:38912\n"
"s_waitcnt lgkmcnt(9)\n"
"v_mfma_f32_16x16x32_bf16 v[192:195],v[224:227],v[16:19],v[192:195]\n"
"ds_read_b64_tr_b16 v[216:217],v249\n"
"v_mfma_f32_16x16x32_bf16 v[192:195],v[228:231],v[20:23],v[192:195]\n"
"ds_read_b64_tr_b16 v[218:219],v249 offset:2048\n"
"v_mfma_f32_16x16x32_bf16 v[196:199],v[224:227],v[88:91],v[196:199]\n"
"ds_read_b64_tr_b16 v[220:221],v249 offset:4096\n"
"v_mfma_f32_16x16x32_bf16 v[196:199],v[228:231],v[92:95],v[196:199]\n"
"ds_read_b64_tr_b16 v[222:223],v249 offset:6144\n"
"s_waitcnt lgkmcnt(8)\n"
"v_mfma_f32_16x16x32_bf16 v[196:199],v[232:235],v[96:99],v[196:199]\n"
"ds_read_b64_tr_b16 v[224:225],v249 offset:8192\n"
"buffer_store_dwordx4 v[148:151],v252,s[32:35],s78 offen offset:1024 sc1\n"
"v_mfma_f32_16x16x32_bf16 v[196:199],v[236:239],v[100:103],v[196:199]\n"
"ds_read_b64_tr_b16 v[226:227],v249 offset:10240\n"
"s_add_u32 s92,s80,590080\n"
"v_mfma_f32_16x16x32_bf16 v[192:195],v[232:235],v[24:27],v[192:195]\n"
"ds_read_b64_tr_b16 v[228:229],v249 offset:12288\n"
"s_add_u32 m0,s91,8192\n"
"v_mfma_f32_16x16x32_bf16 v[192:195],v[236:239],v[28:31],v[192:195]\n"
"ds_read_b64_tr_b16 v[230:231],v249 offset:14336\n"
"s_waitcnt lgkmcnt(8)\n"
"v_mfma_f32_16x16x32_bf16 v[192:195],v[208:211],v[32:35],v[192:195]\n"
"ds_read_b64_tr_b16 v[232:233],v249 offset:16384\n"
"v_mfma_f32_16x16x32_bf16 v[192:195],v[212:215],v[36:39],v[192:195]\n"
"ds_read_b64_tr_b16 v[234:235],v249 offset:18432\n"
"v_mfma_f32_16x16x32_bf16 v[196:199],v[208:211],v[104:107],v[196:199]\n"
"ds_read_b64_tr_b16 v[236:237],v249 offset:20480\n"
"v_mfma_f32_16x16x32_bf16 v[196:199],v[212:215],v[108:111],v[196:199]\n"
"ds_read_b64_tr_b16 v[238:239],v249 offset:22528\n"
"s_waitcnt vmcnt(2)\n"
"s_waitcnt lgkmcnt(8)\n"
"v_mfma_f32_16x16x32_bf16 v[200:203],v[216:219],v[0:3],v[244:247]\n"
"ds_read_b64_tr_b16 v[208:209],v249 offset:24576\n"
"v_mfma_f32_16x16x32_bf16 v[200:203],v[220:223],v[4:7],v[200:203]\n"
"ds_read_b64_tr_b16 v[210:211],v249 offset:26624\n"
"s_barrier\n"
"v_mfma_f32_16x16x32_bf16 v[204:207],v[216:219],v[72:75],v[244:247]\n"
"ds_read_b64_tr_b16 v[212:213],v249 offset:28672\n"
"buffer_load_dwordx4 v250,s[16:19],s92 offen lds\n"
"v_mfma_f32_16x16x32_bf16 v[204:207],v[220:223],v[76:79],v[204:207]\n"
"ds_read_b64_tr_b16 v[214:215],v249 offset:30720\n"
"s_waitcnt lgkmcnt(8)\n"
"v_mfma_f32_16x16x32_bf16 v[204:207],v[224:227],v[80:83],v[204:207]\n"
"ds_read_b64_tr_b16 v[216:217],v249 offset:32768\n"
"s_add_u32 s90,s92,s93\n"
"v_mfma_f32_16x16x32_bf16 v[204:207],v[228:231],v[84:87],v[204:207]\n"
"ds_read_b64_tr_b16 v[218:219],v249 offset:34816\n"
"buffer_load_dwordx4 v191,s[16:19],s92 offen offset:1024 lds\n"
"v_mfma_f32_16x16x32_bf16 v[200:203],v[224:227],v[8:11],v[200:203]\n"
"ds_read_b64_tr_b16 v[220:221],v249 offset:36864\n"
"s_addk_i32 m0,8192\n"
"v_mfma_f32_16x16x32_bf16 v[200:203],v[228:231],v[12:15],v[200:203]\n"
"ds_read_b64_tr_b16 v[222:223],v249 offset:38912\n"
"s_waitcnt lgkmcnt(8)\n"
"v_mfma_f32_16x16x32_bf16 v[200:203],v[232:235],v[16:19],v[200:203]\n"
"ds_read_b64_tr_b16 v[224:225],v249 offset:40960\n"
"v_mfma_f32_16x16x32_bf16 v[200:203],v[236:239],v[20:23],v[200:203]\n"
"ds_read_b64_tr_b16 v[226:227],v249 offset:43008\n"
"v_mfma_f32_16x16x32_bf16 v[204:207],v[232:235],v[88:91],v[204:207]\n"
"ds_read_b64_tr_b16 v[228:229],v249 offset:45056\n"
"v_mfma_f32_16x16x32_bf16 v[204:207],v[236:239],v[92:95],v[204:207]\n"
"ds_read_b64_tr_b16 v[230:231],v249 offset:47104\n"
"s_waitcnt lgkmcnt(8)\n"
"v_mfma_f32_16x16x32_bf16 v[204:207],v[208:211],v[96:99],v[204:207]\n"
"ds_read_b64_tr_b16 v[232:233],v249 offset:49152\n"
"buffer_load_dwordx4 v250,s[16:19],s90 offen lds\n"
"v_mfma_f32_16x16x32_bf16 v[204:207],v[212:215],v[100:103],v[204:207]\n"
"ds_read_b64_tr_b16 v[234:235],v249 offset:51200\n"
"s_add_u32 s92,s90,s93\n"
"v_mfma_f32_16x16x32_bf16 v[200:203],v[208:211],v[24:27],v[200:203]\n"
"ds_read_b64_tr_b16 v[236:237],v249 offset:53248\n"
"v_mfma_f32_16x16x32_bf16 v[200:203],v[212:215],v[28:31],v[200:203]\n"
"ds_read_b64_tr_b16 v[238:239],v249 offset:55296\n"
"s_waitcnt lgkmcnt(8)\n"
"v_mfma_f32_16x16x32_bf16 v[200:203],v[216:219],v[32:35],v[200:203]\n"
"ds_read_b64_tr_b16 v[208:209],v249 offset:57344\n"
"v_mfma_f32_16x16x32_bf16 v[200:203],v[220:223],v[36:39],v[200:203]\n"
"ds_read_b64_tr_b16 v[210:211],v249 offset:59392\n"
"v_mfma_f32_16x16x32_bf16 v[204:207],v[216:219],v[104:107],v[204:207]\n"
"ds_read_b64_tr_b16 v[212:213],v249 offset:61440\n"
"buffer_load_dwordx4 v191,s[16:19],s90 offen offset:1024 lds\n"
"v_mfma_f32_16x16x32_bf16 v[204:207],v[220:223],v[108:111],v[204:207]\n"
"ds_read_b64_tr_b16 v[214:215],v249 offset:63488\n"
".Lk_padm3:\n"
"s_cbranch_execz .Lk_padm7\n"
"s_waitcnt lgkmcnt(8)\n"
"v_mfma_f32_16x16x32_bf16 v[204:207],v[224:227],v[112:115],v[204:207]\n"
"v_add_u32 v249,0x10000,v249\n"
"ds_read_b64_tr_b16 v[216:217],v249\n"
"s_addk_i32 m0,8192\n"
"v_mfma_f32_16x16x32_bf16 v[204:207],v[228:231],v[116:119],v[204:207]\n"
"ds_read_b64_tr_b16 v[218:219],v249 offset:2048\n"
"s_setprio 2\n"
"v_mfma_f32_16x16x32_bf16 v[200:203],v[224:227],v[40:43],v[200:203]\n"
"ds_read_b64_tr_b16 v[220:221],v249 offset:4096\n"
"v_mfma_f32_16x16x32_bf16 v[200:203],v[228:231],v[44:47],v[200:203]\n"
"ds_read_b64_tr_b16 v[222:223],v249 offset:6144\n"
"s_waitcnt lgkmcnt(8)\n"
"v_mfma_f32_16x16x32_bf16 v[200:203],v[232:235],v[48:51],v[200:203]\n"
"ds_read_b64_tr_b16 v[224:225],v248 offset:40960\n"
"buffer_load_dwordx4 v250,s[16:19],s92 offen lds\n"
"v_mfma_f32_16x16x32_bf16 v[200:203],v[236:239],v[52:55],v[200:203]\n"
"ds_read_b64_tr_b16 v[226:227],v248 offset:43008\n"
"s_add_u32 s90,s92,s93\n"
"v_mfma_f32_16x16x32_bf16 v[204:207],v[232:235],v[120:123],v[204:207]\n"
"ds_read_b64_tr_b16 v[228:229],v248 offset:45056\n"
"v_mfma_f32_16x16x32_bf16 v[204:207],v[236:239],v[124:127],v[204:207]\n"
"ds_read_b64_tr_b16 v[230:231],v248 offset:47104\n"
"s_waitcnt lgkmcnt(8)\n"
"v_mfma_f32_16x16x32_bf16 v[204:207],v[208:211],v[128:131],v[204:207]\n"
"ds_read_b64_tr_b16 v[232:233],v248 offset:49152\n"
"v_mfma_f32_16x16x32_bf16 v[204:207],v[212:215],v[132:135],v[204:207]\n"
"ds_read_b64_tr_b16 v[234:235],v248 offset:51200\n"
"v_mfma_f32_16x16x32_bf16 v[200:203],v[208:211],v[56:59],v[200:203]\n"
"ds_read_b64_tr_b16 v[236:237],v248 offset:53248\n"
"buffer_load_dwordx4 v191,s[16:19],s92 offen offset:1024 lds\n"
"v_mfma_f32_16x16x32_bf16 v[200:203],v[212:215],v[60:63],v[200:203]\n"
"ds_read_b64_tr_b16 v[238:239],v248 offset:55296\n"
"s_waitcnt lgkmcnt(8)\n"
"v_mfma_f32_16x16x32_bf16 v[200:203],v[216:219],v[64:67],v[200:203]\n"
"ds_read_b64_tr_b16 v[208:209],v248 offset:57344\n"
"s_addk_i32 m0,8192\n"
"v_mfma_f32_16x16x32_bf16 v[200:203],v[220:223],v[68:71],v[200:203]\n"
"ds_read_b64_tr_b16 v[210:211],v248 offset:59392\n"
"v_mfma_f32_16x16x32_bf16 v[204:207],v[216:219],v[136:139],v[204:207]\n"
"ds_read_b64_tr_b16 v[212:213],v248 offset:61440\n"
"v_mfma_f32_16x16x32_bf16 v[204:207],v[220:223],v[140:143],v[204:207]\n"
"ds_read_b64_tr_b16 v[214:215],v248 offset:63488\n"
"s_waitcnt lgkmcnt(8)\n"
"v_mfma_f32_16x16x32_bf16 v[192:195],v[224:227],v[40:43],v[192:195]\n"
"v_add_u32 v248,0x10000,v248\n"
"ds_read_b64_tr_b16 v[216:217],v248\n"
"v_mfma_f32_16x16x32_bf16 v[192:195],v[228:231],v[44:47],v[192:195]\n"
"ds_read_b64_tr_b16 v[218:219],v248 offset:2048\n"
"buffer_load_dwordx4 v250,s[16:19],s90 offen lds\n"
"v_mfma_f32_16x16x32_bf16 v[196:199],v[224:227],v[112:115],v[196:199]\n"
"ds_read_b64_tr_b16 v[220:221],v248 offset:4096\n"
"v_mfma_f32_16x16x32_bf16 v[196:199],v[228:231],v[116:119],v[196:199]\n"
"ds_read_b64_tr_b16 v[222:223],v248 offset:6144\n"
"s_waitcnt lgkmcnt(8)\n"
"v_mfma_f32_16x16x32_bf16 v[196:199],v[232:235],v[120:123],v[196:199]\n"
"v_mfma_f32_16x16x32_bf16 v[196:199],v[236:239],v[124:127],v[196:199]\n"
"v_mfma_f32_16x16x32_bf16 v[192:195],v[232:235],v[48:51],v[192:195]\n"
"v_mfma_f32_16x16x32_bf16 v[192:195],v[236:239],v[52:55],v[192:195]\n"
"buffer_load_dwordx4 v191,s[16:19],s90 offen offset:1024 lds\n"
"s_waitcnt lgkmcnt(4)\n"
"v_mfma_f32_16x16x32_bf16 v[192:195],v[208:211],v[56:59],v[192:195]\n"
"v_mfma_f32_16x16x32_bf16 v[192:195],v[212:215],v[60:63],v[192:195]\n"
"v_add_u32 v248,4294828032,v248\n"
"v_mfma_f32_16x16x32_bf16 v[196:199],v[208:211],v[128:131],v[196:199]\n"
"v_mfma_f32_16x16x32_bf16 v[196:199],v[212:215],v[132:135],v[196:199]\n"
"v_add_u32 v249,4294828032,v249\n"
"s_waitcnt lgkmcnt(0)\n"
"v_mfma_f32_16x16x32_bf16 v[196:199],v[216:219],v[136:139],v[196:199]\n"
"v_mfma_f32_16x16x32_bf16 v[196:199],v[220:223],v[140:143],v[196:199]\n"
"v_mfma_f32_16x16x32_bf16 v[192:195],v[216:219],v[64:67],v[192:195]\n"
"v_mfma_f32_16x16x32_bf16 v[192:195],v[220:223],v[68:71],v[192:195]\n"
"s_setprio 0\n"
"v_fma_f32 v244,-v200,v200,s79\n"
"v_fma_f32 v245,-v201,v201,s79\n"
"v_fma_f32 v246,-v202,v202,s79\n"
"v_fma_f32 v247,-v203,v203,s79\n"
"s_add_u32 s92,s80,2949376\n"
"s_addk_i32 m0,8192\n"
"v_mul_f32 v244,v244,v200\n"
"v_mul_f32 v245,v245,v201\n"
"buffer_load_dwordx4 v250,s[16:19],s92 offen lds\n"
"v_mul_f32 v246,v246,v202\n"
"v_mul_f32 v247,v247,v203\n"
"v_exp_f32 v244,v244\n"
"v_exp_f32 v245,v245\n"
"v_exp_f32 v246,v246\n"
"v_exp_f32 v247,v247\n"
"v_add_f32 v244,1.0,v244\n"
"v_add_f32 v245,1.0,v245\n"
"v_add_f32 v246,1.0,v246\n"
"v_add_f32 v247,1.0,v247\n"
"v_rcp_f32 v244,v244\n"
"v_rcp_f32 v245,v245\n"
"v_rcp_f32 v246,v246\n"
"s_add_u32 s90,s92,s93\n"
"v_rcp_f32 v247,v247\n"
"buffer_load_dwordx4 v191,s[16:19],s92 offen offset:1024 lds\n"
"v_mul_f32 v244,v244,v200\n"
"v_mul_f32 v245,v245,v201\n"
"v_mul_f32 v246,v246,v202\n"
"v_mul_f32 v247,v247,v203\n"
"v_cvt_pk_bf16_f32 v154,v244,v245\n"
"v_cvt_pk_bf16_f32 v155,v246,v247\n"
"v_fma_f32 v240,-v192,v192,s79\n"
"v_fma_f32 v241,-v193,v193,s79\n"
"v_fma_f32 v242,-v194,v194,s79\n"
"v_fma_f32 v243,-v195,v195,s79\n"
"v_mul_f32 v240,v240,v192\n"
"v_mul_f32 v241,v241,v193\n"
"v_mul_f32 v242,v242,v194\n"
"v_mul_f32 v243,v243,v195\n"
"v_exp_f32 v240,v240\n"
"v_exp_f32 v241,v241\n"
"v_exp_f32 v242,v242\n"
"s_addk_i32 m0,8192\n"
"v_exp_f32 v243,v243\n"
"buffer_load_dwordx4 v250,s[16:19],s90 offen lds\n"
"v_add_f32 v240,1.0,v240\n"
"v_add_f32 v241,1.0,v241\n"
"v_add_f32 v242,1.0,v242\n"
"v_add_f32 v243,1.0,v243\n"
"v_rcp_f32 v240,v240\n"
"v_rcp_f32 v241,v241\n"
"v_rcp_f32 v242,v242\n"
"v_rcp_f32 v243,v243\n"
"v_mul_f32 v240,v240,v192\n"
"v_mul_f32 v241,v241,v193\n"
"v_mul_f32 v242,v242,v194\n"
"v_mul_f32 v243,v243,v195\n"
"v_cvt_pk_bf16_f32 v152,v240,v241\n"
"v_cvt_pk_bf16_f32 v153,v242,v243\n"
"v_fma_f32 v240,-v196,v196,s79\n"
"v_fma_f32 v241,-v197,v197,s79\n"
"v_fma_f32 v242,-v198,v198,s79\n"
"v_fma_f32 v243,-v199,v199,s79\n"
"buffer_load_dwordx4 v191,s[16:19],s90 offen offset:1024 lds\n"
"v_mul_f32 v240,v240,v196\n"
"v_mul_f32 v241,v241,v197\n"
"v_mul_f32 v242,v242,v198\n"
"v_mul_f32 v243,v243,v199\n"
"v_exp_f32 v240,v240\n"
"v_exp_f32 v241,v241\n"
"v_exp_f32 v242,v242\n"
"v_exp_f32 v243,v243\n"
"v_add_f32 v240,1.0,v240\n"
"v_add_f32 v241,1.0,v241\n"
"v_add_f32 v242,1.0,v242\n"
"v_add_f32 v243,1.0,v243\n"
"v_rcp_f32 v240,v240\n"
"v_rcp_f32 v241,v241\n"
"v_rcp_f32 v242,v242\n"
"v_rcp_f32 v243,v243\n"
"v_mul_f32 v240,v240,v196\n"
"v_mul_f32 v241,v241,v197\n"
"v_mul_f32 v242,v242,v198\n"
"v_mul_f32 v243,v243,v199\n"
"s_waitcnt vmcnt(4)\n"
"s_barrier\n"
"v_cvt_pk_bf16_f32 v156,v240,v241\n"
"v_cvt_pk_bf16_f32 v157,v242,v243\n"
"v_fma_f32 v244,-v204,v204,s79\n"
"v_fma_f32 v245,-v205,v205,s79\n"
"v_fma_f32 v246,-v206,v206,s79\n"
"v_fma_f32 v247,-v207,v207,s79\n"
"v_mul_f32 v244,v244,v204\n"
"v_mul_f32 v245,v245,v205\n"
"v_mul_f32 v246,v246,v206\n"
"v_mul_f32 v247,v247,v207\n"
"v_exp_f32 v244,v244\n"
"v_exp_f32 v245,v245\n"
"v_exp_f32 v246,v246\n"
"v_exp_f32 v247,v247\n"
"v_add_f32 v244,1.0,v244\n"
"v_add_f32 v245,1.0,v245\n"
"v_add_f32 v246,1.0,v246\n"
"v_add_f32 v247,1.0,v247\n"
"v_rcp_f32 v244,v244\n"
"s_setprio 3\n"
"v_rcp_f32 v245,v245\n"
"ds_read_b128 v[240:243],v253 offset:512\n"
"ds_read_b64_tr_b16 v[208:209],v248\n"
"v_rcp_f32 v246,v246\n"
"ds_read_b64_tr_b16 v[210:211],v248 offset:2048\n"
"ds_read_b64_tr_b16 v[212:213],v248 offset:4096\n"
"v_rcp_f32 v247,v247\n"
"ds_read_b64_tr_b16 v[214:215],v248 offset:6144\n"
"ds_read_b64_tr_b16 v[216:217],v248 offset:8192\n"
"v_mul_f32 v244,v244,v204\n"
"v_mul_f32 v245,v245,v205\n"
"ds_read_b64_tr_b16 v[218:219],v248 offset:10240\n"
"ds_read_b64_tr_b16 v[220:221],v248 offset:12288\n"
"v_mul_f32 v246,v246,v206\n"
"v_mul_f32 v247,v247,v207\n"
"ds_read_b64_tr_b16 v[222:223],v248 offset:14336\n"
"ds_read_b64_tr_b16 v[224:225],v248 offset:16384\n"
"v_cvt_pk_bf16_f32 v158,v244,v245\n"
"v_cvt_pk_bf16_f32 v159,v246,v247\n"
"ds_read_b64_tr_b16 v[226:227],v248 offset:18432\n"
"ds_read_b64_tr_b16 v[228:229],v248 offset:20480\n"
"ds_read_b64_tr_b16 v[230:231],v248 offset:22528\n"
"s_setprio 1\n"
"s_waitcnt lgkmcnt(12)\n"
"s_waitcnt lgkmcnt(8)\n"
"v_mfma_f32_16x16x32_bf16 v[192:195],v[208:211],v[0:3],v[240:243]\n"
"ds_read_b64_tr_b16 v[232:233],v248 offset:24576\n"
"ds_read_b128 v[244:247],v253 offset:576\n"
"v_mfma_f32_16x16x32_bf16 v[192:195],v[212:215],v[4:7],v[192:195]\n"
"ds_read_b64_tr_b16 v[234:235],v248 offset:26624\n"
"s_add_u32 s78,s84,2048\n"
"v_mfma_f32_16x16x32_bf16 v[196:199],v[208:211],v[72:75],v[240:243]\n"
"ds_read_b64_tr_b16 v[236:237],v248 offset:28672\n"
"v_mfma_f32_16x16x32_bf16 v[196:199],v[212:215],v[76:79],v[196:199]\n"
"ds_read_b64_tr_b16 v[238:239],v248 offset:30720\n"
"s_waitcnt lgkmcnt(9)\n"
"v_mfma_f32_16x16x32_bf16 v[196:199],v[216:219],v[80:83],v[196:199]\n"
"ds_read_b64_tr_b16 v[208:209],v248 offset:32768\n"
"v_mfma_f32_16x16x32_bf16 v[196:199],v[220:223],v[84:87],v[196:199]\n"
"ds_read_b64_tr_b16 v[210:211],v248 offset:34816\n"
"v_mfma_f32_16x16x32_bf16 v[192:195],v[216:219],v[8:11],v[192:195]\n"
"ds_read_b64_tr_b16 v[212:213],v248 offset:36864\n"
"buffer_store_dwordx4 v[152:155],v252,s[32:35],s78 offen sc1\n"
"v_mfma_f32_16x16x32_bf16 v[192:195],v[220:223],v[12:15],v[192:195]\n"
"ds_read_b64_tr_b16 v[214:215],v248 offset:38912\n"
"s_waitcnt lgkmcnt(9)\n"
"v_mfma_f32_16x16x32_bf16 v[192:195],v[224:227],v[16:19],v[192:195]\n"
"ds_read_b64_tr_b16 v[216:217],v249\n"
"v_mfma_f32_16x16x32_bf16 v[192:195],v[228:231],v[20:23],v[192:195]\n"
"ds_read_b64_tr_b16 v[218:219],v249 offset:2048\n"
"v_mfma_f32_16x16x32_bf16 v[196:199],v[224:227],v[88:91],v[196:199]\n"
"ds_read_b64_tr_b16 v[220:221],v249 offset:4096\n"
"v_mfma_f32_16x16x32_bf16 v[196:199],v[228:231],v[92:95],v[196:199]\n"
"ds_read_b64_tr_b16 v[222:223],v249 offset:6144\n"
"s_waitcnt lgkmcnt(8)\n"
"v_mfma_f32_16x16x32_bf16 v[196:199],v[232:235],v[96:99],v[196:199]\n"
"ds_read_b64_tr_b16 v[224:225],v249 offset:8192\n"
"buffer_store_dwordx4 v[156:159],v252,s[32:35],s78 offen offset:1024 sc1\n"
"v_mfma_f32_16x16x32_bf16 v[196:199],v[236:239],v[100:103],v[196:199]\n"
"ds_read_b64_tr_b16 v[226:227],v249 offset:10240\n"
"s_add_u32 s92,s80,590208\n"
"v_mfma_f32_16x16x32_bf16 v[192:195],v[232:235],v[24:27],v[192:195]\n"
"ds_read_b64_tr_b16 v[228:229],v249 offset:12288\n"
"s_add_u32 m0,s91,81920\n"
"v_mfma_f32_16x16x32_bf16 v[192:195],v[236:239],v[28:31],v[192:195]\n"
"ds_read_b64_tr_b16 v[230:231],v249 offset:14336\n"
"s_waitcnt lgkmcnt(8)\n"
"v_mfma_f32_16x16x32_bf16 v[192:195],v[208:211],v[32:35],v[192:195]\n"
"ds_read_b64_tr_b16 v[232:233],v249 offset:16384\n"
"v_mfma_f32_16x16x32_bf16 v[192:195],v[212:215],v[36:39],v[192:195]\n"
"ds_read_b64_tr_b16 v[234:235],v249 offset:18432\n"
"v_mfma_f32_16x16x32_bf16 v[196:199],v[208:211],v[104:107],v[196:199]\n"
"ds_read_b64_tr_b16 v[236:237],v249 offset:20480\n"
"v_mfma_f32_16x16x32_bf16 v[196:199],v[212:215],v[108:111],v[196:199]\n"
"ds_read_b64_tr_b16 v[238:239],v249 offset:22528\n"
"s_waitcnt vmcnt(2)\n"
"s_waitcnt lgkmcnt(8)\n"
"v_mfma_f32_16x16x32_bf16 v[200:203],v[216:219],v[0:3],v[244:247]\n"
"ds_read_b64_tr_b16 v[208:209],v249 offset:24576\n"
"v_mfma_f32_16x16x32_bf16 v[200:203],v[220:223],v[4:7],v[200:203]\n"
"ds_read_b64_tr_b16 v[210:211],v249 offset:26624\n"
"s_barrier\n"
"v_mfma_f32_16x16x32_bf16 v[204:207],v[216:219],v[72:75],v[244:247]\n"
"ds_read_b64_tr_b16 v[212:213],v249 offset:28672\n"
"buffer_load_dwordx4 v250,s[16:19],s92 offen lds\n"
"v_mfma_f32_16x16x32_bf16 v[204:207],v[220:223],v[76:79],v[204:207]\n"
"ds_read_b64_tr_b16 v[214:215],v249 offset:30720\n"
"s_waitcnt lgkmcnt(8)\n"
"v_mfma_f32_16x16x32_bf16 v[204:207],v[224:227],v[80:83],v[204:207]\n"
"ds_read_b64_tr_b16 v[216:217],v249 offset:32768\n"
"s_add_u32 s90,s92,s93\n"
"v_mfma_f32_16x16x32_bf16 v[204:207],v[228:231],v[84:87],v[204:207]\n"
"ds_read_b64_tr_b16 v[218:219],v249 offset:34816\n"
"buffer_load_dwordx4 v191,s[16:19],s92 offen offset:1024 lds\n"
"v_mfma_f32_16x16x32_bf16 v[200:203],v[224:227],v[8:11],v[200:203]\n"
"ds_read_b64_tr_b16 v[220:221],v249 offset:36864\n"
"s_addk_i32 m0,8192\n"
"v_mfma_f32_16x16x32_bf16 v[200:203],v[228:231],v[12:15],v[200:203]\n"
"ds_read_b64_tr_b16 v[222:223],v249 offset:38912\n"
"s_waitcnt lgkmcnt(8)\n"
"v_mfma_f32_16x16x32_bf16 v[200:203],v[232:235],v[16:19],v[200:203]\n"
"ds_read_b64_tr_b16 v[224:225],v249 offset:40960\n"
"v_mfma_f32_16x16x32_bf16 v[200:203],v[236:239],v[20:23],v[200:203]\n"
"ds_read_b64_tr_b16 v[226:227],v249 offset:43008\n"
"v_mfma_f32_16x16x32_bf16 v[204:207],v[232:235],v[88:91],v[204:207]\n"
"ds_read_b64_tr_b16 v[228:229],v249 offset:45056\n"
"v_mfma_f32_16x16x32_bf16 v[204:207],v[236:239],v[92:95],v[204:207]\n"
"ds_read_b64_tr_b16 v[230:231],v249 offset:47104\n"
"s_waitcnt lgkmcnt(8)\n"
"v_mfma_f32_16x16x32_bf16 v[204:207],v[208:211],v[96:99],v[204:207]\n"
"ds_read_b64_tr_b16 v[232:233],v249 offset:49152\n"
"buffer_load_dwordx4 v250,s[16:19],s90 offen lds\n"
"v_mfma_f32_16x16x32_bf16 v[204:207],v[212:215],v[100:103],v[204:207]\n"
"ds_read_b64_tr_b16 v[234:235],v249 offset:51200\n"
"s_add_u32 s92,s90,s93\n"
"v_mfma_f32_16x16x32_bf16 v[200:203],v[208:211],v[24:27],v[200:203]\n"
"ds_read_b64_tr_b16 v[236:237],v249 offset:53248\n"
"v_mfma_f32_16x16x32_bf16 v[200:203],v[212:215],v[28:31],v[200:203]\n"
"ds_read_b64_tr_b16 v[238:239],v249 offset:55296\n"
"s_waitcnt lgkmcnt(8)\n"
"v_mfma_f32_16x16x32_bf16 v[200:203],v[216:219],v[32:35],v[200:203]\n"
"ds_read_b64_tr_b16 v[208:209],v249 offset:57344\n"
"v_mfma_f32_16x16x32_bf16 v[200:203],v[220:223],v[36:39],v[200:203]\n"
"ds_read_b64_tr_b16 v[210:211],v249 offset:59392\n"
"v_mfma_f32_16x16x32_bf16 v[204:207],v[216:219],v[104:107],v[204:207]\n"
"ds_read_b64_tr_b16 v[212:213],v249 offset:61440\n"
"buffer_load_dwordx4 v191,s[16:19],s90 offen offset:1024 lds\n"
"v_mfma_f32_16x16x32_bf16 v[204:207],v[220:223],v[108:111],v[204:207]\n"
"ds_read_b64_tr_b16 v[214:215],v249 offset:63488\n"
"s_waitcnt lgkmcnt(8)\n"
"v_mfma_f32_16x16x32_bf16 v[204:207],v[224:227],v[112:115],v[204:207]\n"
"v_add_u32 v249,0x10000,v249\n"
"ds_read_b64_tr_b16 v[216:217],v249\n"
"s_addk_i32 m0,8192\n"
"v_mfma_f32_16x16x32_bf16 v[204:207],v[228:231],v[116:119],v[204:207]\n"
"ds_read_b64_tr_b16 v[218:219],v249 offset:2048\n"
"s_setprio 2\n"
"v_mfma_f32_16x16x32_bf16 v[200:203],v[224:227],v[40:43],v[200:203]\n"
"ds_read_b64_tr_b16 v[220:221],v249 offset:4096\n"
"v_mfma_f32_16x16x32_bf16 v[200:203],v[228:231],v[44:47],v[200:203]\n"
"ds_read_b64_tr_b16 v[222:223],v249 offset:6144\n"
"s_waitcnt lgkmcnt(8)\n"
"v_mfma_f32_16x16x32_bf16 v[200:203],v[232:235],v[48:51],v[200:203]\n"
"ds_read_b64_tr_b16 v[224:225],v248 offset:40960\n"
"buffer_load_dwordx4 v250,s[16:19],s92 offen lds\n"
"v_mfma_f32_16x16x32_bf16 v[200:203],v[236:239],v[52:55],v[200:203]\n"
"ds_read_b64_tr_b16 v[226:227],v248 offset:43008\n"
"s_add_u32 s90,s92,s93\n"
"v_mfma_f32_16x16x32_bf16 v[204:207],v[232:235],v[120:123],v[204:207]\n"
"ds_read_b64_tr_b16 v[228:229],v248 offset:45056\n"
"v_mfma_f32_16x16x32_bf16 v[204:207],v[236:239],v[124:127],v[204:207]\n"
"ds_read_b64_tr_b16 v[230:231],v248 offset:47104\n"
"s_waitcnt lgkmcnt(8)\n"
"v_mfma_f32_16x16x32_bf16 v[204:207],v[208:211],v[128:131],v[204:207]\n"
"ds_read_b64_tr_b16 v[232:233],v248 offset:49152\n"
"v_mfma_f32_16x16x32_bf16 v[204:207],v[212:215],v[132:135],v[204:207]\n"
"ds_read_b64_tr_b16 v[234:235],v248 offset:51200\n"
"v_mfma_f32_16x16x32_bf16 v[200:203],v[208:211],v[56:59],v[200:203]\n"
"ds_read_b64_tr_b16 v[236:237],v248 offset:53248\n"
"buffer_load_dwordx4 v191,s[16:19],s92 offen offset:1024 lds\n"
"v_mfma_f32_16x16x32_bf16 v[200:203],v[212:215],v[60:63],v[200:203]\n"
"ds_read_b64_tr_b16 v[238:239],v248 offset:55296\n"
"s_waitcnt lgkmcnt(8)\n"
"v_mfma_f32_16x16x32_bf16 v[200:203],v[216:219],v[64:67],v[200:203]\n"
"ds_read_b64_tr_b16 v[208:209],v248 offset:57344\n"
"s_addk_i32 m0,8192\n"
"v_mfma_f32_16x16x32_bf16 v[200:203],v[220:223],v[68:71],v[200:203]\n"
"ds_read_b64_tr_b16 v[210:211],v248 offset:59392\n"
"v_mfma_f32_16x16x32_bf16 v[204:207],v[216:219],v[136:139],v[204:207]\n"
"ds_read_b64_tr_b16 v[212:213],v248 offset:61440\n"
"v_mfma_f32_16x16x32_bf16 v[204:207],v[220:223],v[140:143],v[204:207]\n"
"ds_read_b64_tr_b16 v[214:215],v248 offset:63488\n"
"s_waitcnt lgkmcnt(8)\n"
"v_mfma_f32_16x16x32_bf16 v[192:195],v[224:227],v[40:43],v[192:195]\n"
"v_add_u32 v248,0x10000,v248\n"
"ds_read_b64_tr_b16 v[216:217],v248\n"
"v_mfma_f32_16x16x32_bf16 v[192:195],v[228:231],v[44:47],v[192:195]\n"
"ds_read_b64_tr_b16 v[218:219],v248 offset:2048\n"
"buffer_load_dwordx4 v250,s[16:19],s90 offen lds\n"
"v_mfma_f32_16x16x32_bf16 v[196:199],v[224:227],v[112:115],v[196:199]\n"
"ds_read_b64_tr_b16 v[220:221],v248 offset:4096\n"
"v_mfma_f32_16x16x32_bf16 v[196:199],v[228:231],v[116:119],v[196:199]\n"
"ds_read_b64_tr_b16 v[222:223],v248 offset:6144\n"
"s_waitcnt lgkmcnt(8)\n"
"v_mfma_f32_16x16x32_bf16 v[196:199],v[232:235],v[120:123],v[196:199]\n"
"v_mfma_f32_16x16x32_bf16 v[196:199],v[236:239],v[124:127],v[196:199]\n"
"v_mfma_f32_16x16x32_bf16 v[192:195],v[232:235],v[48:51],v[192:195]\n"
"v_mfma_f32_16x16x32_bf16 v[192:195],v[236:239],v[52:55],v[192:195]\n"
"buffer_load_dwordx4 v191,s[16:19],s90 offen offset:1024 lds\n"
"s_waitcnt lgkmcnt(4)\n"
"v_mfma_f32_16x16x32_bf16 v[192:195],v[208:211],v[56:59],v[192:195]\n"
"v_mfma_f32_16x16x32_bf16 v[192:195],v[212:215],v[60:63],v[192:195]\n"
"v_add_u32 v248,8192,v248\n"
"v_mfma_f32_16x16x32_bf16 v[196:199],v[208:211],v[128:131],v[196:199]\n"
"v_mfma_f32_16x16x32_bf16 v[196:199],v[212:215],v[132:135],v[196:199]\n"
"v_add_u32 v249,8192,v249\n"
"s_waitcnt lgkmcnt(0)\n"
"v_mfma_f32_16x16x32_bf16 v[196:199],v[216:219],v[136:139],v[196:199]\n"
"v_mfma_f32_16x16x32_bf16 v[196:199],v[220:223],v[140:143],v[196:199]\n"
"v_mfma_f32_16x16x32_bf16 v[192:195],v[216:219],v[64:67],v[192:195]\n"
"v_mfma_f32_16x16x32_bf16 v[192:195],v[220:223],v[68:71],v[192:195]\n"
"s_setprio 0\n"
"v_fma_f32 v244,-v200,v200,s79\n"
"v_fma_f32 v245,-v201,v201,s79\n"
"v_fma_f32 v246,-v202,v202,s79\n"
"v_fma_f32 v247,-v203,v203,s79\n"
"s_add_u32 s92,s80,2949504\n"
"s_addk_i32 m0,8192\n"
"v_mul_f32 v244,v244,v200\n"
"v_mul_f32 v245,v245,v201\n"
"buffer_load_dwordx4 v250,s[16:19],s92 offen lds\n"
"v_mul_f32 v246,v246,v202\n"
"v_mul_f32 v247,v247,v203\n"
"v_exp_f32 v244,v244\n"
"v_exp_f32 v245,v245\n"
"v_exp_f32 v246,v246\n"
"v_exp_f32 v247,v247\n"
"v_add_f32 v244,1.0,v244\n"
"v_add_f32 v245,1.0,v245\n"
"v_add_f32 v246,1.0,v246\n"
"v_add_f32 v247,1.0,v247\n"
"v_rcp_f32 v244,v244\n"
"v_rcp_f32 v245,v245\n"
"v_rcp_f32 v246,v246\n"
"s_add_u32 s90,s92,s93\n"
"v_rcp_f32 v247,v247\n"
"buffer_load_dwordx4 v191,s[16:19],s92 offen offset:1024 lds\n"
"v_mul_f32 v244,v244,v200\n"
"v_mul_f32 v245,v245,v201\n"
"v_mul_f32 v246,v246,v202\n"
"v_mul_f32 v247,v247,v203\n"
"v_cvt_pk_bf16_f32 v162,v244,v245\n"
"v_cvt_pk_bf16_f32 v163,v246,v247\n"
"v_fma_f32 v240,-v192,v192,s79\n"
"v_fma_f32 v241,-v193,v193,s79\n"
"v_fma_f32 v242,-v194,v194,s79\n"
"v_fma_f32 v243,-v195,v195,s79\n"
"v_mul_f32 v240,v240,v192\n"
"v_mul_f32 v241,v241,v193\n"
"v_mul_f32 v242,v242,v194\n"
"v_mul_f32 v243,v243,v195\n"
"v_exp_f32 v240,v240\n"
"v_exp_f32 v241,v241\n"
"v_exp_f32 v242,v242\n"
"s_addk_i32 m0,8192\n"
"v_exp_f32 v243,v243\n"
"buffer_load_dwordx4 v250,s[16:19],s90 offen lds\n"
"v_add_f32 v240,1.0,v240\n"
"v_add_f32 v241,1.0,v241\n"
"v_add_f32 v242,1.0,v242\n"
"v_add_f32 v243,1.0,v243\n"
"v_rcp_f32 v240,v240\n"
"v_rcp_f32 v241,v241\n"
"v_rcp_f32 v242,v242\n"
"v_rcp_f32 v243,v243\n"
"v_mul_f32 v240,v240,v192\n"
"v_mul_f32 v241,v241,v193\n"
"v_mul_f32 v242,v242,v194\n"
"v_mul_f32 v243,v243,v195\n"
"v_cvt_pk_bf16_f32 v160,v240,v241\n"
"v_cvt_pk_bf16_f32 v161,v242,v243\n"
"v_fma_f32 v240,-v196,v196,s79\n"
"v_fma_f32 v241,-v197,v197,s79\n"
"v_fma_f32 v242,-v198,v198,s79\n"
"v_fma_f32 v243,-v199,v199,s79\n"
"buffer_load_dwordx4 v191,s[16:19],s90 offen offset:1024 lds\n"
"v_mul_f32 v240,v240,v196\n"
"v_mul_f32 v241,v241,v197\n"
"v_mul_f32 v242,v242,v198\n"
"v_mul_f32 v243,v243,v199\n"
"v_exp_f32 v240,v240\n"
"v_exp_f32 v241,v241\n"
"v_exp_f32 v242,v242\n"
"v_exp_f32 v243,v243\n"
"v_add_f32 v240,1.0,v240\n"
"v_add_f32 v241,1.0,v241\n"
"v_add_f32 v242,1.0,v242\n"
"v_add_f32 v243,1.0,v243\n"
"v_rcp_f32 v240,v240\n"
"v_rcp_f32 v241,v241\n"
"v_rcp_f32 v242,v242\n"
"v_rcp_f32 v243,v243\n"
"v_mul_f32 v240,v240,v196\n"
"v_mul_f32 v241,v241,v197\n"
"v_mul_f32 v242,v242,v198\n"
"v_mul_f32 v243,v243,v199\n"
"s_waitcnt vmcnt(4)\n"
"s_barrier\n"
"v_cvt_pk_bf16_f32 v164,v240,v241\n"
"v_cvt_pk_bf16_f32 v165,v242,v243\n"
"v_fma_f32 v244,-v204,v204,s79\n"
"v_fma_f32 v245,-v205,v205,s79\n"
"v_fma_f32 v246,-v206,v206,s79\n"
"v_fma_f32 v247,-v207,v207,s79\n"
"v_mul_f32 v244,v244,v204\n"
"v_mul_f32 v245,v245,v205\n"
"v_mul_f32 v246,v246,v206\n"
"v_mul_f32 v247,v247,v207\n"
"v_exp_f32 v244,v244\n"
"v_exp_f32 v245,v245\n"
"v_exp_f32 v246,v246\n"
"v_exp_f32 v247,v247\n"
"v_add_f32 v244,1.0,v244\n"
"v_add_f32 v245,1.0,v245\n"
"v_add_f32 v246,1.0,v246\n"
"v_add_f32 v247,1.0,v247\n"
"v_rcp_f32 v244,v244\n"
"s_setprio 3\n"
"v_rcp_f32 v245,v245\n"
"ds_read_b128 v[240:243],v253 offset:768\n"
"ds_read_b64_tr_b16 v[208:209],v248\n"
"v_rcp_f32 v246,v246\n"
"ds_read_b64_tr_b16 v[210:211],v248 offset:2048\n"
"ds_read_b64_tr_b16 v[212:213],v248 offset:4096\n"
"v_rcp_f32 v247,v247\n"
"ds_read_b64_tr_b16 v[214:215],v248 offset:6144\n"
"ds_read_b64_tr_b16 v[216:217],v248 offset:8192\n"
"v_mul_f32 v244,v244,v204\n"
"v_mul_f32 v245,v245,v205\n"
"ds_read_b64_tr_b16 v[218:219],v248 offset:10240\n"
"ds_read_b64_tr_b16 v[220:221],v248 offset:12288\n"
"v_mul_f32 v246,v246,v206\n"
"v_mul_f32 v247,v247,v207\n"
"ds_read_b64_tr_b16 v[222:223],v248 offset:14336\n"
"ds_read_b64_tr_b16 v[224:225],v248 offset:16384\n"
"v_cvt_pk_bf16_f32 v166,v244,v245\n"
"v_cvt_pk_bf16_f32 v167,v246,v247\n"
"ds_read_b64_tr_b16 v[226:227],v248 offset:18432\n"
"ds_read_b64_tr_b16 v[228:229],v248 offset:20480\n"
"ds_read_b64_tr_b16 v[230:231],v248 offset:22528\n"
"s_setprio 1\n"
"s_waitcnt lgkmcnt(12)\n"
"s_waitcnt lgkmcnt(8)\n"
"v_mfma_f32_16x16x32_bf16 v[192:195],v[208:211],v[0:3],v[240:243]\n"
"ds_read_b64_tr_b16 v[232:233],v248 offset:24576\n"
"ds_read_b128 v[244:247],v253 offset:832\n"
"v_mfma_f32_16x16x32_bf16 v[192:195],v[212:215],v[4:7],v[192:195]\n"
"ds_read_b64_tr_b16 v[234:235],v248 offset:26624\n"
"s_add_u32 s78,s84,4096\n"
"v_mfma_f32_16x16x32_bf16 v[196:199],v[208:211],v[72:75],v[240:243]\n"
"ds_read_b64_tr_b16 v[236:237],v248 offset:28672\n"
"v_mfma_f32_16x16x32_bf16 v[196:199],v[212:215],v[76:79],v[196:199]\n"
"ds_read_b64_tr_b16 v[238:239],v248 offset:30720\n"
"s_waitcnt lgkmcnt(9)\n"
"v_mfma_f32_16x16x32_bf16 v[196:199],v[216:219],v[80:83],v[196:199]\n"
"ds_read_b64_tr_b16 v[208:209],v248 offset:32768\n"
"v_mfma_f32_16x16x32_bf16 v[196:199],v[220:223],v[84:87],v[196:199]\n"
"ds_read_b64_tr_b16 v[210:211],v248 offset:34816\n"
"v_mfma_f32_16x16x32_bf16 v[192:195],v[216:219],v[8:11],v[192:195]\n"
"ds_read_b64_tr_b16 v[212:213],v248 offset:36864\n"
"buffer_store_dwordx4 v[160:163],v252,s[32:35],s78 offen sc1\n"
"v_mfma_f32_16x16x32_bf16 v[192:195],v[220:223],v[12:15],v[192:195]\n"
"ds_read_b64_tr_b16 v[214:215],v248 offset:38912\n"
"s_waitcnt lgkmcnt(9)\n"
"v_mfma_f32_16x16x32_bf16 v[192:195],v[224:227],v[16:19],v[192:195]\n"
"ds_read_b64_tr_b16 v[216:217],v249\n"
"v_mfma_f32_16x16x32_bf16 v[192:195],v[228:231],v[20:23],v[192:195]\n"
"ds_read_b64_tr_b16 v[218:219],v249 offset:2048\n"
"v_mfma_f32_16x16x32_bf16 v[196:199],v[224:227],v[88:91],v[196:199]\n"
"ds_read_b64_tr_b16 v[220:221],v249 offset:4096\n"
"v_mfma_f32_16x16x32_bf16 v[196:199],v[228:231],v[92:95],v[196:199]\n"
"ds_read_b64_tr_b16 v[222:223],v249 offset:6144\n"
"s_waitcnt lgkmcnt(8)\n"
"v_mfma_f32_16x16x32_bf16 v[196:199],v[232:235],v[96:99],v[196:199]\n"
"ds_read_b64_tr_b16 v[224:225],v249 offset:8192\n"
"buffer_store_dwordx4 v[164:167],v252,s[32:35],s78 offen offset:1024 sc1\n"
"v_mfma_f32_16x16x32_bf16 v[196:199],v[236:239],v[100:103],v[196:199]\n"
"ds_read_b64_tr_b16 v[226:227],v249 offset:10240\n"
"s_add_u32 s92,s80,590336\n"
"v_mfma_f32_16x16x32_bf16 v[192:195],v[232:235],v[24:27],v[192:195]\n"
"ds_read_b64_tr_b16 v[228:229],v249 offset:12288\n"
"s_add_u32 m0,s91,8192\n"
"v_mfma_f32_16x16x32_bf16 v[192:195],v[236:239],v[28:31],v[192:195]\n"
"ds_read_b64_tr_b16 v[230:231],v249 offset:14336\n"
"s_waitcnt lgkmcnt(8)\n"
"v_mfma_f32_16x16x32_bf16 v[192:195],v[208:211],v[32:35],v[192:195]\n"
"ds_read_b64_tr_b16 v[232:233],v249 offset:16384\n"
"v_mfma_f32_16x16x32_bf16 v[192:195],v[212:215],v[36:39],v[192:195]\n"
"ds_read_b64_tr_b16 v[234:235],v249 offset:18432\n"
"v_mfma_f32_16x16x32_bf16 v[196:199],v[208:211],v[104:107],v[196:199]\n"
"ds_read_b64_tr_b16 v[236:237],v249 offset:20480\n"
"v_mfma_f32_16x16x32_bf16 v[196:199],v[212:215],v[108:111],v[196:199]\n"
"ds_read_b64_tr_b16 v[238:239],v249 offset:22528\n"
"s_waitcnt vmcnt(2)\n"
"s_waitcnt lgkmcnt(8)\n"
"v_mfma_f32_16x16x32_bf16 v[200:203],v[216:219],v[0:3],v[244:247]\n"
"ds_read_b64_tr_b16 v[208:209],v249 offset:24576\n"
"v_mfma_f32_16x16x32_bf16 v[200:203],v[220:223],v[4:7],v[200:203]\n"
"ds_read_b64_tr_b16 v[210:211],v249 offset:26624\n"
"s_barrier\n"
"v_mfma_f32_16x16x32_bf16 v[204:207],v[216:219],v[72:75],v[244:247]\n"
"ds_read_b64_tr_b16 v[212:213],v249 offset:28672\n"
"buffer_load_dwordx4 v250,s[16:19],s92 offen lds\n"
"v_mfma_f32_16x16x32_bf16 v[204:207],v[220:223],v[76:79],v[204:207]\n"
"ds_read_b64_tr_b16 v[214:215],v249 offset:30720\n"
"s_waitcnt lgkmcnt(8)\n"
"v_mfma_f32_16x16x32_bf16 v[204:207],v[224:227],v[80:83],v[204:207]\n"
"ds_read_b64_tr_b16 v[216:217],v249 offset:32768\n"
"s_add_u32 s90,s92,s93\n"
"v_mfma_f32_16x16x32_bf16 v[204:207],v[228:231],v[84:87],v[204:207]\n"
"ds_read_b64_tr_b16 v[218:219],v249 offset:34816\n"
"buffer_load_dwordx4 v191,s[16:19],s92 offen offset:1024 lds\n"
"v_mfma_f32_16x16x32_bf16 v[200:203],v[224:227],v[8:11],v[200:203]\n"
"ds_read_b64_tr_b16 v[220:221],v249 offset:36864\n"
"s_addk_i32 m0,8192\n"
"v_mfma_f32_16x16x32_bf16 v[200:203],v[228:231],v[12:15],v[200:203]\n"
"ds_read_b64_tr_b16 v[222:223],v249 offset:38912\n"
"s_waitcnt lgkmcnt(8)\n"
"v_mfma_f32_16x16x32_bf16 v[200:203],v[232:235],v[16:19],v[200:203]\n"
"ds_read_b64_tr_b16 v[224:225],v249 offset:40960\n"
"v_mfma_f32_16x16x32_bf16 v[200:203],v[236:239],v[20:23],v[200:203]\n"
"ds_read_b64_tr_b16 v[226:227],v249 offset:43008\n"
"v_mfma_f32_16x16x32_bf16 v[204:207],v[232:235],v[88:91],v[204:207]\n"
"ds_read_b64_tr_b16 v[228:229],v249 offset:45056\n"
"v_mfma_f32_16x16x32_bf16 v[204:207],v[236:239],v[92:95],v[204:207]\n"
"ds_read_b64_tr_b16 v[230:231],v249 offset:47104\n"
"s_waitcnt lgkmcnt(8)\n"
"v_mfma_f32_16x16x32_bf16 v[204:207],v[208:211],v[96:99],v[204:207]\n"
"ds_read_b64_tr_b16 v[232:233],v249 offset:49152\n"
"buffer_load_dwordx4 v250,s[16:19],s90 offen lds\n"
"v_mfma_f32_16x16x32_bf16 v[204:207],v[212:215],v[100:103],v[204:207]\n"
"ds_read_b64_tr_b16 v[234:235],v249 offset:51200\n"
"s_add_u32 s92,s90,s93\n"
"v_mfma_f32_16x16x32_bf16 v[200:203],v[208:211],v[24:27],v[200:203]\n"
"ds_read_b64_tr_b16 v[236:237],v249 offset:53248\n"
"v_mfma_f32_16x16x32_bf16 v[200:203],v[212:215],v[28:31],v[200:203]\n"
"ds_read_b64_tr_b16 v[238:239],v249 offset:55296\n"
"s_waitcnt lgkmcnt(8)\n"
"v_mfma_f32_16x16x32_bf16 v[200:203],v[216:219],v[32:35],v[200:203]\n"
"ds_read_b64_tr_b16 v[208:209],v249 offset:57344\n"
"v_mfma_f32_16x16x32_bf16 v[200:203],v[220:223],v[36:39],v[200:203]\n"
"ds_read_b64_tr_b16 v[210:211],v249 offset:59392\n"
"v_mfma_f32_16x16x32_bf16 v[204:207],v[216:219],v[104:107],v[204:207]\n"
"ds_read_b64_tr_b16 v[212:213],v249 offset:61440\n"
"buffer_load_dwordx4 v191,s[16:19],s90 offen offset:1024 lds\n"
"v_mfma_f32_16x16x32_bf16 v[204:207],v[220:223],v[108:111],v[204:207]\n"
"ds_read_b64_tr_b16 v[214:215],v249 offset:63488\n"
"s_waitcnt lgkmcnt(8)\n"
"v_mfma_f32_16x16x32_bf16 v[204:207],v[224:227],v[112:115],v[204:207]\n"
"v_add_u32 v249,0x10000,v249\n"
"ds_read_b64_tr_b16 v[216:217],v249\n"
"s_addk_i32 m0,8192\n"
"v_mfma_f32_16x16x32_bf16 v[204:207],v[228:231],v[116:119],v[204:207]\n"
"ds_read_b64_tr_b16 v[218:219],v249 offset:2048\n"
"s_setprio 2\n"
"v_mfma_f32_16x16x32_bf16 v[200:203],v[224:227],v[40:43],v[200:203]\n"
"ds_read_b64_tr_b16 v[220:221],v249 offset:4096\n"
"v_mfma_f32_16x16x32_bf16 v[200:203],v[228:231],v[44:47],v[200:203]\n"
"ds_read_b64_tr_b16 v[222:223],v249 offset:6144\n"
"s_waitcnt lgkmcnt(8)\n"
"v_mfma_f32_16x16x32_bf16 v[200:203],v[232:235],v[48:51],v[200:203]\n"
".Lk_padm5:\n"
"s_cbranch_execz .Lk_hop_retm\n"
"ds_read_b64_tr_b16 v[224:225],v248 offset:40960\n"
"buffer_load_dwordx4 v250,s[16:19],s92 offen lds\n"
"v_mfma_f32_16x16x32_bf16 v[200:203],v[236:239],v[52:55],v[200:203]\n"
"ds_read_b64_tr_b16 v[226:227],v248 offset:43008\n"
"s_add_u32 s90,s92,s93\n"
"v_mfma_f32_16x16x32_bf16 v[204:207],v[232:235],v[120:123],v[204:207]\n"
"ds_read_b64_tr_b16 v[228:229],v248 offset:45056\n"
"v_mfma_f32_16x16x32_bf16 v[204:207],v[236:239],v[124:127],v[204:207]\n"
"ds_read_b64_tr_b16 v[230:231],v248 offset:47104\n"
"s_waitcnt lgkmcnt(8)\n"
"v_mfma_f32_16x16x32_bf16 v[204:207],v[208:211],v[128:131],v[204:207]\n"
"ds_read_b64_tr_b16 v[232:233],v248 offset:49152\n"
"v_mfma_f32_16x16x32_bf16 v[204:207],v[212:215],v[132:135],v[204:207]\n"
"ds_read_b64_tr_b16 v[234:235],v248 offset:51200\n"
"v_mfma_f32_16x16x32_bf16 v[200:203],v[208:211],v[56:59],v[200:203]\n"
"ds_read_b64_tr_b16 v[236:237],v248 offset:53248\n"
"buffer_load_dwordx4 v191,s[16:19],s92 offen offset:1024 lds\n"
"v_mfma_f32_16x16x32_bf16 v[200:203],v[212:215],v[60:63],v[200:203]\n"
"ds_read_b64_tr_b16 v[238:239],v248 offset:55296\n"
"s_waitcnt lgkmcnt(8)\n"
"v_mfma_f32_16x16x32_bf16 v[200:203],v[216:219],v[64:67],v[200:203]\n"
"ds_read_b64_tr_b16 v[208:209],v248 offset:57344\n"
"s_addk_i32 m0,8192\n"
"v_mfma_f32_16x16x32_bf16 v[200:203],v[220:223],v[68:71],v[200:203]\n"
"ds_read_b64_tr_b16 v[210:211],v248 offset:59392\n"
"v_mfma_f32_16x16x32_bf16 v[204:207],v[216:219],v[136:139],v[204:207]\n"
"ds_read_b64_tr_b16 v[212:213],v248 offset:61440\n"
"v_mfma_f32_16x16x32_bf16 v[204:207],v[220:223],v[140:143],v[204:207]\n"
"ds_read_b64_tr_b16 v[214:215],v248 offset:63488\n"
"s_waitcnt lgkmcnt(8)\n"
"v_mfma_f32_16x16x32_bf16 v[192:195],v[224:227],v[40:43],v[192:195]\n"
"v_add_u32 v248,0x10000,v248\n"
"ds_read_b64_tr_b16 v[216:217],v248\n"
"v_mfma_f32_16x16x32_bf16 v[192:195],v[228:231],v[44:47],v[192:195]\n"
"ds_read_b64_tr_b16 v[218:219],v248 offset:2048\n"
"buffer_load_dwordx4 v250,s[16:19],s90 offen lds\n"
"v_mfma_f32_16x16x32_bf16 v[196:199],v[224:227],v[112:115],v[196:199]\n"
"ds_read_b64_tr_b16 v[220:221],v248 offset:4096\n"
"v_mfma_f32_16x16x32_bf16 v[196:199],v[228:231],v[116:119],v[196:199]\n"
"ds_read_b64_tr_b16 v[222:223],v248 offset:6144\n"
"s_waitcnt lgkmcnt(8)\n"
"v_mfma_f32_16x16x32_bf16 v[196:199],v[232:235],v[120:123],v[196:199]\n"
"v_mfma_f32_16x16x32_bf16 v[196:199],v[236:239],v[124:127],v[196:199]\n"
"v_mfma_f32_16x16x32_bf16 v[192:195],v[232:235],v[48:51],v[192:195]\n"
"v_mfma_f32_16x16x32_bf16 v[192:195],v[236:239],v[52:55],v[192:195]\n"
"buffer_load_dwordx4 v191,s[16:19],s90 offen offset:1024 lds\n"
"s_waitcnt lgkmcnt(4)\n"
"v_mfma_f32_16x16x32_bf16 v[192:195],v[208:211],v[56:59],v[192:195]\n"
"v_mfma_f32_16x16x32_bf16 v[192:195],v[212:215],v[60:63],v[192:195]\n"
"v_add_u32 v248,4294828032,v248\n"
"v_mfma_f32_16x16x32_bf16 v[196:199],v[208:211],v[128:131],v[196:199]\n"
"v_mfma_f32_16x16x32_bf16 v[196:199],v[212:215],v[132:135],v[196:199]\n"
"v_add_u32 v249,4294828032,v249\n"
"s_waitcnt lgkmcnt(0)\n"
"v_mfma_f32_16x16x32_bf16 v[196:199],v[216:219],v[136:139],v[196:199]\n"
"v_mfma_f32_16x16x32_bf16 v[196:199],v[220:223],v[140:143],v[196:199]\n"
"v_mfma_f32_16x16x32_bf16 v[192:195],v[216:219],v[64:67],v[192:195]\n"
"v_mfma_f32_16x16x32_bf16 v[192:195],v[220:223],v[68:71],v[192:195]\n"
"s_setprio 0\n"
"v_fma_f32 v244,-v200,v200,s79\n"
"v_fma_f32 v245,-v201,v201,s79\n"
"v_fma_f32 v246,-v202,v202,s79\n"
"v_fma_f32 v247,-v203,v203,s79\n"
"s_add_u32 s92,s80,2949632\n"
"s_addk_i32 m0,8192\n"
"v_mul_f32 v244,v244,v200\n"
"v_mul_f32 v245,v245,v201\n"
"buffer_load_dwordx4 v250,s[16:19],s92 offen lds\n"
"v_mul_f32 v246,v246,v202\n"
"v_mul_f32 v247,v247,v203\n"
"v_exp_f32 v244,v244\n"
"v_exp_f32 v245,v245\n"
"v_exp_f32 v246,v246\n"
"v_exp_f32 v247,v247\n"
"v_add_f32 v244,1.0,v244\n"
"v_add_f32 v245,1.0,v245\n"
"v_add_f32 v246,1.0,v246\n"
"v_add_f32 v247,1.0,v247\n"
"v_rcp_f32 v244,v244\n"
"v_rcp_f32 v245,v245\n"
"v_rcp_f32 v246,v246\n"
"s_add_u32 s90,s92,s93\n"
"v_rcp_f32 v247,v247\n"
"buffer_load_dwordx4 v191,s[16:19],s92 offen offset:1024 lds\n"
"v_mul_f32 v244,v244,v200\n"
"v_mul_f32 v245,v245,v201\n"
"v_mul_f32 v246,v246,v202\n"
"v_mul_f32 v247,v247,v203\n"
"v_cvt_pk_bf16_f32 v170,v244,v245\n"
"v_cvt_pk_bf16_f32 v171,v246,v247\n"
"v_fma_f32 v240,-v192,v192,s79\n"
"v_fma_f32 v241,-v193,v193,s79\n"
"v_fma_f32 v242,-v194,v194,s79\n"
"v_fma_f32 v243,-v195,v195,s79\n"
"v_mul_f32 v240,v240,v192\n"
"v_mul_f32 v241,v241,v193\n"
"v_mul_f32 v242,v242,v194\n"
"v_mul_f32 v243,v243,v195\n"
"v_exp_f32 v240,v240\n"
"v_exp_f32 v241,v241\n"
"v_exp_f32 v242,v242\n"
"s_addk_i32 m0,8192\n"
"v_exp_f32 v243,v243\n"
"buffer_load_dwordx4 v250,s[16:19],s90 offen lds\n"
"v_add_f32 v240,1.0,v240\n"
"v_add_f32 v241,1.0,v241\n"
"v_add_f32 v242,1.0,v242\n"
"v_add_f32 v243,1.0,v243\n"
"v_rcp_f32 v240,v240\n"
"v_rcp_f32 v241,v241\n"
"v_rcp_f32 v242,v242\n"
"v_rcp_f32 v243,v243\n"
"v_mul_f32 v240,v240,v192\n"
"v_mul_f32 v241,v241,v193\n"
"v_mul_f32 v242,v242,v194\n"
"v_mul_f32 v243,v243,v195\n"
"v_cvt_pk_bf16_f32 v168,v240,v241\n"
"v_cvt_pk_bf16_f32 v169,v242,v243\n"
"v_fma_f32 v240,-v196,v196,s79\n"
"v_fma_f32 v241,-v197,v197,s79\n"
"v_fma_f32 v242,-v198,v198,s79\n"
"v_fma_f32 v243,-v199,v199,s79\n"
"buffer_load_dwordx4 v191,s[16:19],s90 offen offset:1024 lds\n"
"v_mul_f32 v240,v240,v196\n"
"v_mul_f32 v241,v241,v197\n"
"v_mul_f32 v242,v242,v198\n"
"v_mul_f32 v243,v243,v199\n"
"v_exp_f32 v240,v240\n"
"v_exp_f32 v241,v241\n"
"v_exp_f32 v242,v242\n"
"v_exp_f32 v243,v243\n"
"v_add_f32 v240,1.0,v240\n"
"v_add_f32 v241,1.0,v241\n"
"v_add_f32 v242,1.0,v242\n"
"v_add_f32 v243,1.0,v243\n"
"v_rcp_f32 v240,v240\n"
"v_rcp_f32 v241,v241\n"
"v_rcp_f32 v242,v242\n"
"v_rcp_f32 v243,v243\n"
"v_mul_f32 v240,v240,v196\n"
"v_mul_f32 v241,v241,v197\n"
"v_mul_f32 v242,v242,v198\n"
"v_mul_f32 v243,v243,v199\n"
"s_waitcnt vmcnt(4)\n"
"s_barrier\n"
"v_cvt_pk_bf16_f32 v172,v240,v241\n"
"v_cvt_pk_bf16_f32 v173,v242,v243\n"
"v_fma_f32 v244,-v204,v204,s79\n"
"v_fma_f32 v245,-v205,v205,s79\n"
"v_fma_f32 v246,-v206,v206,s79\n"
"v_fma_f32 v247,-v207,v207,s79\n"
"v_mul_f32 v244,v244,v204\n"
"v_mul_f32 v245,v245,v205\n"
"v_mov_b32 v208,1\n"
"s_mov_b64 exec,1\n"
"buffer_store_dword v208,off,s[32:35],s88 sc1\n"
"s_mov_b64 exec,-1\n"
"v_mul_f32 v246,v246,v206\n"
"v_mul_f32 v247,v247,v207\n"
"v_exp_f32 v244,v244\n"
"v_exp_f32 v245,v245\n"
"v_exp_f32 v246,v246\n"
"v_exp_f32 v247,v247\n"
"v_add_f32 v244,1.0,v244\n"
"v_add_f32 v245,1.0,v245\n"
"v_add_f32 v246,1.0,v246\n"
"v_add_f32 v247,1.0,v247\n"
"v_rcp_f32 v244,v244\n"
"s_setprio 3\n"
"v_rcp_f32 v245,v245\n"
"ds_read_b128 v[240:243],v253 offset:1024\n"
"ds_read_b64_tr_b16 v[208:209],v248\n"
"v_rcp_f32 v246,v246\n"
"ds_read_b64_tr_b16 v[210:211],v248 offset:2048\n"
"ds_read_b64_tr_b16 v[212:213],v248 offset:4096\n"
"v_rcp_f32 v247,v247\n"
"ds_read_b64_tr_b16 v[214:215],v248 offset:6144\n"
"ds_read_b64_tr_b16 v[216:217],v248 offset:8192\n"
"v_mul_f32 v244,v244,v204\n"
"v_mul_f32 v245,v245,v205\n"
"ds_read_b64_tr_b16 v[218:219],v248 offset:10240\n"
"ds_read_b64_tr_b16 v[220:221],v248 offset:12288\n"
"v_mul_f32 v246,v246,v206\n"
"v_mul_f32 v247,v247,v207\n"
"ds_read_b64_tr_b16 v[222:223],v248 offset:14336\n"
"ds_read_b64_tr_b16 v[224:225],v248 offset:16384\n"
"v_cvt_pk_bf16_f32 v174,v244,v245\n"
"v_cvt_pk_bf16_f32 v175,v246,v247\n"
"ds_read_b64_tr_b16 v[226:227],v248 offset:18432\n"
"ds_read_b64_tr_b16 v[228:229],v248 offset:20480\n"
"ds_read_b64_tr_b16 v[230:231],v248 offset:22528\n"
"s_setprio 1\n"
"s_waitcnt lgkmcnt(12)\n"
"s_waitcnt lgkmcnt(8)\n"
"v_mfma_f32_16x16x32_bf16 v[192:195],v[208:211],v[0:3],v[240:243]\n"
"ds_read_b64_tr_b16 v[232:233],v248 offset:24576\n"
"ds_read_b128 v[244:247],v253 offset:1088\n"
"v_mfma_f32_16x16x32_bf16 v[192:195],v[212:215],v[4:7],v[192:195]\n"
"ds_read_b64_tr_b16 v[234:235],v248 offset:26624\n"
"s_add_u32 s78,s84,6144\n"
"v_mfma_f32_16x16x32_bf16 v[196:199],v[208:211],v[72:75],v[240:243]\n"
"ds_read_b64_tr_b16 v[236:237],v248 offset:28672\n"
"v_mfma_f32_16x16x32_bf16 v[196:199],v[212:215],v[76:79],v[196:199]\n"
"ds_read_b64_tr_b16 v[238:239],v248 offset:30720\n"
"s_waitcnt lgkmcnt(9)\n"
"v_mfma_f32_16x16x32_bf16 v[196:199],v[216:219],v[80:83],v[196:199]\n"
"ds_read_b64_tr_b16 v[208:209],v248 offset:32768\n"
"v_mfma_f32_16x16x32_bf16 v[196:199],v[220:223],v[84:87],v[196:199]\n"
"ds_read_b64_tr_b16 v[210:211],v248 offset:34816\n"
"v_mfma_f32_16x16x32_bf16 v[192:195],v[216:219],v[8:11],v[192:195]\n"
"ds_read_b64_tr_b16 v[212:213],v248 offset:36864\n"
"buffer_store_dwordx4 v[168:171],v252,s[32:35],s78 offen sc1\n"
"v_mfma_f32_16x16x32_bf16 v[192:195],v[220:223],v[12:15],v[192:195]\n"
"ds_read_b64_tr_b16 v[214:215],v248 offset:38912\n"
"s_waitcnt lgkmcnt(9)\n"
"v_mfma_f32_16x16x32_bf16 v[192:195],v[224:227],v[16:19],v[192:195]\n"
"ds_read_b64_tr_b16 v[216:217],v249\n"
"v_mfma_f32_16x16x32_bf16 v[192:195],v[228:231],v[20:23],v[192:195]\n"
"ds_read_b64_tr_b16 v[218:219],v249 offset:2048\n"
"v_mfma_f32_16x16x32_bf16 v[196:199],v[224:227],v[88:91],v[196:199]\n"
"ds_read_b64_tr_b16 v[220:221],v249 offset:4096\n"
"v_mfma_f32_16x16x32_bf16 v[196:199],v[228:231],v[92:95],v[196:199]\n"
"ds_read_b64_tr_b16 v[222:223],v249 offset:6144\n"
"s_waitcnt lgkmcnt(8)\n"
"v_mfma_f32_16x16x32_bf16 v[196:199],v[232:235],v[96:99],v[196:199]\n"
"ds_read_b64_tr_b16 v[224:225],v249 offset:8192\n"
"buffer_store_dwordx4 v[172:175],v252,s[32:35],s78 offen offset:1024 sc1\n"
"v_mfma_f32_16x16x32_bf16 v[196:199],v[236:239],v[100:103],v[196:199]\n"
"ds_read_b64_tr_b16 v[226:227],v249 offset:10240\n"
"s_add_u32 s92,s80,590464\n"
"v_mfma_f32_16x16x32_bf16 v[192:195],v[232:235],v[24:27],v[192:195]\n"
"ds_read_b64_tr_b16 v[228:229],v249 offset:12288\n"
"s_add_u32 m0,s91,81920\n"
"v_mfma_f32_16x16x32_bf16 v[192:195],v[236:239],v[28:31],v[192:195]\n"
"ds_read_b64_tr_b16 v[230:231],v249 offset:14336\n"
"s_waitcnt lgkmcnt(8)\n"
"v_mfma_f32_16x16x32_bf16 v[192:195],v[208:211],v[32:35],v[192:195]\n"
"ds_read_b64_tr_b16 v[232:233],v249 offset:16384\n"
"v_mfma_f32_16x16x32_bf16 v[192:195],v[212:215],v[36:39],v[192:195]\n"
"ds_read_b64_tr_b16 v[234:235],v249 offset:18432\n"
"v_mfma_f32_16x16x32_bf16 v[196:199],v[208:211],v[104:107],v[196:199]\n"
"ds_read_b64_tr_b16 v[236:237],v249 offset:20480\n"
"v_mfma_f32_16x16x32_bf16 v[196:199],v[212:215],v[108:111],v[196:199]\n"
"ds_read_b64_tr_b16 v[238:239],v249 offset:22528\n"
"s_waitcnt vmcnt(3)\n"
"s_waitcnt lgkmcnt(8)\n"
"v_mfma_f32_16x16x32_bf16 v[200:203],v[216:219],v[0:3],v[244:247]\n"
"ds_read_b64_tr_b16 v[208:209],v249 offset:24576\n"
"v_mfma_f32_16x16x32_bf16 v[200:203],v[220:223],v[4:7],v[200:203]\n"
"ds_read_b64_tr_b16 v[210:211],v249 offset:26624\n"
"s_barrier\n"
"v_mfma_f32_16x16x32_bf16 v[204:207],v[216:219],v[72:75],v[244:247]\n"
"ds_read_b64_tr_b16 v[212:213],v249 offset:28672\n"
"buffer_load_dwordx4 v250,s[16:19],s92 offen lds\n"
"v_mfma_f32_16x16x32_bf16 v[204:207],v[220:223],v[76:79],v[204:207]\n"
"ds_read_b64_tr_b16 v[214:215],v249 offset:30720\n"
"s_waitcnt lgkmcnt(8)\n"
"v_mfma_f32_16x16x32_bf16 v[204:207],v[224:227],v[80:83],v[204:207]\n"
"ds_read_b64_tr_b16 v[216:217],v249 offset:32768\n"
"s_add_u32 s90,s92,s93\n"
"v_mfma_f32_16x16x32_bf16 v[204:207],v[228:231],v[84:87],v[204:207]\n"
"ds_read_b64_tr_b16 v[218:219],v249 offset:34816\n"
"buffer_load_dwordx4 v191,s[16:19],s92 offen offset:1024 lds\n"
"v_mfma_f32_16x16x32_bf16 v[200:203],v[224:227],v[8:11],v[200:203]\n"
"ds_read_b64_tr_b16 v[220:221],v249 offset:36864\n"
"s_addk_i32 m0,8192\n"
"v_mfma_f32_16x16x32_bf16 v[200:203],v[228:231],v[12:15],v[200:203]\n"
"ds_read_b64_tr_b16 v[222:223],v249 offset:38912\n"
"s_waitcnt lgkmcnt(8)\n"
"v_mfma_f32_16x16x32_bf16 v[200:203],v[232:235],v[16:19],v[200:203]\n"
"ds_read_b64_tr_b16 v[224:225],v249 offset:40960\n"
"v_mfma_f32_16x16x32_bf16 v[200:203],v[236:239],v[20:23],v[200:203]\n"
"ds_read_b64_tr_b16 v[226:227],v249 offset:43008\n"
"v_mfma_f32_16x16x32_bf16 v[204:207],v[232:235],v[88:91],v[204:207]\n"
"ds_read_b64_tr_b16 v[228:229],v249 offset:45056\n"
"v_mfma_f32_16x16x32_bf16 v[204:207],v[236:239],v[92:95],v[204:207]\n"
"ds_read_b64_tr_b16 v[230:231],v249 offset:47104\n"
"s_waitcnt lgkmcnt(8)\n"
"v_mfma_f32_16x16x32_bf16 v[204:207],v[208:211],v[96:99],v[204:207]\n"
"ds_read_b64_tr_b16 v[232:233],v249 offset:49152\n"
"buffer_load_dwordx4 v250,s[16:19],s90 offen lds\n"
"v_mfma_f32_16x16x32_bf16 v[204:207],v[212:215],v[100:103],v[204:207]\n"
"ds_read_b64_tr_b16 v[234:235],v249 offset:51200\n"
"s_add_u32 s92,s90,s93\n"
"v_mfma_f32_16x16x32_bf16 v[200:203],v[208:211],v[24:27],v[200:203]\n"
"ds_read_b64_tr_b16 v[236:237],v249 offset:53248\n"
"v_mfma_f32_16x16x32_bf16 v[200:203],v[212:215],v[28:31],v[200:203]\n"
"ds_read_b64_tr_b16 v[238:239],v249 offset:55296\n"
"s_waitcnt lgkmcnt(8)\n"
"v_mfma_f32_16x16x32_bf16 v[200:203],v[216:219],v[32:35],v[200:203]\n"
"ds_read_b64_tr_b16 v[208:209],v249 offset:57344\n"
"v_mfma_f32_16x16x32_bf16 v[200:203],v[220:223],v[36:39],v[200:203]\n"
"ds_read_b64_tr_b16 v[210:211],v249 offset:59392\n"
"v_mfma_f32_16x16x32_bf16 v[204:207],v[216:219],v[104:107],v[204:207]\n"
"ds_read_b64_tr_b16 v[212:213],v249 offset:61440\n"
"buffer_load_dwordx4 v191,s[16:19],s90 offen offset:1024 lds\n"
"v_mfma_f32_16x16x32_bf16 v[204:207],v[220:223],v[108:111],v[204:207]\n"
"ds_read_b64_tr_b16 v[214:215],v249 offset:63488\n"
"s_waitcnt lgkmcnt(8)\n"
"v_mfma_f32_16x16x32_bf16 v[204:207],v[224:227],v[112:115],v[204:207]\n"
"v_add_u32 v249,0x10000,v249\n"
"ds_read_b64_tr_b16 v[216:217],v249\n"
"s_addk_i32 m0,8192\n"
"v_mfma_f32_16x16x32_bf16 v[204:207],v[228:231],v[116:119],v[204:207]\n"
"ds_read_b64_tr_b16 v[218:219],v249 offset:2048\n"
"s_setprio 2\n"
"v_mfma_f32_16x16x32_bf16 v[200:203],v[224:227],v[40:43],v[200:203]\n"
"ds_read_b64_tr_b16 v[220:221],v249 offset:4096\n"
"v_mfma_f32_16x16x32_bf16 v[200:203],v[228:231],v[44:47],v[200:203]\n"
"ds_read_b64_tr_b16 v[222:223],v249 offset:6144\n"
"s_waitcnt lgkmcnt(8)\n"
"v_mfma_f32_16x16x32_bf16 v[200:203],v[232:235],v[48:51],v[200:203]\n"
"ds_read_b64_tr_b16 v[224:225],v248 offset:40960\n"
"buffer_load_dwordx4 v250,s[16:19],s92 offen lds\n"
"v_mfma_f32_16x16x32_bf16 v[200:203],v[236:239],v[52:55],v[200:203]\n"
"ds_read_b64_tr_b16 v[226:227],v248 offset:43008\n"
"s_add_u32 s90,s92,s93\n"
"v_mfma_f32_16x16x32_bf16 v[204:207],v[232:235],v[120:123],v[204:207]\n"
"ds_read_b64_tr_b16 v[228:229],v248 offset:45056\n"
"v_mfma_f32_16x16x32_bf16 v[204:207],v[236:239],v[124:127],v[204:207]\n"
"ds_read_b64_tr_b16 v[230:231],v248 offset:47104\n"
"s_waitcnt lgkmcnt(8)\n"
"v_mfma_f32_16x16x32_bf16 v[204:207],v[208:211],v[128:131],v[204:207]\n"
"ds_read_b64_tr_b16 v[232:233],v248 offset:49152\n"
"v_mfma_f32_16x16x32_bf16 v[204:207],v[212:215],v[132:135],v[204:207]\n"
"ds_read_b64_tr_b16 v[234:235],v248 offset:51200\n"
"v_mfma_f32_16x16x32_bf16 v[200:203],v[208:211],v[56:59],v[200:203]\n"
"ds_read_b64_tr_b16 v[236:237],v248 offset:53248\n"
"buffer_load_dwordx4 v191,s[16:19],s92 offen offset:1024 lds\n"
"v_mfma_f32_16x16x32_bf16 v[200:203],v[212:215],v[60:63],v[200:203]\n"
"ds_read_b64_tr_b16 v[238:239],v248 offset:55296\n"
"s_waitcnt lgkmcnt(8)\n"
"v_mfma_f32_16x16x32_bf16 v[200:203],v[216:219],v[64:67],v[200:203]\n"
"ds_read_b64_tr_b16 v[208:209],v248 offset:57344\n"
"s_addk_i32 m0,8192\n"
"v_mfma_f32_16x16x32_bf16 v[200:203],v[220:223],v[68:71],v[200:203]\n"
"ds_read_b64_tr_b16 v[210:211],v248 offset:59392\n"
"v_mfma_f32_16x16x32_bf16 v[204:207],v[216:219],v[136:139],v[204:207]\n"
"ds_read_b64_tr_b16 v[212:213],v248 offset:61440\n"
"v_mfma_f32_16x16x32_bf16 v[204:207],v[220:223],v[140:143],v[204:207]\n"
"ds_read_b64_tr_b16 v[214:215],v248 offset:63488\n"
"s_waitcnt lgkmcnt(8)\n"
"v_mfma_f32_16x16x32_bf16 v[192:195],v[224:227],v[40:43],v[192:195]\n"
"v_add_u32 v248,0x10000,v248\n"
"ds_read_b64_tr_b16 v[216:217],v248\n"
"v_mfma_f32_16x16x32_bf16 v[192:195],v[228:231],v[44:47],v[192:195]\n"
"ds_read_b64_tr_b16 v[218:219],v248 offset:2048\n"
"buffer_load_dwordx4 v250,s[16:19],s90 offen lds\n"
"v_mfma_f32_16x16x32_bf16 v[196:199],v[224:227],v[112:115],v[196:199]\n"
"ds_read_b64_tr_b16 v[220:221],v248 offset:4096\n"
"v_mfma_f32_16x16x32_bf16 v[196:199],v[228:231],v[116:119],v[196:199]\n"
"ds_read_b64_tr_b16 v[222:223],v248 offset:6144\n"
"s_waitcnt lgkmcnt(8)\n"
"v_mfma_f32_16x16x32_bf16 v[196:199],v[232:235],v[120:123],v[196:199]\n"
"v_mfma_f32_16x16x32_bf16 v[196:199],v[236:239],v[124:127],v[196:199]\n"
"v_mfma_f32_16x16x32_bf16 v[192:195],v[232:235],v[48:51],v[192:195]\n"
"v_mfma_f32_16x16x32_bf16 v[192:195],v[236:239],v[52:55],v[192:195]\n"
"buffer_load_dwordx4 v191,s[16:19],s90 offen offset:1024 lds\n"
"s_waitcnt lgkmcnt(4)\n"
"v_mfma_f32_16x16x32_bf16 v[192:195],v[208:211],v[56:59],v[192:195]\n"
"v_mfma_f32_16x16x32_bf16 v[192:195],v[212:215],v[60:63],v[192:195]\n"
"v_add_u32 v248,8192,v248\n"
"v_mfma_f32_16x16x32_bf16 v[196:199],v[208:211],v[128:131],v[196:199]\n"
"v_mfma_f32_16x16x32_bf16 v[196:199],v[212:215],v[132:135],v[196:199]\n"
"v_add_u32 v249,8192,v249\n"
"s_waitcnt lgkmcnt(0)\n"
"v_mfma_f32_16x16x32_bf16 v[196:199],v[216:219],v[136:139],v[196:199]\n"
"v_mfma_f32_16x16x32_bf16 v[196:199],v[220:223],v[140:143],v[196:199]\n"
"v_mfma_f32_16x16x32_bf16 v[192:195],v[216:219],v[64:67],v[192:195]\n"
"v_mfma_f32_16x16x32_bf16 v[192:195],v[220:223],v[68:71],v[192:195]\n"
"s_setprio 0\n"
"v_fma_f32 v244,-v200,v200,s79\n"
"v_fma_f32 v245,-v201,v201,s79\n"
"v_fma_f32 v246,-v202,v202,s79\n"
"v_fma_f32 v247,-v203,v203,s79\n"
"s_add_u32 s92,s80,2949760\n"
"s_addk_i32 m0,8192\n"
"v_mul_f32 v244,v244,v200\n"
"v_mul_f32 v245,v245,v201\n"
"buffer_load_dwordx4 v250,s[16:19],s92 offen lds\n"
"v_mul_f32 v246,v246,v202\n"
"v_mul_f32 v247,v247,v203\n"
"v_exp_f32 v244,v244\n"
"v_exp_f32 v245,v245\n"
"v_exp_f32 v246,v246\n"
"v_exp_f32 v247,v247\n"
"v_add_f32 v244,1.0,v244\n"
"v_add_f32 v245,1.0,v245\n"
"v_add_f32 v246,1.0,v246\n"
"v_add_f32 v247,1.0,v247\n"
"v_rcp_f32 v244,v244\n"
"v_rcp_f32 v245,v245\n"
"v_rcp_f32 v246,v246\n"
"s_add_u32 s90,s92,s93\n"
"v_rcp_f32 v247,v247\n"
"buffer_load_dwordx4 v191,s[16:19],s92 offen offset:1024 lds\n"
"v_mul_f32 v244,v244,v200\n"
"v_mul_f32 v245,v245,v201\n"
"v_mul_f32 v246,v246,v202\n"
"v_mul_f32 v247,v247,v203\n"
"v_cvt_pk_bf16_f32 v178,v244,v245\n"
"v_cvt_pk_bf16_f32 v179,v246,v247\n"
"v_fma_f32 v240,-v192,v192,s79\n"
"v_fma_f32 v241,-v193,v193,s79\n"
"v_fma_f32 v242,-v194,v194,s79\n"
"v_fma_f32 v243,-v195,v195,s79\n"
"v_mul_f32 v240,v240,v192\n"
"v_mul_f32 v241,v241,v193\n"
"v_mul_f32 v242,v242,v194\n"
"v_mul_f32 v243,v243,v195\n"
"v_exp_f32 v240,v240\n"
"v_exp_f32 v241,v241\n"
"v_exp_f32 v242,v242\n"
"s_addk_i32 m0,8192\n"
"v_exp_f32 v243,v243\n"
"buffer_load_dwordx4 v250,s[16:19],s90 offen lds\n"
"v_add_f32 v240,1.0,v240\n"
"v_add_f32 v241,1.0,v241\n"
"v_add_f32 v242,1.0,v242\n"
"v_add_f32 v243,1.0,v243\n"
"v_rcp_f32 v240,v240\n"
"v_rcp_f32 v241,v241\n"
"v_rcp_f32 v242,v242\n"
"v_rcp_f32 v243,v243\n"
"v_mul_f32 v240,v240,v192\n"
"v_mul_f32 v241,v241,v193\n"
"v_mul_f32 v242,v242,v194\n"
"v_mul_f32 v243,v243,v195\n"
"v_cvt_pk_bf16_f32 v176,v240,v241\n"
"v_cvt_pk_bf16_f32 v177,v242,v243\n"
"v_fma_f32 v240,-v196,v196,s79\n"
"v_fma_f32 v241,-v197,v197,s79\n"
"v_fma_f32 v242,-v198,v198,s79\n"
"v_fma_f32 v243,-v199,v199,s79\n"
"buffer_load_dwordx4 v191,s[16:19],s90 offen offset:1024 lds\n"
"v_mul_f32 v240,v240,v196\n"
"v_mul_f32 v241,v241,v197\n"
"v_mul_f32 v242,v242,v198\n"
"v_mul_f32 v243,v243,v199\n"
"v_exp_f32 v240,v240\n"
"v_exp_f32 v241,v241\n"
"v_exp_f32 v242,v242\n"
"v_exp_f32 v243,v243\n"
"v_add_f32 v240,1.0,v240\n"
"v_add_f32 v241,1.0,v241\n"
"v_add_f32 v242,1.0,v242\n"
"v_add_f32 v243,1.0,v243\n"
"v_rcp_f32 v240,v240\n"
"v_rcp_f32 v241,v241\n"
"v_rcp_f32 v242,v242\n"
"v_rcp_f32 v243,v243\n"
"v_mul_f32 v240,v240,v196\n"
"v_mul_f32 v241,v241,v197\n"
"v_mul_f32 v242,v242,v198\n"
"v_mul_f32 v243,v243,v199\n"
"s_waitcnt vmcnt(4)\n"
"s_barrier\n"
"v_cvt_pk_bf16_f32 v180,v240,v241\n"
"v_cvt_pk_bf16_f32 v181,v242,v243\n"
"v_fma_f32 v244,-v204,v204,s79\n"
"v_fma_f32 v245,-v205,v205,s79\n"
"v_fma_f32 v246,-v206,v206,s79\n"
"v_fma_f32 v247,-v207,v207,s79\n"
"v_mul_f32 v244,v244,v204\n"
"v_mul_f32 v245,v245,v205\n"
"v_mul_f32 v246,v246,v206\n"
"v_mul_f32 v247,v247,v207\n"
"v_exp_f32 v244,v244\n"
"v_exp_f32 v245,v245\n"
"v_exp_f32 v246,v246\n"
"v_exp_f32 v247,v247\n"
"v_add_f32 v244,1.0,v244\n"
"v_add_f32 v245,1.0,v245\n"
"v_add_f32 v246,1.0,v246\n"
"v_add_f32 v247,1.0,v247\n"
"v_rcp_f32 v244,v244\n"
"s_setprio 3\n"
"v_rcp_f32 v245,v245\n"
"ds_read_b128 v[240:243],v253 offset:1280\n"
"ds_read_b64_tr_b16 v[208:209],v248\n"
"v_rcp_f32 v246,v246\n"
"ds_read_b64_tr_b16 v[210:211],v248 offset:2048\n"
"ds_read_b64_tr_b16 v[212:213],v248 offset:4096\n"
"v_rcp_f32 v247,v247\n"
"ds_read_b64_tr_b16 v[214:215],v248 offset:6144\n"
"ds_read_b64_tr_b16 v[216:217],v248 offset:8192\n"
"v_mul_f32 v244,v244,v204\n"
"v_mul_f32 v245,v245,v205\n"
"ds_read_b64_tr_b16 v[218:219],v248 offset:10240\n"
"ds_read_b64_tr_b16 v[220:221],v248 offset:12288\n"
"v_mul_f32 v246,v246,v206\n"
"v_mul_f32 v247,v247,v207\n"
"ds_read_b64_tr_b16 v[222:223],v248 offset:14336\n"
"ds_read_b64_tr_b16 v[224:225],v248 offset:16384\n"
"v_cvt_pk_bf16_f32 v182,v244,v245\n"
"v_cvt_pk_bf16_f32 v183,v246,v247\n"
"ds_read_b64_tr_b16 v[226:227],v248 offset:18432\n"
"ds_read_b64_tr_b16 v[228:229],v248 offset:20480\n"
"ds_read_b64_tr_b16 v[230:231],v248 offset:22528\n"
"s_setprio 1\n"
"s_waitcnt lgkmcnt(12)\n"
"s_waitcnt lgkmcnt(8)\n"
"v_mfma_f32_16x16x32_bf16 v[192:195],v[208:211],v[0:3],v[240:243]\n"
"ds_read_b64_tr_b16 v[232:233],v248 offset:24576\n"
"ds_read_b128 v[244:247],v253 offset:1344\n"
"v_mfma_f32_16x16x32_bf16 v[192:195],v[212:215],v[4:7],v[192:195]\n"
"ds_read_b64_tr_b16 v[234:235],v248 offset:26624\n"
"s_add_u32 s78,s84,8192\n"
"v_mfma_f32_16x16x32_bf16 v[196:199],v[208:211],v[72:75],v[240:243]\n"
"ds_read_b64_tr_b16 v[236:237],v248 offset:28672\n"
"v_mfma_f32_16x16x32_bf16 v[196:199],v[212:215],v[76:79],v[196:199]\n"
"ds_read_b64_tr_b16 v[238:239],v248 offset:30720\n"
"s_waitcnt lgkmcnt(9)\n"
"v_mfma_f32_16x16x32_bf16 v[196:199],v[216:219],v[80:83],v[196:199]\n"
"ds_read_b64_tr_b16 v[208:209],v248 offset:32768\n"
"v_mfma_f32_16x16x32_bf16 v[196:199],v[220:223],v[84:87],v[196:199]\n"
"ds_read_b64_tr_b16 v[210:211],v248 offset:34816\n"
"buffer_store_dwordx4 v[176:179],v252,s[32:35],s78 offen sc1\n"
"v_mfma_f32_16x16x32_bf16 v[192:195],v[216:219],v[8:11],v[192:195]\n"
"ds_read_b64_tr_b16 v[212:213],v248 offset:36864\n"
"v_mfma_f32_16x16x32_bf16 v[192:195],v[220:223],v[12:15],v[192:195]\n"
"ds_read_b64_tr_b16 v[214:215],v248 offset:38912\n"
"s_waitcnt lgkmcnt(9)\n"
"v_mfma_f32_16x16x32_bf16 v[192:195],v[224:227],v[16:19],v[192:195]\n"
"ds_read_b64_tr_b16 v[216:217],v249\n"
"v_mfma_f32_16x16x32_bf16 v[192:195],v[228:231],v[20:23],v[192:195]\n"
"ds_read_b64_tr_b16 v[218:219],v249 offset:2048\n"
"v_mfma_f32_16x16x32_bf16 v[196:199],v[224:227],v[88:91],v[196:199]\n"
"ds_read_b64_tr_b16 v[220:221],v249 offset:4096\n"
"buffer_store_dwordx4 v[180:183],v252,s[32:35],s78 offen offset:1024 sc1\n"
"v_mfma_f32_16x16x32_bf16 v[196:199],v[228:231],v[92:95],v[196:199]\n"
"ds_read_b64_tr_b16 v[222:223],v249 offset:6144\n"
"s_waitcnt lgkmcnt(8)\n"
"v_mfma_f32_16x16x32_bf16 v[196:199],v[232:235],v[96:99],v[196:199]\n"
"ds_read_b64_tr_b16 v[224:225],v249 offset:8192\n"
"s_add_u32 m0,s73,0\n"
"v_mfma_f32_16x16x32_bf16 v[196:199],v[236:239],v[100:103],v[196:199]\n"
"ds_read_b64_tr_b16 v[226:227],v249 offset:10240\n"
"s_add_u32 s74,s81,0\n"
"v_mfma_f32_16x16x32_bf16 v[192:195],v[232:235],v[24:27],v[192:195]\n"
"ds_read_b64_tr_b16 v[228:229],v249 offset:12288\n"
"v_mfma_f32_16x16x32_bf16 v[192:195],v[236:239],v[28:31],v[192:195]\n"
"ds_read_b64_tr_b16 v[230:231],v249 offset:14336\n"
"s_waitcnt lgkmcnt(8)\n"
"v_mfma_f32_16x16x32_bf16 v[192:195],v[208:211],v[32:35],v[192:195]\n"
"ds_read_b64_tr_b16 v[232:233],v249 offset:16384\n"
"v_mfma_f32_16x16x32_bf16 v[192:195],v[212:215],v[36:39],v[192:195]\n"
"ds_read_b64_tr_b16 v[234:235],v249 offset:18432\n"
"v_mfma_f32_16x16x32_bf16 v[196:199],v[208:211],v[104:107],v[196:199]\n"
"ds_read_b64_tr_b16 v[236:237],v249 offset:20480\n"
"v_mfma_f32_16x16x32_bf16 v[196:199],v[212:215],v[108:111],v[196:199]\n"
"ds_read_b64_tr_b16 v[238:239],v249 offset:22528\n"
"s_waitcnt vmcnt(2)\n"
"s_waitcnt lgkmcnt(8)\n"
"v_mfma_f32_16x16x32_bf16 v[200:203],v[216:219],v[0:3],v[244:247]\n"
"ds_read_b64_tr_b16 v[208:209],v249 offset:24576\n"
"v_mfma_f32_16x16x32_bf16 v[200:203],v[220:223],v[4:7],v[200:203]\n"
"ds_read_b64_tr_b16 v[210:211],v249 offset:26624\n"
"s_barrier\n"
"v_mfma_f32_16x16x32_bf16 v[204:207],v[216:219],v[72:75],v[244:247]\n"
"ds_read_b64_tr_b16 v[212:213],v249 offset:28672\n"
"buffer_load_dwordx4 v251,s[20:23],s74 offen lds\n"
"v_mfma_f32_16x16x32_bf16 v[204:207],v[220:223],v[76:79],v[204:207]\n"
"ds_read_b64_tr_b16 v[214:215],v249 offset:30720\n"
"s_waitcnt lgkmcnt(8)\n"
"v_mfma_f32_16x16x32_bf16 v[204:207],v[224:227],v[80:83],v[204:207]\n"
"ds_read_b64_tr_b16 v[216:217],v249 offset:32768\n"
"s_addk_i32 m0,4096\n"
"v_mfma_f32_16x16x32_bf16 v[204:207],v[228:231],v[84:87],v[204:207]\n"
"ds_read_b64_tr_b16 v[218:219],v249 offset:34816\n"
"s_addk_i32 s74,128\n"
"v_mfma_f32_16x16x32_bf16 v[200:203],v[224:227],v[8:11],v[200:203]\n"
"ds_read_b64_tr_b16 v[220:221],v249 offset:36864\n"
"buffer_load_dwordx4 v251,s[20:23],s74 offen lds\n"
"v_mfma_f32_16x16x32_bf16 v[200:203],v[228:231],v[12:15],v[200:203]\n"
"ds_read_b64_tr_b16 v[222:223],v249 offset:38912\n"
"s_waitcnt lgkmcnt(8)\n"
"v_mfma_f32_16x16x32_bf16 v[200:203],v[232:235],v[16:19],v[200:203]\n"
"ds_read_b64_tr_b16 v[224:225],v249 offset:40960\n"
"s_addk_i32 m0,4096\n"
"v_mfma_f32_16x16x32_bf16 v[200:203],v[236:239],v[20:23],v[200:203]\n"
"ds_read_b64_tr_b16 v[226:227],v249 offset:43008\n"
"s_addk_i32 s74,128\n"
"v_mfma_f32_16x16x32_bf16 v[204:207],v[232:235],v[88:91],v[204:207]\n"
"ds_read_b64_tr_b16 v[228:229],v249 offset:45056\n"
"buffer_load_dwordx4 v251,s[20:23],s74 offen lds\n"
"v_mfma_f32_16x16x32_bf16 v[204:207],v[236:239],v[92:95],v[204:207]\n"
"ds_read_b64_tr_b16 v[230:231],v249 offset:47104\n"
"s_waitcnt lgkmcnt(8)\n"
"v_mfma_f32_16x16x32_bf16 v[204:207],v[208:211],v[96:99],v[204:207]\n"
"ds_read_b64_tr_b16 v[232:233],v249 offset:49152\n"
"s_addk_i32 m0,4096\n"
"v_mfma_f32_16x16x32_bf16 v[204:207],v[212:215],v[100:103],v[204:207]\n"
"ds_read_b64_tr_b16 v[234:235],v249 offset:51200\n"
"s_add_u32 s74,s81,73728\n"
"v_mfma_f32_16x16x32_bf16 v[200:203],v[208:211],v[24:27],v[200:203]\n"
"ds_read_b64_tr_b16 v[236:237],v249 offset:53248\n"
"buffer_load_dwordx4 v251,s[20:23],s74 offen lds\n"
"v_mfma_f32_16x16x32_bf16 v[200:203],v[212:215],v[28:31],v[200:203]\n"
"ds_read_b64_tr_b16 v[238:239],v249 offset:55296\n"
"s_waitcnt lgkmcnt(8)\n"
"v_mfma_f32_16x16x32_bf16 v[200:203],v[216:219],v[32:35],v[200:203]\n"
"ds_read_b64_tr_b16 v[208:209],v249 offset:57344\n"
"s_addk_i32 m0,4096\n"
"v_mfma_f32_16x16x32_bf16 v[200:203],v[220:223],v[36:39],v[200:203]\n"
"ds_read_b64_tr_b16 v[210:211],v249 offset:59392\n"
"s_addk_i32 s74,128\n"
"v_mfma_f32_16x16x32_bf16 v[204:207],v[216:219],v[104:107],v[204:207]\n"
"ds_read_b64_tr_b16 v[212:213],v249 offset:61440\n"
"buffer_load_dwordx4 v251,s[20:23],s74 offen lds\n"
"v_mfma_f32_16x16x32_bf16 v[204:207],v[220:223],v[108:111],v[204:207]\n"
"ds_read_b64_tr_b16 v[214:215],v249 offset:63488\n"
"s_waitcnt lgkmcnt(8)\n"
"v_mfma_f32_16x16x32_bf16 v[204:207],v[224:227],v[112:115],v[204:207]\n"
"v_add_u32 v249,0x10000,v249\n"
"ds_read_b64_tr_b16 v[216:217],v249\n"
"s_addk_i32 m0,4096\n"
"v_mfma_f32_16x16x32_bf16 v[204:207],v[228:231],v[116:119],v[204:207]\n"
"ds_read_b64_tr_b16 v[218:219],v249 offset:2048\n"
"s_setprio 2\n"
"v_mfma_f32_16x16x32_bf16 v[200:203],v[224:227],v[40:43],v[200:203]\n"
"ds_read_b64_tr_b16 v[220:221],v249 offset:4096\n"
"s_addk_i32 s74,128\n"
"v_mfma_f32_16x16x32_bf16 v[200:203],v[228:231],v[44:47],v[200:203]\n"
"ds_read_b64_tr_b16 v[222:223],v249 offset:6144\n"
"s_waitcnt lgkmcnt(8)\n"
"v_mfma_f32_16x16x32_bf16 v[200:203],v[232:235],v[48:51],v[200:203]\n"
"ds_read_b64_tr_b16 v[224:225],v248 offset:40960\n"
"buffer_load_dwordx4 v251,s[20:23],s74 offen lds\n"
"v_mfma_f32_16x16x32_bf16 v[200:203],v[236:239],v[52:55],v[200:203]\n"
"ds_read_b64_tr_b16 v[226:227],v248 offset:43008\n"
"v_mfma_f32_16x16x32_bf16 v[204:207],v[232:235],v[120:123],v[204:207]\n"
".Lk_padm7:\n"
"s_cbranch_execz .Lk_hop_retm\n"
"ds_read_b64_tr_b16 v[228:229],v248 offset:45056\n"
"v_mfma_f32_16x16x32_bf16 v[204:207],v[236:239],v[124:127],v[204:207]\n"
"ds_read_b64_tr_b16 v[230:231],v248 offset:47104\n"
"s_waitcnt lgkmcnt(8)\n"
"v_mfma_f32_16x16x32_bf16 v[204:207],v[208:211],v[128:131],v[204:207]\n"
"ds_read_b64_tr_b16 v[232:233],v248 offset:49152\n"
"buffer_load_dword v240,v255,s[32:35],0 offen sc1\n"
"v_mfma_f32_16x16x32_bf16 v[204:207],v[212:215],v[132:135],v[204:207]\n"
"ds_read_b64_tr_b16 v[234:235],v248 offset:51200\n"
"s_addk_i32 m0,4096\n"
"v_mfma_f32_16x16x32_bf16 v[200:203],v[208:211],v[56:59],v[200:203]\n"
"ds_read_b64_tr_b16 v[236:237],v248 offset:53248\n"
"s_add_u32 s74,s81,147456\n"
"v_mfma_f32_16x16x32_bf16 v[200:203],v[212:215],v[60:63],v[200:203]\n"
"ds_read_b64_tr_b16 v[238:239],v248 offset:55296\n"
"s_waitcnt lgkmcnt(8)\n"
"v_mfma_f32_16x16x32_bf16 v[200:203],v[216:219],v[64:67],v[200:203]\n"
"ds_read_b64_tr_b16 v[208:209],v248 offset:57344\n"
"v_mfma_f32_16x16x32_bf16 v[200:203],v[220:223],v[68:71],v[200:203]\n"
"ds_read_b64_tr_b16 v[210:211],v248 offset:59392\n"
"buffer_load_dwordx4 v251,s[20:23],s74 offen lds\n"
"v_mfma_f32_16x16x32_bf16 v[204:207],v[216:219],v[136:139],v[204:207]\n"
"ds_read_b64_tr_b16 v[212:213],v248 offset:61440\n"
"s_addk_i32 m0,4096\n"
"v_mfma_f32_16x16x32_bf16 v[204:207],v[220:223],v[140:143],v[204:207]\n"
"ds_read_b64_tr_b16 v[214:215],v248 offset:63488\n"
"s_waitcnt lgkmcnt(8)\n"
"v_mfma_f32_16x16x32_bf16 v[192:195],v[224:227],v[40:43],v[192:195]\n"
"v_add_u32 v248,0x10000,v248\n"
"ds_read_b64_tr_b16 v[216:217],v248\n"
"s_addk_i32 s74,128\n"
"v_mfma_f32_16x16x32_bf16 v[192:195],v[228:231],v[44:47],v[192:195]\n"
"ds_read_b64_tr_b16 v[218:219],v248 offset:2048\n"
"v_mfma_f32_16x16x32_bf16 v[196:199],v[224:227],v[112:115],v[196:199]\n"
"ds_read_b64_tr_b16 v[220:221],v248 offset:4096\n"
"v_mfma_f32_16x16x32_bf16 v[196:199],v[228:231],v[116:119],v[196:199]\n"
"ds_read_b64_tr_b16 v[222:223],v248 offset:6144\n"
"s_waitcnt lgkmcnt(8)\n"
"v_mfma_f32_16x16x32_bf16 v[196:199],v[232:235],v[120:123],v[196:199]\n"
"buffer_load_dwordx4 v251,s[20:23],s74 offen lds\n"
"v_mfma_f32_16x16x32_bf16 v[196:199],v[236:239],v[124:127],v[196:199]\n"
"s_addk_i32 m0,4096\n"
"s_addk_i32 s74,128\n"
"v_mfma_f32_16x16x32_bf16 v[192:195],v[232:235],v[48:51],v[192:195]\n"
"v_mfma_f32_16x16x32_bf16 v[192:195],v[236:239],v[52:55],v[192:195]\n"
"s_waitcnt lgkmcnt(4)\n"
"v_mfma_f32_16x16x32_bf16 v[192:195],v[208:211],v[56:59],v[192:195]\n"
"buffer_load_dwordx4 v251,s[20:23],s74 offen lds\n"
"v_mfma_f32_16x16x32_bf16 v[192:195],v[212:215],v[60:63],v[192:195]\n"
"v_add_u32 v248,4294828032,v248\n"
"v_mfma_f32_16x16x32_bf16 v[196:199],v[208:211],v[128:131],v[196:199]\n"
"v_mfma_f32_16x16x32_bf16 v[196:199],v[212:215],v[132:135],v[196:199]\n"
"v_add_u32 v249,4294828032,v249\n"
"s_waitcnt lgkmcnt(0)\n"
"v_mfma_f32_16x16x32_bf16 v[196:199],v[216:219],v[136:139],v[196:199]\n"
"v_mfma_f32_16x16x32_bf16 v[196:199],v[220:223],v[140:143],v[196:199]\n"
"v_mfma_f32_16x16x32_bf16 v[192:195],v[216:219],v[64:67],v[192:195]\n"
"v_mfma_f32_16x16x32_bf16 v[192:195],v[220:223],v[68:71],v[192:195]\n"
"s_setprio 0\n"
"s_waitcnt vmcnt(3)\n"
"v_cmp_gt_u32 vcc,1,v240\n"
"s_nop 1\n"
"s_cbranch_vccz .Lk_h1g0_ok\n"
"s_mov_b32 s89,0x100000\n"
".Lk_h1g0_spin:\n"
"s_sleep 2\n"
"buffer_load_dword v240,v255,s[32:35],0 offen sc1\n"
"s_waitcnt vmcnt(0)\n"
"v_cmp_gt_u32 vcc,1,v240\n"
"s_nop 1\n"
"s_cbranch_vccz .Lk_h1g0_ok\n"
"s_sub_u32 s89,s89,1\n"
"s_cmp_lg_u32 s89,0\n"
"s_cbranch_scc1 .Lk_h1g0_spin\n"
".Lk_h1g0_ok:\n"
"v_xor_b32 v253,96,v248\n"
"v_xor_b32 v250,64,v248\n"
"v_xor_b32 v249,32,v248\n"
"v_xor_b32 v248,0,v248\n"
"v_fma_f32 v244,-v200,v200,s79\n"
"v_fma_f32 v245,-v201,v201,s79\n"
"v_fma_f32 v246,-v202,v202,s79\n"
"v_fma_f32 v247,-v203,v203,s79\n"
"v_mul_f32 v244,v244,v200\n"
"v_mul_f32 v245,v245,v201\n"
"v_mul_f32 v246,v246,v202\n"
"v_mul_f32 v247,v247,v203\n"
"v_exp_f32 v244,v244\n"
"v_exp_f32 v245,v245\n"
"s_add_u32 s74,s85,0\n"
"buffer_load_dwordx4 v[96:99],v252,s[32:35],s74 offen sc1\n"
"v_exp_f32 v246,v246\n"
"v_exp_f32 v247,v247\n"
"v_add_f32 v244,1.0,v244\n"
"v_add_f32 v245,1.0,v245\n"
"v_add_f32 v246,1.0,v246\n"
"v_add_f32 v247,1.0,v247\n"
"v_rcp_f32 v244,v244\n"
"v_rcp_f32 v245,v245\n"
"v_rcp_f32 v246,v246\n"
"v_rcp_f32 v247,v247\n"
"buffer_load_dwordx4 v[100:103],v252,s[32:35],s74 offen offset:1024 sc1\n"
"v_mul_f32 v244,v244,v200\n"
"v_mul_f32 v245,v245,v201\n"
"v_mul_f32 v246,v246,v202\n"
"v_mul_f32 v247,v247,v203\n"
"v_cvt_pk_bf16_f32 v186,v244,v245\n"
"v_cvt_pk_bf16_f32 v187,v246,v247\n"
"v_fma_f32 v240,-v192,v192,s79\n"
"v_fma_f32 v241,-v193,v193,s79\n"
"v_fma_f32 v242,-v194,v194,s79\n"
"v_fma_f32 v243,-v195,v195,s79\n"
"v_mul_f32 v240,v240,v192\n"
"v_mul_f32 v241,v241,v193\n"
"v_mul_f32 v242,v242,v194\n"
"v_mul_f32 v243,v243,v195\n"
"v_exp_f32 v240,v240\n"
"s_add_u32 s74,s86,0\n"
"buffer_load_dwordx4 v[104:107],v252,s[32:35],s74 offen sc1\n"
"v_exp_f32 v241,v241\n"
"v_exp_f32 v242,v242\n"
"v_exp_f32 v243,v243\n"
"v_add_f32 v240,1.0,v240\n"
"v_add_f32 v241,1.0,v241\n"
"v_add_f32 v242,1.0,v242\n"
"v_add_f32 v243,1.0,v243\n"
"v_rcp_f32 v240,v240\n"
"v_rcp_f32 v241,v241\n"
"v_rcp_f32 v242,v242\n"
"buffer_load_dwordx4 v[108:111],v252,s[32:35],s74 offen offset:1024 sc1\n"
"v_rcp_f32 v243,v243\n"
"v_mul_f32 v240,v240,v192\n"
"v_mul_f32 v241,v241,v193\n"
"v_mul_f32 v242,v242,v194\n"
"v_mul_f32 v243,v243,v195\n"
"v_cvt_pk_bf16_f32 v184,v240,v241\n"
"v_cvt_pk_bf16_f32 v185,v242,v243\n"
"v_fma_f32 v240,-v196,v196,s79\n"
"v_fma_f32 v241,-v197,v197,s79\n"
"v_fma_f32 v242,-v198,v198,s79\n"
"v_fma_f32 v243,-v199,v199,s79\n"
"v_mul_f32 v240,v240,v196\n"
"v_mul_f32 v241,v241,v197\n"
"v_mul_f32 v242,v242,v198\n"
"v_mul_f32 v243,v243,v199\n"
"s_add_u32 s74,s85,2048\n"
"buffer_load_dwordx4 v[112:115],v252,s[32:35],s74 offen sc1\n"
"v_exp_f32 v240,v240\n"
"v_exp_f32 v241,v241\n"
"v_exp_f32 v242,v242\n"
"v_exp_f32 v243,v243\n"
"v_add_f32 v240,1.0,v240\n"
"v_add_f32 v241,1.0,v241\n"
"v_add_f32 v242,1.0,v242\n"
"v_add_f32 v243,1.0,v243\n"
"v_rcp_f32 v240,v240\n"
"v_rcp_f32 v241,v241\n"
"buffer_load_dwordx4 v[116:119],v252,s[32:35],s74 offen offset:1024 sc1\n"
"v_rcp_f32 v242,v242\n"
"v_rcp_f32 v243,v243\n"
"v_mul_f32 v240,v240,v196\n"
"v_mul_f32 v241,v241,v197\n"
"v_mul_f32 v242,v242,v198\n"
"v_mul_f32 v243,v243,v199\n"
"s_waitcnt vmcnt(6)\n"
"s_barrier\n"
"v_cvt_pk_bf16_f32 v188,v240,v241\n"
"v_cvt_pk_bf16_f32 v189,v242,v243\n"
"v_fma_f32 v244,-v204,v204,s79\n"
"v_fma_f32 v245,-v205,v205,s79\n"
"v_fma_f32 v246,-v206,v206,s79\n"
"v_fma_f32 v247,-v207,v207,s79\n"
"v_mul_f32 v244,v244,v204\n"
"v_mul_f32 v245,v245,v205\n"
"v_mov_b32 v208,2\n"
"s_mov_b64 exec,1\n"
"buffer_store_dword v208,off,s[32:35],s88 sc1\n"
"s_mov_b64 exec,-1\n"
"v_mul_f32 v246,v246,v206\n"
"v_mul_f32 v247,v247,v207\n"
"v_exp_f32 v244,v244\n"
"v_exp_f32 v245,v245\n"
"v_exp_f32 v246,v246\n"
"v_exp_f32 v247,v247\n"
"v_add_f32 v244,1.0,v244\n"
"v_add_f32 v245,1.0,v245\n"
"v_add_f32 v246,1.0,v246\n"
"v_add_f32 v247,1.0,v247\n"
"v_rcp_f32 v244,v244\n"
"s_setprio 3\n"
"v_rcp_f32 v245,v245\n"
"ds_read_b64_tr_b16 v[208:209],v248\n"
"ds_read_b64_tr_b16 v[210:211],v248 offset:2048\n"
"v_rcp_f32 v246,v246\n"
"ds_read_b64_tr_b16 v[212:213],v248 offset:12288\n"
"ds_read_b64_tr_b16 v[214:215],v248 offset:14336\n"
"v_rcp_f32 v247,v247\n"
"ds_read_b64_tr_b16 v[216:217],v248 offset:24576\n"
"ds_read_b64_tr_b16 v[218:219],v248 offset:26624\n"
"v_mul_f32 v244,v244,v204\n"
"v_mul_f32 v245,v245,v205\n"
"ds_read_b64_tr_b16 v[220:221],v249\n"
"ds_read_b64_tr_b16 v[222:223],v249 offset:2048\n"
"v_mul_f32 v246,v246,v206\n"
"v_mul_f32 v247,v247,v207\n"
"ds_read_b64_tr_b16 v[224:225],v249 offset:12288\n"
"ds_read_b64_tr_b16 v[226:227],v249 offset:14336\n"
"v_cvt_pk_bf16_f32 v190,v244,v245\n"
"v_cvt_pk_bf16_f32 v191,v246,v247\n"
"ds_read_b64_tr_b16 v[228:229],v249 offset:24576\n"
"ds_read_b64_tr_b16 v[230:231],v249 offset:26624\n"
"s_setprio 1\n"
"s_waitcnt lgkmcnt(6)\n"
"v_mfma_f32_16x16x32_bf16 v[0:3],v[208:211],v[144:147],0\n"
"ds_read_b64_tr_b16 v[232:233],v250\n"
"s_add_u32 s78,s84,10240\n"
"v_mfma_f32_16x16x32_bf16 v[0:3],v[212:215],v[152:155],v[0:3]\n"
"ds_read_b64_tr_b16 v[234:235],v250 offset:2048\n"
"buffer_store_dwordx4 v[184:187],v252,s[32:35],s78 offen sc1\n"
"v_mfma_f32_16x16x32_bf16 v[0:3],v[216:219],v[160:163],v[0:3]\n"
"ds_read_b64_tr_b16 v[236:237],v250 offset:12288\n"
"v_mfma_f32_16x16x32_bf16 v[4:7],v[208:211],v[148:151],0\n"
"ds_read_b64_tr_b16 v[238:239],v250 offset:14336\n"
"v_mfma_f32_16x16x32_bf16 v[4:7],v[212:215],v[156:159],v[4:7]\n"
"ds_read_b64_tr_b16 v[208:209],v250 offset:24576\n"
"v_mfma_f32_16x16x32_bf16 v[4:7],v[216:219],v[164:167],v[4:7]\n"
"ds_read_b64_tr_b16 v[210:211],v250 offset:26624\n"
"s_waitcnt lgkmcnt(6)\n"
"v_mfma_f32_16x16x32_bf16 v[8:11],v[220:223],v[144:147],0\n"
"ds_read_b64_tr_b16 v[212:213],v253\n"
"buffer_store_dwordx4 v[188:191],v252,s[32:35],s78 offen offset:1024 sc1\n"
"v_mfma_f32_16x16x32_bf16 v[8:11],v[224:227],v[152:155],v[8:11]\n"
"ds_read_b64_tr_b16 v[214:215],v253 offset:2048\n"
"s_add_u32 m0,s73,73728\n"
"v_mfma_f32_16x16x32_bf16 v[8:11],v[228:231],v[160:163],v[8:11]\n"
"ds_read_b64_tr_b16 v[216:217],v253 offset:12288\n"
"s_add_u32 s74,s81,221184\n"
"v_mfma_f32_16x16x32_bf16 v[12:15],v[220:223],v[148:151],0\n"
"ds_read_b64_tr_b16 v[218:219],v253 offset:14336\n"
"v_mfma_f32_16x16x32_bf16 v[12:15],v[224:227],v[156:159],v[12:15]\n"
"ds_read_b64_tr_b16 v[220:221],v253 offset:24576\n"
"v_mfma_f32_16x16x32_bf16 v[12:15],v[228:231],v[164:167],v[12:15]\n"
"ds_read_b64_tr_b16 v[222:223],v253 offset:26624\n"
"s_waitcnt lgkmcnt(6)\n"
"v_mfma_f32_16x16x32_bf16 v[16:19],v[232:235],v[144:147],0\n"
"ds_read_b64_tr_b16 v[224:225],v248 offset:4096\n"
"v_mfma_f32_16x16x32_bf16 v[16:19],v[236:239],v[152:155],v[16:19]\n"
"ds_read_b64_tr_b16 v[226:227],v248 offset:6144\n"
"v_mfma_f32_16x16x32_bf16 v[16:19],v[208:211],v[160:163],v[16:19]\n"
"ds_read_b64_tr_b16 v[228:229],v248 offset:16384\n"
"v_mfma_f32_16x16x32_bf16 v[20:23],v[232:235],v[148:151],0\n"
"ds_read_b64_tr_b16 v[230:231],v248 offset:18432\n"
"v_mfma_f32_16x16x32_bf16 v[20:23],v[236:239],v[156:159],v[20:23]\n"
"ds_read_b64_tr_b16 v[232:233],v248 offset:28672\n"
"v_mfma_f32_16x16x32_bf16 v[20:23],v[208:211],v[164:167],v[20:23]\n"
"ds_read_b64_tr_b16 v[234:235],v248 offset:30720\n"
"s_waitcnt lgkmcnt(6)\n"
"v_mfma_f32_16x16x32_bf16 v[24:27],v[212:215],v[144:147],0\n"
"ds_read_b64_tr_b16 v[236:237],v249 offset:4096\n"
"v_mfma_f32_16x16x32_bf16 v[24:27],v[216:219],v[152:155],v[24:27]\n"
"ds_read_b64_tr_b16 v[238:239],v249 offset:6144\n"
"v_mfma_f32_16x16x32_bf16 v[24:27],v[220:223],v[160:163],v[24:27]\n"
"ds_read_b64_tr_b16 v[208:209],v249 offset:16384\n"
"v_mfma_f32_16x16x32_bf16 v[28:31],v[212:215],v[148:151],0\n"
"ds_read_b64_tr_b16 v[210:211],v249 offset:18432\n"
"s_barrier\n"
"v_mfma_f32_16x16x32_bf16 v[28:31],v[216:219],v[156:159],v[28:31]\n"
"ds_read_b64_tr_b16 v[212:213],v249 offset:28672\n"
"buffer_load_dwordx4 v251,s[20:23],s74 offen lds\n"
"v_mfma_f32_16x16x32_bf16 v[28:31],v[220:223],v[164:167],v[28:31]\n"
"ds_read_b64_tr_b16 v[214:215],v249 offset:30720\n"
"s_waitcnt lgkmcnt(6)\n"
"v_mfma_f32_16x16x32_bf16 v[32:35],v[224:227],v[144:147],0\n"
"ds_read_b64_tr_b16 v[216:217],v250 offset:4096\n"
"s_addk_i32 m0,4096\n"
"v_mfma_f32_16x16x32_bf16 v[32:35],v[228:231],v[152:155],v[32:35]\n"
"ds_read_b64_tr_b16 v[218:219],v250 offset:6144\n"
"s_addk_i32 s74,128\n"
"v_mfma_f32_16x16x32_bf16 v[32:35],v[232:235],v[160:163],v[32:35]\n"
"ds_read_b64_tr_b16 v[220:221],v250 offset:16384\n"
"buffer_load_dwordx4 v251,s[20:23],s74 offen lds\n"
"v_mfma_f32_16x16x32_bf16 v[36:39],v[224:227],v[148:151],0\n"
"ds_read_b64_tr_b16 v[222:223],v250 offset:18432\n"
"s_addk_i32 m0,4096\n"
"v_mfma_f32_16x16x32_bf16 v[36:39],v[228:231],v[156:159],v[36:39]\n"
"ds_read_b64_tr_b16 v[224:225],v250 offset:28672\n"
"s_addk_i32 s74,128\n"
"v_mfma_f32_16x16x32_bf16 v[36:39],v[232:235],v[164:167],v[36:39]\n"
"ds_read_b64_tr_b16 v[226:227],v250 offset:30720\n"
"s_waitcnt lgkmcnt(6)\n"
"v_mfma_f32_16x16x32_bf16 v[40:43],v[236:239],v[144:147],0\n"
"ds_read_b64_tr_b16 v[228:229],v253 offset:4096\n"
"buffer_load_dwordx4 v251,s[20:23],s74 offen lds\n"
"v_mfma_f32_16x16x32_bf16 v[40:43],v[208:211],v[152:155],v[40:43]\n"
"ds_read_b64_tr_b16 v[230:231],v253 offset:6144\n"
"s_addk_i32 m0,4096\n"
"v_mfma_f32_16x16x32_bf16 v[40:43],v[212:215],v[160:163],v[40:43]\n"
"ds_read_b64_tr_b16 v[232:233],v253 offset:16384\n"
"s_add_u32 s74,s81,294912\n"
"v_mfma_f32_16x16x32_bf16 v[44:47],v[236:239],v[148:151],0\n"
"ds_read_b64_tr_b16 v[234:235],v253 offset:18432\n"
"buffer_load_dwordx4 v251,s[20:23],s74 offen lds\n"
"v_mfma_f32_16x16x32_bf16 v[44:47],v[208:211],v[156:159],v[44:47]\n"
"ds_read_b64_tr_b16 v[236:237],v253 offset:28672\n"
"s_addk_i32 m0,4096\n"
"v_mfma_f32_16x16x32_bf16 v[44:47],v[212:215],v[164:167],v[44:47]\n"
"ds_read_b64_tr_b16 v[238:239],v253 offset:30720\n"
"s_waitcnt lgkmcnt(6)\n"
"v_mfma_f32_16x16x32_bf16 v[48:51],v[216:219],v[144:147],0\n"
"ds_read_b64_tr_b16 v[208:209],v248 offset:8192\n"
"s_addk_i32 s74,128\n"
"v_mfma_f32_16x16x32_bf16 v[48:51],v[220:223],v[152:155],v[48:51]\n"
"ds_read_b64_tr_b16 v[210:211],v248 offset:10240\n"
"v_mfma_f32_16x16x32_bf16 v[48:51],v[224:227],v[160:163],v[48:51]\n"
"ds_read_b64_tr_b16 v[212:213],v248 offset:20480\n"
"buffer_load_dwordx4 v251,s[20:23],s74 offen lds\n"
"v_mfma_f32_16x16x32_bf16 v[52:55],v[216:219],v[148:151],0\n"
"ds_read_b64_tr_b16 v[214:215],v248 offset:22528\n"
"s_addk_i32 m0,4096\n"
"v_mfma_f32_16x16x32_bf16 v[52:55],v[220:223],v[156:159],v[52:55]\n"
"ds_read_b64_tr_b16 v[216:217],v248 offset:32768\n"
"s_addk_i32 s74,128\n"
"v_mfma_f32_16x16x32_bf16 v[52:55],v[224:227],v[164:167],v[52:55]\n"
"ds_read_b64_tr_b16 v[218:219],v248 offset:34816\n"
"s_setprio 2\n"
"s_waitcnt lgkmcnt(6)\n"
"v_mfma_f32_16x16x32_bf16 v[56:59],v[228:231],v[144:147],0\n"
"ds_read_b64_tr_b16 v[220:221],v249 offset:8192\n"
"v_mfma_f32_16x16x32_bf16 v[56:59],v[232:235],v[152:155],v[56:59]\n"
"ds_read_b64_tr_b16 v[222:223],v249 offset:10240\n"
"v_mfma_f32_16x16x32_bf16 v[56:59],v[236:239],v[160:163],v[56:59]\n"
"ds_read_b64_tr_b16 v[224:225],v249 offset:20480\n"
"buffer_load_dwordx4 v251,s[20:23],s74 offen lds\n"
"v_mfma_f32_16x16x32_bf16 v[60:63],v[228:231],v[148:151],0\n"
"ds_read_b64_tr_b16 v[226:227],v249 offset:22528\n"
"s_addk_i32 m0,4096\n"
"v_mfma_f32_16x16x32_bf16 v[60:63],v[232:235],v[156:159],v[60:63]\n"
"ds_read_b64_tr_b16 v[228:229],v249 offset:32768\n"
"s_add_u32 s74,s81,368640\n"
"v_mfma_f32_16x16x32_bf16 v[60:63],v[236:239],v[164:167],v[60:63]\n"
"ds_read_b64_tr_b16 v[230:231],v249 offset:34816\n"
"s_waitcnt lgkmcnt(6)\n"
"v_mfma_f32_16x16x32_bf16 v[64:67],v[208:211],v[144:147],0\n"
"ds_read_b64_tr_b16 v[232:233],v250 offset:8192\n"
"v_mfma_f32_16x16x32_bf16 v[64:67],v[212:215],v[152:155],v[64:67]\n"
"ds_read_b64_tr_b16 v[234:235],v250 offset:10240\n"
"v_mfma_f32_16x16x32_bf16 v[64:67],v[216:219],v[160:163],v[64:67]\n"
"ds_read_b64_tr_b16 v[236:237],v250 offset:20480\n"
"buffer_load_dwordx4 v251,s[20:23],s74 offen lds\n"
"v_mfma_f32_16x16x32_bf16 v[68:71],v[208:211],v[148:151],0\n"
"ds_read_b64_tr_b16 v[238:239],v250 offset:22528\n"
"s_addk_i32 m0,4096\n"
"v_mfma_f32_16x16x32_bf16 v[68:71],v[212:215],v[156:159],v[68:71]\n"
"ds_read_b64_tr_b16 v[208:209],v250 offset:32768\n"
"s_addk_i32 s74,128\n"
"v_mfma_f32_16x16x32_bf16 v[68:71],v[216:219],v[164:167],v[68:71]\n"
"ds_read_b64_tr_b16 v[210:211],v250 offset:34816\n"
"s_waitcnt lgkmcnt(6)\n"
"v_mfma_f32_16x16x32_bf16 v[72:75],v[220:223],v[144:147],0\n"
"ds_read_b64_tr_b16 v[212:213],v253 offset:8192\n"
"v_mfma_f32_16x16x32_bf16 v[72:75],v[224:227],v[152:155],v[72:75]\n"
"ds_read_b64_tr_b16 v[214:215],v253 offset:10240\n"
"v_mfma_f32_16x16x32_bf16 v[72:75],v[228:231],v[160:163],v[72:75]\n"
"ds_read_b64_tr_b16 v[216:217],v253 offset:20480\n"
"v_mfma_f32_16x16x32_bf16 v[76:79],v[220:223],v[148:151],0\n"
"ds_read_b64_tr_b16 v[218:219],v253 offset:22528\n"
"buffer_load_dwordx4 v251,s[20:23],s74 offen lds\n"
"v_mfma_f32_16x16x32_bf16 v[76:79],v[224:227],v[156:159],v[76:79]\n"
"ds_read_b64_tr_b16 v[220:221],v253 offset:32768\n"
"s_addk_i32 m0,4096\n"
"v_mfma_f32_16x16x32_bf16 v[76:79],v[228:231],v[164:167],v[76:79]\n"
"ds_read_b64_tr_b16 v[222:223],v253 offset:34816\n"
"s_waitcnt lgkmcnt(6)\n"
"v_mfma_f32_16x16x32_bf16 v[80:83],v[232:235],v[144:147],0\n"
"s_addk_i32 s74,128\n"
"v_mfma_f32_16x16x32_bf16 v[80:83],v[236:239],v[152:155],v[80:83]\n"
"v_mfma_f32_16x16x32_bf16 v[80:83],v[208:211],v[160:163],v[80:83]\n"
"v_mfma_f32_16x16x32_bf16 v[84:87],v[232:235],v[148:151],0\n"
"buffer_load_dwordx4 v251,s[20:23],s74 offen lds\n"
"v_mfma_f32_16x16x32_bf16 v[84:87],v[236:239],v[156:159],v[84:87]\n"
"v_mfma_f32_16x16x32_bf16 v[84:87],v[208:211],v[164:167],v[84:87]\n"
"s_waitcnt lgkmcnt(0)\n"
"v_mfma_f32_16x16x32_bf16 v[88:91],v[212:215],v[144:147],0\n"
"v_mfma_f32_16x16x32_bf16 v[88:91],v[216:219],v[152:155],v[88:91]\n"
"v_mfma_f32_16x16x32_bf16 v[88:91],v[220:223],v[160:163],v[88:91]\n"
"v_mfma_f32_16x16x32_bf16 v[92:95],v[212:215],v[148:151],0\n"
"v_mfma_f32_16x16x32_bf16 v[92:95],v[216:219],v[156:159],v[92:95]\n"
"v_mfma_f32_16x16x32_bf16 v[92:95],v[220:223],v[164:167],v[92:95]\n"
"s_setprio 0\n"
"v_add_u32 v248,73728,v248\n"
"v_add_u32 v249,73728,v249\n"
"v_add_u32 v250,73728,v250\n"
"v_add_u32 v253,73728,v253\n"
"s_lshl_b32 s75,s71,12\n"
"s_add_u32 s75,s75,147456\n"
"v_add_u32 v247,s75,v252\n"
"s_sleep 2\n"
"s_addk_i32 m0,4096\n"
"s_add_u32 s74,s81,258048\n"
"buffer_load_dwordx4 v251,s[20:23],s74 offen lds\n"
"s_waitcnt vmcnt(10)\n"
"v_mov_b32 v208,3\n"
"s_mov_b64 exec,1\n"
"buffer_store_dword v208,off,s[32:35],s88 sc1\n"
"s_mov_b64 exec,-1\n"
"s_add_u32 s74,s86,2048\n"
"buffer_load_dwordx4 v[120:123],v252,s[32:35],s74 offen sc1\n"
"s_addk_i32 m0,4096\n"
"s_add_u32 s74,s81,258176\n"
"buffer_load_dwordx4 v251,s[20:23],s74 offen lds\n"
"s_add_u32 s74,s86,2048\n"
"buffer_load_dwordx4 v[124:127],v252,s[32:35],s74 offen offset:1024 sc1\n"
"s_addk_i32 m0,4096\n"
"s_add_u32 s74,s81,258304\n"
"buffer_load_dwordx4 v251,s[20:23],s74 offen lds\n"
"s_addk_i32 m0,4096\n"
"s_add_u32 s74,s81,331776\n"
"buffer_load_dwordx4 v251,s[20:23],s74 offen lds\n"
"s_add_u32 s74,s85,4096\n"
"buffer_load_dwordx4 v[128:131],v252,s[32:35],s74 offen sc1\n"
"s_addk_i32 m0,4096\n"
"s_add_u32 s74,s81,331904\n"
"buffer_load_dwordx4 v251,s[20:23],s74 offen lds\n"
"s_add_u32 s74,s85,4096\n"
"buffer_load_dwordx4 v[132:135],v252,s[32:35],s74 offen offset:1024 sc1\n"
"s_addk_i32 m0,4096\n"
"s_add_u32 s74,s81,332032\n"
"buffer_load_dwordx4 v251,s[20:23],s74 offen lds\n"
"s_addk_i32 m0,4096\n"
"s_add_u32 s74,s81,405504\n"
"buffer_load_dwordx4 v251,s[20:23],s74 offen lds\n"
"s_add_u32 s74,s86,4096\n"
"buffer_load_dwordx4 v[136:139],v252,s[32:35],s74 offen sc1\n"
"s_addk_i32 m0,4096\n"
"s_add_u32 s74,s81,405632\n"
"buffer_load_dwordx4 v251,s[20:23],s74 offen lds\n"
"s_add_u32 s74,s86,4096\n"
"buffer_load_dwordx4 v[140:143],v252,s[32:35],s74 offen offset:1024 sc1\n"
"s_addk_i32 m0,4096\n"
"s_add_u32 s74,s81,405760\n"
"buffer_load_dwordx4 v251,s[20:23],s74 offen lds\n"
"s_waitcnt lgkmcnt(0)\n"
"s_setprio 3\n"
"s_waitcnt vmcnt(16)\n"
"s_barrier\n"
"ds_read_b64_tr_b16 v[208:209],v248\n"
"ds_read_b64_tr_b16 v[210:211],v248 offset:2048\n"
"ds_read_b64_tr_b16 v[212:213],v248 offset:12288\n"
"ds_read_b64_tr_b16 v[214:215],v248 offset:14336\n"
"ds_read_b64_tr_b16 v[216:217],v248 offset:24576\n"
"ds_read_b64_tr_b16 v[218:219],v248 offset:26624\n"
"ds_read_b64_tr_b16 v[220:221],v249\n"
"ds_read_b64_tr_b16 v[222:223],v249 offset:2048\n"
"ds_read_b64_tr_b16 v[224:225],v249 offset:12288\n"
"ds_read_b64_tr_b16 v[226:227],v249 offset:14336\n"
"ds_read_b64_tr_b16 v[228:229],v249 offset:24576\n"
"ds_read_b64_tr_b16 v[230:231],v249 offset:26624\n"
"s_setprio 1\n"
"s_waitcnt lgkmcnt(6)\n"
"v_mfma_f32_16x16x32_bf16 v[0:3],v[208:211],v[168:171],v[0:3]\n"
"ds_read_b64_tr_b16 v[232:233],v250\n"
"v_mfma_f32_16x16x32_bf16 v[0:3],v[212:215],v[176:179],v[0:3]\n"
"ds_read_b64_tr_b16 v[234:235],v250 offset:2048\n"
"v_mfma_f32_16x16x32_bf16 v[0:3],v[216:219],v[184:187],v[0:3]\n"
"ds_read_b64_tr_b16 v[236:237],v250 offset:12288\n"
"v_mfma_f32_16x16x32_bf16 v[4:7],v[208:211],v[172:175],v[4:7]\n"
"ds_read_b64_tr_b16 v[238:239],v250 offset:14336\n"
"v_mfma_f32_16x16x32_bf16 v[4:7],v[212:215],v[180:183],v[4:7]\n"
"ds_read_b64_tr_b16 v[208:209],v250 offset:24576\n"
"v_mfma_f32_16x16x32_bf16 v[4:7],v[216:219],v[188:191],v[4:7]\n"
"ds_read_b64_tr_b16 v[210:211],v250 offset:26624\n"
"s_waitcnt lgkmcnt(6)\n"
"v_mfma_f32_16x16x32_bf16 v[8:11],v[220:223],v[168:171],v[8:11]\n"
"ds_read_b64_tr_b16 v[212:213],v253\n"
"v_mfma_f32_16x16x32_bf16 v[8:11],v[224:227],v[176:179],v[8:11]\n"
"ds_read_b64_tr_b16 v[214:215],v253 offset:2048\n"
"v_mfma_f32_16x16x32_bf16 v[8:11],v[228:231],v[184:187],v[8:11]\n"
"ds_read_b64_tr_b16 v[216:217],v253 offset:12288\n"
"v_mfma_f32_16x16x32_bf16 v[12:15],v[220:223],v[172:175],v[12:15]\n"
"ds_read_b64_tr_b16 v[218:219],v253 offset:14336\n"
"v_mfma_f32_16x16x32_bf16 v[12:15],v[224:227],v[180:183],v[12:15]\n"
"ds_read_b64_tr_b16 v[220:221],v253 offset:24576\n"
"v_mfma_f32_16x16x32_bf16 v[12:15],v[228:231],v[188:191],v[12:15]\n"
"ds_read_b64_tr_b16 v[222:223],v253 offset:26624\n"
"s_waitcnt lgkmcnt(6)\n"
"v_mfma_f32_16x16x32_bf16 v[16:19],v[232:235],v[168:171],v[16:19]\n"
"ds_read_b64_tr_b16 v[224:225],v248 offset:4096\n"
"v_mfma_f32_16x16x32_bf16 v[16:19],v[236:239],v[176:179],v[16:19]\n"
"ds_read_b64_tr_b16 v[226:227],v248 offset:6144\n"
"v_mfma_f32_16x16x32_bf16 v[16:19],v[208:211],v[184:187],v[16:19]\n"
"ds_read_b64_tr_b16 v[228:229],v248 offset:16384\n"
"v_mfma_f32_16x16x32_bf16 v[20:23],v[232:235],v[172:175],v[20:23]\n"
"ds_read_b64_tr_b16 v[230:231],v248 offset:18432\n"
"v_mfma_f32_16x16x32_bf16 v[20:23],v[236:239],v[180:183],v[20:23]\n"
"ds_read_b64_tr_b16 v[232:233],v248 offset:28672\n"
"v_mfma_f32_16x16x32_bf16 v[20:23],v[208:211],v[188:191],v[20:23]\n"
"ds_read_b64_tr_b16 v[234:235],v248 offset:30720\n"
"s_waitcnt lgkmcnt(6)\n"
"v_mfma_f32_16x16x32_bf16 v[24:27],v[212:215],v[168:171],v[24:27]\n"
"ds_read_b64_tr_b16 v[236:237],v249 offset:4096\n"
"v_mfma_f32_16x16x32_bf16 v[24:27],v[216:219],v[176:179],v[24:27]\n"
"ds_read_b64_tr_b16 v[238:239],v249 offset:6144\n"
"v_mfma_f32_16x16x32_bf16 v[24:27],v[220:223],v[184:187],v[24:27]\n"
"ds_read_b64_tr_b16 v[208:209],v249 offset:16384\n"
"v_mfma_f32_16x16x32_bf16 v[28:31],v[212:215],v[172:175],v[28:31]\n"
"ds_read_b64_tr_b16 v[210:211],v249 offset:18432\n"
"v_mfma_f32_16x16x32_bf16 v[28:31],v[216:219],v[180:183],v[28:31]\n"
"ds_read_b64_tr_b16 v[212:213],v249 offset:28672\n"
"v_mfma_f32_16x16x32_bf16 v[28:31],v[220:223],v[188:191],v[28:31]\n"
"ds_read_b64_tr_b16 v[214:215],v249 offset:30720\n"
"s_waitcnt lgkmcnt(6)\n"
"v_mfma_f32_16x16x32_bf16 v[32:35],v[224:227],v[168:171],v[32:35]\n"
"ds_read_b64_tr_b16 v[216:217],v250 offset:4096\n"
"v_mfma_f32_16x16x32_bf16 v[32:35],v[228:231],v[176:179],v[32:35]\n"
"ds_read_b64_tr_b16 v[218:219],v250 offset:6144\n"
"v_mfma_f32_16x16x32_bf16 v[32:35],v[232:235],v[184:187],v[32:35]\n"
"ds_read_b64_tr_b16 v[220:221],v250 offset:16384\n"
"v_mfma_f32_16x16x32_bf16 v[36:39],v[224:227],v[172:175],v[36:39]\n"
"ds_read_b64_tr_b16 v[222:223],v250 offset:18432\n"
"v_mfma_f32_16x16x32_bf16 v[36:39],v[228:231],v[180:183],v[36:39]\n"
"ds_read_b64_tr_b16 v[224:225],v250 offset:28672\n"
"v_mfma_f32_16x16x32_bf16 v[36:39],v[232:235],v[188:191],v[36:39]\n"
"ds_read_b64_tr_b16 v[226:227],v250 offset:30720\n"
"buffer_load_dword v240,v255,s[32:35],0 offen sc1\n"
"s_waitcnt lgkmcnt(6)\n"
"v_mfma_f32_16x16x32_bf16 v[40:43],v[236:239],v[168:171],v[40:43]\n"
"ds_read_b64_tr_b16 v[228:229],v253 offset:4096\n"
"v_mfma_f32_16x16x32_bf16 v[40:43],v[208:211],v[176:179],v[40:43]\n"
"ds_read_b64_tr_b16 v[230:231],v253 offset:6144\n"
"v_mfma_f32_16x16x32_bf16 v[40:43],v[212:215],v[184:187],v[40:43]\n"
"ds_read_b64_tr_b16 v[232:233],v253 offset:16384\n"
"v_mfma_f32_16x16x32_bf16 v[44:47],v[236:239],v[172:175],v[44:47]\n"
"ds_read_b64_tr_b16 v[234:235],v253 offset:18432\n"
"v_mfma_f32_16x16x32_bf16 v[44:47],v[208:211],v[180:183],v[44:47]\n"
"ds_read_b64_tr_b16 v[236:237],v253 offset:28672\n"
"v_mfma_f32_16x16x32_bf16 v[44:47],v[212:215],v[188:191],v[44:47]\n"
"ds_read_b64_tr_b16 v[238:239],v253 offset:30720\n"
"s_waitcnt lgkmcnt(6)\n"
"v_mfma_f32_16x16x32_bf16 v[48:51],v[216:219],v[168:171],v[48:51]\n"
"ds_read_b64_tr_b16 v[208:209],v248 offset:8192\n"
"v_mfma_f32_16x16x32_bf16 v[48:51],v[220:223],v[176:179],v[48:51]\n"
"ds_read_b64_tr_b16 v[210:211],v248 offset:10240\n"
"v_mfma_f32_16x16x32_bf16 v[48:51],v[224:227],v[184:187],v[48:51]\n"
"ds_read_b64_tr_b16 v[212:213],v248 offset:20480\n"
"v_mfma_f32_16x16x32_bf16 v[52:55],v[216:219],v[172:175],v[52:55]\n"
"ds_read_b64_tr_b16 v[214:215],v248 offset:22528\n"
"v_mfma_f32_16x16x32_bf16 v[52:55],v[220:223],v[180:183],v[52:55]\n"
"ds_read_b64_tr_b16 v[216:217],v248 offset:32768\n"
"v_mfma_f32_16x16x32_bf16 v[52:55],v[224:227],v[188:191],v[52:55]\n"
"ds_read_b64_tr_b16 v[218:219],v248 offset:34816\n"
"s_setprio 2\n"
"s_waitcnt lgkmcnt(6)\n"
"v_mfma_f32_16x16x32_bf16 v[56:59],v[228:231],v[168:171],v[56:59]\n"
"ds_read_b64_tr_b16 v[220:221],v249 offset:8192\n"
"v_mfma_f32_16x16x32_bf16 v[56:59],v[232:235],v[176:179],v[56:59]\n"
"ds_read_b64_tr_b16 v[222:223],v249 offset:10240\n"
"v_mfma_f32_16x16x32_bf16 v[56:59],v[236:239],v[184:187],v[56:59]\n"
"ds_read_b64_tr_b16 v[224:225],v249 offset:20480\n"
"v_mfma_f32_16x16x32_bf16 v[60:63],v[228:231],v[172:175],v[60:63]\n"
"ds_read_b64_tr_b16 v[226:227],v249 offset:22528\n"
"v_mfma_f32_16x16x32_bf16 v[60:63],v[232:235],v[180:183],v[60:63]\n"
"ds_read_b64_tr_b16 v[228:229],v249 offset:32768\n"
"v_mfma_f32_16x16x32_bf16 v[60:63],v[236:239],v[188:191],v[60:63]\n"
"ds_read_b64_tr_b16 v[230:231],v249 offset:34816\n"
"s_waitcnt lgkmcnt(6)\n"
"v_mfma_f32_16x16x32_bf16 v[64:67],v[208:211],v[168:171],v[64:67]\n"
"ds_read_b64_tr_b16 v[232:233],v250 offset:8192\n"
"v_mfma_f32_16x16x32_bf16 v[64:67],v[212:215],v[176:179],v[64:67]\n"
"ds_read_b64_tr_b16 v[234:235],v250 offset:10240\n"
"v_mfma_f32_16x16x32_bf16 v[64:67],v[216:219],v[184:187],v[64:67]\n"
"ds_read_b64_tr_b16 v[236:237],v250 offset:20480\n"
"v_mfma_f32_16x16x32_bf16 v[68:71],v[208:211],v[172:175],v[68:71]\n"
"ds_read_b64_tr_b16 v[238:239],v250 offset:22528\n"
"v_mfma_f32_16x16x32_bf16 v[68:71],v[212:215],v[180:183],v[68:71]\n"
"ds_read_b64_tr_b16 v[208:209],v250 offset:32768\n"
"v_mfma_f32_16x16x32_bf16 v[68:71],v[216:219],v[188:191],v[68:71]\n"
"ds_read_b64_tr_b16 v[210:211],v250 offset:34816\n"
"s_waitcnt lgkmcnt(6)\n"
"v_mfma_f32_16x16x32_bf16 v[72:75],v[220:223],v[168:171],v[72:75]\n"
"ds_read_b64_tr_b16 v[212:213],v253 offset:8192\n"
"v_mfma_f32_16x16x32_bf16 v[72:75],v[224:227],v[176:179],v[72:75]\n"
"ds_read_b64_tr_b16 v[214:215],v253 offset:10240\n"
"v_mfma_f32_16x16x32_bf16 v[72:75],v[228:231],v[184:187],v[72:75]\n"
"ds_read_b64_tr_b16 v[216:217],v253 offset:20480\n"
"v_mfma_f32_16x16x32_bf16 v[76:79],v[220:223],v[172:175],v[76:79]\n"
"ds_read_b64_tr_b16 v[218:219],v253 offset:22528\n"
"v_mfma_f32_16x16x32_bf16 v[76:79],v[224:227],v[180:183],v[76:79]\n"
"ds_read_b64_tr_b16 v[220:221],v253 offset:32768\n"
"v_mfma_f32_16x16x32_bf16 v[76:79],v[228:231],v[188:191],v[76:79]\n"
"ds_read_b64_tr_b16 v[222:223],v253 offset:34816\n"
"s_waitcnt vmcnt(1)\n"
"s_waitcnt lgkmcnt(6)\n"
"v_mfma_f32_16x16x32_bf16 v[80:83],v[232:235],v[168:171],v[80:83]\n"
"v_mfma_f32_16x16x32_bf16 v[80:83],v[236:239],v[176:179],v[80:83]\n"
"s_barrier\n"
"v_mfma_f32_16x16x32_bf16 v[80:83],v[208:211],v[184:187],v[80:83]\n"
"v_mfma_f32_16x16x32_bf16 v[84:87],v[232:235],v[172:175],v[84:87]\n"
"v_mfma_f32_16x16x32_bf16 v[84:87],v[236:239],v[180:183],v[84:87]\n"
"v_mfma_f32_16x16x32_bf16 v[84:87],v[208:211],v[188:191],v[84:87]\n"
"s_waitcnt lgkmcnt(0)\n"
"v_mfma_f32_16x16x32_bf16 v[88:91],v[212:215],v[168:171],v[88:91]\n"
"v_mfma_f32_16x16x32_bf16 v[88:91],v[216:219],v[176:179],v[88:91]\n"
"v_mfma_f32_16x16x32_bf16 v[88:91],v[220:223],v[184:187],v[88:91]\n"
"v_mfma_f32_16x16x32_bf16 v[92:95],v[212:215],v[172:175],v[92:95]\n"
".Lk_padm9:\n"
"s_cbranch_execz .Lk_padm13\n"
"v_mfma_f32_16x16x32_bf16 v[92:95],v[216:219],v[180:183],v[92:95]\n"
"v_mfma_f32_16x16x32_bf16 v[92:95],v[220:223],v[188:191],v[92:95]\n"
"s_setprio 0\n"
"v_add_u32 v248,4294893568,v248\n"
"v_add_u32 v249,4294893568,v249\n"
"v_add_u32 v250,4294893568,v250\n"
"v_add_u32 v253,4294893568,v253\n"
"s_add_u32 m0,s73,36864\n"
"s_add_u32 s74,s82,36864\n"
"buffer_load_dwordx4 v251,s[20:23],s74 offen lds\n"
"s_addk_i32 m0,4096\n"
"s_addk_i32 s74,128\n"
"buffer_load_dwordx4 v251,s[20:23],s74 offen lds\n"
"s_waitcnt vmcnt(2)\n"
"v_cmp_gt_u32 vcc,2,v240\n"
"s_nop 1\n"
"s_cbranch_vccz .Lk_h2g0_ok\n"
"s_mov_b32 s89,0x100000\n"
".Lk_h2g0_spin:\n"
"s_sleep 2\n"
"buffer_load_dword v240,v255,s[32:35],0 offen sc1\n"
"s_waitcnt vmcnt(0)\n"
"v_cmp_gt_u32 vcc,2,v240\n"
"s_nop 1\n"
"s_cbranch_vccz .Lk_h2g0_ok\n"
"s_sub_u32 s89,s89,1\n"
"s_cmp_lg_u32 s89,0\n"
"s_cbranch_scc1 .Lk_h2g0_spin\n"
".Lk_h2g0_ok:\n"
"s_add_u32 m0,s73,45056\n"
"s_add_u32 s74,s82,37120\n"
"buffer_load_dwordx4 v251,s[20:23],s74 offen lds\n"
"s_add_u32 s74,s85,6144\n"
"buffer_load_dwordx4 v[192:195],v252,s[32:35],s74 offen sc1\n"
"s_addk_i32 m0,4096\n"
"s_add_u32 s74,s83,36864\n"
"buffer_load_dwordx4 v251,s[20:23],s74 offen lds\n"
"s_add_u32 s74,s85,6144\n"
"buffer_load_dwordx4 v[196:199],v252,s[32:35],s74 offen offset:1024 sc1\n"
"s_addk_i32 m0,4096\n"
"s_add_u32 s74,s83,36992\n"
"buffer_load_dwordx4 v251,s[20:23],s74 offen lds\n"
"s_add_u32 s74,s86,6144\n"
"buffer_load_dwordx4 v[200:203],v252,s[32:35],s74 offen sc1\n"
"s_addk_i32 m0,4096\n"
"s_add_u32 s74,s83,37120\n"
"buffer_load_dwordx4 v251,s[20:23],s74 offen lds\n"
"s_add_u32 s74,s86,6144\n"
"buffer_load_dwordx4 v[204:207],v252,s[32:35],s74 offen offset:1024 sc1\n"
"s_addk_i32 m0,4096\n"
"s_add_u32 s74,s82,110592\n"
"buffer_load_dwordx4 v251,s[20:23],s74 offen lds\n"
"s_add_u32 s74,s85,8192\n"
"buffer_load_dwordx4 v[144:147],v252,s[32:35],s74 offen sc1\n"
"s_addk_i32 m0,4096\n"
"s_add_u32 s74,s82,110720\n"
"buffer_load_dwordx4 v251,s[20:23],s74 offen lds\n"
"s_add_u32 s74,s85,8192\n"
"buffer_load_dwordx4 v[148:151],v252,s[32:35],s74 offen offset:1024 sc1\n"
"s_addk_i32 m0,4096\n"
"s_add_u32 s74,s82,110848\n"
"buffer_load_dwordx4 v251,s[20:23],s74 offen lds\n"
"s_waitcnt lgkmcnt(0)\n"
"s_setprio 3\n"
"s_barrier\n"
"ds_read_b64_tr_b16 v[208:209],v248\n"
"ds_read_b64_tr_b16 v[210:211],v248 offset:2048\n"
"ds_read_b64_tr_b16 v[212:213],v248 offset:12288\n"
"ds_read_b64_tr_b16 v[214:215],v248 offset:14336\n"
"ds_read_b64_tr_b16 v[216:217],v248 offset:24576\n"
"ds_read_b64_tr_b16 v[218:219],v248 offset:26624\n"
"ds_read_b64_tr_b16 v[220:221],v249\n"
"ds_read_b64_tr_b16 v[222:223],v249 offset:2048\n"
"ds_read_b64_tr_b16 v[224:225],v249 offset:12288\n"
"ds_read_b64_tr_b16 v[226:227],v249 offset:14336\n"
"ds_read_b64_tr_b16 v[228:229],v249 offset:24576\n"
"ds_read_b64_tr_b16 v[230:231],v249 offset:26624\n"
"s_setprio 1\n"
"s_waitcnt lgkmcnt(6)\n"
"v_mfma_f32_16x16x32_bf16 v[0:3],v[208:211],v[96:99],v[0:3]\n"
"ds_read_b64_tr_b16 v[232:233],v250\n"
"v_mfma_f32_16x16x32_bf16 v[0:3],v[212:215],v[104:107],v[0:3]\n"
"ds_read_b64_tr_b16 v[234:235],v250 offset:2048\n"
"v_mfma_f32_16x16x32_bf16 v[0:3],v[216:219],v[112:115],v[0:3]\n"
"ds_read_b64_tr_b16 v[236:237],v250 offset:12288\n"
"v_mfma_f32_16x16x32_bf16 v[4:7],v[208:211],v[100:103],v[4:7]\n"
"ds_read_b64_tr_b16 v[238:239],v250 offset:14336\n"
"v_mfma_f32_16x16x32_bf16 v[4:7],v[212:215],v[108:111],v[4:7]\n"
"ds_read_b64_tr_b16 v[208:209],v250 offset:24576\n"
"v_mfma_f32_16x16x32_bf16 v[4:7],v[216:219],v[116:119],v[4:7]\n"
"ds_read_b64_tr_b16 v[210:211],v250 offset:26624\n"
"s_waitcnt lgkmcnt(6)\n"
"v_mfma_f32_16x16x32_bf16 v[8:11],v[220:223],v[96:99],v[8:11]\n"
"ds_read_b64_tr_b16 v[212:213],v253\n"
"v_mfma_f32_16x16x32_bf16 v[8:11],v[224:227],v[104:107],v[8:11]\n"
"ds_read_b64_tr_b16 v[214:215],v253 offset:2048\n"
"v_mfma_f32_16x16x32_bf16 v[8:11],v[228:231],v[112:115],v[8:11]\n"
"ds_read_b64_tr_b16 v[216:217],v253 offset:12288\n"
"v_mfma_f32_16x16x32_bf16 v[12:15],v[220:223],v[100:103],v[12:15]\n"
"ds_read_b64_tr_b16 v[218:219],v253 offset:14336\n"
"v_mfma_f32_16x16x32_bf16 v[12:15],v[224:227],v[108:111],v[12:15]\n"
"ds_read_b64_tr_b16 v[220:221],v253 offset:24576\n"
"v_mfma_f32_16x16x32_bf16 v[12:15],v[228:231],v[116:119],v[12:15]\n"
"ds_read_b64_tr_b16 v[222:223],v253 offset:26624\n"
"s_waitcnt lgkmcnt(6)\n"
"v_mfma_f32_16x16x32_bf16 v[16:19],v[232:235],v[96:99],v[16:19]\n"
"ds_read_b64_tr_b16 v[224:225],v248 offset:4096\n"
"v_mfma_f32_16x16x32_bf16 v[16:19],v[236:239],v[104:107],v[16:19]\n"
"ds_read_b64_tr_b16 v[226:227],v248 offset:6144\n"
"v_mfma_f32_16x16x32_bf16 v[16:19],v[208:211],v[112:115],v[16:19]\n"
"ds_read_b64_tr_b16 v[228:229],v248 offset:16384\n"
"v_mfma_f32_16x16x32_bf16 v[20:23],v[232:235],v[100:103],v[20:23]\n"
"ds_read_b64_tr_b16 v[230:231],v248 offset:18432\n"
"v_mfma_f32_16x16x32_bf16 v[20:23],v[236:239],v[108:111],v[20:23]\n"
"ds_read_b64_tr_b16 v[232:233],v248 offset:28672\n"
"v_mfma_f32_16x16x32_bf16 v[20:23],v[208:211],v[116:119],v[20:23]\n"
"ds_read_b64_tr_b16 v[234:235],v248 offset:30720\n"
"s_waitcnt lgkmcnt(6)\n"
"v_mfma_f32_16x16x32_bf16 v[24:27],v[212:215],v[96:99],v[24:27]\n"
"ds_read_b64_tr_b16 v[236:237],v249 offset:4096\n"
"v_mfma_f32_16x16x32_bf16 v[24:27],v[216:219],v[104:107],v[24:27]\n"
"ds_read_b64_tr_b16 v[238:239],v249 offset:6144\n"
"v_mfma_f32_16x16x32_bf16 v[24:27],v[220:223],v[112:115],v[24:27]\n"
"ds_read_b64_tr_b16 v[208:209],v249 offset:16384\n"
"v_mfma_f32_16x16x32_bf16 v[28:31],v[212:215],v[100:103],v[28:31]\n"
"ds_read_b64_tr_b16 v[210:211],v249 offset:18432\n"
"v_mfma_f32_16x16x32_bf16 v[28:31],v[216:219],v[108:111],v[28:31]\n"
"ds_read_b64_tr_b16 v[212:213],v249 offset:28672\n"
"v_mfma_f32_16x16x32_bf16 v[28:31],v[220:223],v[116:119],v[28:31]\n"
"ds_read_b64_tr_b16 v[214:215],v249 offset:30720\n"
"s_waitcnt lgkmcnt(6)\n"
"v_mfma_f32_16x16x32_bf16 v[32:35],v[224:227],v[96:99],v[32:35]\n"
"ds_read_b64_tr_b16 v[216:217],v250 offset:4096\n"
"v_mfma_f32_16x16x32_bf16 v[32:35],v[228:231],v[104:107],v[32:35]\n"
"ds_read_b64_tr_b16 v[218:219],v250 offset:6144\n"
"v_mfma_f32_16x16x32_bf16 v[32:35],v[232:235],v[112:115],v[32:35]\n"
"ds_read_b64_tr_b16 v[220:221],v250 offset:16384\n"
"v_mfma_f32_16x16x32_bf16 v[36:39],v[224:227],v[100:103],v[36:39]\n"
"ds_read_b64_tr_b16 v[222:223],v250 offset:18432\n"
"v_mfma_f32_16x16x32_bf16 v[36:39],v[228:231],v[108:111],v[36:39]\n"
"ds_read_b64_tr_b16 v[224:225],v250 offset:28672\n"
"v_mfma_f32_16x16x32_bf16 v[36:39],v[232:235],v[116:119],v[36:39]\n"
"ds_read_b64_tr_b16 v[226:227],v250 offset:30720\n"
"buffer_load_dword v240,v255,s[32:35],0 offen sc1\n"
"s_waitcnt lgkmcnt(6)\n"
"v_mfma_f32_16x16x32_bf16 v[40:43],v[236:239],v[96:99],v[40:43]\n"
"ds_read_b64_tr_b16 v[228:229],v253 offset:4096\n"
"v_mfma_f32_16x16x32_bf16 v[40:43],v[208:211],v[104:107],v[40:43]\n"
"ds_read_b64_tr_b16 v[230:231],v253 offset:6144\n"
"v_mfma_f32_16x16x32_bf16 v[40:43],v[212:215],v[112:115],v[40:43]\n"
"ds_read_b64_tr_b16 v[232:233],v253 offset:16384\n"
"v_mfma_f32_16x16x32_bf16 v[44:47],v[236:239],v[100:103],v[44:47]\n"
"ds_read_b64_tr_b16 v[234:235],v253 offset:18432\n"
"v_mfma_f32_16x16x32_bf16 v[44:47],v[208:211],v[108:111],v[44:47]\n"
"ds_read_b64_tr_b16 v[236:237],v253 offset:28672\n"
"v_mfma_f32_16x16x32_bf16 v[44:47],v[212:215],v[116:119],v[44:47]\n"
"ds_read_b64_tr_b16 v[238:239],v253 offset:30720\n"
"s_waitcnt lgkmcnt(6)\n"
"v_mfma_f32_16x16x32_bf16 v[48:51],v[216:219],v[96:99],v[48:51]\n"
"ds_read_b64_tr_b16 v[208:209],v248 offset:8192\n"
"v_mfma_f32_16x16x32_bf16 v[48:51],v[220:223],v[104:107],v[48:51]\n"
"ds_read_b64_tr_b16 v[210:211],v248 offset:10240\n"
"v_mfma_f32_16x16x32_bf16 v[48:51],v[224:227],v[112:115],v[48:51]\n"
"ds_read_b64_tr_b16 v[212:213],v248 offset:20480\n"
"v_mfma_f32_16x16x32_bf16 v[52:55],v[216:219],v[100:103],v[52:55]\n"
"ds_read_b64_tr_b16 v[214:215],v248 offset:22528\n"
"v_mfma_f32_16x16x32_bf16 v[52:55],v[220:223],v[108:111],v[52:55]\n"
"ds_read_b64_tr_b16 v[216:217],v248 offset:32768\n"
"v_mfma_f32_16x16x32_bf16 v[52:55],v[224:227],v[116:119],v[52:55]\n"
"ds_read_b64_tr_b16 v[218:219],v248 offset:34816\n"
"s_setprio 2\n"
"s_waitcnt lgkmcnt(6)\n"
"v_mfma_f32_16x16x32_bf16 v[56:59],v[228:231],v[96:99],v[56:59]\n"
"ds_read_b64_tr_b16 v[220:221],v249 offset:8192\n"
"v_mfma_f32_16x16x32_bf16 v[56:59],v[232:235],v[104:107],v[56:59]\n"
"ds_read_b64_tr_b16 v[222:223],v249 offset:10240\n"
"v_mfma_f32_16x16x32_bf16 v[56:59],v[236:239],v[112:115],v[56:59]\n"
"ds_read_b64_tr_b16 v[224:225],v249 offset:20480\n"
"v_mfma_f32_16x16x32_bf16 v[60:63],v[228:231],v[100:103],v[60:63]\n"
"ds_read_b64_tr_b16 v[226:227],v249 offset:22528\n"
"v_mfma_f32_16x16x32_bf16 v[60:63],v[232:235],v[108:111],v[60:63]\n"
"ds_read_b64_tr_b16 v[228:229],v249 offset:32768\n"
"v_mfma_f32_16x16x32_bf16 v[60:63],v[236:239],v[116:119],v[60:63]\n"
"ds_read_b64_tr_b16 v[230:231],v249 offset:34816\n"
"s_waitcnt lgkmcnt(6)\n"
"v_mfma_f32_16x16x32_bf16 v[64:67],v[208:211],v[96:99],v[64:67]\n"
"ds_read_b64_tr_b16 v[232:233],v250 offset:8192\n"
"v_mfma_f32_16x16x32_bf16 v[64:67],v[212:215],v[104:107],v[64:67]\n"
"ds_read_b64_tr_b16 v[234:235],v250 offset:10240\n"
"v_mfma_f32_16x16x32_bf16 v[64:67],v[216:219],v[112:115],v[64:67]\n"
"ds_read_b64_tr_b16 v[236:237],v250 offset:20480\n"
"v_mfma_f32_16x16x32_bf16 v[68:71],v[208:211],v[100:103],v[68:71]\n"
"ds_read_b64_tr_b16 v[238:239],v250 offset:22528\n"
"v_mfma_f32_16x16x32_bf16 v[68:71],v[212:215],v[108:111],v[68:71]\n"
"ds_read_b64_tr_b16 v[208:209],v250 offset:32768\n"
"v_mfma_f32_16x16x32_bf16 v[68:71],v[216:219],v[116:119],v[68:71]\n"
"ds_read_b64_tr_b16 v[210:211],v250 offset:34816\n"
"s_waitcnt lgkmcnt(6)\n"
"v_mfma_f32_16x16x32_bf16 v[72:75],v[220:223],v[96:99],v[72:75]\n"
"ds_read_b64_tr_b16 v[212:213],v253 offset:8192\n"
"v_mfma_f32_16x16x32_bf16 v[72:75],v[224:227],v[104:107],v[72:75]\n"
"ds_read_b64_tr_b16 v[214:215],v253 offset:10240\n"
"v_mfma_f32_16x16x32_bf16 v[72:75],v[228:231],v[112:115],v[72:75]\n"
"ds_read_b64_tr_b16 v[216:217],v253 offset:20480\n"
"v_mfma_f32_16x16x32_bf16 v[76:79],v[220:223],v[100:103],v[76:79]\n"
"ds_read_b64_tr_b16 v[218:219],v253 offset:22528\n"
"v_mfma_f32_16x16x32_bf16 v[76:79],v[224:227],v[108:111],v[76:79]\n"
"ds_read_b64_tr_b16 v[220:221],v253 offset:32768\n"
"v_mfma_f32_16x16x32_bf16 v[76:79],v[228:231],v[116:119],v[76:79]\n"
"ds_read_b64_tr_b16 v[222:223],v253 offset:34816\n"
"s_waitcnt vmcnt(1)\n"
"s_waitcnt lgkmcnt(6)\n"
"v_mfma_f32_16x16x32_bf16 v[80:83],v[232:235],v[96:99],v[80:83]\n"
"v_mfma_f32_16x16x32_bf16 v[80:83],v[236:239],v[104:107],v[80:83]\n"
"s_barrier\n"
"v_mfma_f32_16x16x32_bf16 v[80:83],v[208:211],v[112:115],v[80:83]\n"
"v_mfma_f32_16x16x32_bf16 v[84:87],v[232:235],v[100:103],v[84:87]\n"
"v_mfma_f32_16x16x32_bf16 v[84:87],v[236:239],v[108:111],v[84:87]\n"
"v_mfma_f32_16x16x32_bf16 v[84:87],v[208:211],v[116:119],v[84:87]\n"
"s_waitcnt lgkmcnt(0)\n"
"v_mfma_f32_16x16x32_bf16 v[88:91],v[212:215],v[96:99],v[88:91]\n"
"v_mfma_f32_16x16x32_bf16 v[88:91],v[216:219],v[104:107],v[88:91]\n"
"v_mfma_f32_16x16x32_bf16 v[88:91],v[220:223],v[112:115],v[88:91]\n"
"v_mfma_f32_16x16x32_bf16 v[92:95],v[212:215],v[100:103],v[92:95]\n"
"v_mfma_f32_16x16x32_bf16 v[92:95],v[216:219],v[108:111],v[92:95]\n"
"v_mfma_f32_16x16x32_bf16 v[92:95],v[220:223],v[116:119],v[92:95]\n"
"s_setprio 0\n"
"v_add_u32 v248,73728,v248\n"
"v_add_u32 v249,73728,v249\n"
"v_add_u32 v250,73728,v250\n"
"v_add_u32 v253,73728,v253\n"
"s_add_u32 m0,s73,110592\n"
"s_add_u32 s74,s83,110592\n"
"buffer_load_dwordx4 v251,s[20:23],s74 offen lds\n"
"s_addk_i32 m0,4096\n"
"s_add_u32 s74,s82,184320\n"
"buffer_load_dwordx4 v251,s[20:23],s74 offen lds\n"
"s_waitcnt vmcnt(2)\n"
"v_cmp_gt_u32 vcc,3,v240\n"
"s_nop 1\n"
"s_cbranch_vccz .Lk_h3g0_ok\n"
"s_mov_b32 s89,0x100000\n"
".Lk_h3g0_spin:\n"
"s_sleep 2\n"
"buffer_load_dword v240,v255,s[32:35],0 offen sc1\n"
"s_waitcnt vmcnt(0)\n"
"v_cmp_gt_u32 vcc,3,v240\n"
"s_nop 1\n"
"s_cbranch_vccz .Lk_h3g0_ok\n"
"s_sub_u32 s89,s89,1\n"
"s_cmp_lg_u32 s89,0\n"
"s_cbranch_scc1 .Lk_h3g0_spin\n"
".Lk_h3g0_ok:\n"
"s_add_u32 m0,s73,118784\n"
"s_add_u32 s74,s83,184320\n"
"buffer_load_dwordx4 v251,s[20:23],s74 offen lds\n"
"v_lshrrev_b32 v254,4,v252\n"
"v_bfe_u32 v182,v254,2,2\n"
"v_bfe_u32 v183,v254,4,1\n"
"v_lshl_add_u32 v182,v182,2,v183\n"
"v_lshrrev_b32 v189,5,v254\n"
"s_add_u32 s74,s86,8192\n"
"buffer_load_dwordx4 v[152:155],v252,s[32:35],s74 offen sc1\n"
"v_and_b32 v183,3,v254\n"
"v_lshl_add_u32 v189,v189,2,v183\n"
"v_lshlrev_b32 v189,3,v189\n"
"s_lshr_b32 s75,s71,1\n"
"s_and_b32 s76,s71,1\n"
"s_addk_i32 m0,4096\n"
"s_add_u32 s74,s82,258048\n"
"buffer_load_dwordx4 v251,s[20:23],s74 offen lds\n"
"s_lshl_b32 s77,s69,3\n"
"s_add_u32 s77,s77,s75\n"
"s_lshl_b32 s78,s77,5\n"
"s_add_u32 s78,s78,147456\n"
"v_lshl_add_u32 v188,v182,4,s78\n"
"s_add_u32 s74,s86,8192\n"
"buffer_load_dwordx4 v[156:159],v252,s[32:35],s74 offen offset:1024 sc1\n"
"s_setprio 3\n"
"ds_read_b128 v[184:187],v188\n"
"s_setprio 0\n"
"v_mul_u32_u24 v188,2304,v182\n"
"v_add_u32 v188,v188,v189\n"
"s_mul_i32 s89,s70,6\n"
"s_add_u32 s89,s89,s76\n"
"s_addk_i32 m0,4096\n"
"s_add_u32 s74,s83,258048\n"
"buffer_load_dwordx4 v251,s[20:23],s74 offen lds\n"
"s_lshl_b32 s78,s89,6\n"
"v_add_u32 v189,s78,v189\n"
"s_lshl_b32 s84,s68,5\n"
"s_add_u32 s84,s84,s77\n"
"s_mul_i32 s84,s84,4608\n"
"s_add_u32 s74,s85,10240\n"
"buffer_load_dwordx4 v[160:163],v252,s[32:35],s74 offen sc1\n"
"s_add_u32 s84,s84,s78\n"
"s_lshl_b32 s78,s68,2\n"
"s_add_u32 s78,s78,s69\n"
"s_lshl_b32 s78,s78,1\n"
"s_add_u32 s78,s78,s75\n"
"s_addk_i32 m0,4096\n"
"s_add_u32 s74,s82,331776\n"
"buffer_load_dwordx4 v251,s[20:23],s74 offen lds\n"
"s_mul_i32 s80,s78,18\n"
"s_add_u32 s80,s80,s89\n"
"s_lshl_b32 s80,s80,10\n"
"s_add_u32 s80,s80,20971520\n"
"s_mul_i32 s75,s68,12\n"
"s_add_u32 s75,s75,s70\n"
"s_add_u32 s74,s85,10240\n"
"buffer_load_dwordx4 v[164:167],v252,s[32:35],s74 offen offset:1024 sc1\n"
"s_lshl_b32 s75,s75,10\n"
"s_add_u32 s75,s75,256\n"
"s_lshl_b32 s77,s68,12\n"
"s_lshl_b32 s78,s76,8\n"
"s_add_u32 s77,s77,s78\n"
"s_addk_i32 m0,4096\n"
"s_add_u32 s74,s83,331776\n"
"buffer_load_dwordx4 v251,s[20:23],s74 offen lds\n"
"s_add_u32 s77,s77,196608\n"
"v_mul_u32_u24 v190,3072,v183\n"
"v_add_u32 v190,s75,v190\n"
"v_lshl_add_u32 v183,v183,10,s77\n"
"s_mov_b32 s96,0xf0f0f0f0\n"
"s_add_u32 s74,s86,10240\n"
"buffer_load_dwordx4 v[168:171],v252,s[32:35],s74 offen sc1\n"
"s_mov_b32 s97,0xf0f0f0f0\n"
"v_cndmask_b32 v190,v190,v183,s[96:97]\n"
"s_lshl_b32 s95,s69,6\n"
"s_mov_b32 s79,0x40088e99\n"
"v_add_u32 v191,s73,v252\n"
"s_addk_i32 m0,4096\n"
"s_add_u32 s74,s82,405504\n"
"buffer_load_dwordx4 v251,s[20:23],s74 offen lds\n"
"v_mov_b32 v176,0\n"
"v_mov_b32 v177,0\n"
"v_mov_b32 v180,0\n"
"v_mov_b32 v181,0\n"
"s_mov_b32 s90,0xcccccccc\n"
"s_add_u32 s74,s86,10240\n"
"buffer_load_dwordx4 v[172:175],v252,s[32:35],s74 offen offset:1024 sc1\n"
"s_mov_b32 s91,0xcccccccc\n"
"s_mov_b32 s92,0xaaaaaaaa\n"
"s_mov_b32 s93,0xaaaaaaaa\n"
"v_mov_b32 v208,0x3f80\n"
"v_cndmask_b32 v208,v208,1.0,s[92:93]\n"
"s_addk_i32 m0,4096\n"
"s_add_u32 s74,s83,405504\n"
"buffer_load_dwordx4 v251,s[20:23],s74 offen lds\n"
"v_bfe_u32 v209,v254,2,2\n"
"v_lshrrev_b32 v210,4,v254\n"
"v_cmp_eq_u32 vcc,v209,v210\n"
"s_nop 1\n"
"v_cndmask_b32 v208,0,v208,vcc\n"
"v_cndmask_b32 v178,v208,0,s[90:91]\n"
"v_cndmask_b32 v179,0,v208,s[90:91]\n"
"s_waitcnt lgkmcnt(0)\n"
"s_setprio 3\n"
"s_barrier\n"
"ds_read_b64_tr_b16 v[208:209],v248\n"
"ds_read_b64_tr_b16 v[210:211],v248 offset:2048\n"
"ds_read_b64_tr_b16 v[212:213],v248 offset:4096\n"
"ds_read_b64_tr_b16 v[214:215],v248 offset:6144\n"
"ds_read_b64_tr_b16 v[216:217],v248 offset:8192\n"
"ds_read_b64_tr_b16 v[218:219],v248 offset:10240\n"
"ds_read_b64_tr_b16 v[220:221],v248 offset:12288\n"
"ds_read_b64_tr_b16 v[222:223],v248 offset:14336\n"
"ds_read_b64_tr_b16 v[224:225],v248 offset:16384\n"
"ds_read_b64_tr_b16 v[226:227],v248 offset:18432\n"
"ds_read_b64_tr_b16 v[228:229],v248 offset:20480\n"
"ds_read_b64_tr_b16 v[230:231],v248 offset:22528\n"
"s_setprio 1\n"
"s_waitcnt lgkmcnt(8)\n"
"v_mfma_f32_16x16x32_bf16 v[0:3],v[208:211],v[120:123],v[0:3]\n"
"ds_read_b64_tr_b16 v[232:233],v249\n"
"v_mfma_f32_16x16x32_bf16 v[0:3],v[212:215],v[128:131],v[0:3]\n"
"ds_read_b64_tr_b16 v[234:235],v249 offset:2048\n"
"v_mfma_f32_16x16x32_bf16 v[4:7],v[208:211],v[124:127],v[4:7]\n"
"ds_read_b64_tr_b16 v[236:237],v249 offset:4096\n"
"v_mfma_f32_16x16x32_bf16 v[4:7],v[212:215],v[132:135],v[4:7]\n"
"ds_read_b64_tr_b16 v[238:239],v249 offset:6144\n"
"s_waitcnt lgkmcnt(8)\n"
"v_mfma_f32_16x16x32_bf16 v[4:7],v[216:219],v[140:143],v[4:7]\n"
"ds_read_b64_tr_b16 v[208:209],v249 offset:8192\n"
"v_mfma_f32_16x16x32_bf16 v[4:7],v[220:223],v[196:199],v[4:7]\n"
"ds_read_b64_tr_b16 v[210:211],v249 offset:10240\n"
"v_mfma_f32_16x16x32_bf16 v[0:3],v[216:219],v[136:139],v[0:3]\n"
"ds_read_b64_tr_b16 v[212:213],v249 offset:12288\n"
"v_mfma_f32_16x16x32_bf16 v[0:3],v[220:223],v[192:195],v[0:3]\n"
"ds_read_b64_tr_b16 v[214:215],v249 offset:14336\n"
"s_waitcnt lgkmcnt(8)\n"
"v_mfma_f32_16x16x32_bf16 v[0:3],v[224:227],v[200:203],v[0:3]\n"
"ds_read_b64_tr_b16 v[216:217],v249 offset:16384\n"
"v_mfma_f32_16x16x32_bf16 v[0:3],v[228:231],v[144:147],v[0:3]\n"
"ds_read_b64_tr_b16 v[218:219],v249 offset:18432\n"
"v_mfma_f32_16x16x32_bf16 v[4:7],v[224:227],v[204:207],v[4:7]\n"
"ds_read_b64_tr_b16 v[220:221],v249 offset:20480\n"
"v_mfma_f32_16x16x32_bf16 v[4:7],v[228:231],v[148:151],v[4:7]\n"
"ds_read_b64_tr_b16 v[222:223],v249 offset:22528\n"
"s_waitcnt lgkmcnt(8)\n"
"v_mfma_f32_16x16x32_bf16 v[8:11],v[232:235],v[120:123],v[8:11]\n"
"ds_read_b64_tr_b16 v[224:225],v250\n"
"v_mfma_f32_16x16x32_bf16 v[8:11],v[236:239],v[128:131],v[8:11]\n"
"ds_read_b64_tr_b16 v[226:227],v250 offset:2048\n"
"v_mfma_f32_16x16x32_bf16 v[12:15],v[232:235],v[124:127],v[12:15]\n"
"ds_read_b64_tr_b16 v[228:229],v250 offset:4096\n"
"v_mfma_f32_16x16x32_bf16 v[12:15],v[236:239],v[132:135],v[12:15]\n"
"ds_read_b64_tr_b16 v[230:231],v250 offset:6144\n"
"s_waitcnt lgkmcnt(8)\n"
"v_mfma_f32_16x16x32_bf16 v[12:15],v[208:211],v[140:143],v[12:15]\n"
"ds_read_b64_tr_b16 v[232:233],v250 offset:8192\n"
"v_mfma_f32_16x16x32_bf16 v[12:15],v[212:215],v[196:199],v[12:15]\n"
"ds_read_b64_tr_b16 v[234:235],v250 offset:10240\n"
"v_mfma_f32_16x16x32_bf16 v[8:11],v[208:211],v[136:139],v[8:11]\n"
"ds_read_b64_tr_b16 v[236:237],v250 offset:12288\n"
"v_mfma_f32_16x16x32_bf16 v[8:11],v[212:215],v[192:195],v[8:11]\n"
"ds_read_b64_tr_b16 v[238:239],v250 offset:14336\n"
"s_waitcnt lgkmcnt(8)\n"
"v_mfma_f32_16x16x32_bf16 v[8:11],v[216:219],v[200:203],v[8:11]\n"
"ds_read_b64_tr_b16 v[208:209],v250 offset:16384\n"
"v_mfma_f32_16x16x32_bf16 v[8:11],v[220:223],v[144:147],v[8:11]\n"
"ds_read_b64_tr_b16 v[210:211],v250 offset:18432\n"
"v_mfma_f32_16x16x32_bf16 v[12:15],v[216:219],v[204:207],v[12:15]\n"
"ds_read_b64_tr_b16 v[212:213],v250 offset:20480\n"
"v_mfma_f32_16x16x32_bf16 v[12:15],v[220:223],v[148:151],v[12:15]\n"
"ds_read_b64_tr_b16 v[214:215],v250 offset:22528\n"
"s_waitcnt lgkmcnt(8)\n"
"v_mfma_f32_16x16x32_bf16 v[16:19],v[224:227],v[120:123],v[16:19]\n"
"ds_read_b64_tr_b16 v[216:217],v253\n"
"v_mfma_f32_16x16x32_bf16 v[16:19],v[228:231],v[128:131],v[16:19]\n"
"ds_read_b64_tr_b16 v[218:219],v253 offset:2048\n"
"v_mfma_f32_16x16x32_bf16 v[20:23],v[224:227],v[124:127],v[20:23]\n"
"ds_read_b64_tr_b16 v[220:221],v253 offset:4096\n"
"v_mfma_f32_16x16x32_bf16 v[20:23],v[228:231],v[132:135],v[20:23]\n"
"ds_read_b64_tr_b16 v[222:223],v253 offset:6144\n"
"s_waitcnt lgkmcnt(8)\n"
"v_mfma_f32_16x16x32_bf16 v[20:23],v[232:235],v[140:143],v[20:23]\n"
"ds_read_b64_tr_b16 v[224:225],v253 offset:8192\n"
"v_mfma_f32_16x16x32_bf16 v[20:23],v[236:239],v[196:199],v[20:23]\n"
"ds_read_b64_tr_b16 v[226:227],v253 offset:10240\n"
"v_mfma_f32_16x16x32_bf16 v[16:19],v[232:235],v[136:139],v[16:19]\n"
"ds_read_b64_tr_b16 v[228:229],v253 offset:12288\n"
"v_mfma_f32_16x16x32_bf16 v[16:19],v[236:239],v[192:195],v[16:19]\n"
"ds_read_b64_tr_b16 v[230:231],v253 offset:14336\n"
"s_waitcnt lgkmcnt(8)\n"
"v_mfma_f32_16x16x32_bf16 v[16:19],v[208:211],v[200:203],v[16:19]\n"
"ds_read_b64_tr_b16 v[232:233],v253 offset:16384\n"
"v_mfma_f32_16x16x32_bf16 v[16:19],v[212:215],v[144:147],v[16:19]\n"
"ds_read_b64_tr_b16 v[234:235],v253 offset:18432\n"
"v_mfma_f32_16x16x32_bf16 v[20:23],v[208:211],v[204:207],v[20:23]\n"
"ds_read_b64_tr_b16 v[236:237],v253 offset:20480\n"
"v_mfma_f32_16x16x32_bf16 v[20:23],v[212:215],v[148:151],v[20:23]\n"
"ds_read_b64_tr_b16 v[238:239],v253 offset:22528\n"
"s_waitcnt lgkmcnt(8)\n"
"v_mfma_f32_16x16x32_bf16 v[24:27],v[216:219],v[120:123],v[24:27]\n"
"ds_read_b64_tr_b16 v[208:209],v248 offset:24576\n"
"v_mfma_f32_16x16x32_bf16 v[24:27],v[220:223],v[128:131],v[24:27]\n"
"ds_read_b64_tr_b16 v[210:211],v248 offset:26624\n"
"v_mfma_f32_16x16x32_bf16 v[28:31],v[216:219],v[124:127],v[28:31]\n"
"ds_read_b64_tr_b16 v[212:213],v248 offset:28672\n"
"v_mfma_f32_16x16x32_bf16 v[28:31],v[220:223],v[132:135],v[28:31]\n"
"ds_read_b64_tr_b16 v[214:215],v248 offset:30720\n"
"s_waitcnt lgkmcnt(8)\n"
"v_mfma_f32_16x16x32_bf16 v[28:31],v[224:227],v[140:143],v[28:31]\n"
"ds_read_b64_tr_b16 v[216:217],v248 offset:32768\n"
"v_mfma_f32_16x16x32_bf16 v[28:31],v[228:231],v[196:199],v[28:31]\n"
"ds_read_b64_tr_b16 v[218:219],v248 offset:34816\n"
"s_setprio 2\n"
"v_mfma_f32_16x16x32_bf16 v[24:27],v[224:227],v[136:139],v[24:27]\n"
"ds_read_b64_tr_b16 v[220:221],v249 offset:24576\n"
"v_mfma_f32_16x16x32_bf16 v[24:27],v[228:231],v[192:195],v[24:27]\n"
"ds_read_b64_tr_b16 v[222:223],v249 offset:26624\n"
"s_waitcnt lgkmcnt(8)\n"
"v_mfma_f32_16x16x32_bf16 v[24:27],v[232:235],v[200:203],v[24:27]\n"
"ds_read_b64_tr_b16 v[224:225],v249 offset:28672\n"
"v_mfma_f32_16x16x32_bf16 v[24:27],v[236:239],v[144:147],v[24:27]\n"
"ds_read_b64_tr_b16 v[226:227],v249 offset:30720\n"
"v_mfma_f32_16x16x32_bf16 v[28:31],v[232:235],v[204:207],v[28:31]\n"
"ds_read_b64_tr_b16 v[228:229],v249 offset:32768\n"
"v_mfma_f32_16x16x32_bf16 v[28:31],v[236:239],v[148:151],v[28:31]\n"
"ds_read_b64_tr_b16 v[230:231],v249 offset:34816\n"
"s_waitcnt vmcnt(1)\n"
"s_waitcnt lgkmcnt(8)\n"
"v_mfma_f32_16x16x32_bf16 v[0:3],v[208:211],v[152:155],v[0:3]\n"
"ds_read_b64_tr_b16 v[232:233],v250 offset:24576\n"
"v_mfma_f32_16x16x32_bf16 v[0:3],v[212:215],v[160:163],v[0:3]\n"
"ds_read_b64_tr_b16 v[234:235],v250 offset:26624\n"
"v_mfma_f32_16x16x32_bf16 v[4:7],v[208:211],v[156:159],v[4:7]\n"
"ds_read_b64_tr_b16 v[236:237],v250 offset:28672\n"
"v_mfma_f32_16x16x32_bf16 v[4:7],v[212:215],v[164:167],v[4:7]\n"
"ds_read_b64_tr_b16 v[238:239],v250 offset:30720\n"
"s_waitcnt lgkmcnt(8)\n"
"v_mfma_f32_16x16x32_bf16 v[4:7],v[216:219],v[172:175],v[4:7]\n"
"ds_read_b64_tr_b16 v[208:209],v250 offset:32768\n"
"v_mfma_f32_16x16x32_bf16 v[0:3],v[216:219],v[168:171],v[0:3]\n"
"ds_read_b64_tr_b16 v[210:211],v250 offset:34816\n"
"v_mfma_f32_16x16x32_bf16 v[8:11],v[220:223],v[152:155],v[8:11]\n"
"ds_read_b64_tr_b16 v[212:213],v253 offset:24576\n"
"s_waitcnt lgkmcnt(7)\n"
"v_mfma_f32_16x16x32_bf16 v[8:11],v[224:227],v[160:163],v[8:11]\n"
"ds_read_b64_tr_b16 v[214:215],v253 offset:26624\n"
"v_mfma_f32_16x16x32_bf16 v[12:15],v[220:223],v[156:159],v[12:15]\n"
"ds_read_b64_tr_b16 v[216:217],v253 offset:28672\n"
"v_mfma_f32_16x16x32_bf16 v[12:15],v[224:227],v[164:167],v[12:15]\n"
"ds_read_b64_tr_b16 v[218:219],v253 offset:30720\n"
"v_mfma_f32_16x16x32_bf16 v[12:15],v[228:231],v[172:175],v[12:15]\n"
"ds_read_b64_tr_b16 v[220:221],v253 offset:32768\n"
"v_mfma_f32_16x16x32_bf16 v[8:11],v[228:231],v[168:171],v[8:11]\n"
"ds_read_b64_tr_b16 v[222:223],v253 offset:34816\n"
"s_waitcnt vmcnt(0)\n"
"s_waitcnt lgkmcnt(8)\n"
"v_mfma_f32_16x16x32_bf16 v[16:19],v[232:235],v[152:155],v[16:19]\n"
"v_mfma_f32_16x16x32_bf16 v[16:19],v[236:239],v[160:163],v[16:19]\n"
"s_barrier\n"
"v_mfma_f32_16x16x32_bf16 v[20:23],v[232:235],v[156:159],v[20:23]\n"
"v_mfma_f32_16x16x32_bf16 v[20:23],v[236:239],v[164:167],v[20:23]\n"
"s_waitcnt lgkmcnt(4)\n"
"v_mfma_f32_16x16x32_bf16 v[20:23],v[208:211],v[172:175],v[20:23]\n"
"v_mov_b32 v255,0\n"
"v_mfma_f32_16x16x32_bf16 v[16:19],v[208:211],v[168:171],v[16:19]\n"
"v_cndmask_b32 v255,v255,1,s[96:97]\n"
"v_mfma_f32_16x16x32_bf16 v[24:27],v[212:215],v[152:155],v[24:27]\n"
"buffer_load_dword v254,v190,s[32:35],s95 offen sc1\n"
"s_waitcnt lgkmcnt(0)\n"
"v_mfma_f32_16x16x32_bf16 v[24:27],v[216:219],v[160:163],v[24:27]\n"
"v_mfma_f32_16x16x32_bf16 v[28:31],v[212:215],v[156:159],v[28:31]\n"
"v_mfma_f32_16x16x32_bf16 v[28:31],v[216:219],v[164:167],v[28:31]\n"
"v_mfma_f32_16x16x32_bf16 v[28:31],v[220:223],v[172:175],v[28:31]\n"
"v_mfma_f32_16x16x32_bf16 v[24:27],v[220:223],v[168:171],v[24:27]\n"
"s_setprio 0\n"
"v_add_u32 v248,4294893568,v248\n"
"v_add_u32 v249,4294893568,v249\n"
"v_add_u32 v250,4294893568,v250\n"
"v_add_u32 v253,4294893568,v253\n"
"v_cvt_pk_bf16_f32 v0,v0,v1\n"
"v_cvt_pk_bf16_f32 v1,v2,v3\n"
"v_cvt_pk_bf16_f32 v2,v8,v9\n"
"v_cvt_pk_bf16_f32 v3,v10,v11\n"
"s_setprio 3\n"
"ds_write_b128 v247,v[0:3]\n"
"s_setprio 0\n"
"v_cvt_pk_bf16_f32 v4,v4,v5\n"
"v_cvt_pk_bf16_f32 v5,v6,v7\n"
"v_cvt_pk_bf16_f32 v6,v12,v13\n"
"v_cvt_pk_bf16_f32 v7,v14,v15\n"
"s_setprio 3\n"
"ds_write_b128 v247,v[4:7] offset:1024\n"
"s_setprio 0\n"
"v_cvt_pk_bf16_f32 v16,v16,v17\n"
"v_cvt_pk_bf16_f32 v17,v18,v19\n"
"v_cvt_pk_bf16_f32 v18,v24,v25\n"
"v_cvt_pk_bf16_f32 v19,v26,v27\n"
"s_setprio 3\n"
"ds_write_b128 v247,v[16:19] offset:2048\n"
"s_setprio 0\n"
"v_cvt_pk_bf16_f32 v20,v20,v21\n"
"v_cvt_pk_bf16_f32 v21,v22,v23\n"
"v_cvt_pk_bf16_f32 v22,v28,v29\n"
"v_cvt_pk_bf16_f32 v23,v30,v31\n"
"s_setprio 3\n"
"ds_write_b128 v247,v[20:23] offset:3072\n"
"s_setprio 0\n"
"s_add_u32 m0,s73,36864\n"
"s_add_u32 s74,s83,110720\n"
"buffer_load_dwordx4 v251,s[20:23],s74 offen lds\n"
"s_addk_i32 m0,4096\n"
"s_add_u32 s74,s82,184448\n"
"buffer_load_dwordx4 v251,s[20:23],s74 offen lds\n"
"s_addk_i32 m0,4096\n"
"s_add_u32 s74,s83,184448\n"
"buffer_load_dwordx4 v251,s[20:23],s74 offen lds\n"
"s_addk_i32 m0,4096\n"
"s_add_u32 s74,s82,258176\n"
"buffer_load_dwordx4 v251,s[20:23],s74 offen lds\n"
"s_addk_i32 m0,4096\n"
"s_add_u32 s74,s83,258176\n"
"buffer_load_dwordx4 v251,s[20:23],s74 offen lds\n"
"s_addk_i32 m0,4096\n"
"s_add_u32 s74,s82,331904\n"
"buffer_load_dwordx4 v251,s[20:23],s74 offen lds\n"
"s_addk_i32 m0,4096\n"
"s_add_u32 s74,s83,331904\n"
"buffer_load_dwordx4 v251,s[20:23],s74 offen lds\n"
"s_addk_i32 m0,4096\n"
"s_add_u32 s74,s82,405632\n"
"buffer_load_dwordx4 v251,s[20:23],s74 offen lds\n"
"s_addk_i32 m0,4096\n"
"s_add_u32 s74,s83,405632\n"
"buffer_load_dwordx4 v251,s[20:23],s74 offen lds\n"
".Lk_padm11:\n"
"s_cbranch_execz .Lk_hop_retm\n"
"s_waitcnt lgkmcnt(0)\n"
"s_waitcnt vmcnt(9)\n"
"v_cmp_gt_u32 vcc,v255,v254\n"
"s_nop 1\n"
"s_cbranch_vccz .Lk_rv18_ok\n"
"s_mov_b32 s89,0x100000\n"
".Lk_rv18_spin:\n"
"s_sleep 2\n"
"buffer_load_dword v254,v190,s[32:35],s95 offen sc1\n"
"s_waitcnt vmcnt(0)\n"
"v_cmp_gt_u32 vcc,v255,v254\n"
"s_nop 1\n"
"s_cbranch_vccz .Lk_rv18_ok\n"
"s_sub_u32 s89,s89,1\n"
"s_cmp_lg_u32 s89,0\n"
"s_cbranch_scc1 .Lk_rv18_spin\n"
".Lk_rv18_ok:\n"
"s_add_u32 s77,s80,9437184\n"
"buffer_load_dwordx4 v[0:3],v252,s[32:35],s77 offen sc1\n"
"s_add_u32 s77,s80,11796480\n"
"buffer_load_dwordx4 v[4:7],v252,s[32:35],s77 offen sc1\n"
"s_add_u32 s77,s80,14155776\n"
"buffer_load_dwordx4 v[8:11],v252,s[32:35],s77 offen sc1\n"
"s_add_u32 s77,s80,16515072\n"
"buffer_load_dwordx4 v[12:15],v252,s[32:35],s77 offen sc1\n"
"s_waitcnt lgkmcnt(0)\n"
"s_setprio 3\n"
"s_barrier\n"
"ds_read_b64_tr_b16 v[208:209],v248\n"
"ds_read_b64_tr_b16 v[210:211],v248 offset:2048\n"
"ds_read_b64_tr_b16 v[212:213],v248 offset:4096\n"
"ds_read_b64_tr_b16 v[214:215],v248 offset:6144\n"
"ds_read_b64_tr_b16 v[216:217],v248 offset:8192\n"
"ds_read_b64_tr_b16 v[218:219],v248 offset:10240\n"
"ds_read_b64_tr_b16 v[220:221],v248 offset:12288\n"
"ds_read_b64_tr_b16 v[222:223],v248 offset:14336\n"
"ds_read_b64_tr_b16 v[224:225],v248 offset:16384\n"
"ds_read_b64_tr_b16 v[226:227],v248 offset:18432\n"
"ds_read_b64_tr_b16 v[228:229],v248 offset:20480\n"
"ds_read_b64_tr_b16 v[230:231],v248 offset:22528\n"
"s_setprio 1\n"
"s_waitcnt lgkmcnt(8)\n"
"v_mfma_f32_16x16x32_bf16 v[32:35],v[208:211],v[120:123],v[32:35]\n"
"ds_read_b64_tr_b16 v[232:233],v248 offset:24576\n"
"v_mfma_f32_16x16x32_bf16 v[32:35],v[212:215],v[128:131],v[32:35]\n"
"ds_read_b64_tr_b16 v[234:235],v248 offset:26624\n"
"v_mfma_f32_16x16x32_bf16 v[36:39],v[208:211],v[124:127],v[36:39]\n"
"ds_read_b64_tr_b16 v[236:237],v248 offset:28672\n"
"v_mfma_f32_16x16x32_bf16 v[36:39],v[212:215],v[132:135],v[36:39]\n"
"ds_read_b64_tr_b16 v[238:239],v248 offset:30720\n"
"s_waitcnt lgkmcnt(8)\n"
"v_mfma_f32_16x16x32_bf16 v[36:39],v[216:219],v[140:143],v[36:39]\n"
"ds_read_b64_tr_b16 v[208:209],v248 offset:32768\n"
"v_mfma_f32_16x16x32_bf16 v[36:39],v[220:223],v[196:199],v[36:39]\n"
"ds_read_b64_tr_b16 v[210:211],v248 offset:34816\n"
"v_mfma_f32_16x16x32_bf16 v[32:35],v[216:219],v[136:139],v[32:35]\n"
"ds_read_b64_tr_b16 v[212:213],v249\n"
"v_mfma_f32_16x16x32_bf16 v[32:35],v[220:223],v[192:195],v[32:35]\n"
"ds_read_b64_tr_b16 v[214:215],v249 offset:2048\n"
"s_waitcnt lgkmcnt(8)\n"
"v_mfma_f32_16x16x32_bf16 v[32:35],v[224:227],v[200:203],v[32:35]\n"
"ds_read_b64_tr_b16 v[216:217],v249 offset:4096\n"
"v_mfma_f32_16x16x32_bf16 v[32:35],v[228:231],v[144:147],v[32:35]\n"
"ds_read_b64_tr_b16 v[218:219],v249 offset:6144\n"
"v_mfma_f32_16x16x32_bf16 v[36:39],v[224:227],v[204:207],v[36:39]\n"
"ds_read_b64_tr_b16 v[220:221],v249 offset:8192\n"
"v_mfma_f32_16x16x32_bf16 v[36:39],v[228:231],v[148:151],v[36:39]\n"
"ds_read_b64_tr_b16 v[222:223],v249 offset:10240\n"
"s_waitcnt lgkmcnt(8)\n"
"v_mfma_f32_16x16x32_bf16 v[36:39],v[232:235],v[156:159],v[36:39]\n"
"ds_read_b64_tr_b16 v[224:225],v249 offset:12288\n"
"v_mfma_f32_16x16x32_bf16 v[36:39],v[236:239],v[164:167],v[36:39]\n"
"ds_read_b64_tr_b16 v[226:227],v249 offset:14336\n"
"v_mfma_f32_16x16x32_bf16 v[32:35],v[232:235],v[152:155],v[32:35]\n"
"ds_read_b64_tr_b16 v[228:229],v249 offset:16384\n"
"v_mfma_f32_16x16x32_bf16 v[32:35],v[236:239],v[160:163],v[32:35]\n"
"ds_read_b64_tr_b16 v[230:231],v249 offset:18432\n"
"s_waitcnt lgkmcnt(8)\n"
"v_mfma_f32_16x16x32_bf16 v[32:35],v[208:211],v[168:171],v[32:35]\n"
"ds_read_b64_tr_b16 v[232:233],v249 offset:20480\n"
"v_mfma_f32_16x16x32_bf16 v[36:39],v[208:211],v[172:175],v[36:39]\n"
"ds_read_b64_tr_b16 v[234:235],v249 offset:22528\n"
"v_mfma_f32_16x16x32_bf16 v[40:43],v[212:215],v[120:123],v[40:43]\n"
"ds_read_b64_tr_b16 v[236:237],v249 offset:24576\n"
"s_waitcnt lgkmcnt(7)\n"
"v_mfma_f32_16x16x32_bf16 v[40:43],v[216:219],v[128:131],v[40:43]\n"
"ds_read_b64_tr_b16 v[238:239],v249 offset:26624\n"
"v_mfma_f32_16x16x32_bf16 v[44:47],v[212:215],v[124:127],v[44:47]\n"
"ds_read_b64_tr_b16 v[208:209],v249 offset:28672\n"
"v_mfma_f32_16x16x32_bf16 v[44:47],v[216:219],v[132:135],v[44:47]\n"
"ds_read_b64_tr_b16 v[210:211],v249 offset:30720\n"
"v_mfma_f32_16x16x32_bf16 v[44:47],v[220:223],v[140:143],v[44:47]\n"
"ds_read_b64_tr_b16 v[212:213],v249 offset:32768\n"
"s_waitcnt lgkmcnt(7)\n"
"v_mfma_f32_16x16x32_bf16 v[44:47],v[224:227],v[196:199],v[44:47]\n"
"ds_read_b64_tr_b16 v[214:215],v249 offset:34816\n"
"v_mfma_f32_16x16x32_bf16 v[40:43],v[220:223],v[136:139],v[40:43]\n"
"ds_read_b64_tr_b16 v[216:217],v250\n"
"v_mfma_f32_16x16x32_bf16 v[40:43],v[224:227],v[192:195],v[40:43]\n"
"ds_read_b64_tr_b16 v[218:219],v250 offset:2048\n"
"v_mfma_f32_16x16x32_bf16 v[40:43],v[228:231],v[200:203],v[40:43]\n"
"ds_read_b64_tr_b16 v[220:221],v250 offset:4096\n"
"s_waitcnt lgkmcnt(7)\n"
"v_mfma_f32_16x16x32_bf16 v[40:43],v[232:235],v[144:147],v[40:43]\n"
"ds_read_b64_tr_b16 v[222:223],v250 offset:6144\n"
"v_mfma_f32_16x16x32_bf16 v[44:47],v[228:231],v[204:207],v[44:47]\n"
"ds_read_b64_tr_b16 v[224:225],v250 offset:8192\n"
"v_mfma_f32_16x16x32_bf16 v[44:47],v[232:235],v[148:151],v[44:47]\n"
"ds_read_b64_tr_b16 v[226:227],v250 offset:10240\n"
"v_mfma_f32_16x16x32_bf16 v[44:47],v[236:239],v[156:159],v[44:47]\n"
"ds_read_b64_tr_b16 v[228:229],v250 offset:12288\n"
"s_waitcnt lgkmcnt(7)\n"
"v_mfma_f32_16x16x32_bf16 v[44:47],v[208:211],v[164:167],v[44:47]\n"
"ds_read_b64_tr_b16 v[230:231],v250 offset:14336\n"
"v_mfma_f32_16x16x32_bf16 v[40:43],v[236:239],v[152:155],v[40:43]\n"
"ds_read_b64_tr_b16 v[232:233],v250 offset:16384\n"
"v_mfma_f32_16x16x32_bf16 v[40:43],v[208:211],v[160:163],v[40:43]\n"
"ds_read_b64_tr_b16 v[234:235],v250 offset:18432\n"
"v_mfma_f32_16x16x32_bf16 v[40:43],v[212:215],v[168:171],v[40:43]\n"
"ds_read_b64_tr_b16 v[236:237],v250 offset:20480\n"
"v_mfma_f32_16x16x32_bf16 v[44:47],v[212:215],v[172:175],v[44:47]\n"
"ds_read_b64_tr_b16 v[238:239],v250 offset:22528\n"
"s_waitcnt lgkmcnt(8)\n"
"v_mfma_f32_16x16x32_bf16 v[48:51],v[216:219],v[120:123],v[48:51]\n"
"ds_read_b64_tr_b16 v[208:209],v250 offset:24576\n"
"v_mfma_f32_16x16x32_bf16 v[48:51],v[220:223],v[128:131],v[48:51]\n"
"ds_read_b64_tr_b16 v[210:211],v250 offset:26624\n"
"v_mfma_f32_16x16x32_bf16 v[52:55],v[216:219],v[124:127],v[52:55]\n"
"ds_read_b64_tr_b16 v[212:213],v250 offset:28672\n"
"v_mfma_f32_16x16x32_bf16 v[52:55],v[220:223],v[132:135],v[52:55]\n"
"ds_read_b64_tr_b16 v[214:215],v250 offset:30720\n"
"s_waitcnt lgkmcnt(8)\n"
"v_mfma_f32_16x16x32_bf16 v[52:55],v[224:227],v[140:143],v[52:55]\n"
"ds_read_b64_tr_b16 v[216:217],v250 offset:32768\n"
"v_mfma_f32_16x16x32_bf16 v[52:55],v[228:231],v[196:199],v[52:55]\n"
"ds_read_b64_tr_b16 v[218:219],v250 offset:34816\n"
"s_setprio 2\n"
"v_mfma_f32_16x16x32_bf16 v[48:51],v[224:227],v[136:139],v[48:51]\n"
"ds_read_b64_tr_b16 v[220:221],v253\n"
"v_mfma_f32_16x16x32_bf16 v[48:51],v[228:231],v[192:195],v[48:51]\n"
"ds_read_b64_tr_b16 v[222:223],v253 offset:2048\n"
"s_waitcnt lgkmcnt(8)\n"
"v_mfma_f32_16x16x32_bf16 v[48:51],v[232:235],v[200:203],v[48:51]\n"
"ds_read_b64_tr_b16 v[224:225],v253 offset:4096\n"
"v_mfma_f32_16x16x32_bf16 v[48:51],v[236:239],v[144:147],v[48:51]\n"
"ds_read_b64_tr_b16 v[226:227],v253 offset:6144\n"
"v_mfma_f32_16x16x32_bf16 v[52:55],v[232:235],v[204:207],v[52:55]\n"
"ds_read_b64_tr_b16 v[228:229],v253 offset:8192\n"
"v_mfma_f32_16x16x32_bf16 v[52:55],v[236:239],v[148:151],v[52:55]\n"
"ds_read_b64_tr_b16 v[230:231],v253 offset:10240\n"
"s_waitcnt lgkmcnt(8)\n"
"v_mfma_f32_16x16x32_bf16 v[52:55],v[208:211],v[156:159],v[52:55]\n"
"ds_read_b64_tr_b16 v[232:233],v253 offset:12288\n"
"v_mfma_f32_16x16x32_bf16 v[52:55],v[212:215],v[164:167],v[52:55]\n"
"ds_read_b64_tr_b16 v[234:235],v253 offset:14336\n"
"v_mfma_f32_16x16x32_bf16 v[48:51],v[208:211],v[152:155],v[48:51]\n"
"ds_read_b64_tr_b16 v[236:237],v253 offset:16384\n"
"v_mfma_f32_16x16x32_bf16 v[48:51],v[212:215],v[160:163],v[48:51]\n"
"ds_read_b64_tr_b16 v[238:239],v253 offset:18432\n"
"s_waitcnt lgkmcnt(8)\n"
"v_mfma_f32_16x16x32_bf16 v[48:51],v[216:219],v[168:171],v[48:51]\n"
"ds_read_b64_tr_b16 v[208:209],v253 offset:20480\n"
"v_mfma_f32_16x16x32_bf16 v[52:55],v[216:219],v[172:175],v[52:55]\n"
"ds_read_b64_tr_b16 v[210:211],v253 offset:22528\n"
"v_mfma_f32_16x16x32_bf16 v[56:59],v[220:223],v[120:123],v[56:59]\n"
"ds_read_b64_tr_b16 v[212:213],v253 offset:24576\n"
"s_waitcnt lgkmcnt(7)\n"
"v_mfma_f32_16x16x32_bf16 v[56:59],v[224:227],v[128:131],v[56:59]\n"
"ds_read_b64_tr_b16 v[214:215],v253 offset:26624\n"
"v_mfma_f32_16x16x32_bf16 v[60:63],v[220:223],v[124:127],v[60:63]\n"
"ds_read_b64_tr_b16 v[216:217],v253 offset:28672\n"
"v_mfma_f32_16x16x32_bf16 v[60:63],v[224:227],v[132:135],v[60:63]\n"
"ds_read_b64_tr_b16 v[218:219],v253 offset:30720\n"
"v_mfma_f32_16x16x32_bf16 v[60:63],v[228:231],v[140:143],v[60:63]\n"
"ds_read_b64_tr_b16 v[220:221],v253 offset:32768\n"
"s_waitcnt lgkmcnt(7)\n"
"v_mfma_f32_16x16x32_bf16 v[60:63],v[232:235],v[196:199],v[60:63]\n"
"ds_read_b64_tr_b16 v[222:223],v253 offset:34816\n"
"s_waitcnt vmcnt(4)\n"
"v_mfma_f32_16x16x32_bf16 v[56:59],v[228:231],v[136:139],v[56:59]\n"
"v_mfma_f32_16x16x32_bf16 v[56:59],v[232:235],v[192:195],v[56:59]\n"
"s_barrier\n"
"v_mfma_f32_16x16x32_bf16 v[56:59],v[236:239],v[200:203],v[56:59]\n"
"s_waitcnt lgkmcnt(4)\n"
"v_mfma_f32_16x16x32_bf16 v[56:59],v[208:211],v[144:147],v[56:59]\n"
"v_mfma_f32_16x16x32_bf16 v[60:63],v[236:239],v[204:207],v[60:63]\n"
"v_mfma_f32_16x16x32_bf16 v[60:63],v[208:211],v[148:151],v[60:63]\n"
"v_mfma_f32_16x16x32_bf16 v[60:63],v[212:215],v[156:159],v[60:63]\n"
"s_waitcnt lgkmcnt(0)\n"
"v_mfma_f32_16x16x32_bf16 v[60:63],v[216:219],v[164:167],v[60:63]\n"
"v_mfma_f32_16x16x32_bf16 v[56:59],v[212:215],v[152:155],v[56:59]\n"
"v_mfma_f32_16x16x32_bf16 v[56:59],v[216:219],v[160:163],v[56:59]\n"
"v_mfma_f32_16x16x32_bf16 v[56:59],v[220:223],v[168:171],v[56:59]\n"
"v_mfma_f32_16x16x32_bf16 v[60:63],v[220:223],v[172:175],v[60:63]\n"
"s_setprio 0\n"
"v_add_u32 v248,73728,v248\n"
"v_add_u32 v249,73728,v249\n"
"v_add_u32 v250,73728,v250\n"
"v_add_u32 v253,73728,v253\n"
"v_cvt_pk_bf16_f32 v32,v32,v33\n"
"v_cvt_pk_bf16_f32 v33,v34,v35\n"
"v_cvt_pk_bf16_f32 v34,v40,v41\n"
"v_cvt_pk_bf16_f32 v35,v42,v43\n"
"s_setprio 3\n"
"ds_write_b128 v247,v[32:35]\n"
"s_setprio 0\n"
"v_cvt_pk_bf16_f32 v36,v36,v37\n"
"v_cvt_pk_bf16_f32 v37,v38,v39\n"
"v_cvt_pk_bf16_f32 v38,v44,v45\n"
"v_cvt_pk_bf16_f32 v39,v46,v47\n"
"s_setprio 3\n"
"ds_write_b128 v247,v[36:39] offset:1024\n"
"s_setprio 0\n"
"v_cvt_pk_bf16_f32 v48,v48,v49\n"
"v_cvt_pk_bf16_f32 v49,v50,v51\n"
"v_cvt_pk_bf16_f32 v50,v56,v57\n"
"v_cvt_pk_bf16_f32 v51,v58,v59\n"
"s_setprio 3\n"
"ds_write_b128 v247,v[48:51] offset:2048\n"
"s_setprio 0\n"
"v_cvt_pk_bf16_f32 v52,v52,v53\n"
"v_cvt_pk_bf16_f32 v53,v54,v55\n"
"v_cvt_pk_bf16_f32 v54,v60,v61\n"
"v_cvt_pk_bf16_f32 v55,v62,v63\n"
"s_setprio 3\n"
"ds_write_b128 v247,v[52:55] offset:3072\n"
"s_setprio 0\n"
"s_add_u32 m0,s73,110592\n"
"s_add_u32 s74,s83,110848\n"
"buffer_load_dwordx4 v251,s[20:23],s74 offen lds\n"
"s_addk_i32 m0,4096\n"
"s_add_u32 s74,s82,184576\n"
"buffer_load_dwordx4 v251,s[20:23],s74 offen lds\n"
"s_addk_i32 m0,4096\n"
"s_add_u32 s74,s83,184576\n"
"buffer_load_dwordx4 v251,s[20:23],s74 offen lds\n"
"s_addk_i32 m0,4096\n"
"s_add_u32 s74,s82,258304\n"
"buffer_load_dwordx4 v251,s[20:23],s74 offen lds\n"
"s_addk_i32 m0,4096\n"
"s_add_u32 s74,s83,258304\n"
"buffer_load_dwordx4 v251,s[20:23],s74 offen lds\n"
"s_addk_i32 m0,4096\n"
"s_add_u32 s74,s82,332032\n"
"buffer_load_dwordx4 v251,s[20:23],s74 offen lds\n"
"s_addk_i32 m0,4096\n"
"s_add_u32 s74,s83,332032\n"
"buffer_load_dwordx4 v251,s[20:23],s74 offen lds\n"
"s_addk_i32 m0,4096\n"
"s_add_u32 s74,s82,405760\n"
"buffer_load_dwordx4 v251,s[20:23],s74 offen lds\n"
"s_addk_i32 m0,4096\n"
"s_add_u32 s74,s83,405760\n"
"buffer_load_dwordx4 v251,s[20:23],s74 offen lds\n"
"buffer_load_dwordx2 v[96:97],v188,s[12:15],s84 offen\n"
"buffer_load_dwordx2 v[98:99],v188,s[12:15],s84 offen offset:1152\n"
"global_load_dwordx2 v[100:101],v189,s[4:5]\n"
"global_load_dwordx2 v[102:103],v189,s[6:7]\n"
"buffer_load_dwordx2 v[112:113],v188,s[12:15],s84 offen offset:256\n"
"buffer_load_dwordx2 v[114:115],v188,s[12:15],s84 offen offset:1408\n"
"global_load_dwordx2 v[116:117],v189,s[4:5] offset:256\n"
"global_load_dwordx2 v[118:119],v189,s[6:7] offset:256\n"
"s_waitcnt lgkmcnt(0)\n"
"s_setprio 3\n"
"s_barrier\n"
"ds_read_b64_tr_b16 v[208:209],v248\n"
"ds_read_b64_tr_b16 v[210:211],v248 offset:2048\n"
"ds_read_b64_tr_b16 v[212:213],v248 offset:4096\n"
"ds_read_b64_tr_b16 v[214:215],v248 offset:6144\n"
"ds_read_b64_tr_b16 v[216:217],v248 offset:8192\n"
"ds_read_b64_tr_b16 v[218:219],v248 offset:10240\n"
"ds_read_b64_tr_b16 v[220:221],v248 offset:12288\n"
"ds_read_b64_tr_b16 v[222:223],v248 offset:14336\n"
"ds_read_b64_tr_b16 v[224:225],v248 offset:16384\n"
"ds_read_b64_tr_b16 v[226:227],v248 offset:18432\n"
"ds_read_b64_tr_b16 v[228:229],v248 offset:20480\n"
"ds_read_b64_tr_b16 v[230:231],v248 offset:22528\n"
"s_setprio 1\n"
"s_waitcnt lgkmcnt(8)\n"
"v_mfma_f32_16x16x32_bf16 v[64:67],v[208:211],v[120:123],v[64:67]\n"
"ds_read_b64_tr_b16 v[232:233],v248 offset:24576\n"
"v_mfma_f32_16x16x32_bf16 v[64:67],v[212:215],v[128:131],v[64:67]\n"
"ds_read_b64_tr_b16 v[234:235],v248 offset:26624\n"
"v_mfma_f32_16x16x32_bf16 v[68:71],v[208:211],v[124:127],v[68:71]\n"
"ds_read_b64_tr_b16 v[236:237],v248 offset:28672\n"
"v_mfma_f32_16x16x32_bf16 v[68:71],v[212:215],v[132:135],v[68:71]\n"
"ds_read_b64_tr_b16 v[238:239],v248 offset:30720\n"
"s_waitcnt lgkmcnt(8)\n"
"v_mfma_f32_16x16x32_bf16 v[68:71],v[216:219],v[140:143],v[68:71]\n"
"ds_read_b64_tr_b16 v[208:209],v248 offset:32768\n"
"v_mfma_f32_16x16x32_bf16 v[68:71],v[220:223],v[196:199],v[68:71]\n"
"ds_read_b64_tr_b16 v[210:211],v248 offset:34816\n"
"v_mfma_f32_16x16x32_bf16 v[64:67],v[216:219],v[136:139],v[64:67]\n"
"ds_read_b64_tr_b16 v[212:213],v249\n"
"v_mfma_f32_16x16x32_bf16 v[64:67],v[220:223],v[192:195],v[64:67]\n"
"ds_read_b64_tr_b16 v[214:215],v249 offset:2048\n"
"s_waitcnt lgkmcnt(8)\n"
"v_mfma_f32_16x16x32_bf16 v[64:67],v[224:227],v[200:203],v[64:67]\n"
"ds_read_b64_tr_b16 v[216:217],v249 offset:4096\n"
"v_mfma_f32_16x16x32_bf16 v[64:67],v[228:231],v[144:147],v[64:67]\n"
"ds_read_b64_tr_b16 v[218:219],v249 offset:6144\n"
"v_mfma_f32_16x16x32_bf16 v[68:71],v[224:227],v[204:207],v[68:71]\n"
"ds_read_b64_tr_b16 v[220:221],v249 offset:8192\n"
"v_mfma_f32_16x16x32_bf16 v[68:71],v[228:231],v[148:151],v[68:71]\n"
"ds_read_b64_tr_b16 v[222:223],v249 offset:10240\n"
"s_waitcnt lgkmcnt(8)\n"
"v_mfma_f32_16x16x32_bf16 v[68:71],v[232:235],v[156:159],v[68:71]\n"
"ds_read_b64_tr_b16 v[224:225],v249 offset:12288\n"
"v_mfma_f32_16x16x32_bf16 v[68:71],v[236:239],v[164:167],v[68:71]\n"
"ds_read_b64_tr_b16 v[226:227],v249 offset:14336\n"
"v_mfma_f32_16x16x32_bf16 v[64:67],v[232:235],v[152:155],v[64:67]\n"
"ds_read_b64_tr_b16 v[228:229],v249 offset:16384\n"
"v_mfma_f32_16x16x32_bf16 v[64:67],v[236:239],v[160:163],v[64:67]\n"
"ds_read_b64_tr_b16 v[230:231],v249 offset:18432\n"
"s_waitcnt lgkmcnt(8)\n"
"v_mfma_f32_16x16x32_bf16 v[64:67],v[208:211],v[168:171],v[64:67]\n"
"ds_read_b64_tr_b16 v[232:233],v249 offset:20480\n"
"v_mfma_f32_16x16x32_bf16 v[68:71],v[208:211],v[172:175],v[68:71]\n"
"ds_read_b64_tr_b16 v[234:235],v249 offset:22528\n"
"v_mfma_f32_16x16x32_bf16 v[72:75],v[212:215],v[120:123],v[72:75]\n"
"ds_read_b64_tr_b16 v[236:237],v249 offset:24576\n"
"s_waitcnt lgkmcnt(7)\n"
"v_mfma_f32_16x16x32_bf16 v[72:75],v[216:219],v[128:131],v[72:75]\n"
"ds_read_b64_tr_b16 v[238:239],v249 offset:26624\n"
"v_mfma_f32_16x16x32_bf16 v[76:79],v[212:215],v[124:127],v[76:79]\n"
"ds_read_b64_tr_b16 v[208:209],v249 offset:28672\n"
"v_mfma_f32_16x16x32_bf16 v[76:79],v[216:219],v[132:135],v[76:79]\n"
"ds_read_b64_tr_b16 v[210:211],v249 offset:30720\n"
"v_mfma_f32_16x16x32_bf16 v[76:79],v[220:223],v[140:143],v[76:79]\n"
"ds_read_b64_tr_b16 v[212:213],v249 offset:32768\n"
"s_waitcnt lgkmcnt(7)\n"
"v_mfma_f32_16x16x32_bf16 v[76:79],v[224:227],v[196:199],v[76:79]\n"
"ds_read_b64_tr_b16 v[214:215],v249 offset:34816\n"
"v_mfma_f32_16x16x32_bf16 v[72:75],v[220:223],v[136:139],v[72:75]\n"
"ds_read_b64_tr_b16 v[216:217],v250\n"
"v_mfma_f32_16x16x32_bf16 v[72:75],v[224:227],v[192:195],v[72:75]\n"
"ds_read_b64_tr_b16 v[218:219],v250 offset:2048\n"
"v_mfma_f32_16x16x32_bf16 v[72:75],v[228:231],v[200:203],v[72:75]\n"
"ds_read_b64_tr_b16 v[220:221],v250 offset:4096\n"
"s_waitcnt lgkmcnt(7)\n"
"v_mfma_f32_16x16x32_bf16 v[72:75],v[232:235],v[144:147],v[72:75]\n"
"ds_read_b64_tr_b16 v[222:223],v250 offset:6144\n"
"v_mfma_f32_16x16x32_bf16 v[76:79],v[228:231],v[204:207],v[76:79]\n"
"ds_read_b64_tr_b16 v[224:225],v250 offset:8192\n"
"v_mfma_f32_16x16x32_bf16 v[76:79],v[232:235],v[148:151],v[76:79]\n"
"ds_read_b64_tr_b16 v[226:227],v250 offset:10240\n"
"v_mfma_f32_16x16x32_bf16 v[76:79],v[236:239],v[156:159],v[76:79]\n"
"ds_read_b64_tr_b16 v[228:229],v250 offset:12288\n"
"s_waitcnt lgkmcnt(7)\n"
"v_mfma_f32_16x16x32_bf16 v[76:79],v[208:211],v[164:167],v[76:79]\n"
"ds_read_b64_tr_b16 v[230:231],v250 offset:14336\n"
"v_mfma_f32_16x16x32_bf16 v[72:75],v[236:239],v[152:155],v[72:75]\n"
"ds_read_b64_tr_b16 v[232:233],v250 offset:16384\n"
"v_mfma_f32_16x16x32_bf16 v[72:75],v[208:211],v[160:163],v[72:75]\n"
"ds_read_b64_tr_b16 v[234:235],v250 offset:18432\n"
"v_mfma_f32_16x16x32_bf16 v[72:75],v[212:215],v[168:171],v[72:75]\n"
"ds_read_b64_tr_b16 v[236:237],v250 offset:20480\n"
"v_mfma_f32_16x16x32_bf16 v[76:79],v[212:215],v[172:175],v[76:79]\n"
"ds_read_b64_tr_b16 v[238:239],v250 offset:22528\n"
"s_waitcnt lgkmcnt(8)\n"
"v_mfma_f32_16x16x32_bf16 v[80:83],v[216:219],v[120:123],v[80:83]\n"
"ds_read_b64_tr_b16 v[208:209],v250 offset:24576\n"
"v_mfma_f32_16x16x32_bf16 v[80:83],v[220:223],v[128:131],v[80:83]\n"
"ds_read_b64_tr_b16 v[210:211],v250 offset:26624\n"
"v_mfma_f32_16x16x32_bf16 v[84:87],v[216:219],v[124:127],v[84:87]\n"
"ds_read_b64_tr_b16 v[212:213],v250 offset:28672\n"
"v_mfma_f32_16x16x32_bf16 v[84:87],v[220:223],v[132:135],v[84:87]\n"
"ds_read_b64_tr_b16 v[214:215],v250 offset:30720\n"
"s_waitcnt lgkmcnt(8)\n"
"v_mfma_f32_16x16x32_bf16 v[84:87],v[224:227],v[140:143],v[84:87]\n"
"ds_read_b64_tr_b16 v[216:217],v250 offset:32768\n"
"v_mfma_f32_16x16x32_bf16 v[84:87],v[228:231],v[196:199],v[84:87]\n"
"ds_read_b64_tr_b16 v[218:219],v250 offset:34816\n"
"s_setprio 2\n"
"v_mfma_f32_16x16x32_bf16 v[80:83],v[224:227],v[136:139],v[80:83]\n"
"ds_read_b64_tr_b16 v[220:221],v253\n"
"v_mfma_f32_16x16x32_bf16 v[80:83],v[228:231],v[192:195],v[80:83]\n"
"ds_read_b64_tr_b16 v[222:223],v253 offset:2048\n"
"s_waitcnt lgkmcnt(8)\n"
"v_mfma_f32_16x16x32_bf16 v[80:83],v[232:235],v[200:203],v[80:83]\n"
"ds_read_b64_tr_b16 v[224:225],v253 offset:4096\n"
"v_mfma_f32_16x16x32_bf16 v[80:83],v[236:239],v[144:147],v[80:83]\n"
"ds_read_b64_tr_b16 v[226:227],v253 offset:6144\n"
"v_mfma_f32_16x16x32_bf16 v[84:87],v[232:235],v[204:207],v[84:87]\n"
"ds_read_b64_tr_b16 v[228:229],v253 offset:8192\n"
"v_mfma_f32_16x16x32_bf16 v[84:87],v[236:239],v[148:151],v[84:87]\n"
"ds_read_b64_tr_b16 v[230:231],v253 offset:10240\n"
"s_waitcnt lgkmcnt(8)\n"
"v_mfma_f32_16x16x32_bf16 v[84:87],v[208:211],v[156:159],v[84:87]\n"
"ds_read_b64_tr_b16 v[232:233],v253 offset:12288\n"
"v_mfma_f32_16x16x32_bf16 v[84:87],v[212:215],v[164:167],v[84:87]\n"
"ds_read_b64_tr_b16 v[234:235],v253 offset:14336\n"
"v_mfma_f32_16x16x32_bf16 v[80:83],v[208:211],v[152:155],v[80:83]\n"
"ds_read_b64_tr_b16 v[236:237],v253 offset:16384\n"
"v_mfma_f32_16x16x32_bf16 v[80:83],v[212:215],v[160:163],v[80:83]\n"
"ds_read_b64_tr_b16 v[238:239],v253 offset:18432\n"
"s_waitcnt lgkmcnt(8)\n"
"v_mfma_f32_16x16x32_bf16 v[80:83],v[216:219],v[168:171],v[80:83]\n"
"ds_read_b64_tr_b16 v[208:209],v253 offset:20480\n"
"v_mfma_f32_16x16x32_bf16 v[84:87],v[216:219],v[172:175],v[84:87]\n"
"ds_read_b64_tr_b16 v[210:211],v253 offset:22528\n"
"v_mfma_f32_16x16x32_bf16 v[88:91],v[220:223],v[120:123],v[88:91]\n"
"ds_read_b64_tr_b16 v[212:213],v253 offset:24576\n"
"s_waitcnt lgkmcnt(7)\n"
"v_mfma_f32_16x16x32_bf16 v[88:91],v[224:227],v[128:131],v[88:91]\n"
"ds_read_b64_tr_b16 v[214:215],v253 offset:26624\n"
"v_mfma_f32_16x16x32_bf16 v[92:95],v[220:223],v[124:127],v[92:95]\n"
"ds_read_b64_tr_b16 v[216:217],v253 offset:28672\n"
"v_mfma_f32_16x16x32_bf16 v[92:95],v[224:227],v[132:135],v[92:95]\n"
"ds_read_b64_tr_b16 v[218:219],v253 offset:30720\n"
"v_mfma_f32_16x16x32_bf16 v[92:95],v[228:231],v[140:143],v[92:95]\n"
"ds_read_b64_tr_b16 v[220:221],v253 offset:32768\n"
"s_waitcnt lgkmcnt(7)\n"
"v_mfma_f32_16x16x32_bf16 v[92:95],v[232:235],v[196:199],v[92:95]\n"
"ds_read_b64_tr_b16 v[222:223],v253 offset:34816\n"
"s_waitcnt vmcnt(8)\n"
"v_mfma_f32_16x16x32_bf16 v[88:91],v[228:231],v[136:139],v[88:91]\n"
"v_mfma_f32_16x16x32_bf16 v[88:91],v[232:235],v[192:195],v[88:91]\n"
"s_barrier\n"
"v_mfma_f32_16x16x32_bf16 v[88:91],v[236:239],v[200:203],v[88:91]\n"
"s_waitcnt lgkmcnt(4)\n"
"v_mfma_f32_16x16x32_bf16 v[88:91],v[208:211],v[144:147],v[88:91]\n"
"v_mfma_f32_16x16x32_bf16 v[92:95],v[236:239],v[204:207],v[92:95]\n"
"v_mov_b32 v255,4\n"
"v_mfma_f32_16x16x32_bf16 v[92:95],v[208:211],v[148:151],v[92:95]\n"
"v_cndmask_b32 v255,v255,0,s[96:97]\n"
"v_mfma_f32_16x16x32_bf16 v[92:95],v[212:215],v[156:159],v[92:95]\n"
"buffer_load_dword v254,v190,s[32:35],s95 offen sc1\n"
"s_waitcnt lgkmcnt(0)\n"
"v_mfma_f32_16x16x32_bf16 v[92:95],v[216:219],v[164:167],v[92:95]\n"
"v_mfma_f32_16x16x32_bf16 v[88:91],v[212:215],v[152:155],v[88:91]\n"
"v_mfma_f32_16x16x32_bf16 v[88:91],v[216:219],v[160:163],v[88:91]\n"
"v_mfma_f32_16x16x32_bf16 v[88:91],v[220:223],v[168:171],v[88:91]\n"
"v_mfma_f32_16x16x32_bf16 v[92:95],v[220:223],v[172:175],v[92:95]\n"
"s_setprio 0\n"
"v_add_u32 v248,4294893568,v248\n"
"v_add_u32 v249,4294893568,v249\n"
"v_add_u32 v250,4294893568,v250\n"
"v_add_u32 v253,4294893568,v253\n"
"v_cvt_pk_bf16_f32 v64,v64,v65\n"
"v_cvt_pk_bf16_f32 v65,v66,v67\n"
"v_cvt_pk_bf16_f32 v66,v72,v73\n"
"v_cvt_pk_bf16_f32 v67,v74,v75\n"
"s_setprio 3\n"
"ds_write_b128 v247,v[64:67]\n"
"s_setprio 0\n"
"v_cvt_pk_bf16_f32 v68,v68,v69\n"
"v_cvt_pk_bf16_f32 v69,v70,v71\n"
"v_cvt_pk_bf16_f32 v70,v76,v77\n"
"v_cvt_pk_bf16_f32 v71,v78,v79\n"
"s_setprio 3\n"
"ds_write_b128 v247,v[68:71] offset:1024\n"
"s_setprio 0\n"
"v_cvt_pk_bf16_f32 v80,v80,v81\n"
"v_cvt_pk_bf16_f32 v81,v82,v83\n"
"v_cvt_pk_bf16_f32 v82,v88,v89\n"
"v_cvt_pk_bf16_f32 v83,v90,v91\n"
"s_setprio 3\n"
"ds_write_b128 v247,v[80:83] offset:2048\n"
"s_setprio 0\n"
"v_cvt_pk_bf16_f32 v84,v84,v85\n"
"v_cvt_pk_bf16_f32 v85,v86,v87\n"
"v_cvt_pk_bf16_f32 v86,v92,v93\n"
"v_cvt_pk_bf16_f32 v87,v94,v95\n"
"s_setprio 3\n"
"ds_write_b128 v247,v[84:87] offset:3072\n"
"s_setprio 0\n"
"s_waitcnt lgkmcnt(0)\n"
"s_barrier\n"
"s_add_u32 s81,s69,1\n"
"s_and_b32 s81,s81,3\n"
"s_mul_i32 s81,s81,2359296\n"
"s_add_u32 s81,s81,s80\n"
"s_add_u32 s82,s69,2\n"
"s_and_b32 s82,s82,3\n"
"s_mul_i32 s82,s82,2359296\n"
"s_add_u32 s82,s82,s80\n"
"s_add_u32 s83,s69,3\n"
"s_and_b32 s83,s83,3\n"
"s_mul_i32 s83,s83,2359296\n"
"s_add_u32 s83,s83,s80\n"
"s_waitcnt vmcnt(0)\n"
"v_cmp_gt_u32 vcc,v255,v254\n"
"s_nop 1\n"
"s_cbranch_vccz .Lk_rv22_ok\n"
"s_mov_b32 s89,0x100000\n"
".Lk_rv22_spin:\n"
"s_sleep 2\n"
"buffer_load_dword v254,v190,s[32:35],s95 offen sc1\n"
"s_waitcnt vmcnt(0)\n"
"v_cmp_gt_u32 vcc,v255,v254\n"
"s_nop 1\n"
"s_cbranch_vccz .Lk_rv22_ok\n"
"s_sub_u32 s89,s89,1\n"
"s_cmp_lg_u32 s89,0\n"
"s_cbranch_scc1 .Lk_rv22_spin\n"
".Lk_rv22_ok:\n"
"v_mov_b32 v255,0\n"
"v_cndmask_b32 v255,v255,3,s[96:97]\n"
"buffer_load_dword v254,v190,s[32:35],s95 offen sc1\n"
"s_add_u32 s77,s81,0\n"
"buffer_load_dwordx4 v[32:35],v252,s[32:35],s77 offen sc1\n"
"s_add_u32 s77,s82,0\n"
"buffer_load_dwordx4 v[36:39],v252,s[32:35],s77 offen sc1\n"
"s_add_u32 s77,s83,0\n"
"buffer_load_dwordx4 v[40:43],v252,s[32:35],s77 offen sc1\n"
"s_setprio 0\n"
"v_mul_f32_e64 v184,-v184,v185\n"
"v_mul_f32_e64 v186,-v186,v187\n"
"v_lshlrev_b32 v188,1,v188\n"
"s_lshl_b32 s84,s84,1\n"
"v_lshlrev_b32 v16,16,v96\n"
"v_and_b32 v17,0xffff0000,v96\n"
"v_lshlrev_b32 v18,16,v97\n"
"v_and_b32 v19,0xffff0000,v97\n"
"v_lshlrev_b32 v20,16,v98\n"
"v_and_b32 v21,0xffff0000,v98\n"
"v_lshlrev_b32 v22,16,v99\n"
"v_and_b32 v23,0xffff0000,v99\n"
"v_lshlrev_b32 v24,16,v100\n"
"v_and_b32 v25,0xffff0000,v100\n"
"v_lshlrev_b32 v26,16,v101\n"
"v_and_b32 v27,0xffff0000,v101\n"
"v_lshlrev_b32 v28,16,v102\n"
"v_and_b32 v29,0xffff0000,v102\n"
"v_lshlrev_b32 v30,16,v103\n"
"v_and_b32 v31,0xffff0000,v103\n"
"v_add_f32 v96,v16,v28\n"
"v_add_f32 v97,v17,v29\n"
"v_add_f32 v98,v18,v30\n"
"v_add_f32 v99,v19,v31\n"
"v_fma_f32 v16,v16,v185,v184\n"
"v_fma_f32 v17,v17,v185,v184\n"
"v_fma_f32 v18,v18,v185,v184\n"
"v_fma_f32 v19,v19,v185,v184\n"
"v_fmac_f32 v96,v16,v24\n"
"v_fmac_f32 v97,v17,v25\n"
"v_fmac_f32 v98,v18,v26\n"
"v_fmac_f32 v99,v19,v27\n"
"v_add_f32 v100,v20,v28\n"
"v_add_f32 v101,v21,v29\n"
"v_add_f32 v102,v22,v30\n"
"v_add_f32 v103,v23,v31\n"
"v_fma_f32 v20,v20,v187,v186\n"
"v_fma_f32 v21,v21,v187,v186\n"
"v_fma_f32 v22,v22,v187,v186\n"
"v_fma_f32 v23,v23,v187,v186\n"
"v_fmac_f32 v100,v20,v24\n"
"v_fmac_f32 v101,v21,v25\n"
"v_fmac_f32 v102,v22,v26\n"
"v_fmac_f32 v103,v23,v27\n"
"v_lshlrev_b32 v16,16,v112\n"
"v_and_b32 v17,0xffff0000,v112\n"
"v_lshlrev_b32 v18,16,v113\n"
"v_and_b32 v19,0xffff0000,v113\n"
"v_lshlrev_b32 v20,16,v114\n"
"v_and_b32 v21,0xffff0000,v114\n"
"v_lshlrev_b32 v22,16,v115\n"
"v_and_b32 v23,0xffff0000,v115\n"
"v_lshlrev_b32 v24,16,v116\n"
"v_and_b32 v25,0xffff0000,v116\n"
"v_lshlrev_b32 v26,16,v117\n"
"v_and_b32 v27,0xffff0000,v117\n"
"v_lshlrev_b32 v28,16,v118\n"
"v_and_b32 v29,0xffff0000,v118\n"
"v_lshlrev_b32 v30,16,v119\n"
"v_and_b32 v31,0xffff0000,v119\n"
"v_add_f32 v112,v16,v28\n"
"v_add_f32 v113,v17,v29\n"
"v_add_f32 v114,v18,v30\n"
"v_add_f32 v115,v19,v31\n"
"v_fma_f32 v16,v16,v185,v184\n"
"v_fma_f32 v17,v17,v185,v184\n"
"v_fma_f32 v18,v18,v185,v184\n"
"v_fma_f32 v19,v19,v185,v184\n"
"v_fmac_f32 v112,v16,v24\n"
"v_fmac_f32 v113,v17,v25\n"
"v_fmac_f32 v114,v18,v26\n"
"v_fmac_f32 v115,v19,v27\n"
"v_add_f32 v116,v20,v28\n"
"v_add_f32 v117,v21,v29\n"
"v_add_f32 v118,v22,v30\n"
"v_add_f32 v119,v23,v31\n"
"v_fma_f32 v20,v20,v187,v186\n"
"v_fma_f32 v21,v21,v187,v186\n"
"v_fma_f32 v22,v22,v187,v186\n"
"v_fma_f32 v23,v23,v187,v186\n"
"v_fmac_f32 v116,v20,v24\n"
"v_fmac_f32 v117,v21,v25\n"
"v_fmac_f32 v118,v22,v26\n"
"v_fmac_f32 v119,v23,v27\n"
"s_barrier\n"
"s_setprio 1\n"
"s_waitcnt vmcnt(3)\n"
"v_cmp_gt_u32 vcc,v255,v254\n"
"s_nop 1\n"
"s_cbranch_vccz .Lk_r2_ok\n"
"s_mov_b32 s89,0x100000\n"
".Lk_r2_spin:\n"
"s_sleep 2\n"
"buffer_load_dword v254,v190,s[32:35],s95 offen sc1\n"
"s_waitcnt vmcnt(0)\n"
"v_cmp_gt_u32 vcc,v255,v254\n"
"s_nop 1\n"
"s_cbranch_vccz .Lk_r2_ok\n"
"s_sub_u32 s89,s89,1\n"
"s_cmp_lg_u32 s89,0\n"
".Lk_padm13:\n"
"s_cbranch_execz .Lk_hop_retm\n"
"s_cbranch_scc1 .Lk_r2_spin\n"
".Lk_r2_ok:\n"
"s_add_u32 s77,s80,9441280\n"
"buffer_load_dwordx4 v[48:51],v252,s[32:35],s77 offen sc1\n"
"s_add_u32 s77,s80,11800576\n"
"buffer_load_dwordx4 v[52:55],v252,s[32:35],s77 offen sc1\n"
"s_add_u32 s77,s80,14159872\n"
"buffer_load_dwordx4 v[56:59],v252,s[32:35],s77 offen sc1\n"
"s_add_u32 s77,s80,16519168\n"
"buffer_load_dwordx4 v[60:63],v252,s[32:35],s77 offen sc1\n"
"s_add_u32 s77,s81,4096\n"
"buffer_load_dwordx4 v[120:123],v252,s[32:35],s77 offen sc1\n"
"s_add_u32 s77,s82,4096\n"
"buffer_load_dwordx4 v[124:127],v252,s[32:35],s77 offen sc1\n"
"s_add_u32 s77,s83,4096\n"
"buffer_load_dwordx4 v[128:131],v252,s[32:35],s77 offen sc1\n"
"v_mfma_f32_16x16x32_bf16 v[208:211],v[178:181],v[0:3],0\n"
"v_mfma_f32_16x16x32_bf16 v[212:215],v[176:179],v[0:3],0\n"
"v_mfma_f32_16x16x32_bf16 v[208:211],v[178:181],v[4:7],v[208:211]\n"
"v_mfma_f32_16x16x32_bf16 v[212:215],v[176:179],v[4:7],v[212:215]\n"
"v_mfma_f32_16x16x32_bf16 v[208:211],v[178:181],v[8:11],v[208:211]\n"
"v_mfma_f32_16x16x32_bf16 v[212:215],v[176:179],v[8:11],v[212:215]\n"
"v_mfma_f32_16x16x32_bf16 v[208:211],v[178:181],v[12:15],v[208:211]\n"
"v_mfma_f32_16x16x32_bf16 v[212:215],v[176:179],v[12:15],v[212:215]\n"
"ds_read_b128 v[44:47],v191\n"
"s_waitcnt lgkmcnt(0)\n"
"v_mfma_f32_16x16x32_bf16 v[208:211],v[178:181],v[44:47],v[208:211]\n"
"v_mfma_f32_16x16x32_bf16 v[212:215],v[176:179],v[44:47],v[212:215]\n"
"s_waitcnt vmcnt(7)\n"
"v_mfma_f32_16x16x32_bf16 v[208:211],v[178:181],v[32:35],v[208:211]\n"
"v_mfma_f32_16x16x32_bf16 v[212:215],v[176:179],v[32:35],v[212:215]\n"
"v_mfma_f32_16x16x32_bf16 v[208:211],v[178:181],v[36:39],v[208:211]\n"
"v_mfma_f32_16x16x32_bf16 v[212:215],v[176:179],v[36:39],v[212:215]\n"
"v_mfma_f32_16x16x32_bf16 v[208:211],v[178:181],v[40:43],v[208:211]\n"
"v_mfma_f32_16x16x32_bf16 v[212:215],v[176:179],v[40:43],v[212:215]\n"
"s_nop 7\n"
"v_permlane32_swap_b32 v208,v212\n"
"v_permlane32_swap_b32 v209,v213\n"
"v_permlane32_swap_b32 v210,v214\n"
"v_permlane32_swap_b32 v211,v215\n"
"s_mov_b64 vcc,s[90:91]\n"
"v_cndmask_b32_dpp v232,v208,v212,vcc quad_perm:[2,3,0,1] row_mask:0xf bank_mask:0xf\n"
"v_cndmask_b32_dpp v233,v209,v213,vcc quad_perm:[2,3,0,1] row_mask:0xf bank_mask:0xf\n"
"v_cndmask_b32_dpp v234,v210,v214,vcc quad_perm:[2,3,0,1] row_mask:0xf bank_mask:0xf\n"
"v_cndmask_b32_dpp v235,v211,v215,vcc quad_perm:[2,3,0,1] row_mask:0xf bank_mask:0xf\n"
"s_not_b64 vcc,vcc\n"
"v_cndmask_b32_dpp v208,v212,v208,vcc quad_perm:[2,3,0,1] row_mask:0xf bank_mask:0xf\n"
"v_cndmask_b32_dpp v209,v213,v209,vcc quad_perm:[2,3,0,1] row_mask:0xf bank_mask:0xf\n"
"v_cndmask_b32_dpp v210,v214,v210,vcc quad_perm:[2,3,0,1] row_mask:0xf bank_mask:0xf\n"
"v_cndmask_b32_dpp v211,v215,v211,vcc quad_perm:[2,3,0,1] row_mask:0xf bank_mask:0xf\n"
"v_permlane16_swap_b32 v208,v232\n"
"v_permlane16_swap_b32 v209,v233\n"
"v_permlane16_swap_b32 v210,v234\n"
"v_permlane16_swap_b32 v211,v235\n"
"s_mov_b64 vcc,s[92:93]\n"
"v_cndmask_b32_dpp v212,v208,v232,vcc quad_perm:[1,0,3,2] row_mask:0xf bank_mask:0xf\n"
"v_cndmask_b32_dpp v213,v209,v233,vcc quad_perm:[1,0,3,2] row_mask:0xf bank_mask:0xf\n"
"v_cndmask_b32_dpp v214,v210,v234,vcc quad_perm:[1,0,3,2] row_mask:0xf bank_mask:0xf\n"
"v_cndmask_b32_dpp v215,v211,v235,vcc quad_perm:[1,0,3,2] row_mask:0xf bank_mask:0xf\n"
"s_not_b64 vcc,vcc\n"
"v_cndmask_b32_dpp v208,v232,v208,vcc quad_perm:[1,0,3,2] row_mask:0xf bank_mask:0xf\n"
"v_cndmask_b32_dpp v209,v233,v209,vcc quad_perm:[1,0,3,2] row_mask:0xf bank_mask:0xf\n"
"v_cndmask_b32_dpp v210,v234,v210,vcc quad_perm:[1,0,3,2] row_mask:0xf bank_mask:0xf\n"
"v_cndmask_b32_dpp v211,v235,v211,vcc quad_perm:[1,0,3,2] row_mask:0xf bank_mask:0xf\n"
"v_fmac_f32 v96,s79,v208\n"
"v_fmac_f32 v97,s79,v209\n"
"v_fmac_f32 v98,s79,v210\n"
"v_fmac_f32 v99,s79,v211\n"
"v_fmac_f32 v100,s79,v212\n"
"v_fmac_f32 v101,s79,v213\n"
"v_fmac_f32 v102,s79,v214\n"
"v_fmac_f32 v103,s79,v215\n"
"s_waitcnt vmcnt(3)\n"
"v_mfma_f32_16x16x32_bf16 v[216:219],v[178:181],v[48:51],0\n"
"v_mfma_f32_16x16x32_bf16 v[220:223],v[176:179],v[48:51],0\n"
"v_mfma_f32_16x16x32_bf16 v[216:219],v[178:181],v[52:55],v[216:219]\n"
"v_mfma_f32_16x16x32_bf16 v[220:223],v[176:179],v[52:55],v[220:223]\n"
"v_mfma_f32_16x16x32_bf16 v[216:219],v[178:181],v[56:59],v[216:219]\n"
"v_mfma_f32_16x16x32_bf16 v[220:223],v[176:179],v[56:59],v[220:223]\n"
"v_mfma_f32_16x16x32_bf16 v[216:219],v[178:181],v[60:63],v[216:219]\n"
"v_mfma_f32_16x16x32_bf16 v[220:223],v[176:179],v[60:63],v[220:223]\n"
"ds_read_b128 v[132:135],v191 offset:8192\n"
"s_waitcnt lgkmcnt(0)\n"
"v_mfma_f32_16x16x32_bf16 v[216:219],v[178:181],v[132:135],v[216:219]\n"
"v_mfma_f32_16x16x32_bf16 v[220:223],v[176:179],v[132:135],v[220:223]\n"
"s_waitcnt vmcnt(0)\n"
"v_max_u32 v236,v120,v121\n"
"v_max3_u32 v236,v236,v122,v123\n"
"v_max3_u32 v236,v236,v124,v125\n"
"v_max3_u32 v236,v236,v126,v127\n"
"v_max3_u32 v236,v236,v128,v129\n"
"v_max3_u32 v236,v236,v130,v131\n"
"v_cmp_eq_u32 vcc,-1,v236\n"
"s_mov_b32 s89,0x10000f\n"
"s_nop 0\n"
"s_cbranch_vccz .Lk_fm2_ok\n"
"v_mov_b32 v255,5\n"
"v_cndmask_b32 v255,v255,0,s[96:97]\n"
".Lk_fm2_spin:\n"
"s_sleep 2\n"
"s_and_b32 s75,s89,15\n"
"s_cbranch_scc1 .Lk_fm2_nw\n"
"buffer_load_dword v254,v190,s[32:35],s95 offen sc1\n"
"s_waitcnt vmcnt(0)\n"
".Lk_fm2_nw:\n"
"s_add_u32 s77,s81,4096\n"
"buffer_load_dwordx4 v[120:123],v252,s[32:35],s77 offen sc1\n"
"s_add_u32 s77,s82,4096\n"
"buffer_load_dwordx4 v[124:127],v252,s[32:35],s77 offen sc1\n"
"s_add_u32 s77,s83,4096\n"
"buffer_load_dwordx4 v[128:131],v252,s[32:35],s77 offen sc1\n"
"s_waitcnt vmcnt(0)\n"
"v_max_u32 v236,v120,v121\n"
"v_max3_u32 v236,v236,v122,v123\n"
"v_max3_u32 v236,v236,v124,v125\n"
"v_max3_u32 v236,v236,v126,v127\n"
"v_max3_u32 v236,v236,v128,v129\n"
"v_max3_u32 v236,v236,v130,v131\n"
"v_cmp_eq_u32 vcc,-1,v236\n"
"s_sub_u32 s89,s89,1\n"
"s_cbranch_scc1 .Lk_fm2_ok\n"
"s_cbranch_vccz .Lk_fm2_ok\n"
"s_cmp_lg_u32 s75,0\n"
"s_cbranch_scc1 .Lk_fm2_spin\n"
"v_cmp_gt_u32 vcc,v255,v254\n"
"s_nop 1\n"
"s_cbranch_vccnz .Lk_fm2_spin\n"
".Lk_fm2_ok:\n"
"buffer_store_dwordx4 v[96:99],v188,s[28:31],s84 offen sc1\n"
"buffer_store_dwordx4 v[100:103],v188,s[28:31],s84 offen offset:2304 sc1\n"
"v_mfma_f32_16x16x32_bf16 v[216:219],v[178:181],v[120:123],v[216:219]\n"
"v_mfma_f32_16x16x32_bf16 v[220:223],v[176:179],v[120:123],v[220:223]\n"
"v_mfma_f32_16x16x32_bf16 v[216:219],v[178:181],v[124:127],v[216:219]\n"
"v_mfma_f32_16x16x32_bf16 v[220:223],v[176:179],v[124:127],v[220:223]\n"
"v_mfma_f32_16x16x32_bf16 v[216:219],v[178:181],v[128:131],v[216:219]\n"
"v_mfma_f32_16x16x32_bf16 v[220:223],v[176:179],v[128:131],v[220:223]\n"
"s_nop 7\n"
"v_permlane32_swap_b32 v216,v220\n"
"v_permlane32_swap_b32 v217,v221\n"
"v_permlane32_swap_b32 v218,v222\n"
"v_permlane32_swap_b32 v219,v223\n"
"s_mov_b64 vcc,s[90:91]\n"
"v_cndmask_b32_dpp v232,v216,v220,vcc quad_perm:[2,3,0,1] row_mask:0xf bank_mask:0xf\n"
"v_cndmask_b32_dpp v233,v217,v221,vcc quad_perm:[2,3,0,1] row_mask:0xf bank_mask:0xf\n"
"v_cndmask_b32_dpp v234,v218,v222,vcc quad_perm:[2,3,0,1] row_mask:0xf bank_mask:0xf\n"
"v_cndmask_b32_dpp v235,v219,v223,vcc quad_perm:[2,3,0,1] row_mask:0xf bank_mask:0xf\n"
"s_not_b64 vcc,vcc\n"
"v_cndmask_b32_dpp v216,v220,v216,vcc quad_perm:[2,3,0,1] row_mask:0xf bank_mask:0xf\n"
"v_cndmask_b32_dpp v217,v221,v217,vcc quad_perm:[2,3,0,1] row_mask:0xf bank_mask:0xf\n"
"v_cndmask_b32_dpp v218,v222,v218,vcc quad_perm:[2,3,0,1] row_mask:0xf bank_mask:0xf\n"
"v_cndmask_b32_dpp v219,v223,v219,vcc quad_perm:[2,3,0,1] row_mask:0xf bank_mask:0xf\n"
"v_permlane16_swap_b32 v216,v232\n"
"v_permlane16_swap_b32 v217,v233\n"
"v_permlane16_swap_b32 v218,v234\n"
"v_permlane16_swap_b32 v219,v235\n"
"s_mov_b64 vcc,s[92:93]\n"
"v_cndmask_b32_dpp v220,v216,v232,vcc quad_perm:[1,0,3,2] row_mask:0xf bank_mask:0xf\n"
"v_cndmask_b32_dpp v221,v217,v233,vcc quad_perm:[1,0,3,2] row_mask:0xf bank_mask:0xf\n"
"v_cndmask_b32_dpp v222,v218,v234,vcc quad_perm:[1,0,3,2] row_mask:0xf bank_mask:0xf\n"
"v_cndmask_b32_dpp v223,v219,v235,vcc quad_perm:[1,0,3,2] row_mask:0xf bank_mask:0xf\n"
"s_not_b64 vcc,vcc\n"
"v_cndmask_b32_dpp v216,v232,v216,vcc quad_perm:[1,0,3,2] row_mask:0xf bank_mask:0xf\n"
"v_cndmask_b32_dpp v217,v233,v217,vcc quad_perm:[1,0,3,2] row_mask:0xf bank_mask:0xf\n"
"v_cndmask_b32_dpp v218,v234,v218,vcc quad_perm:[1,0,3,2] row_mask:0xf bank_mask:0xf\n"
"v_cndmask_b32_dpp v219,v235,v219,vcc quad_perm:[1,0,3,2] row_mask:0xf bank_mask:0xf\n"
"v_fmac_f32 v112,s79,v216\n"
"v_fmac_f32 v113,s79,v217\n"
"v_fmac_f32 v114,s79,v218\n"
"v_fmac_f32 v115,s79,v219\n"
"v_fmac_f32 v116,s79,v220\n"
"v_fmac_f32 v117,s79,v221\n"
"v_fmac_f32 v118,s79,v222\n"
"v_fmac_f32 v119,s79,v223\n"
"buffer_store_dwordx4 v[112:115],v188,s[28:31],s84 offen offset:512 sc1\n"
"buffer_store_dwordx4 v[116:119],v188,s[28:31],s84 offen offset:2816 sc1\n"
"s_endpgm\n"
".Lk_mlp_g1:\n"
".Lk_padm0:\n"
"s_cbranch_execz .Lk_padm4\n"
"s_add_u32 m0,s91,1024\n"
"s_add_u32 s74,s80,0\n"
"buffer_load_dwordx4 v195,s[16:19],s74 offen lds\n"
"s_addk_i32 m0,8192\n"
"s_add_u32 s74,s74,s93\n"
"buffer_load_dwordx4 v195,s[16:19],s74 offen lds\n"
"s_addk_i32 m0,8192\n"
"s_add_u32 s74,s74,s93\n"
"buffer_load_dwordx4 v195,s[16:19],s74 offen lds\n"
"s_addk_i32 m0,8192\n"
"s_add_u32 s74,s74,s93\n"
"buffer_load_dwordx4 v195,s[16:19],s74 offen lds\n"
"s_waitcnt lgkmcnt(0)\n"
"s_barrier\n"
"buffer_load_ushort v192,v193,s[24:27],s80 offen\n"
"v_mov_b32 v210,0\n"
"v_mov_b32 v211,0\n"
"v_mov_b32 v212,0\n"
"v_mov_b32 v213,0\n"
"s_waitcnt vmcnt(13)\n"
"ds_read_b128 v[0:3],v174\n"
"ds_read_b128 v[4:7],v175\n"
"s_addk_i32 m0,8192\n"
"s_add_u32 s74,s74,s93\n"
"buffer_load_dwordx4 v195,s[16:19],s74 offen lds\n"
"v_dot2c_f32_bf16 v210,v72,v208\n"
"v_dot2c_f32_bf16 v212,v72,v72\n"
"v_dot2c_f32_bf16 v211,v73,v208\n"
"v_dot2c_f32_bf16 v213,v73,v73\n"
"v_dot2c_f32_bf16 v210,v74,v208\n"
"v_dot2c_f32_bf16 v212,v74,v74\n"
"v_dot2c_f32_bf16 v211,v75,v208\n"
"v_dot2c_f32_bf16 v213,v75,v75\n"
"s_waitcnt vmcnt(13)\n"
"ds_read_b128 v[8:11],v174 offset:2048\n"
"ds_read_b128 v[12:15],v175 offset:2048\n"
"s_addk_i32 m0,8192\n"
"s_add_u32 s74,s74,s93\n"
"buffer_load_dwordx4 v195,s[16:19],s74 offen lds\n"
"v_dot2c_f32_bf16 v210,v76,v208\n"
"v_dot2c_f32_bf16 v212,v76,v76\n"
"v_dot2c_f32_bf16 v211,v77,v208\n"
"v_dot2c_f32_bf16 v213,v77,v77\n"
"v_dot2c_f32_bf16 v210,v78,v208\n"
"v_dot2c_f32_bf16 v212,v78,v78\n"
"v_dot2c_f32_bf16 v211,v79,v208\n"
"v_dot2c_f32_bf16 v213,v79,v79\n"
"s_waitcnt vmcnt(13)\n"
"s_cmp_lg_u32 s11,4\n"
"s_cbranch_scc1 .Lk_hebm\n"
"s_mul_i32 s96,s69,3\n"
"s_add_u32 s96,s96,s70\n"
"s_lshl_b32 s96,1,s96\n"
"s_mov_b32 s97,0\n"
"v_add_u32 v178,1,v176\n"
"s_mov_b64 exec,s[96:97]\n"
"buffer_store_dword v178,v177,s[32:35],0 offen sc1\n"
"s_mov_b64 exec,-1\n"
".Lk_hebm:\n"
"ds_read_b128 v[16:19],v174 offset:4096\n"
"ds_read_b128 v[20:23],v175 offset:4096\n"
"s_add_u32 m0,s91,50176\n"
"s_add_u32 s74,s80,3538944\n"
"buffer_load_dwordx4 v195,s[16:19],s74 offen lds\n"
"v_dot2c_f32_bf16 v210,v80,v208\n"
"v_dot2c_f32_bf16 v212,v80,v80\n"
"v_dot2c_f32_bf16 v211,v81,v208\n"
"v_dot2c_f32_bf16 v213,v81,v81\n"
"v_dot2c_f32_bf16 v210,v82,v208\n"
"v_dot2c_f32_bf16 v212,v82,v82\n"
"v_dot2c_f32_bf16 v211,v83,v208\n"
"v_dot2c_f32_bf16 v213,v83,v83\n"
"s_waitcnt vmcnt(13)\n"
"ds_read_b128 v[24:27],v174 offset:6144\n"
"ds_read_b128 v[28:31],v175 offset:6144\n"
"s_addk_i32 m0,8192\n"
"s_add_u32 s74,s74,s93\n"
"buffer_load_dwordx4 v195,s[16:19],s74 offen lds\n"
"v_dot2c_f32_bf16 v210,v84,v208\n"
"v_dot2c_f32_bf16 v212,v84,v84\n"
"v_dot2c_f32_bf16 v211,v85,v208\n"
"v_dot2c_f32_bf16 v213,v85,v85\n"
"v_dot2c_f32_bf16 v210,v86,v208\n"
"v_dot2c_f32_bf16 v212,v86,v86\n"
"v_dot2c_f32_bf16 v211,v87,v208\n"
"v_dot2c_f32_bf16 v213,v87,v87\n"
"s_waitcnt vmcnt(13)\n"
"ds_read_b128 v[32:35],v174 offset:8192\n"
"ds_read_b128 v[36:39],v175 offset:8192\n"
"s_addk_i32 m0,8192\n"
"s_add_u32 s74,s74,s93\n"
"buffer_load_dwordx4 v195,s[16:19],s74 offen lds\n"
"v_dot2c_f32_bf16 v210,v88,v208\n"
"v_dot2c_f32_bf16 v212,v88,v88\n"
"v_dot2c_f32_bf16 v211,v89,v208\n"
"v_dot2c_f32_bf16 v213,v89,v89\n"
"v_dot2c_f32_bf16 v210,v90,v208\n"
"v_dot2c_f32_bf16 v212,v90,v90\n"
"v_dot2c_f32_bf16 v211,v91,v208\n"
"v_dot2c_f32_bf16 v213,v91,v91\n"
"s_waitcnt vmcnt(13)\n"
"ds_read_b128 v[40:43],v174 offset:10240\n"
"ds_read_b128 v[44:47],v175 offset:10240\n"
"v_dot2c_f32_bf16 v210,v92,v208\n"
"v_dot2c_f32_bf16 v212,v92,v92\n"
"v_dot2c_f32_bf16 v211,v93,v208\n"
"v_dot2c_f32_bf16 v213,v93,v93\n"
"v_dot2c_f32_bf16 v210,v94,v208\n"
"v_dot2c_f32_bf16 v212,v94,v94\n"
"v_dot2c_f32_bf16 v211,v95,v208\n"
"v_dot2c_f32_bf16 v213,v95,v95\n"
"s_waitcnt vmcnt(12)\n"
"ds_read_b128 v[48:51],v174 offset:12288\n"
"ds_read_b128 v[52:55],v175 offset:12288\n"
"v_dot2c_f32_bf16 v210,v96,v208\n"
"v_dot2c_f32_bf16 v212,v96,v96\n"
"v_dot2c_f32_bf16 v211,v97,v208\n"
"v_dot2c_f32_bf16 v213,v97,v97\n"
"v_dot2c_f32_bf16 v210,v98,v208\n"
"v_dot2c_f32_bf16 v212,v98,v98\n"
"v_dot2c_f32_bf16 v211,v99,v208\n"
"v_dot2c_f32_bf16 v213,v99,v99\n"
"s_waitcnt vmcnt(11)\n"
"ds_read_b128 v[56:59],v174 offset:14336\n"
"ds_read_b128 v[60:63],v175 offset:14336\n"
"v_dot2c_f32_bf16 v210,v100,v208\n"
"v_dot2c_f32_bf16 v212,v100,v100\n"
"v_dot2c_f32_bf16 v211,v101,v208\n"
"v_dot2c_f32_bf16 v213,v101,v101\n"
"v_dot2c_f32_bf16 v210,v102,v208\n"
"v_dot2c_f32_bf16 v212,v102,v102\n"
"v_dot2c_f32_bf16 v211,v103,v208\n"
"v_dot2c_f32_bf16 v213,v103,v103\n"
"s_waitcnt vmcnt(10)\n"
"ds_read_b128 v[64:67],v174 offset:16384\n"
"ds_read_b128 v[68:71],v175 offset:16384\n"
"v_dot2c_f32_bf16 v210,v104,v208\n"
"v_dot2c_f32_bf16 v212,v104,v104\n"
"v_dot2c_f32_bf16 v211,v105,v208\n"
"v_dot2c_f32_bf16 v213,v105,v105\n"
"v_dot2c_f32_bf16 v210,v106,v208\n"
"v_dot2c_f32_bf16 v212,v106,v106\n"
"v_dot2c_f32_bf16 v211,v107,v208\n"
"v_dot2c_f32_bf16 v213,v107,v107\n"
"s_nop 1\n"
"v_add_f32 v210,v210,v211\n"
"v_add_f32 v212,v212,v213\n"
"s_nop 0\n"
"v_add_f32_dpp v210,v210,v210 quad_perm:[1,0,3,2] row_mask:0xf bank_mask:0xf\n"
"v_add_f32_dpp v212,v212,v212 quad_perm:[1,0,3,2] row_mask:0xf bank_mask:0xf\n"
"s_nop 0\n"
"v_add_f32_dpp v210,v210,v210 quad_perm:[2,3,0,1] row_mask:0xf bank_mask:0xf\n"
"v_add_f32_dpp v212,v212,v212 quad_perm:[2,3,0,1] row_mask:0xf bank_mask:0xf\n"
"s_nop 0\n"
"v_add_f32_dpp v210,v210,v210 row_half_mirror row_mask:0xf bank_mask:0xf\n"
"v_add_f32_dpp v212,v212,v212 row_half_mirror row_mask:0xf bank_mask:0xf\n"
"v_mul_f32 v218,0x3ae38e39,v210\n"
"v_mul_f32 v212,0x3ae38e39,v212\n"
"v_fma_f32 v212,-v218,v218,v212\n"
"v_max_f32 v212,0,v212\n"
"v_add_f32 v219,0x3727c5ac,v212\n"
"v_rsq_f32 v219,v219\n"
"s_nop 0\n"
"v_mul_f32 v240,v219,v219\n"
"v_fmaak_f32 v240,v240,v212,0x3727c5ac\n"
"v_rsq_f32 v240,v240\n"
"v_mul_f32 v212,0x3eeff558,v219\n"
"v_mul_f32 v240,v212,v240\n"
"ds_write_b64 v221,v[218:219] offset:32\n"
"v_cvt_pk_bf16_f32 v214,v240,0\n"
"v_lshlrev_b32 v215,16,v214\n"
"v_mul_f32_e64 v216,-v218,v215\n"
"v_dot2_f32_bf16 v240,v72,v214,v216\n"
"v_dot2_f32_bf16 v241,v72,v215,v216\n"
"v_dot2_f32_bf16 v242,v73,v214,v216\n"
"v_dot2_f32_bf16 v243,v73,v215,v216\n"
"v_dot2_f32_bf16 v244,v74,v214,v216\n"
"v_dot2_f32_bf16 v245,v74,v215,v216\n"
"v_cvt_pk_bf16_f32 v72,v240,v241\n"
"v_dot2_f32_bf16 v240,v75,v214,v216\n"
"v_dot2_f32_bf16 v241,v75,v215,v216\n"
"v_cvt_pk_bf16_f32 v73,v242,v243\n"
"v_dot2_f32_bf16 v242,v76,v214,v216\n"
"v_dot2_f32_bf16 v243,v76,v215,v216\n"
"v_cvt_pk_bf16_f32 v74,v244,v245\n"
"v_dot2_f32_bf16 v244,v77,v214,v216\n"
"v_dot2_f32_bf16 v245,v77,v215,v216\n"
"v_cvt_pk_bf16_f32 v75,v240,v241\n"
"s_waitcnt lgkmcnt(0)\n"
"s_barrier\n"
"ds_write_b128 v220,v[72:75]\n"
"v_dot2_f32_bf16 v240,v78,v214,v216\n"
"v_dot2_f32_bf16 v241,v78,v215,v216\n"
"v_cvt_pk_bf16_f32 v76,v242,v243\n"
"v_dot2_f32_bf16 v242,v79,v214,v216\n"
"v_dot2_f32_bf16 v243,v79,v215,v216\n"
"v_cvt_pk_bf16_f32 v77,v244,v245\n"
"v_dot2_f32_bf16 v244,v80,v214,v216\n"
"v_dot2_f32_bf16 v245,v80,v215,v216\n"
"v_cvt_pk_bf16_f32 v78,v240,v241\n"
"v_dot2_f32_bf16 v240,v81,v214,v216\n"
"v_dot2_f32_bf16 v241,v81,v215,v216\n"
"v_cvt_pk_bf16_f32 v79,v242,v243\n"
"ds_write_b128 v220,v[76:79] offset:2048\n"
"v_dot2_f32_bf16 v242,v82,v214,v216\n"
"v_dot2_f32_bf16 v243,v82,v215,v216\n"
"v_cvt_pk_bf16_f32 v80,v244,v245\n"
"v_dot2_f32_bf16 v244,v83,v214,v216\n"
"v_dot2_f32_bf16 v245,v83,v215,v216\n"
"v_cvt_pk_bf16_f32 v81,v240,v241\n"
"v_dot2_f32_bf16 v240,v84,v214,v216\n"
"v_dot2_f32_bf16 v241,v84,v215,v216\n"
"v_cvt_pk_bf16_f32 v82,v242,v243\n"
"v_dot2_f32_bf16 v242,v85,v214,v216\n"
"v_dot2_f32_bf16 v243,v85,v215,v216\n"
"v_cvt_pk_bf16_f32 v83,v244,v245\n"
"ds_write_b128 v220,v[80:83] offset:4096\n"
"v_dot2_f32_bf16 v244,v86,v214,v216\n"
"v_dot2_f32_bf16 v245,v86,v215,v216\n"
"v_cvt_pk_bf16_f32 v84,v240,v241\n"
"v_dot2_f32_bf16 v240,v87,v214,v216\n"
"v_dot2_f32_bf16 v241,v87,v215,v216\n"
"v_cvt_pk_bf16_f32 v85,v242,v243\n"
"v_dot2_f32_bf16 v242,v88,v214,v216\n"
"v_dot2_f32_bf16 v243,v88,v215,v216\n"
"v_cvt_pk_bf16_f32 v86,v244,v245\n"
"v_dot2_f32_bf16 v244,v89,v214,v216\n"
"v_dot2_f32_bf16 v245,v89,v215,v216\n"
"v_cvt_pk_bf16_f32 v87,v240,v241\n"
"ds_write_b128 v220,v[84:87] offset:6144\n"
"v_dot2_f32_bf16 v240,v90,v214,v216\n"
"v_dot2_f32_bf16 v241,v90,v215,v216\n"
"v_cvt_pk_bf16_f32 v88,v242,v243\n"
"v_dot2_f32_bf16 v242,v91,v214,v216\n"
"v_dot2_f32_bf16 v243,v91,v215,v216\n"
"v_cvt_pk_bf16_f32 v89,v244,v245\n"
"v_dot2_f32_bf16 v244,v92,v214,v216\n"
"v_dot2_f32_bf16 v245,v92,v215,v216\n"
"v_cvt_pk_bf16_f32 v90,v240,v241\n"
"v_dot2_f32_bf16 v240,v93,v214,v216\n"
"v_dot2_f32_bf16 v241,v93,v215,v216\n"
"v_cvt_pk_bf16_f32 v91,v242,v243\n"
"ds_write_b128 v220,v[88:91] offset:8192\n"
"v_dot2_f32_bf16 v242,v94,v214,v216\n"
"v_dot2_f32_bf16 v243,v94,v215,v216\n"
"v_cvt_pk_bf16_f32 v92,v244,v245\n"
"v_dot2_f32_bf16 v244,v95,v214,v216\n"
"v_dot2_f32_bf16 v245,v95,v215,v216\n"
"v_cvt_pk_bf16_f32 v93,v240,v241\n"
"v_dot2_f32_bf16 v240,v96,v214,v216\n"
"v_dot2_f32_bf16 v241,v96,v215,v216\n"
"v_cvt_pk_bf16_f32 v94,v242,v243\n"
"v_dot2_f32_bf16 v242,v97,v214,v216\n"
"v_dot2_f32_bf16 v243,v97,v215,v216\n"
"v_cvt_pk_bf16_f32 v95,v244,v245\n"
"ds_write_b128 v220,v[92:95] offset:10240\n"
"v_dot2_f32_bf16 v244,v98,v214,v216\n"
"v_dot2_f32_bf16 v245,v98,v215,v216\n"
"v_cvt_pk_bf16_f32 v96,v240,v241\n"
"v_dot2_f32_bf16 v240,v99,v214,v216\n"
"v_dot2_f32_bf16 v241,v99,v215,v216\n"
"v_cvt_pk_bf16_f32 v97,v242,v243\n"
"v_dot2_f32_bf16 v242,v100,v214,v216\n"
"v_dot2_f32_bf16 v243,v100,v215,v216\n"
"v_cvt_pk_bf16_f32 v98,v244,v245\n"
"v_dot2_f32_bf16 v244,v101,v214,v216\n"
"v_dot2_f32_bf16 v245,v101,v215,v216\n"
"v_cvt_pk_bf16_f32 v99,v240,v241\n"
"ds_write_b128 v220,v[96:99] offset:12288\n"
"v_dot2_f32_bf16 v240,v102,v214,v216\n"
"v_dot2_f32_bf16 v241,v102,v215,v216\n"
"v_cvt_pk_bf16_f32 v100,v242,v243\n"
"v_dot2_f32_bf16 v242,v103,v214,v216\n"
"v_dot2_f32_bf16 v243,v103,v215,v216\n"
"v_cvt_pk_bf16_f32 v101,v244,v245\n"
"v_dot2_f32_bf16 v244,v104,v214,v216\n"
"v_dot2_f32_bf16 v245,v104,v215,v216\n"
"v_cvt_pk_bf16_f32 v102,v240,v241\n"
"v_dot2_f32_bf16 v240,v105,v214,v216\n"
"v_dot2_f32_bf16 v241,v105,v215,v216\n"
"v_cvt_pk_bf16_f32 v103,v242,v243\n"
"ds_write_b128 v220,v[100:103] offset:14336\n"
"v_dot2_f32_bf16 v242,v106,v214,v216\n"
"v_dot2_f32_bf16 v243,v106,v215,v216\n"
"v_cvt_pk_bf16_f32 v104,v244,v245\n"
"v_dot2_f32_bf16 v244,v107,v214,v216\n"
"v_dot2_f32_bf16 v245,v107,v215,v216\n"
"v_cvt_pk_bf16_f32 v105,v240,v241\n"
"v_cvt_pk_bf16_f32 v106,v242,v243\n"
"s_nop 0\n"
"v_cvt_pk_bf16_f32 v107,v244,v245\n"
"ds_write_b128 v220,v[104:107] offset:16384\n"
"s_waitcnt vmcnt(5)\n"
"v_lshlrev_b32 v192,16,v192\n"
"v_mul_f32 v192,0x3eeff558,v192\n"
"ds_write_b32 v194,v192\n"
"s_waitcnt lgkmcnt(0)\n"
"s_waitcnt vmcnt(0)\n"
"s_barrier\n"
"s_setprio 2\n"
"s_sleep 3\n"
"ds_read_b128 v[72:75],v174\n"
"s_nop 1\n"
"ds_read_b128 v[76:79],v175\n"
"s_nop 1\n"
"ds_read_b128 v[80:83],v174 offset:2048\n"
"s_nop 1\n"
"ds_read_b128 v[84:87],v175 offset:2048\n"
"s_nop 1\n"
"ds_read_b128 v[88:91],v174 offset:4096\n"
"s_nop 1\n"
"ds_read_b128 v[92:95],v175 offset:4096\n"
"s_nop 1\n"
"ds_read_b128 v[96:99],v174 offset:6144\n"
"s_nop 1\n"
"ds_read_b128 v[100:103],v175 offset:6144\n"
"s_nop 1\n"
"ds_read_b128 v[104:107],v174 offset:8192\n"
"s_nop 1\n"
"ds_read_b128 v[108:111],v175 offset:8192\n"
"s_nop 1\n"
"ds_read_b128 v[112:115],v174 offset:10240\n"
"s_nop 1\n"
"ds_read_b128 v[116:119],v175 offset:10240\n"
"s_nop 1\n"
"ds_read_b128 v[120:123],v174 offset:12288\n"
"s_nop 1\n"
"ds_read_b128 v[124:127],v175 offset:12288\n"
"s_nop 1\n"
"ds_read_b128 v[128:131],v174 offset:14336\n"
"s_nop 1\n"
"ds_read_b128 v[132:135],v175 offset:14336\n"
"s_nop 1\n"
"ds_read_b128 v[136:139],v174 offset:16384\n"
"s_nop 1\n"
"ds_read_b128 v[140:143],v175 offset:16384\n"
"s_nop 1\n"
"ds_read_b128 v[240:243],v253\n"
"s_nop 1\n"
"ds_read_b128 v[244:247],v253 offset:64\n"
"s_nop 1\n"
"s_waitcnt lgkmcnt(0)\n"
"s_setprio 0\n"
"s_barrier\n"
"s_add_u32 s90,s80,128\n"
"s_addk_i32 m0,7168\n"
"s_nop 0\n"
"buffer_load_dwordx4 v250,s[16:19],s90 offen lds\n"
"s_nop 6\n"
"s_add_u32 s92,s90,s93\n"
"buffer_load_dwordx4 v191,s[16:19],s90 offen offset:1024 lds\n"
"s_nop 7\n"
"s_addk_i32 m0,8192\n"
"s_nop 0\n"
"buffer_load_dwordx4 v250,s[16:19],s92 offen lds\n"
"s_nop 7\n"
"s_add_u32 s90,s92,s93\n"
"buffer_load_dwordx4 v191,s[16:19],s92 offen offset:1024 lds\n"
"s_nop 7\n"
"s_addk_i32 m0,8192\n"
"s_nop 0\n"
"buffer_load_dwordx4 v250,s[16:19],s90 offen lds\n"
"s_nop 7\n"
"s_add_u32 s92,s90,s93\n"
"buffer_load_dwordx4 v191,s[16:19],s90 offen offset:1024 lds\n"
"s_nop 7\n"
"s_addk_i32 m0,8192\n"
"s_nop 0\n"
"buffer_load_dwordx4 v250,s[16:19],s92 offen lds\n"
"s_nop 7\n"
"s_add_u32 s90,s92,s93\n"
"buffer_load_dwordx4 v191,s[16:19],s92 offen offset:1024 lds\n"
"s_nop 7\n"
"s_addk_i32 m0,8192\n"
"s_nop 0\n"
"buffer_load_dwordx4 v250,s[16:19],s90 offen lds\n"
"s_nop 7\n"
"buffer_load_dwordx4 v191,s[16:19],s90 offen offset:1024 lds\n"
"s_nop 8\n"
"s_and_b32 s75,s10,1\n"
"s_mul_i32 s75,s75,24\n"
"s_lshr_b32 s76,s10,3\n"
"s_add_u32 s75,s75,s76\n"
"s_lshl_b32 s75,s75,2\n"
"s_add_u32 s75,s75,s71\n"
"s_mul_i32 s95,s75,6912\n"
"s_mul_i32 s77,s69,1327104\n"
"s_add_u32 s95,s95,s77\n"
"v_lshrrev_b32 v209,4,v254\n"
"v_lshlrev_b32 v254,7,v254\n"
"v_lshl_or_b32 v254,v209,29,v254\n"
"s_setprio 3\n"
"ds_read_b64_tr_b16 v[208:209],v248\n"
"ds_read_b64_tr_b16 v[210:211],v248 offset:2048\n"
"ds_read_b64_tr_b16 v[212:213],v248 offset:4096\n"
"ds_read_b64_tr_b16 v[214:215],v248 offset:6144\n"
"ds_read_b64_tr_b16 v[216:217],v248 offset:8192\n"
"ds_read_b64_tr_b16 v[218:219],v248 offset:10240\n"
"ds_read_b64_tr_b16 v[220:221],v248 offset:12288\n"
"ds_read_b64_tr_b16 v[222:223],v248 offset:14336\n"
"ds_read_b64_tr_b16 v[224:225],v248 offset:16384\n"
"ds_read_b64_tr_b16 v[226:227],v248 offset:18432\n"
"ds_read_b64_tr_b16 v[228:229],v248 offset:20480\n"
"ds_read_b64_tr_b16 v[230:231],v248 offset:22528\n"
"s_setprio 1\n"
"s_waitcnt lgkmcnt(8)\n"
"v_mfma_f32_16x16x32_bf16 v[192:195],v[208:211],v[0:3],v[240:243]\n"
"ds_read_b64_tr_b16 v[232:233],v248 offset:24576\n"
"s_add_u32 s92,s80,4128896\n"
"v_mfma_f32_16x16x32_bf16 v[192:195],v[212:215],v[4:7],v[192:195]\n"
"ds_read_b64_tr_b16 v[234:235],v248 offset:26624\n"
"s_addk_i32 m0,24576\n"
"v_mfma_f32_16x16x32_bf16 v[196:199],v[208:211],v[72:75],v[240:243]\n"
"ds_read_b64_tr_b16 v[236:237],v248 offset:28672\n"
"buffer_load_dwordx4 v250,s[16:19],s92 offen lds\n"
"v_mfma_f32_16x16x32_bf16 v[196:199],v[212:215],v[76:79],v[196:199]\n"
"ds_read_b64_tr_b16 v[238:239],v248 offset:30720\n"
"s_waitcnt lgkmcnt(8)\n"
"v_mfma_f32_16x16x32_bf16 v[196:199],v[216:219],v[80:83],v[196:199]\n"
"ds_read_b64_tr_b16 v[208:209],v248 offset:32768\n"
"s_add_u32 s90,s92,s93\n"
"v_mfma_f32_16x16x32_bf16 v[196:199],v[220:223],v[84:87],v[196:199]\n"
"ds_read_b64_tr_b16 v[210:211],v248 offset:34816\n"
"v_mfma_f32_16x16x32_bf16 v[192:195],v[216:219],v[8:11],v[192:195]\n"
"ds_read_b64_tr_b16 v[212:213],v248 offset:36864\n"
"v_mfma_f32_16x16x32_bf16 v[192:195],v[220:223],v[12:15],v[192:195]\n"
"ds_read_b64_tr_b16 v[214:215],v248 offset:38912\n"
"s_waitcnt lgkmcnt(8)\n"
"v_mfma_f32_16x16x32_bf16 v[192:195],v[224:227],v[16:19],v[192:195]\n"
"ds_read_b64_tr_b16 v[216:217],v249\n"
"v_mfma_f32_16x16x32_bf16 v[192:195],v[228:231],v[20:23],v[192:195]\n"
"ds_read_b64_tr_b16 v[218:219],v249 offset:2048\n"
"v_mfma_f32_16x16x32_bf16 v[196:199],v[224:227],v[88:91],v[196:199]\n"
"ds_read_b64_tr_b16 v[220:221],v249 offset:4096\n"
"v_mfma_f32_16x16x32_bf16 v[196:199],v[228:231],v[92:95],v[196:199]\n"
"ds_read_b64_tr_b16 v[222:223],v249 offset:6144\n"
"s_waitcnt lgkmcnt(8)\n"
"v_mfma_f32_16x16x32_bf16 v[196:199],v[232:235],v[96:99],v[196:199]\n"
"ds_read_b64_tr_b16 v[224:225],v249 offset:8192\n"
"buffer_load_dwordx4 v191,s[16:19],s92 offen offset:1024 lds\n"
"v_mfma_f32_16x16x32_bf16 v[196:199],v[236:239],v[100:103],v[196:199]\n"
"ds_read_b64_tr_b16 v[226:227],v249 offset:10240\n"
"s_addk_i32 m0,8192\n"
"v_mfma_f32_16x16x32_bf16 v[192:195],v[232:235],v[24:27],v[192:195]\n"
"ds_read_b64_tr_b16 v[228:229],v249 offset:12288\n"
"v_mfma_f32_16x16x32_bf16 v[192:195],v[236:239],v[28:31],v[192:195]\n"
"ds_read_b64_tr_b16 v[230:231],v249 offset:14336\n"
"s_waitcnt lgkmcnt(8)\n"
"v_mfma_f32_16x16x32_bf16 v[192:195],v[208:211],v[32:35],v[192:195]\n"
"ds_read_b64_tr_b16 v[232:233],v249 offset:16384\n"
"v_mfma_f32_16x16x32_bf16 v[192:195],v[212:215],v[36:39],v[192:195]\n"
"ds_read_b64_tr_b16 v[234:235],v249 offset:18432\n"
"v_mfma_f32_16x16x32_bf16 v[196:199],v[208:211],v[104:107],v[196:199]\n"
"ds_read_b64_tr_b16 v[236:237],v249 offset:20480\n"
"v_mfma_f32_16x16x32_bf16 v[196:199],v[212:215],v[108:111],v[196:199]\n"
"ds_read_b64_tr_b16 v[238:239],v249 offset:22528\n"
"s_waitcnt lgkmcnt(8)\n"
"v_mfma_f32_16x16x32_bf16 v[200:203],v[216:219],v[0:3],v[244:247]\n"
"ds_read_b64_tr_b16 v[208:209],v249 offset:24576\n"
"v_mfma_f32_16x16x32_bf16 v[200:203],v[220:223],v[4:7],v[200:203]\n"
"ds_read_b64_tr_b16 v[210:211],v249 offset:26624\n"
"v_mfma_f32_16x16x32_bf16 v[204:207],v[216:219],v[72:75],v[244:247]\n"
"ds_read_b64_tr_b16 v[212:213],v249 offset:28672\n"
"v_mfma_f32_16x16x32_bf16 v[204:207],v[220:223],v[76:79],v[204:207]\n"
"ds_read_b64_tr_b16 v[214:215],v249 offset:30720\n"
"s_waitcnt lgkmcnt(8)\n"
"v_mfma_f32_16x16x32_bf16 v[204:207],v[224:227],v[80:83],v[204:207]\n"
"ds_read_b64_tr_b16 v[216:217],v249 offset:32768\n"
"v_mfma_f32_16x16x32_bf16 v[204:207],v[228:231],v[84:87],v[204:207]\n"
"ds_read_b64_tr_b16 v[218:219],v249 offset:34816\n"
"buffer_load_dwordx4 v250,s[16:19],s90 offen lds\n"
"v_mfma_f32_16x16x32_bf16 v[200:203],v[224:227],v[8:11],v[200:203]\n"
"ds_read_b64_tr_b16 v[220:221],v249 offset:36864\n"
"v_mfma_f32_16x16x32_bf16 v[200:203],v[228:231],v[12:15],v[200:203]\n"
"ds_read_b64_tr_b16 v[222:223],v249 offset:38912\n"
"s_waitcnt lgkmcnt(8)\n"
".Lk_padm2:\n"
"s_cbranch_execz .Lk_padm6\n"
"v_mfma_f32_16x16x32_bf16 v[200:203],v[232:235],v[16:19],v[200:203]\n"
"ds_read_b64_tr_b16 v[224:225],v249 offset:40960\n"
"v_mfma_f32_16x16x32_bf16 v[200:203],v[236:239],v[20:23],v[200:203]\n"
"ds_read_b64_tr_b16 v[226:227],v249 offset:43008\n"
"v_mfma_f32_16x16x32_bf16 v[204:207],v[232:235],v[88:91],v[204:207]\n"
"ds_read_b64_tr_b16 v[228:229],v249 offset:45056\n"
"v_mfma_f32_16x16x32_bf16 v[204:207],v[236:239],v[92:95],v[204:207]\n"
"ds_read_b64_tr_b16 v[230:231],v249 offset:47104\n"
"s_waitcnt lgkmcnt(8)\n"
"v_mfma_f32_16x16x32_bf16 v[204:207],v[208:211],v[96:99],v[204:207]\n"
"ds_read_b64_tr_b16 v[232:233],v249 offset:49152\n"
"v_mfma_f32_16x16x32_bf16 v[204:207],v[212:215],v[100:103],v[204:207]\n"
"ds_read_b64_tr_b16 v[234:235],v249 offset:51200\n"
"v_mfma_f32_16x16x32_bf16 v[200:203],v[208:211],v[24:27],v[200:203]\n"
"ds_read_b64_tr_b16 v[236:237],v249 offset:53248\n"
"v_mfma_f32_16x16x32_bf16 v[200:203],v[212:215],v[28:31],v[200:203]\n"
"ds_read_b64_tr_b16 v[238:239],v249 offset:55296\n"
"s_waitcnt lgkmcnt(8)\n"
"v_mfma_f32_16x16x32_bf16 v[200:203],v[216:219],v[32:35],v[200:203]\n"
"ds_read_b64_tr_b16 v[208:209],v249 offset:57344\n"
"v_mfma_f32_16x16x32_bf16 v[200:203],v[220:223],v[36:39],v[200:203]\n"
"ds_read_b64_tr_b16 v[210:211],v249 offset:59392\n"
"v_mfma_f32_16x16x32_bf16 v[204:207],v[216:219],v[104:107],v[204:207]\n"
"ds_read_b64_tr_b16 v[212:213],v249 offset:61440\n"
"buffer_load_dwordx4 v191,s[16:19],s90 offen offset:1024 lds\n"
"v_mfma_f32_16x16x32_bf16 v[204:207],v[220:223],v[108:111],v[204:207]\n"
"ds_read_b64_tr_b16 v[214:215],v249 offset:63488\n"
"s_waitcnt lgkmcnt(8)\n"
"v_mfma_f32_16x16x32_bf16 v[204:207],v[224:227],v[112:115],v[204:207]\n"
"v_add_u32 v249,0x10000,v249\n"
"ds_read_b64_tr_b16 v[216:217],v249\n"
"v_lshlrev_b32 v240,3,v252\n"
"v_mfma_f32_16x16x32_bf16 v[204:207],v[228:231],v[116:119],v[204:207]\n"
"ds_read_b64_tr_b16 v[218:219],v249 offset:2048\n"
"s_setprio 2\n"
"v_mfma_f32_16x16x32_bf16 v[200:203],v[224:227],v[40:43],v[200:203]\n"
"ds_read_b64_tr_b16 v[220:221],v249 offset:4096\n"
"s_mov_b32 m0,161280\n"
"v_mfma_f32_16x16x32_bf16 v[200:203],v[228:231],v[44:47],v[200:203]\n"
"ds_read_b64_tr_b16 v[222:223],v249 offset:6144\n"
"s_waitcnt lgkmcnt(8)\n"
"v_mfma_f32_16x16x32_bf16 v[200:203],v[232:235],v[48:51],v[200:203]\n"
"ds_read_b64_tr_b16 v[224:225],v248 offset:40960\n"
"v_mfma_f32_16x16x32_bf16 v[200:203],v[236:239],v[52:55],v[200:203]\n"
"ds_read_b64_tr_b16 v[226:227],v248 offset:43008\n"
"v_mfma_f32_16x16x32_bf16 v[204:207],v[232:235],v[120:123],v[204:207]\n"
"ds_read_b64_tr_b16 v[228:229],v248 offset:45056\n"
"v_mfma_f32_16x16x32_bf16 v[204:207],v[236:239],v[124:127],v[204:207]\n"
"ds_read_b64_tr_b16 v[230:231],v248 offset:47104\n"
"s_waitcnt vmcnt(4)\n"
"s_waitcnt lgkmcnt(8)\n"
"v_mfma_f32_16x16x32_bf16 v[204:207],v[208:211],v[128:131],v[204:207]\n"
"ds_read_b64_tr_b16 v[232:233],v248 offset:49152\n"
"v_mfma_f32_16x16x32_bf16 v[204:207],v[212:215],v[132:135],v[204:207]\n"
"ds_read_b64_tr_b16 v[234:235],v248 offset:51200\n"
"s_barrier\n"
"v_mfma_f32_16x16x32_bf16 v[200:203],v[208:211],v[56:59],v[200:203]\n"
"ds_read_b64_tr_b16 v[236:237],v248 offset:53248\n"
"v_mfma_f32_16x16x32_bf16 v[200:203],v[212:215],v[60:63],v[200:203]\n"
"ds_read_b64_tr_b16 v[238:239],v248 offset:55296\n"
"s_waitcnt lgkmcnt(8)\n"
"v_mfma_f32_16x16x32_bf16 v[200:203],v[216:219],v[64:67],v[200:203]\n"
"ds_read_b64_tr_b16 v[208:209],v248 offset:57344\n"
"s_bfm_b64 exec,24,0\n"
"buffer_load_dword v240,s[20:23],s95 offen lds\n"
"s_mov_b64 exec,-1\n"
"v_mfma_f32_16x16x32_bf16 v[200:203],v[220:223],v[68:71],v[200:203]\n"
"ds_read_b64_tr_b16 v[210:211],v248 offset:59392\n"
"v_mfma_f32_16x16x32_bf16 v[204:207],v[216:219],v[136:139],v[204:207]\n"
"ds_read_b64_tr_b16 v[212:213],v248 offset:61440\n"
"v_mfma_f32_16x16x32_bf16 v[204:207],v[220:223],v[140:143],v[204:207]\n"
"ds_read_b64_tr_b16 v[214:215],v248 offset:63488\n"
"s_waitcnt lgkmcnt(8)\n"
"v_mfma_f32_16x16x32_bf16 v[192:195],v[224:227],v[40:43],v[192:195]\n"
"v_add_u32 v248,0x10000,v248\n"
"ds_read_b64_tr_b16 v[216:217],v248\n"
"v_mfma_f32_16x16x32_bf16 v[192:195],v[228:231],v[44:47],v[192:195]\n"
"ds_read_b64_tr_b16 v[218:219],v248 offset:2048\n"
"v_mfma_f32_16x16x32_bf16 v[196:199],v[224:227],v[112:115],v[196:199]\n"
"ds_read_b64_tr_b16 v[220:221],v248 offset:4096\n"
"v_mfma_f32_16x16x32_bf16 v[196:199],v[228:231],v[116:119],v[196:199]\n"
"ds_read_b64_tr_b16 v[222:223],v248 offset:6144\n"
"s_waitcnt lgkmcnt(8)\n"
"v_mfma_f32_16x16x32_bf16 v[196:199],v[232:235],v[120:123],v[196:199]\n"
"v_mfma_f32_16x16x32_bf16 v[196:199],v[236:239],v[124:127],v[196:199]\n"
"v_mfma_f32_16x16x32_bf16 v[192:195],v[232:235],v[48:51],v[192:195]\n"
"v_mfma_f32_16x16x32_bf16 v[192:195],v[236:239],v[52:55],v[192:195]\n"
"s_waitcnt lgkmcnt(4)\n"
"v_mfma_f32_16x16x32_bf16 v[192:195],v[208:211],v[56:59],v[192:195]\n"
"v_mfma_f32_16x16x32_bf16 v[192:195],v[212:215],v[60:63],v[192:195]\n"
"v_add_u32 v248,8192,v248\n"
"v_mfma_f32_16x16x32_bf16 v[196:199],v[208:211],v[128:131],v[196:199]\n"
"v_mfma_f32_16x16x32_bf16 v[196:199],v[212:215],v[132:135],v[196:199]\n"
"v_add_u32 v249,8192,v249\n"
"s_waitcnt lgkmcnt(0)\n"
"v_mfma_f32_16x16x32_bf16 v[196:199],v[216:219],v[136:139],v[196:199]\n"
"v_mfma_f32_16x16x32_bf16 v[196:199],v[220:223],v[140:143],v[196:199]\n"
"v_mfma_f32_16x16x32_bf16 v[192:195],v[216:219],v[64:67],v[192:195]\n"
"v_mfma_f32_16x16x32_bf16 v[192:195],v[220:223],v[68:71],v[192:195]\n"
"s_setprio 0\n"
"v_fma_f32 v244,-v200,v200,s79\n"
"v_fma_f32 v245,-v201,v201,s79\n"
"v_fma_f32 v246,-v202,v202,s79\n"
"v_fma_f32 v247,-v203,v203,s79\n"
"v_mul_f32 v244,v244,v200\n"
"v_mul_f32 v245,v245,v201\n"
"v_mul_f32 v246,v246,v202\n"
"v_mul_f32 v247,v247,v203\n"
"v_exp_f32 v244,v244\n"
"v_exp_f32 v245,v245\n"
"v_exp_f32 v246,v246\n"
"v_exp_f32 v247,v247\n"
"v_add_f32 v244,1.0,v244\n"
"v_add_f32 v245,1.0,v245\n"
"v_add_f32 v246,1.0,v246\n"
"v_add_f32 v247,1.0,v247\n"
"v_rcp_f32 v244,v244\n"
"v_rcp_f32 v245,v245\n"
"v_rcp_f32 v246,v246\n"
"v_rcp_f32 v247,v247\n"
"v_mul_f32 v244,v244,v200\n"
"v_mul_f32 v245,v245,v201\n"
"v_mul_f32 v246,v246,v202\n"
"v_mul_f32 v247,v247,v203\n"
"v_cvt_pk_bf16_f32 v146,v244,v245\n"
"v_cvt_pk_bf16_f32 v147,v246,v247\n"
"v_fma_f32 v240,-v192,v192,s79\n"
"v_fma_f32 v241,-v193,v193,s79\n"
"v_fma_f32 v242,-v194,v194,s79\n"
"v_fma_f32 v243,-v195,v195,s79\n"
"v_mul_f32 v240,v240,v192\n"
"v_mul_f32 v241,v241,v193\n"
"v_mul_f32 v242,v242,v194\n"
"v_mul_f32 v243,v243,v195\n"
"v_exp_f32 v240,v240\n"
"s_waitcnt vmcnt(1)\n"
"s_barrier\n"
"v_exp_f32 v241,v241\n"
"v_exp_f32 v242,v242\n"
"s_add_u32 s90,s80,256\n"
"s_add_u32 m0,s91,0\n"
"v_exp_f32 v243,v243\n"
"buffer_load_dwordx4 v250,s[16:19],s90 offen lds\n"
"v_add_f32 v240,1.0,v240\n"
"v_add_f32 v241,1.0,v241\n"
"v_add_f32 v242,1.0,v242\n"
"v_add_f32 v243,1.0,v243\n"
"v_rcp_f32 v240,v240\n"
"v_rcp_f32 v241,v241\n"
"v_rcp_f32 v242,v242\n"
"v_rcp_f32 v243,v243\n"
"v_mul_f32 v240,v240,v192\n"
"v_mul_f32 v241,v241,v193\n"
"v_mul_f32 v242,v242,v194\n"
"v_mul_f32 v243,v243,v195\n"
"v_cvt_pk_bf16_f32 v144,v240,v241\n"
"v_cvt_pk_bf16_f32 v145,v242,v243\n"
"v_fma_f32 v240,-v196,v196,s79\n"
"v_fma_f32 v241,-v197,v197,s79\n"
"v_fma_f32 v242,-v198,v198,s79\n"
"v_fma_f32 v243,-v199,v199,s79\n"
"v_mul_f32 v240,v240,v196\n"
"v_mul_f32 v241,v241,v197\n"
"v_mul_f32 v242,v242,v198\n"
"v_mul_f32 v243,v243,v199\n"
"v_exp_f32 v240,v240\n"
"v_exp_f32 v241,v241\n"
"v_exp_f32 v242,v242\n"
"v_exp_f32 v243,v243\n"
"v_add_f32 v240,1.0,v240\n"
"v_add_f32 v241,1.0,v241\n"
"buffer_load_dwordx4 v191,s[16:19],s90 offen offset:1024 lds\n"
"v_add_f32 v242,1.0,v242\n"
"v_add_f32 v243,1.0,v243\n"
"v_rcp_f32 v240,v240\n"
"v_rcp_f32 v241,v241\n"
"v_rcp_f32 v242,v242\n"
"v_rcp_f32 v243,v243\n"
"v_mul_f32 v240,v240,v196\n"
"v_mul_f32 v241,v241,v197\n"
"v_mul_f32 v242,v242,v198\n"
"v_mul_f32 v243,v243,v199\n"
"v_cvt_pk_bf16_f32 v148,v240,v241\n"
"v_cvt_pk_bf16_f32 v149,v242,v243\n"
"v_fma_f32 v244,-v204,v204,s79\n"
"v_fma_f32 v245,-v205,v205,s79\n"
"v_fma_f32 v246,-v206,v206,s79\n"
"v_fma_f32 v247,-v207,v207,s79\n"
"v_mul_f32 v244,v244,v204\n"
"v_mul_f32 v245,v245,v205\n"
"v_mul_f32 v246,v246,v206\n"
"v_mul_f32 v247,v247,v207\n"
"v_exp_f32 v244,v244\n"
"v_exp_f32 v245,v245\n"
"v_exp_f32 v246,v246\n"
"v_exp_f32 v247,v247\n"
"v_add_f32 v244,1.0,v244\n"
"v_add_f32 v245,1.0,v245\n"
"v_add_f32 v246,1.0,v246\n"
"v_add_f32 v247,1.0,v247\n"
"v_rcp_f32 v244,v244\n"
"s_setprio 3\n"
"v_rcp_f32 v245,v245\n"
"ds_read_b128 v[240:243],v253 offset:256\n"
"ds_read_b64_tr_b16 v[208:209],v248\n"
"v_rcp_f32 v246,v246\n"
"ds_read_b64_tr_b16 v[210:211],v248 offset:2048\n"
"ds_read_b64_tr_b16 v[212:213],v248 offset:4096\n"
"v_rcp_f32 v247,v247\n"
"ds_read_b64_tr_b16 v[214:215],v248 offset:6144\n"
"ds_read_b64_tr_b16 v[216:217],v248 offset:8192\n"
"v_mul_f32 v244,v244,v204\n"
"v_mul_f32 v245,v245,v205\n"
"ds_read_b64_tr_b16 v[218:219],v248 offset:10240\n"
"ds_read_b64_tr_b16 v[220:221],v248 offset:12288\n"
"v_mul_f32 v246,v246,v206\n"
"v_mul_f32 v247,v247,v207\n"
"ds_read_b64_tr_b16 v[222:223],v248 offset:14336\n"
"ds_read_b64_tr_b16 v[224:225],v248 offset:16384\n"
"v_cvt_pk_bf16_f32 v150,v244,v245\n"
"v_cvt_pk_bf16_f32 v151,v246,v247\n"
"ds_read_b64_tr_b16 v[226:227],v248 offset:18432\n"
"ds_read_b64_tr_b16 v[228:229],v248 offset:20480\n"
"ds_read_b64_tr_b16 v[230:231],v248 offset:22528\n"
"s_setprio 1\n"
"s_waitcnt lgkmcnt(12)\n"
"s_waitcnt lgkmcnt(8)\n"
"v_mfma_f32_16x16x32_bf16 v[192:195],v[208:211],v[0:3],v[240:243]\n"
"ds_read_b64_tr_b16 v[232:233],v248 offset:24576\n"
"ds_read_b128 v[244:247],v253 offset:320\n"
"v_mfma_f32_16x16x32_bf16 v[192:195],v[212:215],v[4:7],v[192:195]\n"
"ds_read_b64_tr_b16 v[234:235],v248 offset:26624\n"
"s_add_u32 s92,s80,4129024\n"
"v_mfma_f32_16x16x32_bf16 v[196:199],v[208:211],v[72:75],v[240:243]\n"
"ds_read_b64_tr_b16 v[236:237],v248 offset:28672\n"
"s_add_u32 m0,s91,57344\n"
"v_mfma_f32_16x16x32_bf16 v[196:199],v[212:215],v[76:79],v[196:199]\n"
"ds_read_b64_tr_b16 v[238:239],v248 offset:30720\n"
"s_waitcnt lgkmcnt(9)\n"
"v_mfma_f32_16x16x32_bf16 v[196:199],v[216:219],v[80:83],v[196:199]\n"
"ds_read_b64_tr_b16 v[208:209],v248 offset:32768\n"
"v_mfma_f32_16x16x32_bf16 v[196:199],v[220:223],v[84:87],v[196:199]\n"
"ds_read_b64_tr_b16 v[210:211],v248 offset:34816\n"
"v_mfma_f32_16x16x32_bf16 v[192:195],v[216:219],v[8:11],v[192:195]\n"
"ds_read_b64_tr_b16 v[212:213],v248 offset:36864\n"
"v_mfma_f32_16x16x32_bf16 v[192:195],v[220:223],v[12:15],v[192:195]\n"
"ds_read_b64_tr_b16 v[214:215],v248 offset:38912\n"
"s_waitcnt lgkmcnt(9)\n"
"v_mfma_f32_16x16x32_bf16 v[192:195],v[224:227],v[16:19],v[192:195]\n"
"ds_read_b64_tr_b16 v[216:217],v249\n"
"buffer_load_dwordx4 v250,s[16:19],s92 offen lds\n"
"v_mfma_f32_16x16x32_bf16 v[192:195],v[228:231],v[20:23],v[192:195]\n"
"ds_read_b64_tr_b16 v[218:219],v249 offset:2048\n"
"s_add_u32 s90,s92,s93\n"
"v_mfma_f32_16x16x32_bf16 v[196:199],v[224:227],v[88:91],v[196:199]\n"
"ds_read_b64_tr_b16 v[220:221],v249 offset:4096\n"
"v_mfma_f32_16x16x32_bf16 v[196:199],v[228:231],v[92:95],v[196:199]\n"
"ds_read_b64_tr_b16 v[222:223],v249 offset:6144\n"
"s_waitcnt lgkmcnt(8)\n"
"v_mfma_f32_16x16x32_bf16 v[196:199],v[232:235],v[96:99],v[196:199]\n"
"ds_read_b64_tr_b16 v[224:225],v249 offset:8192\n"
"v_mfma_f32_16x16x32_bf16 v[196:199],v[236:239],v[100:103],v[196:199]\n"
"ds_read_b64_tr_b16 v[226:227],v249 offset:10240\n"
"v_mfma_f32_16x16x32_bf16 v[192:195],v[232:235],v[24:27],v[192:195]\n"
"ds_read_b64_tr_b16 v[228:229],v249 offset:12288\n"
"buffer_load_dwordx4 v191,s[16:19],s92 offen offset:1024 lds\n"
"v_mfma_f32_16x16x32_bf16 v[192:195],v[236:239],v[28:31],v[192:195]\n"
"ds_read_b64_tr_b16 v[230:231],v249 offset:14336\n"
"s_waitcnt lgkmcnt(8)\n"
"v_mfma_f32_16x16x32_bf16 v[192:195],v[208:211],v[32:35],v[192:195]\n"
"ds_read_b64_tr_b16 v[232:233],v249 offset:16384\n"
"s_addk_i32 m0,8192\n"
"v_mfma_f32_16x16x32_bf16 v[192:195],v[212:215],v[36:39],v[192:195]\n"
"ds_read_b64_tr_b16 v[234:235],v249 offset:18432\n"
"v_mfma_f32_16x16x32_bf16 v[196:199],v[208:211],v[104:107],v[196:199]\n"
"ds_read_b64_tr_b16 v[236:237],v249 offset:20480\n"
"v_mfma_f32_16x16x32_bf16 v[196:199],v[212:215],v[108:111],v[196:199]\n"
"ds_read_b64_tr_b16 v[238:239],v249 offset:22528\n"
"s_waitcnt lgkmcnt(8)\n"
"v_mfma_f32_16x16x32_bf16 v[200:203],v[216:219],v[0:3],v[244:247]\n"
"ds_read_b64_tr_b16 v[208:209],v249 offset:24576\n"
"v_mfma_f32_16x16x32_bf16 v[200:203],v[220:223],v[4:7],v[200:203]\n"
"ds_read_b64_tr_b16 v[210:211],v249 offset:26624\n"
"buffer_load_dwordx4 v250,s[16:19],s90 offen lds\n"
"v_mfma_f32_16x16x32_bf16 v[204:207],v[216:219],v[72:75],v[244:247]\n"
"ds_read_b64_tr_b16 v[212:213],v249 offset:28672\n"
"v_mfma_f32_16x16x32_bf16 v[204:207],v[220:223],v[76:79],v[204:207]\n"
"ds_read_b64_tr_b16 v[214:215],v249 offset:30720\n"
"s_waitcnt lgkmcnt(8)\n"
"v_mfma_f32_16x16x32_bf16 v[204:207],v[224:227],v[80:83],v[204:207]\n"
"ds_read_b64_tr_b16 v[216:217],v249 offset:32768\n"
"v_mfma_f32_16x16x32_bf16 v[204:207],v[228:231],v[84:87],v[204:207]\n"
"ds_read_b64_tr_b16 v[218:219],v249 offset:34816\n"
"v_mfma_f32_16x16x32_bf16 v[200:203],v[224:227],v[8:11],v[200:203]\n"
"ds_read_b64_tr_b16 v[220:221],v249 offset:36864\n"
"v_mfma_f32_16x16x32_bf16 v[200:203],v[228:231],v[12:15],v[200:203]\n"
"ds_read_b64_tr_b16 v[222:223],v249 offset:38912\n"
"s_waitcnt lgkmcnt(8)\n"
"v_mfma_f32_16x16x32_bf16 v[200:203],v[232:235],v[16:19],v[200:203]\n"
"ds_read_b64_tr_b16 v[224:225],v249 offset:40960\n"
"buffer_load_dwordx4 v191,s[16:19],s90 offen offset:1024 lds\n"
"v_mfma_f32_16x16x32_bf16 v[200:203],v[236:239],v[20:23],v[200:203]\n"
"ds_read_b64_tr_b16 v[226:227],v249 offset:43008\n"
"s_add_u32 s78,s84,0\n"
"v_mfma_f32_16x16x32_bf16 v[204:207],v[232:235],v[88:91],v[204:207]\n"
"ds_read_b64_tr_b16 v[228:229],v249 offset:45056\n"
"v_mfma_f32_16x16x32_bf16 v[204:207],v[236:239],v[92:95],v[204:207]\n"
"ds_read_b64_tr_b16 v[230:231],v249 offset:47104\n"
"s_waitcnt lgkmcnt(8)\n"
"v_mfma_f32_16x16x32_bf16 v[204:207],v[208:211],v[96:99],v[204:207]\n"
"ds_read_b64_tr_b16 v[232:233],v249 offset:49152\n"
"v_mfma_f32_16x16x32_bf16 v[204:207],v[212:215],v[100:103],v[204:207]\n"
"ds_read_b64_tr_b16 v[234:235],v249 offset:51200\n"
"v_mfma_f32_16x16x32_bf16 v[200:203],v[208:211],v[24:27],v[200:203]\n"
"ds_read_b64_tr_b16 v[236:237],v249 offset:53248\n"
"v_mfma_f32_16x16x32_bf16 v[200:203],v[212:215],v[28:31],v[200:203]\n"
"ds_read_b64_tr_b16 v[238:239],v249 offset:55296\n"
"s_waitcnt lgkmcnt(8)\n"
"v_mfma_f32_16x16x32_bf16 v[200:203],v[216:219],v[32:35],v[200:203]\n"
"ds_read_b64_tr_b16 v[208:209],v249 offset:57344\n"
"buffer_store_dwordx4 v[144:147],v252,s[32:35],s78 offen sc1\n"
"v_mfma_f32_16x16x32_bf16 v[200:203],v[220:223],v[36:39],v[200:203]\n"
"ds_read_b64_tr_b16 v[210:211],v249 offset:59392\n"
"v_mfma_f32_16x16x32_bf16 v[204:207],v[216:219],v[104:107],v[204:207]\n"
"ds_read_b64_tr_b16 v[212:213],v249 offset:61440\n"
"v_mfma_f32_16x16x32_bf16 v[204:207],v[220:223],v[108:111],v[204:207]\n"
"ds_read_b64_tr_b16 v[214:215],v249 offset:63488\n"
"s_waitcnt lgkmcnt(8)\n"
"v_mfma_f32_16x16x32_bf16 v[204:207],v[224:227],v[112:115],v[204:207]\n"
"v_add_u32 v249,0x10000,v249\n"
"ds_read_b64_tr_b16 v[216:217],v249\n"
"v_mfma_f32_16x16x32_bf16 v[204:207],v[228:231],v[116:119],v[204:207]\n"
"ds_read_b64_tr_b16 v[218:219],v249 offset:2048\n"
"s_setprio 2\n"
"v_mfma_f32_16x16x32_bf16 v[200:203],v[224:227],v[40:43],v[200:203]\n"
"ds_read_b64_tr_b16 v[220:221],v249 offset:4096\n"
"buffer_store_dwordx4 v[148:151],v252,s[32:35],s78 offen offset:1024 sc1\n"
"v_mfma_f32_16x16x32_bf16 v[200:203],v[228:231],v[44:47],v[200:203]\n"
"ds_read_b64_tr_b16 v[222:223],v249 offset:6144\n"
"s_waitcnt lgkmcnt(8)\n"
"v_mfma_f32_16x16x32_bf16 v[200:203],v[232:235],v[48:51],v[200:203]\n"
"ds_read_b64_tr_b16 v[224:225],v248 offset:40960\n"
"v_mov_b32 v240,-1\n"
"v_mfma_f32_16x16x32_bf16 v[200:203],v[236:239],v[52:55],v[200:203]\n"
"ds_read_b64_tr_b16 v[226:227],v248 offset:43008\n"
"v_mov_b32 v241,-1\n"
"v_mfma_f32_16x16x32_bf16 v[204:207],v[232:235],v[120:123],v[204:207]\n"
"ds_read_b64_tr_b16 v[228:229],v248 offset:45056\n"
"v_mov_b32 v242,-1\n"
"v_mfma_f32_16x16x32_bf16 v[204:207],v[236:239],v[124:127],v[204:207]\n"
"ds_read_b64_tr_b16 v[230:231],v248 offset:47104\n"
"s_waitcnt vmcnt(6)\n"
"s_waitcnt lgkmcnt(8)\n"
"v_mfma_f32_16x16x32_bf16 v[204:207],v[208:211],v[128:131],v[204:207]\n"
"ds_read_b64_tr_b16 v[232:233],v248 offset:49152\n"
"v_mov_b32 v243,-1\n"
"v_mfma_f32_16x16x32_bf16 v[204:207],v[212:215],v[132:135],v[204:207]\n"
"ds_read_b64_tr_b16 v[234:235],v248 offset:51200\n"
"s_barrier\n"
"v_mfma_f32_16x16x32_bf16 v[200:203],v[208:211],v[56:59],v[200:203]\n"
"ds_read_b64_tr_b16 v[236:237],v248 offset:53248\n"
"s_add_u32 s78,s9,2048\n"
"v_mfma_f32_16x16x32_bf16 v[200:203],v[212:215],v[60:63],v[200:203]\n"
"ds_read_b64_tr_b16 v[238:239],v248 offset:55296\n"
"s_waitcnt lgkmcnt(8)\n"
"v_mfma_f32_16x16x32_bf16 v[200:203],v[216:219],v[64:67],v[200:203]\n"
"ds_read_b64_tr_b16 v[208:209],v248 offset:57344\n"
"buffer_store_dwordx4 v[240:243],v252,s[32:35],s78 offen sc1\n"
"v_mfma_f32_16x16x32_bf16 v[200:203],v[220:223],v[68:71],v[200:203]\n"
"ds_read_b64_tr_b16 v[210:211],v248 offset:59392\n"
"s_add_u32 s78,s9,20480\n"
"v_mfma_f32_16x16x32_bf16 v[204:207],v[216:219],v[136:139],v[204:207]\n"
"ds_read_b64_tr_b16 v[212:213],v248 offset:61440\n"
"v_mfma_f32_16x16x32_bf16 v[204:207],v[220:223],v[140:143],v[204:207]\n"
"ds_read_b64_tr_b16 v[214:215],v248 offset:63488\n"
"s_waitcnt lgkmcnt(8)\n"
"v_mfma_f32_16x16x32_bf16 v[192:195],v[224:227],v[40:43],v[192:195]\n"
"v_add_u32 v248,0x10000,v248\n"
"ds_read_b64_tr_b16 v[216:217],v248\n"
"buffer_store_dwordx4 v[240:243],v252,s[32:35],s78 offen sc1\n"
"v_mfma_f32_16x16x32_bf16 v[192:195],v[228:231],v[44:47],v[192:195]\n"
"ds_read_b64_tr_b16 v[218:219],v248 offset:2048\n"
"v_mfma_f32_16x16x32_bf16 v[196:199],v[224:227],v[112:115],v[196:199]\n"
"ds_read_b64_tr_b16 v[220:221],v248 offset:4096\n"
"v_mfma_f32_16x16x32_bf16 v[196:199],v[228:231],v[116:119],v[196:199]\n"
"ds_read_b64_tr_b16 v[222:223],v248 offset:6144\n"
"s_waitcnt lgkmcnt(8)\n"
"v_mfma_f32_16x16x32_bf16 v[196:199],v[232:235],v[120:123],v[196:199]\n"
"v_mfma_f32_16x16x32_bf16 v[196:199],v[236:239],v[124:127],v[196:199]\n"
"v_mfma_f32_16x16x32_bf16 v[192:195],v[232:235],v[48:51],v[192:195]\n"
"v_mfma_f32_16x16x32_bf16 v[192:195],v[236:239],v[52:55],v[192:195]\n"
"s_waitcnt lgkmcnt(4)\n"
"v_mfma_f32_16x16x32_bf16 v[192:195],v[208:211],v[56:59],v[192:195]\n"
"v_mfma_f32_16x16x32_bf16 v[192:195],v[212:215],v[60:63],v[192:195]\n"
"v_add_u32 v248,4294828032,v248\n"
"v_mfma_f32_16x16x32_bf16 v[196:199],v[208:211],v[128:131],v[196:199]\n"
"v_mfma_f32_16x16x32_bf16 v[196:199],v[212:215],v[132:135],v[196:199]\n"
"v_add_u32 v249,4294828032,v249\n"
"s_waitcnt lgkmcnt(0)\n"
"v_mfma_f32_16x16x32_bf16 v[196:199],v[216:219],v[136:139],v[196:199]\n"
"v_mfma_f32_16x16x32_bf16 v[196:199],v[220:223],v[140:143],v[196:199]\n"
"v_mfma_f32_16x16x32_bf16 v[192:195],v[216:219],v[64:67],v[192:195]\n"
"v_mfma_f32_16x16x32_bf16 v[192:195],v[220:223],v[68:71],v[192:195]\n"
"s_setprio 0\n"
"v_fma_f32 v244,-v200,v200,s79\n"
"v_fma_f32 v245,-v201,v201,s79\n"
"v_fma_f32 v246,-v202,v202,s79\n"
"v_fma_f32 v247,-v203,v203,s79\n"
"v_mul_f32 v244,v244,v200\n"
"v_mul_f32 v245,v245,v201\n"
"v_mul_f32 v246,v246,v202\n"
"v_mul_f32 v247,v247,v203\n"
"v_exp_f32 v244,v244\n"
"v_exp_f32 v245,v245\n"
"v_exp_f32 v246,v246\n"
"v_exp_f32 v247,v247\n"
"v_add_f32 v244,1.0,v244\n"
"v_add_f32 v245,1.0,v245\n"
"v_add_f32 v246,1.0,v246\n"
"v_add_f32 v247,1.0,v247\n"
"v_rcp_f32 v244,v244\n"
"v_rcp_f32 v245,v245\n"
"v_rcp_f32 v246,v246\n"
"v_rcp_f32 v247,v247\n"
"v_mul_f32 v244,v244,v200\n"
"v_mul_f32 v245,v245,v201\n"
"v_mul_f32 v246,v246,v202\n"
"v_mul_f32 v247,v247,v203\n"
"v_cvt_pk_bf16_f32 v154,v244,v245\n"
"v_cvt_pk_bf16_f32 v155,v246,v247\n"
"v_fma_f32 v240,-v192,v192,s79\n"
"v_fma_f32 v241,-v193,v193,s79\n"
"v_fma_f32 v242,-v194,v194,s79\n"
"v_fma_f32 v243,-v195,v195,s79\n"
"v_mul_f32 v240,v240,v192\n"
"v_mul_f32 v241,v241,v193\n"
"v_mul_f32 v242,v242,v194\n"
"v_mul_f32 v243,v243,v195\n"
"v_exp_f32 v240,v240\n"
"s_waitcnt vmcnt(4)\n"
"s_barrier\n"
"v_exp_f32 v241,v241\n"
"v_exp_f32 v242,v242\n"
"s_add_u32 s90,s80,384\n"
"s_addk_i32 m0,8192\n"
"v_exp_f32 v243,v243\n"
"buffer_load_dwordx4 v250,s[16:19],s90 offen lds\n"
"v_add_f32 v240,1.0,v240\n"
"v_add_f32 v241,1.0,v241\n"
"v_add_f32 v242,1.0,v242\n"
"v_add_f32 v243,1.0,v243\n"
"v_rcp_f32 v240,v240\n"
"v_rcp_f32 v241,v241\n"
"v_rcp_f32 v242,v242\n"
"v_rcp_f32 v243,v243\n"
"v_mul_f32 v240,v240,v192\n"
"v_mul_f32 v241,v241,v193\n"
"v_mul_f32 v242,v242,v194\n"
"v_mul_f32 v243,v243,v195\n"
"v_cvt_pk_bf16_f32 v152,v240,v241\n"
"v_cvt_pk_bf16_f32 v153,v242,v243\n"
"v_fma_f32 v240,-v196,v196,s79\n"
"v_fma_f32 v241,-v197,v197,s79\n"
"v_fma_f32 v242,-v198,v198,s79\n"
"v_fma_f32 v243,-v199,v199,s79\n"
"v_mul_f32 v240,v240,v196\n"
"v_mul_f32 v241,v241,v197\n"
"v_mul_f32 v242,v242,v198\n"
"v_mul_f32 v243,v243,v199\n"
"v_exp_f32 v240,v240\n"
"v_exp_f32 v241,v241\n"
"v_exp_f32 v242,v242\n"
"v_exp_f32 v243,v243\n"
"v_add_f32 v240,1.0,v240\n"
"v_add_f32 v241,1.0,v241\n"
"buffer_load_dwordx4 v191,s[16:19],s90 offen offset:1024 lds\n"
"v_add_f32 v242,1.0,v242\n"
"v_add_f32 v243,1.0,v243\n"
"v_rcp_f32 v240,v240\n"
"v_rcp_f32 v241,v241\n"
"v_rcp_f32 v242,v242\n"
"v_rcp_f32 v243,v243\n"
"v_mul_f32 v240,v240,v196\n"
"v_mul_f32 v241,v241,v197\n"
"v_mul_f32 v242,v242,v198\n"
"v_mul_f32 v243,v243,v199\n"
"v_cvt_pk_bf16_f32 v156,v240,v241\n"
"v_cvt_pk_bf16_f32 v157,v242,v243\n"
"v_fma_f32 v244,-v204,v204,s79\n"
"v_fma_f32 v245,-v205,v205,s79\n"
"v_fma_f32 v246,-v206,v206,s79\n"
"v_fma_f32 v247,-v207,v207,s79\n"
"v_mul_f32 v244,v244,v204\n"
"v_mul_f32 v245,v245,v205\n"
"v_mul_f32 v246,v246,v206\n"
"v_mul_f32 v247,v247,v207\n"
"v_exp_f32 v244,v244\n"
"v_exp_f32 v245,v245\n"
"v_exp_f32 v246,v246\n"
"v_exp_f32 v247,v247\n"
"v_add_f32 v244,1.0,v244\n"
"v_add_f32 v245,1.0,v245\n"
"v_add_f32 v246,1.0,v246\n"
"v_add_f32 v247,1.0,v247\n"
"v_rcp_f32 v244,v244\n"
"s_setprio 3\n"
"v_rcp_f32 v245,v245\n"
"ds_read_b128 v[240:243],v253 offset:512\n"
"ds_read_b64_tr_b16 v[208:209],v248\n"
"v_rcp_f32 v246,v246\n"
"ds_read_b64_tr_b16 v[210:211],v248 offset:2048\n"
"ds_read_b64_tr_b16 v[212:213],v248 offset:4096\n"
"v_rcp_f32 v247,v247\n"
"ds_read_b64_tr_b16 v[214:215],v248 offset:6144\n"
"ds_read_b64_tr_b16 v[216:217],v248 offset:8192\n"
"v_mul_f32 v244,v244,v204\n"
"v_mul_f32 v245,v245,v205\n"
"ds_read_b64_tr_b16 v[218:219],v248 offset:10240\n"
"ds_read_b64_tr_b16 v[220:221],v248 offset:12288\n"
"v_mul_f32 v246,v246,v206\n"
"v_mul_f32 v247,v247,v207\n"
"ds_read_b64_tr_b16 v[222:223],v248 offset:14336\n"
"ds_read_b64_tr_b16 v[224:225],v248 offset:16384\n"
"v_cvt_pk_bf16_f32 v158,v244,v245\n"
"v_cvt_pk_bf16_f32 v159,v246,v247\n"
"ds_read_b64_tr_b16 v[226:227],v248 offset:18432\n"
"ds_read_b64_tr_b16 v[228:229],v248 offset:20480\n"
"ds_read_b64_tr_b16 v[230:231],v248 offset:22528\n"
"s_setprio 1\n"
"s_waitcnt lgkmcnt(12)\n"
"s_waitcnt lgkmcnt(8)\n"
"v_mfma_f32_16x16x32_bf16 v[192:195],v[208:211],v[0:3],v[240:243]\n"
"ds_read_b64_tr_b16 v[232:233],v248 offset:24576\n"
"ds_read_b128 v[244:247],v253 offset:576\n"
"v_mfma_f32_16x16x32_bf16 v[192:195],v[212:215],v[4:7],v[192:195]\n"
"ds_read_b64_tr_b16 v[234:235],v248 offset:26624\n"
"v_mfma_f32_16x16x32_bf16 v[196:199],v[208:211],v[72:75],v[240:243]\n"
"ds_read_b64_tr_b16 v[236:237],v248 offset:28672\n"
"v_mfma_f32_16x16x32_bf16 v[196:199],v[212:215],v[76:79],v[196:199]\n"
"ds_read_b64_tr_b16 v[238:239],v248 offset:30720\n"
"s_waitcnt lgkmcnt(9)\n"
"v_mfma_f32_16x16x32_bf16 v[196:199],v[216:219],v[80:83],v[196:199]\n"
"ds_read_b64_tr_b16 v[208:209],v248 offset:32768\n"
"v_mfma_f32_16x16x32_bf16 v[196:199],v[220:223],v[84:87],v[196:199]\n"
"ds_read_b64_tr_b16 v[210:211],v248 offset:34816\n"
"v_mfma_f32_16x16x32_bf16 v[192:195],v[216:219],v[8:11],v[192:195]\n"
"ds_read_b64_tr_b16 v[212:213],v248 offset:36864\n"
"buffer_load_dword v179,v177,s[32:35],0 offen sc1\n"
"v_mfma_f32_16x16x32_bf16 v[192:195],v[220:223],v[12:15],v[192:195]\n"
"ds_read_b64_tr_b16 v[214:215],v248 offset:38912\n"
"s_waitcnt lgkmcnt(9)\n"
"v_mfma_f32_16x16x32_bf16 v[192:195],v[224:227],v[16:19],v[192:195]\n"
"ds_read_b64_tr_b16 v[216:217],v249\n"
"s_add_u32 s92,s80,4129152\n"
"v_mfma_f32_16x16x32_bf16 v[192:195],v[228:231],v[20:23],v[192:195]\n"
"ds_read_b64_tr_b16 v[218:219],v249 offset:2048\n"
"s_add_u32 m0,s91,131072\n"
"v_mfma_f32_16x16x32_bf16 v[196:199],v[224:227],v[88:91],v[196:199]\n"
"ds_read_b64_tr_b16 v[220:221],v249 offset:4096\n"
"v_mfma_f32_16x16x32_bf16 v[196:199],v[228:231],v[92:95],v[196:199]\n"
"ds_read_b64_tr_b16 v[222:223],v249 offset:6144\n"
"s_waitcnt lgkmcnt(8)\n"
"v_mfma_f32_16x16x32_bf16 v[196:199],v[232:235],v[96:99],v[196:199]\n"
"ds_read_b64_tr_b16 v[224:225],v249 offset:8192\n"
"buffer_load_dwordx4 v250,s[16:19],s92 offen lds\n"
"v_mfma_f32_16x16x32_bf16 v[196:199],v[236:239],v[100:103],v[196:199]\n"
"ds_read_b64_tr_b16 v[226:227],v249 offset:10240\n"
"s_add_u32 s90,s92,s93\n"
"v_mfma_f32_16x16x32_bf16 v[192:195],v[232:235],v[24:27],v[192:195]\n"
"ds_read_b64_tr_b16 v[228:229],v249 offset:12288\n"
"v_mfma_f32_16x16x32_bf16 v[192:195],v[236:239],v[28:31],v[192:195]\n"
"ds_read_b64_tr_b16 v[230:231],v249 offset:14336\n"
"s_waitcnt lgkmcnt(8)\n"
"v_mfma_f32_16x16x32_bf16 v[192:195],v[208:211],v[32:35],v[192:195]\n"
"ds_read_b64_tr_b16 v[232:233],v249 offset:16384\n"
"v_mfma_f32_16x16x32_bf16 v[192:195],v[212:215],v[36:39],v[192:195]\n"
"ds_read_b64_tr_b16 v[234:235],v249 offset:18432\n"
"v_mfma_f32_16x16x32_bf16 v[196:199],v[208:211],v[104:107],v[196:199]\n"
"ds_read_b64_tr_b16 v[236:237],v249 offset:20480\n"
"v_mfma_f32_16x16x32_bf16 v[196:199],v[212:215],v[108:111],v[196:199]\n"
"ds_read_b64_tr_b16 v[238:239],v249 offset:22528\n"
"s_waitcnt lgkmcnt(8)\n"
"v_mfma_f32_16x16x32_bf16 v[200:203],v[216:219],v[0:3],v[244:247]\n"
"ds_read_b64_tr_b16 v[208:209],v249 offset:24576\n"
"buffer_load_dwordx4 v191,s[16:19],s92 offen offset:1024 lds\n"
"v_mfma_f32_16x16x32_bf16 v[200:203],v[220:223],v[4:7],v[200:203]\n"
"ds_read_b64_tr_b16 v[210:211],v249 offset:26624\n"
"s_addk_i32 m0,8192\n"
"v_mfma_f32_16x16x32_bf16 v[204:207],v[216:219],v[72:75],v[244:247]\n"
"ds_read_b64_tr_b16 v[212:213],v249 offset:28672\n"
"v_mfma_f32_16x16x32_bf16 v[204:207],v[220:223],v[76:79],v[204:207]\n"
"ds_read_b64_tr_b16 v[214:215],v249 offset:30720\n"
"s_waitcnt lgkmcnt(8)\n"
"v_mfma_f32_16x16x32_bf16 v[204:207],v[224:227],v[80:83],v[204:207]\n"
"ds_read_b64_tr_b16 v[216:217],v249 offset:32768\n"
"v_mfma_f32_16x16x32_bf16 v[204:207],v[228:231],v[84:87],v[204:207]\n"
"ds_read_b64_tr_b16 v[218:219],v249 offset:34816\n"
"buffer_load_dwordx4 v250,s[16:19],s90 offen lds\n"
"v_mfma_f32_16x16x32_bf16 v[200:203],v[224:227],v[8:11],v[200:203]\n"
"ds_read_b64_tr_b16 v[220:221],v249 offset:36864\n"
"v_mfma_f32_16x16x32_bf16 v[200:203],v[228:231],v[12:15],v[200:203]\n"
"ds_read_b64_tr_b16 v[222:223],v249 offset:38912\n"
"s_waitcnt lgkmcnt(8)\n"
"v_mfma_f32_16x16x32_bf16 v[200:203],v[232:235],v[16:19],v[200:203]\n"
"ds_read_b64_tr_b16 v[224:225],v249 offset:40960\n"
"v_mfma_f32_16x16x32_bf16 v[200:203],v[236:239],v[20:23],v[200:203]\n"
"ds_read_b64_tr_b16 v[226:227],v249 offset:43008\n"
"v_mfma_f32_16x16x32_bf16 v[204:207],v[232:235],v[88:91],v[204:207]\n"
"ds_read_b64_tr_b16 v[228:229],v249 offset:45056\n"
"v_mfma_f32_16x16x32_bf16 v[204:207],v[236:239],v[92:95],v[204:207]\n"
"ds_read_b64_tr_b16 v[230:231],v249 offset:47104\n"
"s_waitcnt lgkmcnt(8)\n"
"v_mfma_f32_16x16x32_bf16 v[204:207],v[208:211],v[96:99],v[204:207]\n"
"ds_read_b64_tr_b16 v[232:233],v249 offset:49152\n"
"buffer_load_dwordx4 v191,s[16:19],s90 offen offset:1024 lds\n"
"v_mfma_f32_16x16x32_bf16 v[204:207],v[212:215],v[100:103],v[204:207]\n"
"ds_read_b64_tr_b16 v[234:235],v249 offset:51200\n"
"s_add_u32 s78,s84,2048\n"
"v_mfma_f32_16x16x32_bf16 v[200:203],v[208:211],v[24:27],v[200:203]\n"
"ds_read_b64_tr_b16 v[236:237],v249 offset:53248\n"
"v_mfma_f32_16x16x32_bf16 v[200:203],v[212:215],v[28:31],v[200:203]\n"
"ds_read_b64_tr_b16 v[238:239],v249 offset:55296\n"
"s_waitcnt lgkmcnt(8)\n"
"v_mfma_f32_16x16x32_bf16 v[200:203],v[216:219],v[32:35],v[200:203]\n"
"ds_read_b64_tr_b16 v[208:209],v249 offset:57344\n"
"v_mfma_f32_16x16x32_bf16 v[200:203],v[220:223],v[36:39],v[200:203]\n"
"ds_read_b64_tr_b16 v[210:211],v249 offset:59392\n"
".Lk_padm4:\n"
"s_cbranch_execz .Lk_hop_retm\n"
"v_mfma_f32_16x16x32_bf16 v[204:207],v[216:219],v[104:107],v[204:207]\n"
"ds_read_b64_tr_b16 v[212:213],v249 offset:61440\n"
"buffer_store_dwordx4 v[152:155],v252,s[32:35],s78 offen sc1\n"
"v_mfma_f32_16x16x32_bf16 v[204:207],v[220:223],v[108:111],v[204:207]\n"
"ds_read_b64_tr_b16 v[214:215],v249 offset:63488\n"
"s_waitcnt lgkmcnt(8)\n"
"v_mfma_f32_16x16x32_bf16 v[204:207],v[224:227],v[112:115],v[204:207]\n"
"v_add_u32 v249,0x10000,v249\n"
"ds_read_b64_tr_b16 v[216:217],v249\n"
"v_mfma_f32_16x16x32_bf16 v[204:207],v[228:231],v[116:119],v[204:207]\n"
"ds_read_b64_tr_b16 v[218:219],v249 offset:2048\n"
"s_setprio 2\n"
"v_mfma_f32_16x16x32_bf16 v[200:203],v[224:227],v[40:43],v[200:203]\n"
"ds_read_b64_tr_b16 v[220:221],v249 offset:4096\n"
"v_mfma_f32_16x16x32_bf16 v[200:203],v[228:231],v[44:47],v[200:203]\n"
"ds_read_b64_tr_b16 v[222:223],v249 offset:6144\n"
"s_waitcnt lgkmcnt(8)\n"
"v_mfma_f32_16x16x32_bf16 v[200:203],v[232:235],v[48:51],v[200:203]\n"
"ds_read_b64_tr_b16 v[224:225],v248 offset:40960\n"
"buffer_store_dwordx4 v[156:159],v252,s[32:35],s78 offen offset:1024 sc1\n"
"v_mfma_f32_16x16x32_bf16 v[200:203],v[236:239],v[52:55],v[200:203]\n"
"ds_read_b64_tr_b16 v[226:227],v248 offset:43008\n"
"v_mov_b32 v240,-1\n"
"v_mfma_f32_16x16x32_bf16 v[204:207],v[232:235],v[120:123],v[204:207]\n"
"ds_read_b64_tr_b16 v[228:229],v248 offset:45056\n"
"v_mov_b32 v241,-1\n"
"v_mfma_f32_16x16x32_bf16 v[204:207],v[236:239],v[124:127],v[204:207]\n"
"ds_read_b64_tr_b16 v[230:231],v248 offset:47104\n"
"s_waitcnt vmcnt(7)\n"
"s_waitcnt lgkmcnt(8)\n"
"v_mfma_f32_16x16x32_bf16 v[204:207],v[208:211],v[128:131],v[204:207]\n"
"ds_read_b64_tr_b16 v[232:233],v248 offset:49152\n"
"v_mov_b32 v242,-1\n"
"v_mfma_f32_16x16x32_bf16 v[204:207],v[212:215],v[132:135],v[204:207]\n"
"ds_read_b64_tr_b16 v[234:235],v248 offset:51200\n"
"s_barrier\n"
"v_mfma_f32_16x16x32_bf16 v[200:203],v[208:211],v[56:59],v[200:203]\n"
"ds_read_b64_tr_b16 v[236:237],v248 offset:53248\n"
"v_mov_b32 v243,-1\n"
"v_mfma_f32_16x16x32_bf16 v[200:203],v[212:215],v[60:63],v[200:203]\n"
"ds_read_b64_tr_b16 v[238:239],v248 offset:55296\n"
"s_waitcnt lgkmcnt(8)\n"
"v_mfma_f32_16x16x32_bf16 v[200:203],v[216:219],v[64:67],v[200:203]\n"
"ds_read_b64_tr_b16 v[208:209],v248 offset:57344\n"
"s_add_u32 s78,s9,3072\n"
"v_mfma_f32_16x16x32_bf16 v[200:203],v[220:223],v[68:71],v[200:203]\n"
"ds_read_b64_tr_b16 v[210:211],v248 offset:59392\n"
"buffer_store_dwordx4 v[240:243],v252,s[32:35],s78 offen sc1\n"
"v_mfma_f32_16x16x32_bf16 v[204:207],v[216:219],v[136:139],v[204:207]\n"
"ds_read_b64_tr_b16 v[212:213],v248 offset:61440\n"
"s_add_u32 s78,s9,21504\n"
"v_mfma_f32_16x16x32_bf16 v[204:207],v[220:223],v[140:143],v[204:207]\n"
"ds_read_b64_tr_b16 v[214:215],v248 offset:63488\n"
"s_waitcnt lgkmcnt(8)\n"
"v_mfma_f32_16x16x32_bf16 v[192:195],v[224:227],v[40:43],v[192:195]\n"
"v_add_u32 v248,0x10000,v248\n"
"ds_read_b64_tr_b16 v[216:217],v248\n"
"v_mfma_f32_16x16x32_bf16 v[192:195],v[228:231],v[44:47],v[192:195]\n"
"ds_read_b64_tr_b16 v[218:219],v248 offset:2048\n"
"buffer_store_dwordx4 v[240:243],v252,s[32:35],s78 offen sc1\n"
"v_mfma_f32_16x16x32_bf16 v[196:199],v[224:227],v[112:115],v[196:199]\n"
"ds_read_b64_tr_b16 v[220:221],v248 offset:4096\n"
"v_mfma_f32_16x16x32_bf16 v[196:199],v[228:231],v[116:119],v[196:199]\n"
"ds_read_b64_tr_b16 v[222:223],v248 offset:6144\n"
"s_waitcnt lgkmcnt(8)\n"
"v_mfma_f32_16x16x32_bf16 v[196:199],v[232:235],v[120:123],v[196:199]\n"
"v_mfma_f32_16x16x32_bf16 v[196:199],v[236:239],v[124:127],v[196:199]\n"
"v_mfma_f32_16x16x32_bf16 v[192:195],v[232:235],v[48:51],v[192:195]\n"
"v_mfma_f32_16x16x32_bf16 v[192:195],v[236:239],v[52:55],v[192:195]\n"
"s_waitcnt lgkmcnt(4)\n"
"v_mfma_f32_16x16x32_bf16 v[192:195],v[208:211],v[56:59],v[192:195]\n"
"v_mfma_f32_16x16x32_bf16 v[192:195],v[212:215],v[60:63],v[192:195]\n"
"v_add_u32 v248,8192,v248\n"
"v_mfma_f32_16x16x32_bf16 v[196:199],v[208:211],v[128:131],v[196:199]\n"
"v_mfma_f32_16x16x32_bf16 v[196:199],v[212:215],v[132:135],v[196:199]\n"
"v_add_u32 v249,8192,v249\n"
"s_waitcnt lgkmcnt(0)\n"
"v_mfma_f32_16x16x32_bf16 v[196:199],v[216:219],v[136:139],v[196:199]\n"
"v_mfma_f32_16x16x32_bf16 v[196:199],v[220:223],v[140:143],v[196:199]\n"
"v_mfma_f32_16x16x32_bf16 v[192:195],v[216:219],v[64:67],v[192:195]\n"
"v_mfma_f32_16x16x32_bf16 v[192:195],v[220:223],v[68:71],v[192:195]\n"
"s_setprio 0\n"
"v_fma_f32 v244,-v200,v200,s79\n"
"v_fma_f32 v245,-v201,v201,s79\n"
"v_fma_f32 v246,-v202,v202,s79\n"
"v_fma_f32 v247,-v203,v203,s79\n"
"v_mul_f32 v244,v244,v200\n"
"v_mul_f32 v245,v245,v201\n"
"v_mul_f32 v246,v246,v202\n"
"v_mul_f32 v247,v247,v203\n"
"v_exp_f32 v244,v244\n"
"v_exp_f32 v245,v245\n"
"v_exp_f32 v246,v246\n"
"v_exp_f32 v247,v247\n"
"v_add_f32 v244,1.0,v244\n"
"v_add_f32 v245,1.0,v245\n"
"v_add_f32 v246,1.0,v246\n"
"v_add_f32 v247,1.0,v247\n"
"v_rcp_f32 v244,v244\n"
"v_rcp_f32 v245,v245\n"
"v_rcp_f32 v246,v246\n"
"v_rcp_f32 v247,v247\n"
"v_mul_f32 v244,v244,v200\n"
"v_mul_f32 v245,v245,v201\n"
"v_mul_f32 v246,v246,v202\n"
"v_mul_f32 v247,v247,v203\n"
"v_cvt_pk_bf16_f32 v162,v244,v245\n"
"v_cvt_pk_bf16_f32 v163,v246,v247\n"
"v_fma_f32 v240,-v192,v192,s79\n"
"v_fma_f32 v241,-v193,v193,s79\n"
"v_fma_f32 v242,-v194,v194,s79\n"
"v_fma_f32 v243,-v195,v195,s79\n"
"v_mul_f32 v240,v240,v192\n"
"v_mul_f32 v241,v241,v193\n"
"v_mul_f32 v242,v242,v194\n"
"v_mul_f32 v243,v243,v195\n"
"v_exp_f32 v240,v240\n"
"s_waitcnt vmcnt(4)\n"
"s_barrier\n"
"v_exp_f32 v241,v241\n"
"v_exp_f32 v242,v242\n"
"s_add_u32 s90,s80,512\n"
"s_add_u32 m0,s91,0\n"
"v_exp_f32 v243,v243\n"
"buffer_load_dwordx4 v250,s[16:19],s90 offen lds\n"
"v_add_f32 v240,1.0,v240\n"
"v_add_f32 v241,1.0,v241\n"
"v_add_f32 v242,1.0,v242\n"
"v_add_f32 v243,1.0,v243\n"
"v_rcp_f32 v240,v240\n"
"v_rcp_f32 v241,v241\n"
"v_rcp_f32 v242,v242\n"
"v_rcp_f32 v243,v243\n"
"v_mul_f32 v240,v240,v192\n"
"v_mul_f32 v241,v241,v193\n"
"v_mul_f32 v242,v242,v194\n"
"v_mul_f32 v243,v243,v195\n"
"v_cvt_pk_bf16_f32 v160,v240,v241\n"
"v_cvt_pk_bf16_f32 v161,v242,v243\n"
"v_fma_f32 v240,-v196,v196,s79\n"
"v_fma_f32 v241,-v197,v197,s79\n"
"v_fma_f32 v242,-v198,v198,s79\n"
"v_fma_f32 v243,-v199,v199,s79\n"
"v_mul_f32 v240,v240,v196\n"
"v_mul_f32 v241,v241,v197\n"
"v_mul_f32 v242,v242,v198\n"
"v_mul_f32 v243,v243,v199\n"
"v_exp_f32 v240,v240\n"
"v_exp_f32 v241,v241\n"
"v_exp_f32 v242,v242\n"
"v_exp_f32 v243,v243\n"
"v_add_f32 v240,1.0,v240\n"
"v_add_f32 v241,1.0,v241\n"
"buffer_load_dwordx4 v191,s[16:19],s90 offen offset:1024 lds\n"
"v_add_f32 v242,1.0,v242\n"
"v_add_f32 v243,1.0,v243\n"
"v_rcp_f32 v240,v240\n"
"v_rcp_f32 v241,v241\n"
"v_rcp_f32 v242,v242\n"
"v_rcp_f32 v243,v243\n"
"v_mul_f32 v240,v240,v196\n"
"v_mul_f32 v241,v241,v197\n"
"v_mul_f32 v242,v242,v198\n"
"v_mul_f32 v243,v243,v199\n"
"v_cvt_pk_bf16_f32 v164,v240,v241\n"
"v_cvt_pk_bf16_f32 v165,v242,v243\n"
"v_fma_f32 v244,-v204,v204,s79\n"
"v_fma_f32 v245,-v205,v205,s79\n"
"v_fma_f32 v246,-v206,v206,s79\n"
"v_fma_f32 v247,-v207,v207,s79\n"
"v_mul_f32 v244,v244,v204\n"
"v_mul_f32 v245,v245,v205\n"
"v_mul_f32 v246,v246,v206\n"
"v_mul_f32 v247,v247,v207\n"
"v_exp_f32 v244,v244\n"
"s_cmp_lg_u32 s11,4\n"
"s_cbranch_scc1 .Lk_he2m\n"
"s_mov_b32 s89,0x100000\n"
".Lk_he_spinm:\n"
"v_cmp_ne_u32 vcc,v179,v176\n"
"s_mul_i32 s96,s69,3\n"
"s_add_u32 s96,s96,s70\n"
"s_lshl_b32 s96,1,s96\n"
"s_mov_b32 s97,0\n"
"s_lshl_b64 s[96:97],s[96:97],16\n"
"s_mov_b32 s77,-1\n"
"s_cmp_eq_u32 vcc_lo,0xffff\n"
"s_cselect_b32 s96,s96,0\n"
"s_min_u32 s77,s77,vcc_lo\n"
"s_cmp_eq_u64 s[96:97],0\n"
"s_cbranch_scc1 .Lk_he_nsm\n"
"s_mov_b64 exec,s[96:97]\n"
"buffer_store_dword v178,v177,s[32:35],0 offen sc1\n"
"s_mov_b64 exec,-1\n"
".Lk_he_nsm:\n"
"s_cmp_ge_u32 s77,0xffff\n"
"s_cbranch_scc1 .Lk_he2m\n"
"s_sub_u32 s89,s89,1\n"
"s_cbranch_scc1 .Lk_he2m\n"
"s_sleep 2\n"
"buffer_load_dword v179,v177,s[32:35],0 offen sc1\n"
"s_waitcnt vmcnt(0)\n"
"s_branch .Lk_he_spinm\n"
".Lk_he2m:\n"
"v_exp_f32 v245,v245\n"
"v_exp_f32 v246,v246\n"
"v_exp_f32 v247,v247\n"
"v_add_f32 v244,1.0,v244\n"
"v_add_f32 v245,1.0,v245\n"
"v_add_f32 v246,1.0,v246\n"
"v_add_f32 v247,1.0,v247\n"
"v_rcp_f32 v244,v244\n"
"s_setprio 3\n"
"v_rcp_f32 v245,v245\n"
"ds_read_b128 v[240:243],v253 offset:768\n"
"ds_read_b64_tr_b16 v[208:209],v248\n"
"v_rcp_f32 v246,v246\n"
"ds_read_b64_tr_b16 v[210:211],v248 offset:2048\n"
"ds_read_b64_tr_b16 v[212:213],v248 offset:4096\n"
"v_rcp_f32 v247,v247\n"
"ds_read_b64_tr_b16 v[214:215],v248 offset:6144\n"
"ds_read_b64_tr_b16 v[216:217],v248 offset:8192\n"
"v_mul_f32 v244,v244,v204\n"
"v_mul_f32 v245,v245,v205\n"
"ds_read_b64_tr_b16 v[218:219],v248 offset:10240\n"
"ds_read_b64_tr_b16 v[220:221],v248 offset:12288\n"
"v_mul_f32 v246,v246,v206\n"
"v_mul_f32 v247,v247,v207\n"
"ds_read_b64_tr_b16 v[222:223],v248 offset:14336\n"
"ds_read_b64_tr_b16 v[224:225],v248 offset:16384\n"
"v_cvt_pk_bf16_f32 v166,v244,v245\n"
"v_cvt_pk_bf16_f32 v167,v246,v247\n"
"ds_read_b64_tr_b16 v[226:227],v248 offset:18432\n"
"ds_read_b64_tr_b16 v[228:229],v248 offset:20480\n"
"ds_read_b64_tr_b16 v[230:231],v248 offset:22528\n"
"s_setprio 1\n"
"s_waitcnt lgkmcnt(12)\n"
"s_waitcnt lgkmcnt(8)\n"
"v_mfma_f32_16x16x32_bf16 v[192:195],v[208:211],v[0:3],v[240:243]\n"
"ds_read_b64_tr_b16 v[232:233],v248 offset:24576\n"
"ds_read_b128 v[244:247],v253 offset:832\n"
"v_mfma_f32_16x16x32_bf16 v[192:195],v[212:215],v[4:7],v[192:195]\n"
"ds_read_b64_tr_b16 v[234:235],v248 offset:26624\n"
"s_add_u32 s92,s80,4129280\n"
"v_mfma_f32_16x16x32_bf16 v[196:199],v[208:211],v[72:75],v[240:243]\n"
"ds_read_b64_tr_b16 v[236:237],v248 offset:28672\n"
"s_add_u32 m0,s91,57344\n"
"v_mfma_f32_16x16x32_bf16 v[196:199],v[212:215],v[76:79],v[196:199]\n"
"ds_read_b64_tr_b16 v[238:239],v248 offset:30720\n"
"s_waitcnt lgkmcnt(9)\n"
"v_mfma_f32_16x16x32_bf16 v[196:199],v[216:219],v[80:83],v[196:199]\n"
"ds_read_b64_tr_b16 v[208:209],v248 offset:32768\n"
"v_mfma_f32_16x16x32_bf16 v[196:199],v[220:223],v[84:87],v[196:199]\n"
"ds_read_b64_tr_b16 v[210:211],v248 offset:34816\n"
"v_mfma_f32_16x16x32_bf16 v[192:195],v[216:219],v[8:11],v[192:195]\n"
"ds_read_b64_tr_b16 v[212:213],v248 offset:36864\n"
"v_mfma_f32_16x16x32_bf16 v[192:195],v[220:223],v[12:15],v[192:195]\n"
"ds_read_b64_tr_b16 v[214:215],v248 offset:38912\n"
"s_waitcnt lgkmcnt(9)\n"
"v_mfma_f32_16x16x32_bf16 v[192:195],v[224:227],v[16:19],v[192:195]\n"
"ds_read_b64_tr_b16 v[216:217],v249\n"
"buffer_load_dwordx4 v250,s[16:19],s92 offen lds\n"
"v_mfma_f32_16x16x32_bf16 v[192:195],v[228:231],v[20:23],v[192:195]\n"
"ds_read_b64_tr_b16 v[218:219],v249 offset:2048\n"
"s_add_u32 s90,s92,s93\n"
"v_mfma_f32_16x16x32_bf16 v[196:199],v[224:227],v[88:91],v[196:199]\n"
"ds_read_b64_tr_b16 v[220:221],v249 offset:4096\n"
"v_mfma_f32_16x16x32_bf16 v[196:199],v[228:231],v[92:95],v[196:199]\n"
"ds_read_b64_tr_b16 v[222:223],v249 offset:6144\n"
"s_waitcnt lgkmcnt(8)\n"
"v_mfma_f32_16x16x32_bf16 v[196:199],v[232:235],v[96:99],v[196:199]\n"
"ds_read_b64_tr_b16 v[224:225],v249 offset:8192\n"
"v_mfma_f32_16x16x32_bf16 v[196:199],v[236:239],v[100:103],v[196:199]\n"
"ds_read_b64_tr_b16 v[226:227],v249 offset:10240\n"
"v_mfma_f32_16x16x32_bf16 v[192:195],v[232:235],v[24:27],v[192:195]\n"
"ds_read_b64_tr_b16 v[228:229],v249 offset:12288\n"
"buffer_load_dwordx4 v191,s[16:19],s92 offen offset:1024 lds\n"
"v_mfma_f32_16x16x32_bf16 v[192:195],v[236:239],v[28:31],v[192:195]\n"
"ds_read_b64_tr_b16 v[230:231],v249 offset:14336\n"
"s_waitcnt lgkmcnt(8)\n"
"v_mfma_f32_16x16x32_bf16 v[192:195],v[208:211],v[32:35],v[192:195]\n"
"ds_read_b64_tr_b16 v[232:233],v249 offset:16384\n"
"s_addk_i32 m0,8192\n"
"v_mfma_f32_16x16x32_bf16 v[192:195],v[212:215],v[36:39],v[192:195]\n"
"ds_read_b64_tr_b16 v[234:235],v249 offset:18432\n"
"v_mfma_f32_16x16x32_bf16 v[196:199],v[208:211],v[104:107],v[196:199]\n"
"ds_read_b64_tr_b16 v[236:237],v249 offset:20480\n"
"v_mfma_f32_16x16x32_bf16 v[196:199],v[212:215],v[108:111],v[196:199]\n"
"ds_read_b64_tr_b16 v[238:239],v249 offset:22528\n"
"s_waitcnt lgkmcnt(8)\n"
"v_mfma_f32_16x16x32_bf16 v[200:203],v[216:219],v[0:3],v[244:247]\n"
"ds_read_b64_tr_b16 v[208:209],v249 offset:24576\n"
"v_mfma_f32_16x16x32_bf16 v[200:203],v[220:223],v[4:7],v[200:203]\n"
"ds_read_b64_tr_b16 v[210:211],v249 offset:26624\n"
"buffer_load_dwordx4 v250,s[16:19],s90 offen lds\n"
"v_mfma_f32_16x16x32_bf16 v[204:207],v[216:219],v[72:75],v[244:247]\n"
"ds_read_b64_tr_b16 v[212:213],v249 offset:28672\n"
"v_mfma_f32_16x16x32_bf16 v[204:207],v[220:223],v[76:79],v[204:207]\n"
"ds_read_b64_tr_b16 v[214:215],v249 offset:30720\n"
"s_waitcnt lgkmcnt(8)\n"
"v_mfma_f32_16x16x32_bf16 v[204:207],v[224:227],v[80:83],v[204:207]\n"
"ds_read_b64_tr_b16 v[216:217],v249 offset:32768\n"
"v_mfma_f32_16x16x32_bf16 v[204:207],v[228:231],v[84:87],v[204:207]\n"
"ds_read_b64_tr_b16 v[218:219],v249 offset:34816\n"
"v_mfma_f32_16x16x32_bf16 v[200:203],v[224:227],v[8:11],v[200:203]\n"
"ds_read_b64_tr_b16 v[220:221],v249 offset:36864\n"
"v_mfma_f32_16x16x32_bf16 v[200:203],v[228:231],v[12:15],v[200:203]\n"
"ds_read_b64_tr_b16 v[222:223],v249 offset:38912\n"
"s_waitcnt lgkmcnt(8)\n"
"v_mfma_f32_16x16x32_bf16 v[200:203],v[232:235],v[16:19],v[200:203]\n"
"ds_read_b64_tr_b16 v[224:225],v249 offset:40960\n"
"buffer_load_dwordx4 v191,s[16:19],s90 offen offset:1024 lds\n"
"v_mfma_f32_16x16x32_bf16 v[200:203],v[236:239],v[20:23],v[200:203]\n"
"ds_read_b64_tr_b16 v[226:227],v249 offset:43008\n"
"s_add_u32 s78,s84,4096\n"
"v_mfma_f32_16x16x32_bf16 v[204:207],v[232:235],v[88:91],v[204:207]\n"
"ds_read_b64_tr_b16 v[228:229],v249 offset:45056\n"
"v_mfma_f32_16x16x32_bf16 v[204:207],v[236:239],v[92:95],v[204:207]\n"
"ds_read_b64_tr_b16 v[230:231],v249 offset:47104\n"
"s_waitcnt lgkmcnt(8)\n"
"v_mfma_f32_16x16x32_bf16 v[204:207],v[208:211],v[96:99],v[204:207]\n"
"ds_read_b64_tr_b16 v[232:233],v249 offset:49152\n"
"v_mfma_f32_16x16x32_bf16 v[204:207],v[212:215],v[100:103],v[204:207]\n"
"ds_read_b64_tr_b16 v[234:235],v249 offset:51200\n"
"v_mfma_f32_16x16x32_bf16 v[200:203],v[208:211],v[24:27],v[200:203]\n"
"ds_read_b64_tr_b16 v[236:237],v249 offset:53248\n"
"v_mfma_f32_16x16x32_bf16 v[200:203],v[212:215],v[28:31],v[200:203]\n"
"ds_read_b64_tr_b16 v[238:239],v249 offset:55296\n"
"s_waitcnt lgkmcnt(8)\n"
"v_mfma_f32_16x16x32_bf16 v[200:203],v[216:219],v[32:35],v[200:203]\n"
"ds_read_b64_tr_b16 v[208:209],v249 offset:57344\n"
"buffer_store_dwordx4 v[160:163],v252,s[32:35],s78 offen sc1\n"
"v_mfma_f32_16x16x32_bf16 v[200:203],v[220:223],v[36:39],v[200:203]\n"
"ds_read_b64_tr_b16 v[210:211],v249 offset:59392\n"
"v_mfma_f32_16x16x32_bf16 v[204:207],v[216:219],v[104:107],v[204:207]\n"
"ds_read_b64_tr_b16 v[212:213],v249 offset:61440\n"
"v_mfma_f32_16x16x32_bf16 v[204:207],v[220:223],v[108:111],v[204:207]\n"
"ds_read_b64_tr_b16 v[214:215],v249 offset:63488\n"
"s_waitcnt lgkmcnt(8)\n"
"v_mfma_f32_16x16x32_bf16 v[204:207],v[224:227],v[112:115],v[204:207]\n"
"v_add_u32 v249,0x10000,v249\n"
"ds_read_b64_tr_b16 v[216:217],v249\n"
"v_mfma_f32_16x16x32_bf16 v[204:207],v[228:231],v[116:119],v[204:207]\n"
"ds_read_b64_tr_b16 v[218:219],v249 offset:2048\n"
"s_setprio 2\n"
"v_mfma_f32_16x16x32_bf16 v[200:203],v[224:227],v[40:43],v[200:203]\n"
"ds_read_b64_tr_b16 v[220:221],v249 offset:4096\n"
"buffer_store_dwordx4 v[164:167],v252,s[32:35],s78 offen offset:1024 sc1\n"
"v_mfma_f32_16x16x32_bf16 v[200:203],v[228:231],v[44:47],v[200:203]\n"
"ds_read_b64_tr_b16 v[222:223],v249 offset:6144\n"
"s_waitcnt lgkmcnt(8)\n"
"v_mfma_f32_16x16x32_bf16 v[200:203],v[232:235],v[48:51],v[200:203]\n"
"ds_read_b64_tr_b16 v[224:225],v248 offset:40960\n"
"v_mov_b32 v240,-1\n"
"v_mfma_f32_16x16x32_bf16 v[200:203],v[236:239],v[52:55],v[200:203]\n"
"ds_read_b64_tr_b16 v[226:227],v248 offset:43008\n"
"v_mov_b32 v241,-1\n"
"v_mfma_f32_16x16x32_bf16 v[204:207],v[232:235],v[120:123],v[204:207]\n"
"ds_read_b64_tr_b16 v[228:229],v248 offset:45056\n"
"v_mov_b32 v242,-1\n"
"v_mfma_f32_16x16x32_bf16 v[204:207],v[236:239],v[124:127],v[204:207]\n"
"ds_read_b64_tr_b16 v[230:231],v248 offset:47104\n"
"s_waitcnt vmcnt(6)\n"
"s_waitcnt lgkmcnt(8)\n"
"v_mfma_f32_16x16x32_bf16 v[204:207],v[208:211],v[128:131],v[204:207]\n"
"ds_read_b64_tr_b16 v[232:233],v248 offset:49152\n"
"v_mov_b32 v243,-1\n"
"v_mfma_f32_16x16x32_bf16 v[204:207],v[212:215],v[132:135],v[204:207]\n"
"ds_read_b64_tr_b16 v[234:235],v248 offset:51200\n"
"s_barrier\n"
"v_mfma_f32_16x16x32_bf16 v[200:203],v[208:211],v[56:59],v[200:203]\n"
"ds_read_b64_tr_b16 v[236:237],v248 offset:53248\n"
"s_add_u32 s78,s9,4096\n"
"v_mfma_f32_16x16x32_bf16 v[200:203],v[212:215],v[60:63],v[200:203]\n"
"ds_read_b64_tr_b16 v[238:239],v248 offset:55296\n"
"s_waitcnt lgkmcnt(8)\n"
"v_mfma_f32_16x16x32_bf16 v[200:203],v[216:219],v[64:67],v[200:203]\n"
"ds_read_b64_tr_b16 v[208:209],v248 offset:57344\n"
"buffer_store_dwordx4 v[240:243],v252,s[32:35],s78 offen sc1\n"
"v_mfma_f32_16x16x32_bf16 v[200:203],v[220:223],v[68:71],v[200:203]\n"
"ds_read_b64_tr_b16 v[210:211],v248 offset:59392\n"
"s_add_u32 s78,s9,22528\n"
"v_mfma_f32_16x16x32_bf16 v[204:207],v[216:219],v[136:139],v[204:207]\n"
"ds_read_b64_tr_b16 v[212:213],v248 offset:61440\n"
"v_mfma_f32_16x16x32_bf16 v[204:207],v[220:223],v[140:143],v[204:207]\n"
"ds_read_b64_tr_b16 v[214:215],v248 offset:63488\n"
"s_waitcnt lgkmcnt(8)\n"
"v_mfma_f32_16x16x32_bf16 v[192:195],v[224:227],v[40:43],v[192:195]\n"
"v_add_u32 v248,0x10000,v248\n"
"ds_read_b64_tr_b16 v[216:217],v248\n"
"buffer_store_dwordx4 v[240:243],v252,s[32:35],s78 offen sc1\n"
"v_mfma_f32_16x16x32_bf16 v[192:195],v[228:231],v[44:47],v[192:195]\n"
"ds_read_b64_tr_b16 v[218:219],v248 offset:2048\n"
"v_mfma_f32_16x16x32_bf16 v[196:199],v[224:227],v[112:115],v[196:199]\n"
"ds_read_b64_tr_b16 v[220:221],v248 offset:4096\n"
"v_mfma_f32_16x16x32_bf16 v[196:199],v[228:231],v[116:119],v[196:199]\n"
"ds_read_b64_tr_b16 v[222:223],v248 offset:6144\n"
"s_waitcnt lgkmcnt(8)\n"
"v_mfma_f32_16x16x32_bf16 v[196:199],v[232:235],v[120:123],v[196:199]\n"
"v_mfma_f32_16x16x32_bf16 v[196:199],v[236:239],v[124:127],v[196:199]\n"
"v_mfma_f32_16x16x32_bf16 v[192:195],v[232:235],v[48:51],v[192:195]\n"
"v_mfma_f32_16x16x32_bf16 v[192:195],v[236:239],v[52:55],v[192:195]\n"
"s_waitcnt lgkmcnt(4)\n"
"v_mfma_f32_16x16x32_bf16 v[192:195],v[208:211],v[56:59],v[192:195]\n"
"v_mfma_f32_16x16x32_bf16 v[192:195],v[212:215],v[60:63],v[192:195]\n"
"v_add_u32 v248,4294828032,v248\n"
"v_mfma_f32_16x16x32_bf16 v[196:199],v[208:211],v[128:131],v[196:199]\n"
"v_mfma_f32_16x16x32_bf16 v[196:199],v[212:215],v[132:135],v[196:199]\n"
"v_add_u32 v249,4294828032,v249\n"
"s_waitcnt lgkmcnt(0)\n"
"v_mfma_f32_16x16x32_bf16 v[196:199],v[216:219],v[136:139],v[196:199]\n"
"v_mfma_f32_16x16x32_bf16 v[196:199],v[220:223],v[140:143],v[196:199]\n"
"v_mfma_f32_16x16x32_bf16 v[192:195],v[216:219],v[64:67],v[192:195]\n"
"v_mfma_f32_16x16x32_bf16 v[192:195],v[220:223],v[68:71],v[192:195]\n"
"s_setprio 0\n"
"v_fma_f32 v244,-v200,v200,s79\n"
"v_fma_f32 v245,-v201,v201,s79\n"
"v_fma_f32 v246,-v202,v202,s79\n"
"v_fma_f32 v247,-v203,v203,s79\n"
"v_mul_f32 v244,v244,v200\n"
"v_mul_f32 v245,v245,v201\n"
"v_mul_f32 v246,v246,v202\n"
"v_mul_f32 v247,v247,v203\n"
"v_exp_f32 v244,v244\n"
"v_exp_f32 v245,v245\n"
"v_exp_f32 v246,v246\n"
"v_exp_f32 v247,v247\n"
"v_add_f32 v244,1.0,v244\n"
"v_add_f32 v245,1.0,v245\n"
"v_add_f32 v246,1.0,v246\n"
"v_add_f32 v247,1.0,v247\n"
"v_rcp_f32 v244,v244\n"
"v_rcp_f32 v245,v245\n"
"v_rcp_f32 v246,v246\n"
"v_rcp_f32 v247,v247\n"
"v_mul_f32 v244,v244,v200\n"
"v_mul_f32 v245,v245,v201\n"
"v_mul_f32 v246,v246,v202\n"
"v_mul_f32 v247,v247,v203\n"
"v_cvt_pk_bf16_f32 v170,v244,v245\n"
"v_cvt_pk_bf16_f32 v171,v246,v247\n"
"v_fma_f32 v240,-v192,v192,s79\n"
"v_fma_f32 v241,-v193,v193,s79\n"
"v_fma_f32 v242,-v194,v194,s79\n"
"v_fma_f32 v243,-v195,v195,s79\n"
"v_mul_f32 v240,v240,v192\n"
"v_mul_f32 v241,v241,v193\n"
"v_mul_f32 v242,v242,v194\n"
"v_mul_f32 v243,v243,v195\n"
"v_exp_f32 v240,v240\n"
"s_waitcnt vmcnt(4)\n"
"s_barrier\n"
"v_exp_f32 v241,v241\n"
"v_exp_f32 v242,v242\n"
"s_add_u32 s90,s80,640\n"
"s_addk_i32 m0,8192\n"
"v_exp_f32 v243,v243\n"
"buffer_load_dwordx4 v250,s[16:19],s90 offen lds\n"
"v_add_f32 v240,1.0,v240\n"
"v_add_f32 v241,1.0,v241\n"
"v_add_f32 v242,1.0,v242\n"
"v_add_f32 v243,1.0,v243\n"
"v_rcp_f32 v240,v240\n"
"v_rcp_f32 v241,v241\n"
"v_rcp_f32 v242,v242\n"
"v_rcp_f32 v243,v243\n"
"v_mul_f32 v240,v240,v192\n"
"v_mul_f32 v241,v241,v193\n"
"v_mul_f32 v242,v242,v194\n"
"v_mul_f32 v243,v243,v195\n"
"v_cvt_pk_bf16_f32 v168,v240,v241\n"
"v_cvt_pk_bf16_f32 v169,v242,v243\n"
"v_fma_f32 v240,-v196,v196,s79\n"
"v_fma_f32 v241,-v197,v197,s79\n"
"v_fma_f32 v242,-v198,v198,s79\n"
"v_fma_f32 v243,-v199,v199,s79\n"
"v_mul_f32 v240,v240,v196\n"
"v_mul_f32 v241,v241,v197\n"
"v_mul_f32 v242,v242,v198\n"
"v_mul_f32 v243,v243,v199\n"
"v_exp_f32 v240,v240\n"
"v_exp_f32 v241,v241\n"
"v_exp_f32 v242,v242\n"
"v_exp_f32 v243,v243\n"
"v_add_f32 v240,1.0,v240\n"
"v_add_f32 v241,1.0,v241\n"
"buffer_load_dwordx4 v191,s[16:19],s90 offen offset:1024 lds\n"
"v_add_f32 v242,1.0,v242\n"
"v_add_f32 v243,1.0,v243\n"
"v_rcp_f32 v240,v240\n"
"v_rcp_f32 v241,v241\n"
"v_rcp_f32 v242,v242\n"
"v_rcp_f32 v243,v243\n"
"v_mul_f32 v240,v240,v196\n"
"v_mul_f32 v241,v241,v197\n"
"v_mul_f32 v242,v242,v198\n"
"v_mul_f32 v243,v243,v199\n"
"v_cvt_pk_bf16_f32 v172,v240,v241\n"
"v_cvt_pk_bf16_f32 v173,v242,v243\n"
"v_fma_f32 v244,-v204,v204,s79\n"
"v_fma_f32 v245,-v205,v205,s79\n"
"v_fma_f32 v246,-v206,v206,s79\n"
"v_fma_f32 v247,-v207,v207,s79\n"
"v_mul_f32 v244,v244,v204\n"
"v_mul_f32 v245,v245,v205\n"
"s_waitcnt vmcnt(4)\n"
"v_mov_b32 v208,1\n"
"s_mov_b64 exec,1\n"
"buffer_store_dword v208,off,s[32:35],s88 sc1\n"
"s_mov_b64 exec,-1\n"
"v_mul_f32 v246,v246,v206\n"
"v_mul_f32 v247,v247,v207\n"
"v_exp_f32 v244,v244\n"
"v_exp_f32 v245,v245\n"
"v_exp_f32 v246,v246\n"
"v_exp_f32 v247,v247\n"
"v_add_f32 v244,1.0,v244\n"
"v_add_f32 v245,1.0,v245\n"
"v_add_f32 v246,1.0,v246\n"
"v_add_f32 v247,1.0,v247\n"
"v_rcp_f32 v244,v244\n"
"s_setprio 3\n"
"v_rcp_f32 v245,v245\n"
"ds_read_b128 v[240:243],v253 offset:1024\n"
"ds_read_b64_tr_b16 v[208:209],v248\n"
"v_rcp_f32 v246,v246\n"
"ds_read_b64_tr_b16 v[210:211],v248 offset:2048\n"
"ds_read_b64_tr_b16 v[212:213],v248 offset:4096\n"
"v_rcp_f32 v247,v247\n"
"ds_read_b64_tr_b16 v[214:215],v248 offset:6144\n"
"ds_read_b64_tr_b16 v[216:217],v248 offset:8192\n"
"v_mul_f32 v244,v244,v204\n"
"v_mul_f32 v245,v245,v205\n"
"ds_read_b64_tr_b16 v[218:219],v248 offset:10240\n"
"ds_read_b64_tr_b16 v[220:221],v248 offset:12288\n"
"v_mul_f32 v246,v246,v206\n"
"v_mul_f32 v247,v247,v207\n"
"ds_read_b64_tr_b16 v[222:223],v248 offset:14336\n"
"ds_read_b64_tr_b16 v[224:225],v248 offset:16384\n"
"v_cvt_pk_bf16_f32 v174,v244,v245\n"
"v_cvt_pk_bf16_f32 v175,v246,v247\n"
"ds_read_b64_tr_b16 v[226:227],v248 offset:18432\n"
"ds_read_b64_tr_b16 v[228:229],v248 offset:20480\n"
"ds_read_b64_tr_b16 v[230:231],v248 offset:22528\n"
"s_setprio 1\n"
"s_waitcnt lgkmcnt(12)\n"
"s_waitcnt lgkmcnt(8)\n"
"v_mfma_f32_16x16x32_bf16 v[192:195],v[208:211],v[0:3],v[240:243]\n"
"ds_read_b64_tr_b16 v[232:233],v248 offset:24576\n"
"ds_read_b128 v[244:247],v253 offset:1088\n"
"v_mfma_f32_16x16x32_bf16 v[192:195],v[212:215],v[4:7],v[192:195]\n"
"ds_read_b64_tr_b16 v[234:235],v248 offset:26624\n"
"s_add_u32 s92,s80,4129408\n"
"v_mfma_f32_16x16x32_bf16 v[196:199],v[208:211],v[72:75],v[240:243]\n"
"ds_read_b64_tr_b16 v[236:237],v248 offset:28672\n"
"s_add_u32 m0,s91,131072\n"
"v_mfma_f32_16x16x32_bf16 v[196:199],v[212:215],v[76:79],v[196:199]\n"
"ds_read_b64_tr_b16 v[238:239],v248 offset:30720\n"
"s_waitcnt lgkmcnt(9)\n"
"v_mfma_f32_16x16x32_bf16 v[196:199],v[216:219],v[80:83],v[196:199]\n"
"ds_read_b64_tr_b16 v[208:209],v248 offset:32768\n"
"v_mfma_f32_16x16x32_bf16 v[196:199],v[220:223],v[84:87],v[196:199]\n"
"ds_read_b64_tr_b16 v[210:211],v248 offset:34816\n"
"v_mfma_f32_16x16x32_bf16 v[192:195],v[216:219],v[8:11],v[192:195]\n"
"ds_read_b64_tr_b16 v[212:213],v248 offset:36864\n"
"v_mfma_f32_16x16x32_bf16 v[192:195],v[220:223],v[12:15],v[192:195]\n"
"ds_read_b64_tr_b16 v[214:215],v248 offset:38912\n"
"s_waitcnt lgkmcnt(9)\n"
"v_mfma_f32_16x16x32_bf16 v[192:195],v[224:227],v[16:19],v[192:195]\n"
"ds_read_b64_tr_b16 v[216:217],v249\n"
"buffer_load_dwordx4 v250,s[16:19],s92 offen lds\n"
"v_mfma_f32_16x16x32_bf16 v[192:195],v[228:231],v[20:23],v[192:195]\n"
"ds_read_b64_tr_b16 v[218:219],v249 offset:2048\n"
"s_add_u32 s90,s92,s93\n"
"v_mfma_f32_16x16x32_bf16 v[196:199],v[224:227],v[88:91],v[196:199]\n"
"ds_read_b64_tr_b16 v[220:221],v249 offset:4096\n"
"v_mfma_f32_16x16x32_bf16 v[196:199],v[228:231],v[92:95],v[196:199]\n"
"ds_read_b64_tr_b16 v[222:223],v249 offset:6144\n"
"s_waitcnt lgkmcnt(8)\n"
"v_mfma_f32_16x16x32_bf16 v[196:199],v[232:235],v[96:99],v[196:199]\n"
"ds_read_b64_tr_b16 v[224:225],v249 offset:8192\n"
"v_mfma_f32_16x16x32_bf16 v[196:199],v[236:239],v[100:103],v[196:199]\n"
"ds_read_b64_tr_b16 v[226:227],v249 offset:10240\n"
"v_mfma_f32_16x16x32_bf16 v[192:195],v[232:235],v[24:27],v[192:195]\n"
"ds_read_b64_tr_b16 v[228:229],v249 offset:12288\n"
"buffer_load_dwordx4 v191,s[16:19],s92 offen offset:1024 lds\n"
"v_mfma_f32_16x16x32_bf16 v[192:195],v[236:239],v[28:31],v[192:195]\n"
"ds_read_b64_tr_b16 v[230:231],v249 offset:14336\n"
"s_waitcnt lgkmcnt(8)\n"
"v_mfma_f32_16x16x32_bf16 v[192:195],v[208:211],v[32:35],v[192:195]\n"
"ds_read_b64_tr_b16 v[232:233],v249 offset:16384\n"
"s_addk_i32 m0,8192\n"
"v_mfma_f32_16x16x32_bf16 v[192:195],v[212:215],v[36:39],v[192:195]\n"
"ds_read_b64_tr_b16 v[234:235],v249 offset:18432\n"
"v_mfma_f32_16x16x32_bf16 v[196:199],v[208:211],v[104:107],v[196:199]\n"
"ds_read_b64_tr_b16 v[236:237],v249 offset:20480\n"
"v_mfma_f32_16x16x32_bf16 v[196:199],v[212:215],v[108:111],v[196:199]\n"
"ds_read_b64_tr_b16 v[238:239],v249 offset:22528\n"
"s_waitcnt lgkmcnt(8)\n"
"v_mfma_f32_16x16x32_bf16 v[200:203],v[216:219],v[0:3],v[244:247]\n"
"ds_read_b64_tr_b16 v[208:209],v249 offset:24576\n"
"v_mfma_f32_16x16x32_bf16 v[200:203],v[220:223],v[4:7],v[200:203]\n"
"ds_read_b64_tr_b16 v[210:211],v249 offset:26624\n"
"buffer_load_dwordx4 v250,s[16:19],s90 offen lds\n"
"v_mfma_f32_16x16x32_bf16 v[204:207],v[216:219],v[72:75],v[244:247]\n"
"ds_read_b64_tr_b16 v[212:213],v249 offset:28672\n"
"v_mfma_f32_16x16x32_bf16 v[204:207],v[220:223],v[76:79],v[204:207]\n"
"ds_read_b64_tr_b16 v[214:215],v249 offset:30720\n"
"s_waitcnt lgkmcnt(8)\n"
"v_mfma_f32_16x16x32_bf16 v[204:207],v[224:227],v[80:83],v[204:207]\n"
"ds_read_b64_tr_b16 v[216:217],v249 offset:32768\n"
"v_mfma_f32_16x16x32_bf16 v[204:207],v[228:231],v[84:87],v[204:207]\n"
"ds_read_b64_tr_b16 v[218:219],v249 offset:34816\n"
"v_mfma_f32_16x16x32_bf16 v[200:203],v[224:227],v[8:11],v[200:203]\n"
"ds_read_b64_tr_b16 v[220:221],v249 offset:36864\n"
"v_mfma_f32_16x16x32_bf16 v[200:203],v[228:231],v[12:15],v[200:203]\n"
"ds_read_b64_tr_b16 v[222:223],v249 offset:38912\n"
"s_waitcnt lgkmcnt(8)\n"
"v_mfma_f32_16x16x32_bf16 v[200:203],v[232:235],v[16:19],v[200:203]\n"
"ds_read_b64_tr_b16 v[224:225],v249 offset:40960\n"
"buffer_load_dwordx4 v191,s[16:19],s90 offen offset:1024 lds\n"
"v_mfma_f32_16x16x32_bf16 v[200:203],v[236:239],v[20:23],v[200:203]\n"
"ds_read_b64_tr_b16 v[226:227],v249 offset:43008\n"
"s_add_u32 s78,s84,6144\n"
"v_mfma_f32_16x16x32_bf16 v[204:207],v[232:235],v[88:91],v[204:207]\n"
"ds_read_b64_tr_b16 v[228:229],v249 offset:45056\n"
"v_mfma_f32_16x16x32_bf16 v[204:207],v[236:239],v[92:95],v[204:207]\n"
"ds_read_b64_tr_b16 v[230:231],v249 offset:47104\n"
"s_waitcnt lgkmcnt(8)\n"
"v_mfma_f32_16x16x32_bf16 v[204:207],v[208:211],v[96:99],v[204:207]\n"
"ds_read_b64_tr_b16 v[232:233],v249 offset:49152\n"
"v_mfma_f32_16x16x32_bf16 v[204:207],v[212:215],v[100:103],v[204:207]\n"
"ds_read_b64_tr_b16 v[234:235],v249 offset:51200\n"
"v_mfma_f32_16x16x32_bf16 v[200:203],v[208:211],v[24:27],v[200:203]\n"
"ds_read_b64_tr_b16 v[236:237],v249 offset:53248\n"
"v_mfma_f32_16x16x32_bf16 v[200:203],v[212:215],v[28:31],v[200:203]\n"
"ds_read_b64_tr_b16 v[238:239],v249 offset:55296\n"
"s_waitcnt lgkmcnt(8)\n"
"v_mfma_f32_16x16x32_bf16 v[200:203],v[216:219],v[32:35],v[200:203]\n"
"ds_read_b64_tr_b16 v[208:209],v249 offset:57344\n"
"buffer_store_dwordx4 v[168:171],v252,s[32:35],s78 offen sc1\n"
".Lk_padm6:\n"
"s_cbranch_execz .Lk_hop_retm\n"
"v_mfma_f32_16x16x32_bf16 v[200:203],v[220:223],v[36:39],v[200:203]\n"
"ds_read_b64_tr_b16 v[210:211],v249 offset:59392\n"
"v_mfma_f32_16x16x32_bf16 v[204:207],v[216:219],v[104:107],v[204:207]\n"
"ds_read_b64_tr_b16 v[212:213],v249 offset:61440\n"
"v_mfma_f32_16x16x32_bf16 v[204:207],v[220:223],v[108:111],v[204:207]\n"
"ds_read_b64_tr_b16 v[214:215],v249 offset:63488\n"
"s_waitcnt lgkmcnt(8)\n"
"v_mfma_f32_16x16x32_bf16 v[204:207],v[224:227],v[112:115],v[204:207]\n"
"v_add_u32 v249,0x10000,v249\n"
"ds_read_b64_tr_b16 v[216:217],v249\n"
"v_mfma_f32_16x16x32_bf16 v[204:207],v[228:231],v[116:119],v[204:207]\n"
"ds_read_b64_tr_b16 v[218:219],v249 offset:2048\n"
"s_setprio 2\n"
"v_mfma_f32_16x16x32_bf16 v[200:203],v[224:227],v[40:43],v[200:203]\n"
"ds_read_b64_tr_b16 v[220:221],v249 offset:4096\n"
"buffer_store_dwordx4 v[172:175],v252,s[32:35],s78 offen offset:1024 sc1\n"
"v_mfma_f32_16x16x32_bf16 v[200:203],v[228:231],v[44:47],v[200:203]\n"
"ds_read_b64_tr_b16 v[222:223],v249 offset:6144\n"
"s_waitcnt lgkmcnt(8)\n"
"v_mfma_f32_16x16x32_bf16 v[200:203],v[232:235],v[48:51],v[200:203]\n"
"ds_read_b64_tr_b16 v[224:225],v248 offset:40960\n"
"v_mov_b32 v240,-1\n"
"v_mfma_f32_16x16x32_bf16 v[200:203],v[236:239],v[52:55],v[200:203]\n"
"ds_read_b64_tr_b16 v[226:227],v248 offset:43008\n"
"v_mov_b32 v241,-1\n"
"v_mfma_f32_16x16x32_bf16 v[204:207],v[232:235],v[120:123],v[204:207]\n"
"ds_read_b64_tr_b16 v[228:229],v248 offset:45056\n"
"v_mov_b32 v242,-1\n"
"v_mfma_f32_16x16x32_bf16 v[204:207],v[236:239],v[124:127],v[204:207]\n"
"ds_read_b64_tr_b16 v[230:231],v248 offset:47104\n"
"s_waitcnt vmcnt(7)\n"
"s_waitcnt lgkmcnt(8)\n"
"v_mfma_f32_16x16x32_bf16 v[204:207],v[208:211],v[128:131],v[204:207]\n"
"ds_read_b64_tr_b16 v[232:233],v248 offset:49152\n"
"v_mov_b32 v243,-1\n"
"v_mfma_f32_16x16x32_bf16 v[204:207],v[212:215],v[132:135],v[204:207]\n"
"ds_read_b64_tr_b16 v[234:235],v248 offset:51200\n"
"s_barrier\n"
"v_mfma_f32_16x16x32_bf16 v[200:203],v[208:211],v[56:59],v[200:203]\n"
"ds_read_b64_tr_b16 v[236:237],v248 offset:53248\n"
"s_add_u32 s78,s9,5120\n"
"v_mfma_f32_16x16x32_bf16 v[200:203],v[212:215],v[60:63],v[200:203]\n"
"ds_read_b64_tr_b16 v[238:239],v248 offset:55296\n"
"s_waitcnt lgkmcnt(8)\n"
"v_mfma_f32_16x16x32_bf16 v[200:203],v[216:219],v[64:67],v[200:203]\n"
"ds_read_b64_tr_b16 v[208:209],v248 offset:57344\n"
"buffer_store_dwordx4 v[240:243],v252,s[32:35],s78 offen sc1\n"
"v_mfma_f32_16x16x32_bf16 v[200:203],v[220:223],v[68:71],v[200:203]\n"
"ds_read_b64_tr_b16 v[210:211],v248 offset:59392\n"
"s_add_u32 s78,s9,23552\n"
"v_mfma_f32_16x16x32_bf16 v[204:207],v[216:219],v[136:139],v[204:207]\n"
"ds_read_b64_tr_b16 v[212:213],v248 offset:61440\n"
"v_mfma_f32_16x16x32_bf16 v[204:207],v[220:223],v[140:143],v[204:207]\n"
"ds_read_b64_tr_b16 v[214:215],v248 offset:63488\n"
"s_waitcnt lgkmcnt(8)\n"
"v_mfma_f32_16x16x32_bf16 v[192:195],v[224:227],v[40:43],v[192:195]\n"
"v_add_u32 v248,0x10000,v248\n"
"ds_read_b64_tr_b16 v[216:217],v248\n"
"buffer_store_dwordx4 v[240:243],v252,s[32:35],s78 offen sc1\n"
"v_mfma_f32_16x16x32_bf16 v[192:195],v[228:231],v[44:47],v[192:195]\n"
"ds_read_b64_tr_b16 v[218:219],v248 offset:2048\n"
"v_mfma_f32_16x16x32_bf16 v[196:199],v[224:227],v[112:115],v[196:199]\n"
"ds_read_b64_tr_b16 v[220:221],v248 offset:4096\n"
"v_mfma_f32_16x16x32_bf16 v[196:199],v[228:231],v[116:119],v[196:199]\n"
"ds_read_b64_tr_b16 v[222:223],v248 offset:6144\n"
"s_waitcnt lgkmcnt(8)\n"
"v_mfma_f32_16x16x32_bf16 v[196:199],v[232:235],v[120:123],v[196:199]\n"
"v_mfma_f32_16x16x32_bf16 v[196:199],v[236:239],v[124:127],v[196:199]\n"
"v_mfma_f32_16x16x32_bf16 v[192:195],v[232:235],v[48:51],v[192:195]\n"
"v_mfma_f32_16x16x32_bf16 v[192:195],v[236:239],v[52:55],v[192:195]\n"
"s_waitcnt lgkmcnt(4)\n"
"v_mfma_f32_16x16x32_bf16 v[192:195],v[208:211],v[56:59],v[192:195]\n"
"v_mfma_f32_16x16x32_bf16 v[192:195],v[212:215],v[60:63],v[192:195]\n"
"v_add_u32 v248,8192,v248\n"
"v_mfma_f32_16x16x32_bf16 v[196:199],v[208:211],v[128:131],v[196:199]\n"
"v_mfma_f32_16x16x32_bf16 v[196:199],v[212:215],v[132:135],v[196:199]\n"
"v_add_u32 v249,8192,v249\n"
"s_waitcnt lgkmcnt(0)\n"
"v_mfma_f32_16x16x32_bf16 v[196:199],v[216:219],v[136:139],v[196:199]\n"
"v_mfma_f32_16x16x32_bf16 v[196:199],v[220:223],v[140:143],v[196:199]\n"
"v_mfma_f32_16x16x32_bf16 v[192:195],v[216:219],v[64:67],v[192:195]\n"
"v_mfma_f32_16x16x32_bf16 v[192:195],v[220:223],v[68:71],v[192:195]\n"
"s_setprio 0\n"
"v_fma_f32 v244,-v200,v200,s79\n"
"v_fma_f32 v245,-v201,v201,s79\n"
"v_fma_f32 v246,-v202,v202,s79\n"
"v_fma_f32 v247,-v203,v203,s79\n"
"v_mul_f32 v244,v244,v200\n"
"v_mul_f32 v245,v245,v201\n"
"v_mul_f32 v246,v246,v202\n"
"v_mul_f32 v247,v247,v203\n"
"v_exp_f32 v244,v244\n"
"v_exp_f32 v245,v245\n"
"v_exp_f32 v246,v246\n"
"v_exp_f32 v247,v247\n"
"v_add_f32 v244,1.0,v244\n"
"v_add_f32 v245,1.0,v245\n"
"v_add_f32 v246,1.0,v246\n"
"v_add_f32 v247,1.0,v247\n"
"v_rcp_f32 v244,v244\n"
"v_rcp_f32 v245,v245\n"
"v_rcp_f32 v246,v246\n"
"v_rcp_f32 v247,v247\n"
"v_mul_f32 v244,v244,v200\n"
"v_mul_f32 v245,v245,v201\n"
"v_mul_f32 v246,v246,v202\n"
"v_mul_f32 v247,v247,v203\n"
"v_cvt_pk_bf16_f32 v178,v244,v245\n"
"v_cvt_pk_bf16_f32 v179,v246,v247\n"
"v_fma_f32 v240,-v192,v192,s79\n"
"v_fma_f32 v241,-v193,v193,s79\n"
"v_fma_f32 v242,-v194,v194,s79\n"
"v_fma_f32 v243,-v195,v195,s79\n"
"v_mul_f32 v240,v240,v192\n"
"v_mul_f32 v241,v241,v193\n"
"v_mul_f32 v242,v242,v194\n"
"v_mul_f32 v243,v243,v195\n"
"v_exp_f32 v240,v240\n"
"s_waitcnt vmcnt(4)\n"
"s_barrier\n"
"v_exp_f32 v241,v241\n"
"v_exp_f32 v242,v242\n"
"v_exp_f32 v243,v243\n"
"v_add_f32 v240,1.0,v240\n"
"v_add_f32 v241,1.0,v241\n"
"v_add_f32 v242,1.0,v242\n"
"v_add_f32 v243,1.0,v243\n"
"v_rcp_f32 v240,v240\n"
"v_rcp_f32 v241,v241\n"
"v_rcp_f32 v242,v242\n"
"v_rcp_f32 v243,v243\n"
"v_mul_f32 v240,v240,v192\n"
"v_mul_f32 v241,v241,v193\n"
"v_mul_f32 v242,v242,v194\n"
"v_mul_f32 v243,v243,v195\n"
"v_cvt_pk_bf16_f32 v176,v240,v241\n"
"v_cvt_pk_bf16_f32 v177,v242,v243\n"
"v_fma_f32 v240,-v196,v196,s79\n"
"v_fma_f32 v241,-v197,v197,s79\n"
"v_fma_f32 v242,-v198,v198,s79\n"
"v_fma_f32 v243,-v199,v199,s79\n"
"v_mul_f32 v240,v240,v196\n"
"v_mul_f32 v241,v241,v197\n"
"v_mul_f32 v242,v242,v198\n"
"v_mul_f32 v243,v243,v199\n"
"v_exp_f32 v240,v240\n"
"v_exp_f32 v241,v241\n"
"v_exp_f32 v242,v242\n"
"v_exp_f32 v243,v243\n"
"v_add_f32 v240,1.0,v240\n"
"v_add_f32 v241,1.0,v241\n"
"v_add_f32 v242,1.0,v242\n"
"v_add_f32 v243,1.0,v243\n"
"v_rcp_f32 v240,v240\n"
"v_rcp_f32 v241,v241\n"
"v_rcp_f32 v242,v242\n"
"v_rcp_f32 v243,v243\n"
"v_mul_f32 v240,v240,v196\n"
"v_mul_f32 v241,v241,v197\n"
"v_mul_f32 v242,v242,v198\n"
"v_mul_f32 v243,v243,v199\n"
"v_cvt_pk_bf16_f32 v180,v240,v241\n"
"v_cvt_pk_bf16_f32 v181,v242,v243\n"
"v_fma_f32 v244,-v204,v204,s79\n"
"v_fma_f32 v245,-v205,v205,s79\n"
"v_fma_f32 v246,-v206,v206,s79\n"
"v_fma_f32 v247,-v207,v207,s79\n"
"v_mul_f32 v244,v244,v204\n"
"v_mul_f32 v245,v245,v205\n"
"v_mul_f32 v246,v246,v206\n"
"v_mul_f32 v247,v247,v207\n"
"v_exp_f32 v244,v244\n"
"v_exp_f32 v245,v245\n"
"v_exp_f32 v246,v246\n"
"v_exp_f32 v247,v247\n"
"v_add_f32 v244,1.0,v244\n"
"v_add_f32 v245,1.0,v245\n"
"v_add_f32 v246,1.0,v246\n"
"v_add_f32 v247,1.0,v247\n"
"v_rcp_f32 v244,v244\n"
"s_setprio 3\n"
"v_rcp_f32 v245,v245\n"
"ds_read_b128 v[240:243],v253 offset:1280\n"
"ds_read_b64_tr_b16 v[208:209],v248\n"
"v_rcp_f32 v246,v246\n"
"ds_read_b64_tr_b16 v[210:211],v248 offset:2048\n"
"ds_read_b64_tr_b16 v[212:213],v248 offset:4096\n"
"v_rcp_f32 v247,v247\n"
"ds_read_b64_tr_b16 v[214:215],v248 offset:6144\n"
"ds_read_b64_tr_b16 v[216:217],v248 offset:8192\n"
"v_mul_f32 v244,v244,v204\n"
"v_mul_f32 v245,v245,v205\n"
"ds_read_b64_tr_b16 v[218:219],v248 offset:10240\n"
"ds_read_b64_tr_b16 v[220:221],v248 offset:12288\n"
"v_mul_f32 v246,v246,v206\n"
"v_mul_f32 v247,v247,v207\n"
"ds_read_b64_tr_b16 v[222:223],v248 offset:14336\n"
"ds_read_b64_tr_b16 v[224:225],v248 offset:16384\n"
"v_cvt_pk_bf16_f32 v182,v244,v245\n"
"v_cvt_pk_bf16_f32 v183,v246,v247\n"
"ds_read_b64_tr_b16 v[226:227],v248 offset:18432\n"
"ds_read_b64_tr_b16 v[228:229],v248 offset:20480\n"
"ds_read_b64_tr_b16 v[230:231],v248 offset:22528\n"
"s_setprio 1\n"
"s_waitcnt lgkmcnt(12)\n"
"s_waitcnt lgkmcnt(8)\n"
"v_mfma_f32_16x16x32_bf16 v[192:195],v[208:211],v[0:3],v[240:243]\n"
"ds_read_b64_tr_b16 v[232:233],v248 offset:24576\n"
"ds_read_b128 v[244:247],v253 offset:1344\n"
"v_mfma_f32_16x16x32_bf16 v[192:195],v[212:215],v[4:7],v[192:195]\n"
"ds_read_b64_tr_b16 v[234:235],v248 offset:26624\n"
"s_add_u32 m0,s73,36864\n"
"v_mfma_f32_16x16x32_bf16 v[196:199],v[208:211],v[72:75],v[240:243]\n"
"ds_read_b64_tr_b16 v[236:237],v248 offset:28672\n"
"s_add_u32 s74,s81,36864\n"
"v_mfma_f32_16x16x32_bf16 v[196:199],v[212:215],v[76:79],v[196:199]\n"
"ds_read_b64_tr_b16 v[238:239],v248 offset:30720\n"
"s_waitcnt lgkmcnt(9)\n"
"v_mfma_f32_16x16x32_bf16 v[196:199],v[216:219],v[80:83],v[196:199]\n"
"ds_read_b64_tr_b16 v[208:209],v248 offset:32768\n"
"buffer_load_dwordx4 v251,s[20:23],s74 offen lds\n"
"v_mfma_f32_16x16x32_bf16 v[196:199],v[220:223],v[84:87],v[196:199]\n"
"ds_read_b64_tr_b16 v[210:211],v248 offset:34816\n"
"s_addk_i32 m0,4096\n"
"v_mfma_f32_16x16x32_bf16 v[192:195],v[216:219],v[8:11],v[192:195]\n"
"ds_read_b64_tr_b16 v[212:213],v248 offset:36864\n"
"s_addk_i32 s74,128\n"
"v_mfma_f32_16x16x32_bf16 v[192:195],v[220:223],v[12:15],v[192:195]\n"
"ds_read_b64_tr_b16 v[214:215],v248 offset:38912\n"
"s_waitcnt lgkmcnt(9)\n"
"v_mfma_f32_16x16x32_bf16 v[192:195],v[224:227],v[16:19],v[192:195]\n"
"ds_read_b64_tr_b16 v[216:217],v249\n"
"v_mfma_f32_16x16x32_bf16 v[192:195],v[228:231],v[20:23],v[192:195]\n"
"ds_read_b64_tr_b16 v[218:219],v249 offset:2048\n"
"buffer_load_dwordx4 v251,s[20:23],s74 offen lds\n"
"v_mfma_f32_16x16x32_bf16 v[196:199],v[224:227],v[88:91],v[196:199]\n"
"ds_read_b64_tr_b16 v[220:221],v249 offset:4096\n"
"s_addk_i32 m0,4096\n"
"v_mfma_f32_16x16x32_bf16 v[196:199],v[228:231],v[92:95],v[196:199]\n"
"ds_read_b64_tr_b16 v[222:223],v249 offset:6144\n"
"s_waitcnt lgkmcnt(8)\n"
"v_mfma_f32_16x16x32_bf16 v[196:199],v[232:235],v[96:99],v[196:199]\n"
"ds_read_b64_tr_b16 v[224:225],v249 offset:8192\n"
"s_addk_i32 s74,128\n"
"v_mfma_f32_16x16x32_bf16 v[196:199],v[236:239],v[100:103],v[196:199]\n"
"ds_read_b64_tr_b16 v[226:227],v249 offset:10240\n"
"v_mfma_f32_16x16x32_bf16 v[192:195],v[232:235],v[24:27],v[192:195]\n"
"ds_read_b64_tr_b16 v[228:229],v249 offset:12288\n"
"buffer_load_dwordx4 v251,s[20:23],s74 offen lds\n"
"v_mfma_f32_16x16x32_bf16 v[192:195],v[236:239],v[28:31],v[192:195]\n"
"ds_read_b64_tr_b16 v[230:231],v249 offset:14336\n"
"s_waitcnt lgkmcnt(8)\n"
"v_mfma_f32_16x16x32_bf16 v[192:195],v[208:211],v[32:35],v[192:195]\n"
"ds_read_b64_tr_b16 v[232:233],v249 offset:16384\n"
"s_addk_i32 m0,4096\n"
"v_mfma_f32_16x16x32_bf16 v[192:195],v[212:215],v[36:39],v[192:195]\n"
"ds_read_b64_tr_b16 v[234:235],v249 offset:18432\n"
"s_add_u32 s74,s81,110592\n"
"v_mfma_f32_16x16x32_bf16 v[196:199],v[208:211],v[104:107],v[196:199]\n"
"ds_read_b64_tr_b16 v[236:237],v249 offset:20480\n"
"v_mfma_f32_16x16x32_bf16 v[196:199],v[212:215],v[108:111],v[196:199]\n"
"ds_read_b64_tr_b16 v[238:239],v249 offset:22528\n"
"s_waitcnt lgkmcnt(8)\n"
"v_mfma_f32_16x16x32_bf16 v[200:203],v[216:219],v[0:3],v[244:247]\n"
"ds_read_b64_tr_b16 v[208:209],v249 offset:24576\n"
"buffer_load_dwordx4 v251,s[20:23],s74 offen lds\n"
"v_mfma_f32_16x16x32_bf16 v[200:203],v[220:223],v[4:7],v[200:203]\n"
"ds_read_b64_tr_b16 v[210:211],v249 offset:26624\n"
"s_addk_i32 m0,4096\n"
"v_mfma_f32_16x16x32_bf16 v[204:207],v[216:219],v[72:75],v[244:247]\n"
"ds_read_b64_tr_b16 v[212:213],v249 offset:28672\n"
"s_addk_i32 s74,128\n"
"v_mfma_f32_16x16x32_bf16 v[204:207],v[220:223],v[76:79],v[204:207]\n"
"ds_read_b64_tr_b16 v[214:215],v249 offset:30720\n"
"s_waitcnt lgkmcnt(8)\n"
"v_mfma_f32_16x16x32_bf16 v[204:207],v[224:227],v[80:83],v[204:207]\n"
"ds_read_b64_tr_b16 v[216:217],v249 offset:32768\n"
"buffer_load_dwordx4 v251,s[20:23],s74 offen lds\n"
"v_mfma_f32_16x16x32_bf16 v[204:207],v[228:231],v[84:87],v[204:207]\n"
"ds_read_b64_tr_b16 v[218:219],v249 offset:34816\n"
"s_addk_i32 m0,4096\n"
"v_mfma_f32_16x16x32_bf16 v[200:203],v[224:227],v[8:11],v[200:203]\n"
"ds_read_b64_tr_b16 v[220:221],v249 offset:36864\n"
"s_addk_i32 s74,128\n"
"v_mfma_f32_16x16x32_bf16 v[200:203],v[228:231],v[12:15],v[200:203]\n"
"ds_read_b64_tr_b16 v[222:223],v249 offset:38912\n"
"s_waitcnt lgkmcnt(8)\n"
"v_mfma_f32_16x16x32_bf16 v[200:203],v[232:235],v[16:19],v[200:203]\n"
"ds_read_b64_tr_b16 v[224:225],v249 offset:40960\n"
"v_mfma_f32_16x16x32_bf16 v[200:203],v[236:239],v[20:23],v[200:203]\n"
"ds_read_b64_tr_b16 v[226:227],v249 offset:43008\n"
"buffer_load_dwordx4 v251,s[20:23],s74 offen lds\n"
"v_mfma_f32_16x16x32_bf16 v[204:207],v[232:235],v[88:91],v[204:207]\n"
"ds_read_b64_tr_b16 v[228:229],v249 offset:45056\n"
"v_mfma_f32_16x16x32_bf16 v[204:207],v[236:239],v[92:95],v[204:207]\n"
"ds_read_b64_tr_b16 v[230:231],v249 offset:47104\n"
"s_waitcnt lgkmcnt(8)\n"
"v_mfma_f32_16x16x32_bf16 v[204:207],v[208:211],v[96:99],v[204:207]\n"
"ds_read_b64_tr_b16 v[232:233],v249 offset:49152\n"
"v_mfma_f32_16x16x32_bf16 v[204:207],v[212:215],v[100:103],v[204:207]\n"
"ds_read_b64_tr_b16 v[234:235],v249 offset:51200\n"
"v_mfma_f32_16x16x32_bf16 v[200:203],v[208:211],v[24:27],v[200:203]\n"
"ds_read_b64_tr_b16 v[236:237],v249 offset:53248\n"
"buffer_load_dword v240,v255,s[32:35],0 offen sc1\n"
"v_mfma_f32_16x16x32_bf16 v[200:203],v[212:215],v[28:31],v[200:203]\n"
"ds_read_b64_tr_b16 v[238:239],v249 offset:55296\n"
"s_waitcnt lgkmcnt(8)\n"
"v_mfma_f32_16x16x32_bf16 v[200:203],v[216:219],v[32:35],v[200:203]\n"
"ds_read_b64_tr_b16 v[208:209],v249 offset:57344\n"
"s_addk_i32 m0,4096\n"
"v_mfma_f32_16x16x32_bf16 v[200:203],v[220:223],v[36:39],v[200:203]\n"
"ds_read_b64_tr_b16 v[210:211],v249 offset:59392\n"
"s_add_u32 s74,s81,184320\n"
"v_mfma_f32_16x16x32_bf16 v[204:207],v[216:219],v[104:107],v[204:207]\n"
"ds_read_b64_tr_b16 v[212:213],v249 offset:61440\n"
"v_mfma_f32_16x16x32_bf16 v[204:207],v[220:223],v[108:111],v[204:207]\n"
"ds_read_b64_tr_b16 v[214:215],v249 offset:63488\n"
"s_waitcnt lgkmcnt(8)\n"
"v_mfma_f32_16x16x32_bf16 v[204:207],v[224:227],v[112:115],v[204:207]\n"
"v_add_u32 v249,0x10000,v249\n"
"ds_read_b64_tr_b16 v[216:217],v249\n"
"buffer_load_dwordx4 v251,s[20:23],s74 offen lds\n"
"v_mfma_f32_16x16x32_bf16 v[204:207],v[228:231],v[116:119],v[204:207]\n"
"ds_read_b64_tr_b16 v[218:219],v249 offset:2048\n"
"s_setprio 2\n"
"v_mfma_f32_16x16x32_bf16 v[200:203],v[224:227],v[40:43],v[200:203]\n"
"ds_read_b64_tr_b16 v[220:221],v249 offset:4096\n"
"s_addk_i32 m0,4096\n"
"v_mfma_f32_16x16x32_bf16 v[200:203],v[228:231],v[44:47],v[200:203]\n"
"ds_read_b64_tr_b16 v[222:223],v249 offset:6144\n"
"s_waitcnt lgkmcnt(8)\n"
"v_mfma_f32_16x16x32_bf16 v[200:203],v[232:235],v[48:51],v[200:203]\n"
"ds_read_b64_tr_b16 v[224:225],v248 offset:40960\n"
"s_addk_i32 s74,128\n"
"v_mfma_f32_16x16x32_bf16 v[200:203],v[236:239],v[52:55],v[200:203]\n"
"ds_read_b64_tr_b16 v[226:227],v248 offset:43008\n"
"buffer_load_dwordx4 v251,s[20:23],s74 offen lds\n"
"v_mfma_f32_16x16x32_bf16 v[204:207],v[232:235],v[120:123],v[204:207]\n"
"ds_read_b64_tr_b16 v[228:229],v248 offset:45056\n"
"s_addk_i32 m0,4096\n"
"v_mfma_f32_16x16x32_bf16 v[204:207],v[236:239],v[124:127],v[204:207]\n"
"ds_read_b64_tr_b16 v[230:231],v248 offset:47104\n"
"s_waitcnt lgkmcnt(8)\n"
"v_mfma_f32_16x16x32_bf16 v[204:207],v[208:211],v[128:131],v[204:207]\n"
"ds_read_b64_tr_b16 v[232:233],v248 offset:49152\n"
"s_addk_i32 s74,128\n"
"v_mfma_f32_16x16x32_bf16 v[204:207],v[212:215],v[132:135],v[204:207]\n"
"ds_read_b64_tr_b16 v[234:235],v248 offset:51200\n"
"s_barrier\n"
"v_mfma_f32_16x16x32_bf16 v[200:203],v[208:211],v[56:59],v[200:203]\n"
"ds_read_b64_tr_b16 v[236:237],v248 offset:53248\n"
"buffer_load_dwordx4 v251,s[20:23],s74 offen lds\n"
"v_mfma_f32_16x16x32_bf16 v[200:203],v[212:215],v[60:63],v[200:203]\n"
"ds_read_b64_tr_b16 v[238:239],v248 offset:55296\n"
"s_waitcnt lgkmcnt(8)\n"
"v_mfma_f32_16x16x32_bf16 v[200:203],v[216:219],v[64:67],v[200:203]\n"
"ds_read_b64_tr_b16 v[208:209],v248 offset:57344\n"
"s_add_u32 s78,s84,8192\n"
"v_mfma_f32_16x16x32_bf16 v[200:203],v[220:223],v[68:71],v[200:203]\n"
"ds_read_b64_tr_b16 v[210:211],v248 offset:59392\n"
"v_mfma_f32_16x16x32_bf16 v[204:207],v[216:219],v[136:139],v[204:207]\n"
"ds_read_b64_tr_b16 v[212:213],v248 offset:61440\n"
"buffer_store_dwordx4 v[176:179],v252,s[32:35],s78 offen sc1\n"
"v_mfma_f32_16x16x32_bf16 v[204:207],v[220:223],v[140:143],v[204:207]\n"
"ds_read_b64_tr_b16 v[214:215],v248 offset:63488\n"
"s_waitcnt lgkmcnt(8)\n"
"v_mfma_f32_16x16x32_bf16 v[192:195],v[224:227],v[40:43],v[192:195]\n"
"v_add_u32 v248,0x10000,v248\n"
"ds_read_b64_tr_b16 v[216:217],v248\n"
"v_mfma_f32_16x16x32_bf16 v[192:195],v[228:231],v[44:47],v[192:195]\n"
"ds_read_b64_tr_b16 v[218:219],v248 offset:2048\n"
"v_mfma_f32_16x16x32_bf16 v[196:199],v[224:227],v[112:115],v[196:199]\n"
"ds_read_b64_tr_b16 v[220:221],v248 offset:4096\n"
"v_mfma_f32_16x16x32_bf16 v[196:199],v[228:231],v[116:119],v[196:199]\n"
"ds_read_b64_tr_b16 v[222:223],v248 offset:6144\n"
"s_waitcnt lgkmcnt(8)\n"
"v_mfma_f32_16x16x32_bf16 v[196:199],v[232:235],v[120:123],v[196:199]\n"
"buffer_store_dwordx4 v[180:183],v252,s[32:35],s78 offen offset:1024 sc1\n"
"v_mfma_f32_16x16x32_bf16 v[196:199],v[236:239],v[124:127],v[196:199]\n"
"v_mfma_f32_16x16x32_bf16 v[192:195],v[232:235],v[48:51],v[192:195]\n"
"v_mfma_f32_16x16x32_bf16 v[192:195],v[236:239],v[52:55],v[192:195]\n"
"s_waitcnt lgkmcnt(4)\n"
"v_mfma_f32_16x16x32_bf16 v[192:195],v[208:211],v[56:59],v[192:195]\n"
"v_mfma_f32_16x16x32_bf16 v[192:195],v[212:215],v[60:63],v[192:195]\n"
"v_add_u32 v248,4294828032,v248\n"
"v_mfma_f32_16x16x32_bf16 v[196:199],v[208:211],v[128:131],v[196:199]\n"
"v_mfma_f32_16x16x32_bf16 v[196:199],v[212:215],v[132:135],v[196:199]\n"
"v_add_u32 v249,4294828032,v249\n"
"s_waitcnt lgkmcnt(0)\n"
"v_mfma_f32_16x16x32_bf16 v[196:199],v[216:219],v[136:139],v[196:199]\n"
"v_mfma_f32_16x16x32_bf16 v[196:199],v[220:223],v[140:143],v[196:199]\n"
"v_mfma_f32_16x16x32_bf16 v[192:195],v[216:219],v[64:67],v[192:195]\n"
"v_mfma_f32_16x16x32_bf16 v[192:195],v[220:223],v[68:71],v[192:195]\n"
"s_setprio 0\n"
"s_waitcnt vmcnt(5)\n"
"v_cmp_gt_u32 vcc,1,v240\n"
"s_nop 1\n"
"s_cbranch_vccz .Lk_h1g1_ok\n"
"s_mov_b32 s89,0x100000\n"
".Lk_h1g1_spin:\n"
"s_sleep 2\n"
"buffer_load_dword v240,v255,s[32:35],0 offen sc1\n"
"s_waitcnt vmcnt(0)\n"
"v_cmp_gt_u32 vcc,1,v240\n"
"s_nop 1\n"
"s_cbranch_vccz .Lk_h1g1_ok\n"
"s_sub_u32 s89,s89,1\n"
"s_cmp_lg_u32 s89,0\n"
"s_cbranch_scc1 .Lk_h1g1_spin\n"
".Lk_h1g1_ok:\n"
"v_xor_b32 v253,36896,v248\n"
"v_xor_b32 v250,36864,v248\n"
"v_xor_b32 v249,36960,v248\n"
"v_xor_b32 v248,36928,v248\n"
"v_fma_f32 v244,-v200,v200,s79\n"
"v_fma_f32 v245,-v201,v201,s79\n"
"v_fma_f32 v246,-v202,v202,s79\n"
"v_fma_f32 v247,-v203,v203,s79\n"
"v_mul_f32 v244,v244,v200\n"
"v_mul_f32 v245,v245,v201\n"
"v_mul_f32 v246,v246,v202\n"
"v_mul_f32 v247,v247,v203\n"
"v_exp_f32 v244,v244\n"
"v_exp_f32 v245,v245\n"
"s_add_u32 s74,s85,0\n"
"buffer_load_dwordx4 v[96:99],v252,s[32:35],s74 offen sc1\n"
"v_exp_f32 v246,v246\n"
"v_exp_f32 v247,v247\n"
"v_add_f32 v244,1.0,v244\n"
"v_add_f32 v245,1.0,v245\n"
"v_add_f32 v246,1.0,v246\n"
"v_add_f32 v247,1.0,v247\n"
"v_rcp_f32 v244,v244\n"
"v_rcp_f32 v245,v245\n"
"v_rcp_f32 v246,v246\n"
"v_rcp_f32 v247,v247\n"
"buffer_load_dwordx4 v[100:103],v252,s[32:35],s74 offen offset:1024 sc1\n"
"v_mul_f32 v244,v244,v200\n"
"v_mul_f32 v245,v245,v201\n"
"v_mul_f32 v246,v246,v202\n"
"v_mul_f32 v247,v247,v203\n"
"v_cvt_pk_bf16_f32 v186,v244,v245\n"
"v_cvt_pk_bf16_f32 v187,v246,v247\n"
"v_fma_f32 v240,-v192,v192,s79\n"
"v_fma_f32 v241,-v193,v193,s79\n"
"v_fma_f32 v242,-v194,v194,s79\n"
"v_fma_f32 v243,-v195,v195,s79\n"
"v_mul_f32 v240,v240,v192\n"
"v_mul_f32 v241,v241,v193\n"
"v_mul_f32 v242,v242,v194\n"
"v_mul_f32 v243,v243,v195\n"
"v_exp_f32 v240,v240\n"
"s_add_u32 s74,s86,0\n"
"buffer_load_dwordx4 v[104:107],v252,s[32:35],s74 offen sc1\n"
"s_waitcnt vmcnt(5)\n"
"s_barrier\n"
"v_exp_f32 v241,v241\n"
"v_exp_f32 v242,v242\n"
"v_exp_f32 v243,v243\n"
"v_add_f32 v240,1.0,v240\n"
"v_add_f32 v241,1.0,v241\n"
"v_add_f32 v242,1.0,v242\n"
"v_add_f32 v243,1.0,v243\n"
"v_rcp_f32 v240,v240\n"
"v_rcp_f32 v241,v241\n"
"v_rcp_f32 v242,v242\n"
"buffer_load_dwordx4 v[108:111],v252,s[32:35],s74 offen offset:1024 sc1\n"
"v_rcp_f32 v243,v243\n"
"v_mul_f32 v240,v240,v192\n"
"v_mul_f32 v241,v241,v193\n"
"v_mul_f32 v242,v242,v194\n"
"v_mul_f32 v243,v243,v195\n"
"v_cvt_pk_bf16_f32 v184,v240,v241\n"
"v_cvt_pk_bf16_f32 v185,v242,v243\n"
"v_fma_f32 v240,-v196,v196,s79\n"
"v_fma_f32 v241,-v197,v197,s79\n"
"v_fma_f32 v242,-v198,v198,s79\n"
"v_fma_f32 v243,-v199,v199,s79\n"
"v_mul_f32 v240,v240,v196\n"
"v_mul_f32 v241,v241,v197\n"
"v_mul_f32 v242,v242,v198\n"
"v_mul_f32 v243,v243,v199\n"
"s_add_u32 s74,s85,2048\n"
"buffer_load_dwordx4 v[112:115],v252,s[32:35],s74 offen sc1\n"
"v_exp_f32 v240,v240\n"
"v_exp_f32 v241,v241\n"
"v_exp_f32 v242,v242\n"
"v_exp_f32 v243,v243\n"
"v_add_f32 v240,1.0,v240\n"
"v_add_f32 v241,1.0,v241\n"
"v_add_f32 v242,1.0,v242\n"
"v_add_f32 v243,1.0,v243\n"
"v_rcp_f32 v240,v240\n"
"v_rcp_f32 v241,v241\n"
"buffer_load_dwordx4 v[116:119],v252,s[32:35],s74 offen offset:1024 sc1\n"
"v_rcp_f32 v242,v242\n"
"v_rcp_f32 v243,v243\n"
"v_mul_f32 v240,v240,v196\n"
"v_mul_f32 v241,v241,v197\n"
"v_mul_f32 v242,v242,v198\n"
"v_mul_f32 v243,v243,v199\n"
"v_cvt_pk_bf16_f32 v188,v240,v241\n"
"v_cvt_pk_bf16_f32 v189,v242,v243\n"
"v_fma_f32 v244,-v204,v204,s79\n"
"v_fma_f32 v245,-v205,v205,s79\n"
"v_fma_f32 v246,-v206,v206,s79\n"
"v_fma_f32 v247,-v207,v207,s79\n"
"v_mul_f32 v244,v244,v204\n"
"v_mul_f32 v245,v245,v205\n"
"s_waitcnt vmcnt(6)\n"
"v_mov_b32 v208,2\n"
"s_mov_b64 exec,1\n"
"buffer_store_dword v208,off,s[32:35],s88 sc1\n"
"s_mov_b64 exec,-1\n"
"v_mul_f32 v246,v246,v206\n"
"v_mul_f32 v247,v247,v207\n"
"v_exp_f32 v244,v244\n"
"v_exp_f32 v245,v245\n"
"v_exp_f32 v246,v246\n"
"v_exp_f32 v247,v247\n"
"v_add_f32 v244,1.0,v244\n"
"v_add_f32 v245,1.0,v245\n"
"v_add_f32 v246,1.0,v246\n"
"v_add_f32 v247,1.0,v247\n"
"v_rcp_f32 v244,v244\n"
"s_setprio 3\n"
"v_rcp_f32 v245,v245\n"
"ds_read_b64_tr_b16 v[208:209],v248\n"
"ds_read_b64_tr_b16 v[210:211],v248 offset:2048\n"
"v_rcp_f32 v246,v246\n"
"ds_read_b64_tr_b16 v[212:213],v248 offset:12288\n"
"ds_read_b64_tr_b16 v[214:215],v248 offset:14336\n"
"v_rcp_f32 v247,v247\n"
"ds_read_b64_tr_b16 v[216:217],v248 offset:24576\n"
"ds_read_b64_tr_b16 v[218:219],v248 offset:26624\n"
"v_mul_f32 v244,v244,v204\n"
"v_mul_f32 v245,v245,v205\n"
"ds_read_b64_tr_b16 v[220:221],v249\n"
"ds_read_b64_tr_b16 v[222:223],v249 offset:2048\n"
"v_mul_f32 v246,v246,v206\n"
"v_mul_f32 v247,v247,v207\n"
"ds_read_b64_tr_b16 v[224:225],v249 offset:12288\n"
"ds_read_b64_tr_b16 v[226:227],v249 offset:14336\n"
"v_cvt_pk_bf16_f32 v190,v244,v245\n"
"v_cvt_pk_bf16_f32 v191,v246,v247\n"
"ds_read_b64_tr_b16 v[228:229],v249 offset:24576\n"
"ds_read_b64_tr_b16 v[230:231],v249 offset:26624\n"
"s_setprio 1\n"
"s_waitcnt lgkmcnt(6)\n"
"v_mfma_f32_16x16x32_bf16 v[0:3],v[208:211],v[144:147],0\n"
"ds_read_b64_tr_b16 v[232:233],v250\n"
"s_add_u32 s78,s84,10240\n"
"v_mfma_f32_16x16x32_bf16 v[0:3],v[212:215],v[152:155],v[0:3]\n"
"ds_read_b64_tr_b16 v[234:235],v250 offset:2048\n"
"buffer_store_dwordx4 v[184:187],v252,s[32:35],s78 offen sc1\n"
"v_mfma_f32_16x16x32_bf16 v[0:3],v[216:219],v[160:163],v[0:3]\n"
"ds_read_b64_tr_b16 v[236:237],v250 offset:12288\n"
"buffer_store_dwordx4 v[188:191],v252,s[32:35],s78 offen offset:1024 sc1\n"
"v_mfma_f32_16x16x32_bf16 v[4:7],v[208:211],v[148:151],0\n"
"ds_read_b64_tr_b16 v[238:239],v250 offset:14336\n"
"v_mfma_f32_16x16x32_bf16 v[4:7],v[212:215],v[156:159],v[4:7]\n"
"ds_read_b64_tr_b16 v[208:209],v250 offset:24576\n"
"v_mfma_f32_16x16x32_bf16 v[4:7],v[216:219],v[164:167],v[4:7]\n"
"ds_read_b64_tr_b16 v[210:211],v250 offset:26624\n"
"s_waitcnt lgkmcnt(6)\n"
"v_mfma_f32_16x16x32_bf16 v[8:11],v[220:223],v[144:147],0\n"
"ds_read_b64_tr_b16 v[212:213],v253\n"
"v_mfma_f32_16x16x32_bf16 v[8:11],v[224:227],v[152:155],v[8:11]\n"
"ds_read_b64_tr_b16 v[214:215],v253 offset:2048\n"
"v_mfma_f32_16x16x32_bf16 v[8:11],v[228:231],v[160:163],v[8:11]\n"
"ds_read_b64_tr_b16 v[216:217],v253 offset:12288\n"
"v_mfma_f32_16x16x32_bf16 v[12:15],v[220:223],v[148:151],0\n"
"ds_read_b64_tr_b16 v[218:219],v253 offset:14336\n"
"v_mfma_f32_16x16x32_bf16 v[12:15],v[224:227],v[156:159],v[12:15]\n"
"ds_read_b64_tr_b16 v[220:221],v253 offset:24576\n"
"v_mfma_f32_16x16x32_bf16 v[12:15],v[228:231],v[164:167],v[12:15]\n"
"ds_read_b64_tr_b16 v[222:223],v253 offset:26624\n"
"s_waitcnt lgkmcnt(6)\n"
"v_mfma_f32_16x16x32_bf16 v[16:19],v[232:235],v[144:147],0\n"
"ds_read_b64_tr_b16 v[224:225],v248 offset:4096\n"
"v_mfma_f32_16x16x32_bf16 v[16:19],v[236:239],v[152:155],v[16:19]\n"
"ds_read_b64_tr_b16 v[226:227],v248 offset:6144\n"
"v_mfma_f32_16x16x32_bf16 v[16:19],v[208:211],v[160:163],v[16:19]\n"
"ds_read_b64_tr_b16 v[228:229],v248 offset:16384\n"
"v_mfma_f32_16x16x32_bf16 v[20:23],v[232:235],v[148:151],0\n"
"ds_read_b64_tr_b16 v[230:231],v248 offset:18432\n"
"v_mfma_f32_16x16x32_bf16 v[20:23],v[236:239],v[156:159],v[20:23]\n"
"ds_read_b64_tr_b16 v[232:233],v248 offset:28672\n"
"v_mfma_f32_16x16x32_bf16 v[20:23],v[208:211],v[164:167],v[20:23]\n"
"ds_read_b64_tr_b16 v[234:235],v248 offset:30720\n"
"s_waitcnt lgkmcnt(6)\n"
"v_mfma_f32_16x16x32_bf16 v[24:27],v[212:215],v[144:147],0\n"
"ds_read_b64_tr_b16 v[236:237],v249 offset:4096\n"
"v_mfma_f32_16x16x32_bf16 v[24:27],v[216:219],v[152:155],v[24:27]\n"
"ds_read_b64_tr_b16 v[238:239],v249 offset:6144\n"
"v_mfma_f32_16x16x32_bf16 v[24:27],v[220:223],v[160:163],v[24:27]\n"
"ds_read_b64_tr_b16 v[208:209],v249 offset:16384\n"
"v_mfma_f32_16x16x32_bf16 v[28:31],v[212:215],v[148:151],0\n"
"ds_read_b64_tr_b16 v[210:211],v249 offset:18432\n"
"v_mfma_f32_16x16x32_bf16 v[28:31],v[216:219],v[156:159],v[28:31]\n"
"ds_read_b64_tr_b16 v[212:213],v249 offset:28672\n"
"v_mfma_f32_16x16x32_bf16 v[28:31],v[220:223],v[164:167],v[28:31]\n"
"ds_read_b64_tr_b16 v[214:215],v249 offset:30720\n"
"s_waitcnt lgkmcnt(6)\n"
"v_mfma_f32_16x16x32_bf16 v[32:35],v[224:227],v[144:147],0\n"
"ds_read_b64_tr_b16 v[216:217],v250 offset:4096\n"
"v_mfma_f32_16x16x32_bf16 v[32:35],v[228:231],v[152:155],v[32:35]\n"
"ds_read_b64_tr_b16 v[218:219],v250 offset:6144\n"
"v_mfma_f32_16x16x32_bf16 v[32:35],v[232:235],v[160:163],v[32:35]\n"
"ds_read_b64_tr_b16 v[220:221],v250 offset:16384\n"
"v_mfma_f32_16x16x32_bf16 v[36:39],v[224:227],v[148:151],0\n"
"ds_read_b64_tr_b16 v[222:223],v250 offset:18432\n"
"v_mfma_f32_16x16x32_bf16 v[36:39],v[228:231],v[156:159],v[36:39]\n"
"ds_read_b64_tr_b16 v[224:225],v250 offset:28672\n"
"v_mfma_f32_16x16x32_bf16 v[36:39],v[232:235],v[164:167],v[36:39]\n"
"ds_read_b64_tr_b16 v[226:227],v250 offset:30720\n"
"s_waitcnt lgkmcnt(6)\n"
"v_mfma_f32_16x16x32_bf16 v[40:43],v[236:239],v[144:147],0\n"
"ds_read_b64_tr_b16 v[228:229],v253 offset:4096\n"
"v_mfma_f32_16x16x32_bf16 v[40:43],v[208:211],v[152:155],v[40:43]\n"
"ds_read_b64_tr_b16 v[230:231],v253 offset:6144\n"
"v_mfma_f32_16x16x32_bf16 v[40:43],v[212:215],v[160:163],v[40:43]\n"
"ds_read_b64_tr_b16 v[232:233],v253 offset:16384\n"
"v_mfma_f32_16x16x32_bf16 v[44:47],v[236:239],v[148:151],0\n"
"ds_read_b64_tr_b16 v[234:235],v253 offset:18432\n"
"v_mfma_f32_16x16x32_bf16 v[44:47],v[208:211],v[156:159],v[44:47]\n"
"ds_read_b64_tr_b16 v[236:237],v253 offset:28672\n"
"v_mfma_f32_16x16x32_bf16 v[44:47],v[212:215],v[164:167],v[44:47]\n"
"ds_read_b64_tr_b16 v[238:239],v253 offset:30720\n"
"s_waitcnt lgkmcnt(6)\n"
"v_mfma_f32_16x16x32_bf16 v[48:51],v[216:219],v[144:147],0\n"
"ds_read_b64_tr_b16 v[208:209],v248 offset:8192\n"
"v_mfma_f32_16x16x32_bf16 v[48:51],v[220:223],v[152:155],v[48:51]\n"
"ds_read_b64_tr_b16 v[210:211],v248 offset:10240\n"
"v_mfma_f32_16x16x32_bf16 v[48:51],v[224:227],v[160:163],v[48:51]\n"
"ds_read_b64_tr_b16 v[212:213],v248 offset:20480\n"
".Lk_padm8:\n"
"s_cbranch_execz .Lk_padm12\n"
"v_mfma_f32_16x16x32_bf16 v[52:55],v[216:219],v[148:151],0\n"
"ds_read_b64_tr_b16 v[214:215],v248 offset:22528\n"
"v_mfma_f32_16x16x32_bf16 v[52:55],v[220:223],v[156:159],v[52:55]\n"
"ds_read_b64_tr_b16 v[216:217],v248 offset:32768\n"
"v_mfma_f32_16x16x32_bf16 v[52:55],v[224:227],v[164:167],v[52:55]\n"
"ds_read_b64_tr_b16 v[218:219],v248 offset:34816\n"
"s_setprio 2\n"
"s_waitcnt lgkmcnt(6)\n"
"v_mfma_f32_16x16x32_bf16 v[56:59],v[228:231],v[144:147],0\n"
"ds_read_b64_tr_b16 v[220:221],v249 offset:8192\n"
"v_mfma_f32_16x16x32_bf16 v[56:59],v[232:235],v[152:155],v[56:59]\n"
"ds_read_b64_tr_b16 v[222:223],v249 offset:10240\n"
"v_mfma_f32_16x16x32_bf16 v[56:59],v[236:239],v[160:163],v[56:59]\n"
"ds_read_b64_tr_b16 v[224:225],v249 offset:20480\n"
"v_mfma_f32_16x16x32_bf16 v[60:63],v[228:231],v[148:151],0\n"
"ds_read_b64_tr_b16 v[226:227],v249 offset:22528\n"
"v_mfma_f32_16x16x32_bf16 v[60:63],v[232:235],v[156:159],v[60:63]\n"
"ds_read_b64_tr_b16 v[228:229],v249 offset:32768\n"
"v_mfma_f32_16x16x32_bf16 v[60:63],v[236:239],v[164:167],v[60:63]\n"
"ds_read_b64_tr_b16 v[230:231],v249 offset:34816\n"
"s_waitcnt lgkmcnt(6)\n"
"v_mfma_f32_16x16x32_bf16 v[64:67],v[208:211],v[144:147],0\n"
"ds_read_b64_tr_b16 v[232:233],v250 offset:8192\n"
"v_mfma_f32_16x16x32_bf16 v[64:67],v[212:215],v[152:155],v[64:67]\n"
"ds_read_b64_tr_b16 v[234:235],v250 offset:10240\n"
"v_mfma_f32_16x16x32_bf16 v[64:67],v[216:219],v[160:163],v[64:67]\n"
"ds_read_b64_tr_b16 v[236:237],v250 offset:20480\n"
"v_mfma_f32_16x16x32_bf16 v[68:71],v[208:211],v[148:151],0\n"
"ds_read_b64_tr_b16 v[238:239],v250 offset:22528\n"
"v_mfma_f32_16x16x32_bf16 v[68:71],v[212:215],v[156:159],v[68:71]\n"
"ds_read_b64_tr_b16 v[208:209],v250 offset:32768\n"
"v_mfma_f32_16x16x32_bf16 v[68:71],v[216:219],v[164:167],v[68:71]\n"
"ds_read_b64_tr_b16 v[210:211],v250 offset:34816\n"
"s_waitcnt lgkmcnt(6)\n"
"v_mfma_f32_16x16x32_bf16 v[72:75],v[220:223],v[144:147],0\n"
"ds_read_b64_tr_b16 v[212:213],v253 offset:8192\n"
"v_mfma_f32_16x16x32_bf16 v[72:75],v[224:227],v[152:155],v[72:75]\n"
"ds_read_b64_tr_b16 v[214:215],v253 offset:10240\n"
"v_mfma_f32_16x16x32_bf16 v[72:75],v[228:231],v[160:163],v[72:75]\n"
"ds_read_b64_tr_b16 v[216:217],v253 offset:20480\n"
"v_mfma_f32_16x16x32_bf16 v[76:79],v[220:223],v[148:151],0\n"
"ds_read_b64_tr_b16 v[218:219],v253 offset:22528\n"
"v_mfma_f32_16x16x32_bf16 v[76:79],v[224:227],v[156:159],v[76:79]\n"
"ds_read_b64_tr_b16 v[220:221],v253 offset:32768\n"
"v_mfma_f32_16x16x32_bf16 v[76:79],v[228:231],v[164:167],v[76:79]\n"
"ds_read_b64_tr_b16 v[222:223],v253 offset:34816\n"
"s_waitcnt lgkmcnt(6)\n"
"v_mfma_f32_16x16x32_bf16 v[80:83],v[232:235],v[144:147],0\n"
"v_mfma_f32_16x16x32_bf16 v[80:83],v[236:239],v[152:155],v[80:83]\n"
"s_barrier\n"
"v_mfma_f32_16x16x32_bf16 v[80:83],v[208:211],v[160:163],v[80:83]\n"
"v_mfma_f32_16x16x32_bf16 v[84:87],v[232:235],v[148:151],0\n"
"v_mfma_f32_16x16x32_bf16 v[84:87],v[236:239],v[156:159],v[84:87]\n"
"v_mfma_f32_16x16x32_bf16 v[84:87],v[208:211],v[164:167],v[84:87]\n"
"s_waitcnt lgkmcnt(0)\n"
"v_mfma_f32_16x16x32_bf16 v[88:91],v[212:215],v[144:147],0\n"
"v_mfma_f32_16x16x32_bf16 v[88:91],v[216:219],v[152:155],v[88:91]\n"
"v_mfma_f32_16x16x32_bf16 v[88:91],v[220:223],v[160:163],v[88:91]\n"
"v_mfma_f32_16x16x32_bf16 v[92:95],v[212:215],v[148:151],0\n"
"v_mfma_f32_16x16x32_bf16 v[92:95],v[216:219],v[156:159],v[92:95]\n"
"v_mfma_f32_16x16x32_bf16 v[92:95],v[220:223],v[164:167],v[92:95]\n"
"s_setprio 0\n"
"v_add_u32 v248,73728,v248\n"
"v_add_u32 v249,73728,v249\n"
"v_add_u32 v250,73728,v250\n"
"v_add_u32 v253,73728,v253\n"
"s_lshl_b32 s75,s71,12\n"
"s_add_u32 s75,s75,147456\n"
"v_add_u32 v247,s75,v252\n"
"s_add_u32 m0,s73,0\n"
"s_add_u32 s74,s82,0\n"
"buffer_load_dwordx4 v251,s[20:23],s74 offen lds\n"
"s_waitcnt vmcnt(1)\n"
"v_mov_b32 v208,3\n"
"s_mov_b64 exec,1\n"
"buffer_store_dword v208,off,s[32:35],s88 sc1\n"
"s_mov_b64 exec,-1\n"
"s_add_u32 s74,s86,2048\n"
"buffer_load_dwordx4 v[120:123],v252,s[32:35],s74 offen sc1\n"
"s_addk_i32 m0,4096\n"
"s_add_u32 s74,s82,128\n"
"buffer_load_dwordx4 v251,s[20:23],s74 offen lds\n"
"s_add_u32 s74,s86,2048\n"
"buffer_load_dwordx4 v[124:127],v252,s[32:35],s74 offen offset:1024 sc1\n"
"s_addk_i32 m0,4096\n"
"s_add_u32 s74,s82,256\n"
"buffer_load_dwordx4 v251,s[20:23],s74 offen lds\n"
"s_addk_i32 m0,4096\n"
"s_add_u32 s74,s83,0\n"
"buffer_load_dwordx4 v251,s[20:23],s74 offen lds\n"
"s_add_u32 s74,s85,4096\n"
"buffer_load_dwordx4 v[128:131],v252,s[32:35],s74 offen sc1\n"
"s_addk_i32 m0,4096\n"
"s_add_u32 s74,s83,128\n"
"buffer_load_dwordx4 v251,s[20:23],s74 offen lds\n"
"s_add_u32 s74,s85,4096\n"
"buffer_load_dwordx4 v[132:135],v252,s[32:35],s74 offen offset:1024 sc1\n"
"s_addk_i32 m0,4096\n"
"s_add_u32 s74,s83,256\n"
"buffer_load_dwordx4 v251,s[20:23],s74 offen lds\n"
"s_addk_i32 m0,4096\n"
"s_add_u32 s74,s82,73728\n"
"buffer_load_dwordx4 v251,s[20:23],s74 offen lds\n"
"s_add_u32 s74,s86,4096\n"
"buffer_load_dwordx4 v[136:139],v252,s[32:35],s74 offen sc1\n"
"s_addk_i32 m0,4096\n"
"s_add_u32 s74,s82,73856\n"
"buffer_load_dwordx4 v251,s[20:23],s74 offen lds\n"
"s_add_u32 s74,s86,4096\n"
"buffer_load_dwordx4 v[140:143],v252,s[32:35],s74 offen offset:1024 sc1\n"
"s_addk_i32 m0,4096\n"
"s_add_u32 s74,s82,73984\n"
"buffer_load_dwordx4 v251,s[20:23],s74 offen lds\n"
"s_waitcnt lgkmcnt(0)\n"
"s_setprio 3\n"
"s_barrier\n"
"ds_read_b64_tr_b16 v[208:209],v248\n"
"ds_read_b64_tr_b16 v[210:211],v248 offset:2048\n"
"ds_read_b64_tr_b16 v[212:213],v248 offset:12288\n"
"ds_read_b64_tr_b16 v[214:215],v248 offset:14336\n"
"ds_read_b64_tr_b16 v[216:217],v248 offset:24576\n"
"ds_read_b64_tr_b16 v[218:219],v248 offset:26624\n"
"ds_read_b64_tr_b16 v[220:221],v249\n"
"ds_read_b64_tr_b16 v[222:223],v249 offset:2048\n"
"ds_read_b64_tr_b16 v[224:225],v249 offset:12288\n"
"ds_read_b64_tr_b16 v[226:227],v249 offset:14336\n"
"ds_read_b64_tr_b16 v[228:229],v249 offset:24576\n"
"ds_read_b64_tr_b16 v[230:231],v249 offset:26624\n"
"s_setprio 1\n"
"s_waitcnt lgkmcnt(6)\n"
"v_mfma_f32_16x16x32_bf16 v[0:3],v[208:211],v[168:171],v[0:3]\n"
"ds_read_b64_tr_b16 v[232:233],v250\n"
"v_mfma_f32_16x16x32_bf16 v[0:3],v[212:215],v[176:179],v[0:3]\n"
"ds_read_b64_tr_b16 v[234:235],v250 offset:2048\n"
"v_mfma_f32_16x16x32_bf16 v[0:3],v[216:219],v[184:187],v[0:3]\n"
"ds_read_b64_tr_b16 v[236:237],v250 offset:12288\n"
"v_mfma_f32_16x16x32_bf16 v[4:7],v[208:211],v[172:175],v[4:7]\n"
"ds_read_b64_tr_b16 v[238:239],v250 offset:14336\n"
"v_mfma_f32_16x16x32_bf16 v[4:7],v[212:215],v[180:183],v[4:7]\n"
"ds_read_b64_tr_b16 v[208:209],v250 offset:24576\n"
"v_mfma_f32_16x16x32_bf16 v[4:7],v[216:219],v[188:191],v[4:7]\n"
"ds_read_b64_tr_b16 v[210:211],v250 offset:26624\n"
"s_waitcnt lgkmcnt(6)\n"
"v_mfma_f32_16x16x32_bf16 v[8:11],v[220:223],v[168:171],v[8:11]\n"
"ds_read_b64_tr_b16 v[212:213],v253\n"
"v_mfma_f32_16x16x32_bf16 v[8:11],v[224:227],v[176:179],v[8:11]\n"
"ds_read_b64_tr_b16 v[214:215],v253 offset:2048\n"
"v_mfma_f32_16x16x32_bf16 v[8:11],v[228:231],v[184:187],v[8:11]\n"
"ds_read_b64_tr_b16 v[216:217],v253 offset:12288\n"
"v_mfma_f32_16x16x32_bf16 v[12:15],v[220:223],v[172:175],v[12:15]\n"
"ds_read_b64_tr_b16 v[218:219],v253 offset:14336\n"
"v_mfma_f32_16x16x32_bf16 v[12:15],v[224:227],v[180:183],v[12:15]\n"
"ds_read_b64_tr_b16 v[220:221],v253 offset:24576\n"
"v_mfma_f32_16x16x32_bf16 v[12:15],v[228:231],v[188:191],v[12:15]\n"
"ds_read_b64_tr_b16 v[222:223],v253 offset:26624\n"
"s_waitcnt lgkmcnt(6)\n"
"v_mfma_f32_16x16x32_bf16 v[16:19],v[232:235],v[168:171],v[16:19]\n"
"ds_read_b64_tr_b16 v[224:225],v248 offset:4096\n"
"v_mfma_f32_16x16x32_bf16 v[16:19],v[236:239],v[176:179],v[16:19]\n"
"ds_read_b64_tr_b16 v[226:227],v248 offset:6144\n"
"v_mfma_f32_16x16x32_bf16 v[16:19],v[208:211],v[184:187],v[16:19]\n"
"ds_read_b64_tr_b16 v[228:229],v248 offset:16384\n"
"v_mfma_f32_16x16x32_bf16 v[20:23],v[232:235],v[172:175],v[20:23]\n"
"ds_read_b64_tr_b16 v[230:231],v248 offset:18432\n"
"v_mfma_f32_16x16x32_bf16 v[20:23],v[236:239],v[180:183],v[20:23]\n"
"ds_read_b64_tr_b16 v[232:233],v248 offset:28672\n"
"v_mfma_f32_16x16x32_bf16 v[20:23],v[208:211],v[188:191],v[20:23]\n"
"ds_read_b64_tr_b16 v[234:235],v248 offset:30720\n"
"s_waitcnt lgkmcnt(6)\n"
"v_mfma_f32_16x16x32_bf16 v[24:27],v[212:215],v[168:171],v[24:27]\n"
"ds_read_b64_tr_b16 v[236:237],v249 offset:4096\n"
"v_mfma_f32_16x16x32_bf16 v[24:27],v[216:219],v[176:179],v[24:27]\n"
"ds_read_b64_tr_b16 v[238:239],v249 offset:6144\n"
"v_mfma_f32_16x16x32_bf16 v[24:27],v[220:223],v[184:187],v[24:27]\n"
"ds_read_b64_tr_b16 v[208:209],v249 offset:16384\n"
"v_mfma_f32_16x16x32_bf16 v[28:31],v[212:215],v[172:175],v[28:31]\n"
"ds_read_b64_tr_b16 v[210:211],v249 offset:18432\n"
"v_mfma_f32_16x16x32_bf16 v[28:31],v[216:219],v[180:183],v[28:31]\n"
"ds_read_b64_tr_b16 v[212:213],v249 offset:28672\n"
"v_mfma_f32_16x16x32_bf16 v[28:31],v[220:223],v[188:191],v[28:31]\n"
"ds_read_b64_tr_b16 v[214:215],v249 offset:30720\n"
"s_waitcnt lgkmcnt(6)\n"
"v_mfma_f32_16x16x32_bf16 v[32:35],v[224:227],v[168:171],v[32:35]\n"
"ds_read_b64_tr_b16 v[216:217],v250 offset:4096\n"
"v_mfma_f32_16x16x32_bf16 v[32:35],v[228:231],v[176:179],v[32:35]\n"
"ds_read_b64_tr_b16 v[218:219],v250 offset:6144\n"
"v_mfma_f32_16x16x32_bf16 v[32:35],v[232:235],v[184:187],v[32:35]\n"
"ds_read_b64_tr_b16 v[220:221],v250 offset:16384\n"
"v_mfma_f32_16x16x32_bf16 v[36:39],v[224:227],v[172:175],v[36:39]\n"
"ds_read_b64_tr_b16 v[222:223],v250 offset:18432\n"
"v_mfma_f32_16x16x32_bf16 v[36:39],v[228:231],v[180:183],v[36:39]\n"
"ds_read_b64_tr_b16 v[224:225],v250 offset:28672\n"
"v_mfma_f32_16x16x32_bf16 v[36:39],v[232:235],v[188:191],v[36:39]\n"
"ds_read_b64_tr_b16 v[226:227],v250 offset:30720\n"
"buffer_load_dword v240,v255,s[32:35],0 offen sc1\n"
"s_waitcnt lgkmcnt(6)\n"
"v_mfma_f32_16x16x32_bf16 v[40:43],v[236:239],v[168:171],v[40:43]\n"
"ds_read_b64_tr_b16 v[228:229],v253 offset:4096\n"
"v_mfma_f32_16x16x32_bf16 v[40:43],v[208:211],v[176:179],v[40:43]\n"
"ds_read_b64_tr_b16 v[230:231],v253 offset:6144\n"
"v_mfma_f32_16x16x32_bf16 v[40:43],v[212:215],v[184:187],v[40:43]\n"
"ds_read_b64_tr_b16 v[232:233],v253 offset:16384\n"
"v_mfma_f32_16x16x32_bf16 v[44:47],v[236:239],v[172:175],v[44:47]\n"
"ds_read_b64_tr_b16 v[234:235],v253 offset:18432\n"
"v_mfma_f32_16x16x32_bf16 v[44:47],v[208:211],v[180:183],v[44:47]\n"
"ds_read_b64_tr_b16 v[236:237],v253 offset:28672\n"
"v_mfma_f32_16x16x32_bf16 v[44:47],v[212:215],v[188:191],v[44:47]\n"
"ds_read_b64_tr_b16 v[238:239],v253 offset:30720\n"
"s_waitcnt lgkmcnt(6)\n"
"v_mfma_f32_16x16x32_bf16 v[48:51],v[216:219],v[168:171],v[48:51]\n"
"ds_read_b64_tr_b16 v[208:209],v248 offset:8192\n"
"v_mfma_f32_16x16x32_bf16 v[48:51],v[220:223],v[176:179],v[48:51]\n"
"ds_read_b64_tr_b16 v[210:211],v248 offset:10240\n"
"v_mfma_f32_16x16x32_bf16 v[48:51],v[224:227],v[184:187],v[48:51]\n"
"ds_read_b64_tr_b16 v[212:213],v248 offset:20480\n"
"v_mfma_f32_16x16x32_bf16 v[52:55],v[216:219],v[172:175],v[52:55]\n"
"ds_read_b64_tr_b16 v[214:215],v248 offset:22528\n"
"v_mfma_f32_16x16x32_bf16 v[52:55],v[220:223],v[180:183],v[52:55]\n"
"ds_read_b64_tr_b16 v[216:217],v248 offset:32768\n"
"v_mfma_f32_16x16x32_bf16 v[52:55],v[224:227],v[188:191],v[52:55]\n"
"ds_read_b64_tr_b16 v[218:219],v248 offset:34816\n"
"s_setprio 2\n"
"s_waitcnt lgkmcnt(6)\n"
"v_mfma_f32_16x16x32_bf16 v[56:59],v[228:231],v[168:171],v[56:59]\n"
"ds_read_b64_tr_b16 v[220:221],v249 offset:8192\n"
"v_mfma_f32_16x16x32_bf16 v[56:59],v[232:235],v[176:179],v[56:59]\n"
"ds_read_b64_tr_b16 v[222:223],v249 offset:10240\n"
"v_mfma_f32_16x16x32_bf16 v[56:59],v[236:239],v[184:187],v[56:59]\n"
"ds_read_b64_tr_b16 v[224:225],v249 offset:20480\n"
"v_mfma_f32_16x16x32_bf16 v[60:63],v[228:231],v[172:175],v[60:63]\n"
"ds_read_b64_tr_b16 v[226:227],v249 offset:22528\n"
"v_mfma_f32_16x16x32_bf16 v[60:63],v[232:235],v[180:183],v[60:63]\n"
"ds_read_b64_tr_b16 v[228:229],v249 offset:32768\n"
"v_mfma_f32_16x16x32_bf16 v[60:63],v[236:239],v[188:191],v[60:63]\n"
"ds_read_b64_tr_b16 v[230:231],v249 offset:34816\n"
"s_waitcnt lgkmcnt(6)\n"
"v_mfma_f32_16x16x32_bf16 v[64:67],v[208:211],v[168:171],v[64:67]\n"
"ds_read_b64_tr_b16 v[232:233],v250 offset:8192\n"
"v_mfma_f32_16x16x32_bf16 v[64:67],v[212:215],v[176:179],v[64:67]\n"
"ds_read_b64_tr_b16 v[234:235],v250 offset:10240\n"
"v_mfma_f32_16x16x32_bf16 v[64:67],v[216:219],v[184:187],v[64:67]\n"
"ds_read_b64_tr_b16 v[236:237],v250 offset:20480\n"
"v_mfma_f32_16x16x32_bf16 v[68:71],v[208:211],v[172:175],v[68:71]\n"
"ds_read_b64_tr_b16 v[238:239],v250 offset:22528\n"
"v_mfma_f32_16x16x32_bf16 v[68:71],v[212:215],v[180:183],v[68:71]\n"
"ds_read_b64_tr_b16 v[208:209],v250 offset:32768\n"
"v_mfma_f32_16x16x32_bf16 v[68:71],v[216:219],v[188:191],v[68:71]\n"
"ds_read_b64_tr_b16 v[210:211],v250 offset:34816\n"
"s_waitcnt lgkmcnt(6)\n"
"v_mfma_f32_16x16x32_bf16 v[72:75],v[220:223],v[168:171],v[72:75]\n"
"ds_read_b64_tr_b16 v[212:213],v253 offset:8192\n"
"v_mfma_f32_16x16x32_bf16 v[72:75],v[224:227],v[176:179],v[72:75]\n"
"ds_read_b64_tr_b16 v[214:215],v253 offset:10240\n"
"v_mfma_f32_16x16x32_bf16 v[72:75],v[228:231],v[184:187],v[72:75]\n"
"ds_read_b64_tr_b16 v[216:217],v253 offset:20480\n"
"v_mfma_f32_16x16x32_bf16 v[76:79],v[220:223],v[172:175],v[76:79]\n"
"ds_read_b64_tr_b16 v[218:219],v253 offset:22528\n"
"v_mfma_f32_16x16x32_bf16 v[76:79],v[224:227],v[180:183],v[76:79]\n"
"ds_read_b64_tr_b16 v[220:221],v253 offset:32768\n"
"v_mfma_f32_16x16x32_bf16 v[76:79],v[228:231],v[188:191],v[76:79]\n"
"ds_read_b64_tr_b16 v[222:223],v253 offset:34816\n"
"s_waitcnt vmcnt(1)\n"
"s_waitcnt lgkmcnt(6)\n"
"v_mfma_f32_16x16x32_bf16 v[80:83],v[232:235],v[168:171],v[80:83]\n"
"v_mfma_f32_16x16x32_bf16 v[80:83],v[236:239],v[176:179],v[80:83]\n"
"s_barrier\n"
"v_mfma_f32_16x16x32_bf16 v[80:83],v[208:211],v[184:187],v[80:83]\n"
"v_mfma_f32_16x16x32_bf16 v[84:87],v[232:235],v[172:175],v[84:87]\n"
"v_mfma_f32_16x16x32_bf16 v[84:87],v[236:239],v[180:183],v[84:87]\n"
"v_mfma_f32_16x16x32_bf16 v[84:87],v[208:211],v[188:191],v[84:87]\n"
"s_waitcnt lgkmcnt(0)\n"
"v_mfma_f32_16x16x32_bf16 v[88:91],v[212:215],v[168:171],v[88:91]\n"
"v_mfma_f32_16x16x32_bf16 v[88:91],v[216:219],v[176:179],v[88:91]\n"
"v_mfma_f32_16x16x32_bf16 v[88:91],v[220:223],v[184:187],v[88:91]\n"
"v_mfma_f32_16x16x32_bf16 v[92:95],v[212:215],v[172:175],v[92:95]\n"
"v_mfma_f32_16x16x32_bf16 v[92:95],v[216:219],v[180:183],v[92:95]\n"
"v_mfma_f32_16x16x32_bf16 v[92:95],v[220:223],v[188:191],v[92:95]\n"
"s_setprio 0\n"
"v_add_u32 v248,4294893568,v248\n"
"v_add_u32 v249,4294893568,v249\n"
"v_add_u32 v250,4294893568,v250\n"
"v_add_u32 v253,4294893568,v253\n"
"s_add_u32 m0,s73,73728\n"
"s_add_u32 s74,s83,73728\n"
"buffer_load_dwordx4 v251,s[20:23],s74 offen lds\n"
"s_addk_i32 m0,4096\n"
"s_add_u32 s74,s82,147456\n"
"buffer_load_dwordx4 v251,s[20:23],s74 offen lds\n"
"s_waitcnt vmcnt(2)\n"
"v_cmp_gt_u32 vcc,2,v240\n"
"s_nop 1\n"
"s_cbranch_vccz .Lk_h2g1_ok\n"
"s_mov_b32 s89,0x100000\n"
".Lk_h2g1_spin:\n"
"s_sleep 2\n"
"buffer_load_dword v240,v255,s[32:35],0 offen sc1\n"
"s_waitcnt vmcnt(0)\n"
"v_cmp_gt_u32 vcc,2,v240\n"
"s_nop 1\n"
"s_cbranch_vccz .Lk_h2g1_ok\n"
"s_sub_u32 s89,s89,1\n"
"s_cmp_lg_u32 s89,0\n"
"s_cbranch_scc1 .Lk_h2g1_spin\n"
".Lk_h2g1_ok:\n"
"s_add_u32 m0,s73,81920\n"
"s_add_u32 s74,s83,147456\n"
"buffer_load_dwordx4 v251,s[20:23],s74 offen lds\n"
"s_add_u32 s74,s85,6144\n"
"buffer_load_dwordx4 v[192:195],v252,s[32:35],s74 offen sc1\n"
"s_addk_i32 m0,4096\n"
"s_add_u32 s74,s82,221184\n"
"buffer_load_dwordx4 v251,s[20:23],s74 offen lds\n"
"s_add_u32 s74,s85,6144\n"
"buffer_load_dwordx4 v[196:199],v252,s[32:35],s74 offen offset:1024 sc1\n"
"s_addk_i32 m0,4096\n"
"s_add_u32 s74,s83,221184\n"
"buffer_load_dwordx4 v251,s[20:23],s74 offen lds\n"
"s_add_u32 s74,s86,6144\n"
"buffer_load_dwordx4 v[200:203],v252,s[32:35],s74 offen sc1\n"
"s_addk_i32 m0,4096\n"
"s_add_u32 s74,s82,294912\n"
"buffer_load_dwordx4 v251,s[20:23],s74 offen lds\n"
"s_add_u32 s74,s86,6144\n"
"buffer_load_dwordx4 v[204:207],v252,s[32:35],s74 offen offset:1024 sc1\n"
"s_addk_i32 m0,4096\n"
"s_add_u32 s74,s83,294912\n"
"buffer_load_dwordx4 v251,s[20:23],s74 offen lds\n"
"s_add_u32 s74,s85,8192\n"
"buffer_load_dwordx4 v[144:147],v252,s[32:35],s74 offen sc1\n"
"s_addk_i32 m0,4096\n"
"s_add_u32 s74,s82,368640\n"
"buffer_load_dwordx4 v251,s[20:23],s74 offen lds\n"
"s_add_u32 s74,s85,8192\n"
"buffer_load_dwordx4 v[148:151],v252,s[32:35],s74 offen offset:1024 sc1\n"
"s_addk_i32 m0,4096\n"
"s_add_u32 s74,s83,368640\n"
"buffer_load_dwordx4 v251,s[20:23],s74 offen lds\n"
"s_waitcnt lgkmcnt(0)\n"
"s_setprio 3\n"
"s_barrier\n"
"ds_read_b64_tr_b16 v[208:209],v248\n"
"ds_read_b64_tr_b16 v[210:211],v248 offset:2048\n"
"ds_read_b64_tr_b16 v[212:213],v248 offset:12288\n"
"ds_read_b64_tr_b16 v[214:215],v248 offset:14336\n"
"ds_read_b64_tr_b16 v[216:217],v248 offset:24576\n"
"ds_read_b64_tr_b16 v[218:219],v248 offset:26624\n"
"ds_read_b64_tr_b16 v[220:221],v249\n"
"ds_read_b64_tr_b16 v[222:223],v249 offset:2048\n"
"ds_read_b64_tr_b16 v[224:225],v249 offset:12288\n"
"ds_read_b64_tr_b16 v[226:227],v249 offset:14336\n"
"ds_read_b64_tr_b16 v[228:229],v249 offset:24576\n"
"ds_read_b64_tr_b16 v[230:231],v249 offset:26624\n"
"s_setprio 1\n"
"s_waitcnt lgkmcnt(6)\n"
"v_mfma_f32_16x16x32_bf16 v[0:3],v[208:211],v[96:99],v[0:3]\n"
"ds_read_b64_tr_b16 v[232:233],v250\n"
"v_mfma_f32_16x16x32_bf16 v[0:3],v[212:215],v[104:107],v[0:3]\n"
"ds_read_b64_tr_b16 v[234:235],v250 offset:2048\n"
"v_mfma_f32_16x16x32_bf16 v[0:3],v[216:219],v[112:115],v[0:3]\n"
"ds_read_b64_tr_b16 v[236:237],v250 offset:12288\n"
"v_mfma_f32_16x16x32_bf16 v[4:7],v[208:211],v[100:103],v[4:7]\n"
"ds_read_b64_tr_b16 v[238:239],v250 offset:14336\n"
"v_mfma_f32_16x16x32_bf16 v[4:7],v[212:215],v[108:111],v[4:7]\n"
"ds_read_b64_tr_b16 v[208:209],v250 offset:24576\n"
"v_mfma_f32_16x16x32_bf16 v[4:7],v[216:219],v[116:119],v[4:7]\n"
"ds_read_b64_tr_b16 v[210:211],v250 offset:26624\n"
"s_waitcnt lgkmcnt(6)\n"
"v_mfma_f32_16x16x32_bf16 v[8:11],v[220:223],v[96:99],v[8:11]\n"
"ds_read_b64_tr_b16 v[212:213],v253\n"
"v_mfma_f32_16x16x32_bf16 v[8:11],v[224:227],v[104:107],v[8:11]\n"
"ds_read_b64_tr_b16 v[214:215],v253 offset:2048\n"
"v_mfma_f32_16x16x32_bf16 v[8:11],v[228:231],v[112:115],v[8:11]\n"
"ds_read_b64_tr_b16 v[216:217],v253 offset:12288\n"
"v_mfma_f32_16x16x32_bf16 v[12:15],v[220:223],v[100:103],v[12:15]\n"
"ds_read_b64_tr_b16 v[218:219],v253 offset:14336\n"
"v_mfma_f32_16x16x32_bf16 v[12:15],v[224:227],v[108:111],v[12:15]\n"
"ds_read_b64_tr_b16 v[220:221],v253 offset:24576\n"
"v_mfma_f32_16x16x32_bf16 v[12:15],v[228:231],v[116:119],v[12:15]\n"
"ds_read_b64_tr_b16 v[222:223],v253 offset:26624\n"
"s_waitcnt lgkmcnt(6)\n"
"v_mfma_f32_16x16x32_bf16 v[16:19],v[232:235],v[96:99],v[16:19]\n"
"ds_read_b64_tr_b16 v[224:225],v248 offset:4096\n"
"v_mfma_f32_16x16x32_bf16 v[16:19],v[236:239],v[104:107],v[16:19]\n"
"ds_read_b64_tr_b16 v[226:227],v248 offset:6144\n"
"v_mfma_f32_16x16x32_bf16 v[16:19],v[208:211],v[112:115],v[16:19]\n"
"ds_read_b64_tr_b16 v[228:229],v248 offset:16384\n"
"v_mfma_f32_16x16x32_bf16 v[20:23],v[232:235],v[100:103],v[20:23]\n"
"ds_read_b64_tr_b16 v[230:231],v248 offset:18432\n"
"v_mfma_f32_16x16x32_bf16 v[20:23],v[236:239],v[108:111],v[20:23]\n"
"ds_read_b64_tr_b16 v[232:233],v248 offset:28672\n"
"v_mfma_f32_16x16x32_bf16 v[20:23],v[208:211],v[116:119],v[20:23]\n"
"ds_read_b64_tr_b16 v[234:235],v248 offset:30720\n"
"s_waitcnt lgkmcnt(6)\n"
"v_mfma_f32_16x16x32_bf16 v[24:27],v[212:215],v[96:99],v[24:27]\n"
"ds_read_b64_tr_b16 v[236:237],v249 offset:4096\n"
"v_mfma_f32_16x16x32_bf16 v[24:27],v[216:219],v[104:107],v[24:27]\n"
"ds_read_b64_tr_b16 v[238:239],v249 offset:6144\n"
"v_mfma_f32_16x16x32_bf16 v[24:27],v[220:223],v[112:115],v[24:27]\n"
"ds_read_b64_tr_b16 v[208:209],v249 offset:16384\n"
"v_mfma_f32_16x16x32_bf16 v[28:31],v[212:215],v[100:103],v[28:31]\n"
"ds_read_b64_tr_b16 v[210:211],v249 offset:18432\n"
"v_mfma_f32_16x16x32_bf16 v[28:31],v[216:219],v[108:111],v[28:31]\n"
"ds_read_b64_tr_b16 v[212:213],v249 offset:28672\n"
"v_mfma_f32_16x16x32_bf16 v[28:31],v[220:223],v[116:119],v[28:31]\n"
"ds_read_b64_tr_b16 v[214:215],v249 offset:30720\n"
"s_waitcnt lgkmcnt(6)\n"
"v_mfma_f32_16x16x32_bf16 v[32:35],v[224:227],v[96:99],v[32:35]\n"
"ds_read_b64_tr_b16 v[216:217],v250 offset:4096\n"
"v_mfma_f32_16x16x32_bf16 v[32:35],v[228:231],v[104:107],v[32:35]\n"
"ds_read_b64_tr_b16 v[218:219],v250 offset:6144\n"
"v_mfma_f32_16x16x32_bf16 v[32:35],v[232:235],v[112:115],v[32:35]\n"
"ds_read_b64_tr_b16 v[220:221],v250 offset:16384\n"
"v_mfma_f32_16x16x32_bf16 v[36:39],v[224:227],v[100:103],v[36:39]\n"
"ds_read_b64_tr_b16 v[222:223],v250 offset:18432\n"
"v_mfma_f32_16x16x32_bf16 v[36:39],v[228:231],v[108:111],v[36:39]\n"
"ds_read_b64_tr_b16 v[224:225],v250 offset:28672\n"
"v_mfma_f32_16x16x32_bf16 v[36:39],v[232:235],v[116:119],v[36:39]\n"
"ds_read_b64_tr_b16 v[226:227],v250 offset:30720\n"
"buffer_load_dword v240,v255,s[32:35],0 offen sc1\n"
"s_waitcnt lgkmcnt(6)\n"
"v_mfma_f32_16x16x32_bf16 v[40:43],v[236:239],v[96:99],v[40:43]\n"
"ds_read_b64_tr_b16 v[228:229],v253 offset:4096\n"
"v_mfma_f32_16x16x32_bf16 v[40:43],v[208:211],v[104:107],v[40:43]\n"
"ds_read_b64_tr_b16 v[230:231],v253 offset:6144\n"
"v_mfma_f32_16x16x32_bf16 v[40:43],v[212:215],v[112:115],v[40:43]\n"
"ds_read_b64_tr_b16 v[232:233],v253 offset:16384\n"
"v_mfma_f32_16x16x32_bf16 v[44:47],v[236:239],v[100:103],v[44:47]\n"
"ds_read_b64_tr_b16 v[234:235],v253 offset:18432\n"
"v_mfma_f32_16x16x32_bf16 v[44:47],v[208:211],v[108:111],v[44:47]\n"
"ds_read_b64_tr_b16 v[236:237],v253 offset:28672\n"
"v_mfma_f32_16x16x32_bf16 v[44:47],v[212:215],v[116:119],v[44:47]\n"
"ds_read_b64_tr_b16 v[238:239],v253 offset:30720\n"
"s_waitcnt lgkmcnt(6)\n"
"v_mfma_f32_16x16x32_bf16 v[48:51],v[216:219],v[96:99],v[48:51]\n"
"ds_read_b64_tr_b16 v[208:209],v248 offset:8192\n"
"v_mfma_f32_16x16x32_bf16 v[48:51],v[220:223],v[104:107],v[48:51]\n"
"ds_read_b64_tr_b16 v[210:211],v248 offset:10240\n"
"v_mfma_f32_16x16x32_bf16 v[48:51],v[224:227],v[112:115],v[48:51]\n"
"ds_read_b64_tr_b16 v[212:213],v248 offset:20480\n"
"v_mfma_f32_16x16x32_bf16 v[52:55],v[216:219],v[100:103],v[52:55]\n"
"ds_read_b64_tr_b16 v[214:215],v248 offset:22528\n"
"v_mfma_f32_16x16x32_bf16 v[52:55],v[220:223],v[108:111],v[52:55]\n"
"ds_read_b64_tr_b16 v[216:217],v248 offset:32768\n"
"v_mfma_f32_16x16x32_bf16 v[52:55],v[224:227],v[116:119],v[52:55]\n"
"ds_read_b64_tr_b16 v[218:219],v248 offset:34816\n"
"s_setprio 2\n"
"s_waitcnt lgkmcnt(6)\n"
"v_mfma_f32_16x16x32_bf16 v[56:59],v[228:231],v[96:99],v[56:59]\n"
"ds_read_b64_tr_b16 v[220:221],v249 offset:8192\n"
"v_mfma_f32_16x16x32_bf16 v[56:59],v[232:235],v[104:107],v[56:59]\n"
"ds_read_b64_tr_b16 v[222:223],v249 offset:10240\n"
"v_mfma_f32_16x16x32_bf16 v[56:59],v[236:239],v[112:115],v[56:59]\n"
"ds_read_b64_tr_b16 v[224:225],v249 offset:20480\n"
"v_mfma_f32_16x16x32_bf16 v[60:63],v[228:231],v[100:103],v[60:63]\n"
"ds_read_b64_tr_b16 v[226:227],v249 offset:22528\n"
"v_mfma_f32_16x16x32_bf16 v[60:63],v[232:235],v[108:111],v[60:63]\n"
"ds_read_b64_tr_b16 v[228:229],v249 offset:32768\n"
"v_mfma_f32_16x16x32_bf16 v[60:63],v[236:239],v[116:119],v[60:63]\n"
"ds_read_b64_tr_b16 v[230:231],v249 offset:34816\n"
"s_waitcnt lgkmcnt(6)\n"
"v_mfma_f32_16x16x32_bf16 v[64:67],v[208:211],v[96:99],v[64:67]\n"
"ds_read_b64_tr_b16 v[232:233],v250 offset:8192\n"
"v_mfma_f32_16x16x32_bf16 v[64:67],v[212:215],v[104:107],v[64:67]\n"
"ds_read_b64_tr_b16 v[234:235],v250 offset:10240\n"
"v_mfma_f32_16x16x32_bf16 v[64:67],v[216:219],v[112:115],v[64:67]\n"
"ds_read_b64_tr_b16 v[236:237],v250 offset:20480\n"
"v_mfma_f32_16x16x32_bf16 v[68:71],v[208:211],v[100:103],v[68:71]\n"
"ds_read_b64_tr_b16 v[238:239],v250 offset:22528\n"
"v_mfma_f32_16x16x32_bf16 v[68:71],v[212:215],v[108:111],v[68:71]\n"
"ds_read_b64_tr_b16 v[208:209],v250 offset:32768\n"
"v_mfma_f32_16x16x32_bf16 v[68:71],v[216:219],v[116:119],v[68:71]\n"
"ds_read_b64_tr_b16 v[210:211],v250 offset:34816\n"
"s_waitcnt lgkmcnt(6)\n"
"v_mfma_f32_16x16x32_bf16 v[72:75],v[220:223],v[96:99],v[72:75]\n"
"ds_read_b64_tr_b16 v[212:213],v253 offset:8192\n"
"v_mfma_f32_16x16x32_bf16 v[72:75],v[224:227],v[104:107],v[72:75]\n"
"ds_read_b64_tr_b16 v[214:215],v253 offset:10240\n"
"v_mfma_f32_16x16x32_bf16 v[72:75],v[228:231],v[112:115],v[72:75]\n"
"ds_read_b64_tr_b16 v[216:217],v253 offset:20480\n"
"v_mfma_f32_16x16x32_bf16 v[76:79],v[220:223],v[100:103],v[76:79]\n"
"ds_read_b64_tr_b16 v[218:219],v253 offset:22528\n"
"v_mfma_f32_16x16x32_bf16 v[76:79],v[224:227],v[108:111],v[76:79]\n"
"ds_read_b64_tr_b16 v[220:221],v253 offset:32768\n"
"v_mfma_f32_16x16x32_bf16 v[76:79],v[228:231],v[116:119],v[76:79]\n"
"ds_read_b64_tr_b16 v[222:223],v253 offset:34816\n"
"s_waitcnt vmcnt(1)\n"
"s_waitcnt lgkmcnt(6)\n"
"v_mfma_f32_16x16x32_bf16 v[80:83],v[232:235],v[96:99],v[80:83]\n"
"v_mfma_f32_16x16x32_bf16 v[80:83],v[236:239],v[104:107],v[80:83]\n"
"s_barrier\n"
"v_mfma_f32_16x16x32_bf16 v[80:83],v[208:211],v[112:115],v[80:83]\n"
"v_mfma_f32_16x16x32_bf16 v[84:87],v[232:235],v[100:103],v[84:87]\n"
"v_mfma_f32_16x16x32_bf16 v[84:87],v[236:239],v[108:111],v[84:87]\n"
"v_mfma_f32_16x16x32_bf16 v[84:87],v[208:211],v[116:119],v[84:87]\n"
"s_waitcnt lgkmcnt(0)\n"
"v_mfma_f32_16x16x32_bf16 v[88:91],v[212:215],v[96:99],v[88:91]\n"
"v_mfma_f32_16x16x32_bf16 v[88:91],v[216:219],v[104:107],v[88:91]\n"
"v_mfma_f32_16x16x32_bf16 v[88:91],v[220:223],v[112:115],v[88:91]\n"
"v_mfma_f32_16x16x32_bf16 v[92:95],v[212:215],v[100:103],v[92:95]\n"
"v_mfma_f32_16x16x32_bf16 v[92:95],v[216:219],v[108:111],v[92:95]\n"
"v_mfma_f32_16x16x32_bf16 v[92:95],v[220:223],v[116:119],v[92:95]\n"
"s_setprio 0\n"
"v_add_u32 v248,73728,v248\n"
"v_add_u32 v249,73728,v249\n"
"v_add_u32 v250,73728,v250\n"
"v_add_u32 v253,73728,v253\n"
"s_add_u32 m0,s73,0\n"
"s_add_u32 s74,s83,73856\n"
"buffer_load_dwordx4 v251,s[20:23],s74 offen lds\n"
"s_addk_i32 m0,4096\n"
"s_add_u32 s74,s82,147584\n"
"buffer_load_dwordx4 v251,s[20:23],s74 offen lds\n"
"s_waitcnt vmcnt(2)\n"
"v_cmp_gt_u32 vcc,3,v240\n"
"s_nop 1\n"
"s_cbranch_vccz .Lk_h3g1_ok\n"
"s_mov_b32 s89,0x100000\n"
".Lk_h3g1_spin:\n"
"s_sleep 2\n"
"buffer_load_dword v240,v255,s[32:35],0 offen sc1\n"
"s_waitcnt vmcnt(0)\n"
"v_cmp_gt_u32 vcc,3,v240\n"
"s_nop 1\n"
"s_cbranch_vccz .Lk_h3g1_ok\n"
"s_sub_u32 s89,s89,1\n"
"s_cmp_lg_u32 s89,0\n"
"s_cbranch_scc1 .Lk_h3g1_spin\n"
".Lk_h3g1_ok:\n"
"s_add_u32 m0,s73,8192\n"
"s_add_u32 s74,s83,147584\n"
"buffer_load_dwordx4 v251,s[20:23],s74 offen lds\n"
"v_lshrrev_b32 v254,4,v252\n"
"v_bfe_u32 v182,v254,2,2\n"
"v_bfe_u32 v183,v254,4,1\n"
"v_lshl_add_u32 v182,v182,2,v183\n"
"v_lshrrev_b32 v189,5,v254\n"
"s_add_u32 s74,s86,8192\n"
"buffer_load_dwordx4 v[152:155],v252,s[32:35],s74 offen sc1\n"
"v_and_b32 v183,3,v254\n"
"v_lshl_add_u32 v189,v189,2,v183\n"
"v_lshlrev_b32 v189,3,v189\n"
"s_lshr_b32 s75,s71,1\n"
"s_and_b32 s76,s71,1\n"
"s_addk_i32 m0,4096\n"
"s_add_u32 s74,s82,221312\n"
"buffer_load_dwordx4 v251,s[20:23],s74 offen lds\n"
"s_lshl_b32 s77,s69,3\n"
"s_add_u32 s77,s77,s75\n"
"s_lshl_b32 s78,s77,5\n"
"s_add_u32 s78,s78,147456\n"
"v_lshl_add_u32 v188,v182,4,s78\n"
"s_add_u32 s74,s86,8192\n"
"buffer_load_dwordx4 v[156:159],v252,s[32:35],s74 offen offset:1024 sc1\n"
"s_setprio 3\n"
"ds_read_b128 v[184:187],v188\n"
"s_setprio 0\n"
"v_mul_u32_u24 v188,2304,v182\n"
"v_add_u32 v188,v188,v189\n"
"s_mul_i32 s89,s70,6\n"
"s_add_u32 s89,s89,s76\n"
"s_addk_i32 m0,4096\n"
"s_add_u32 s74,s83,221312\n"
"buffer_load_dwordx4 v251,s[20:23],s74 offen lds\n"
"s_lshl_b32 s78,s89,6\n"
"v_add_u32 v189,s78,v189\n"
"s_lshl_b32 s84,s68,5\n"
"s_add_u32 s84,s84,s77\n"
"s_mul_i32 s84,s84,4608\n"
"s_add_u32 s74,s85,10240\n"
"buffer_load_dwordx4 v[160:163],v252,s[32:35],s74 offen sc1\n"
"s_add_u32 s84,s84,s78\n"
"s_lshl_b32 s78,s68,2\n"
"s_add_u32 s78,s78,s69\n"
"s_lshl_b32 s78,s78,1\n"
"s_add_u32 s78,s78,s75\n"
"s_addk_i32 m0,4096\n"
"s_add_u32 s74,s82,295040\n"
".Lk_padm10:\n"
"s_cbranch_execz .Lk_hop_retm\n"
"buffer_load_dwordx4 v251,s[20:23],s74 offen lds\n"
"s_mul_i32 s80,s78,18\n"
"s_add_u32 s80,s80,s89\n"
"s_lshl_b32 s80,s80,10\n"
"s_add_u32 s80,s80,20971520\n"
"s_mul_i32 s75,s68,12\n"
"s_add_u32 s75,s75,s70\n"
"s_add_u32 s74,s85,10240\n"
"buffer_load_dwordx4 v[164:167],v252,s[32:35],s74 offen offset:1024 sc1\n"
"s_lshl_b32 s75,s75,10\n"
"s_add_u32 s75,s75,256\n"
"s_lshl_b32 s77,s68,12\n"
"s_lshl_b32 s78,s76,8\n"
"s_add_u32 s77,s77,s78\n"
"s_addk_i32 m0,4096\n"
"s_add_u32 s74,s83,295040\n"
"buffer_load_dwordx4 v251,s[20:23],s74 offen lds\n"
"s_add_u32 s77,s77,196608\n"
"v_mul_u32_u24 v190,3072,v183\n"
"v_add_u32 v190,s75,v190\n"
"v_lshl_add_u32 v183,v183,10,s77\n"
"s_mov_b32 s96,0xf0f0f0f0\n"
"s_add_u32 s74,s86,10240\n"
"buffer_load_dwordx4 v[168:171],v252,s[32:35],s74 offen sc1\n"
"s_mov_b32 s97,0xf0f0f0f0\n"
"v_cndmask_b32 v190,v190,v183,s[96:97]\n"
"s_lshl_b32 s95,s69,6\n"
"s_mov_b32 s79,0x40088e99\n"
"v_add_u32 v191,s73,v252\n"
"s_addk_i32 m0,4096\n"
"s_add_u32 s74,s82,368768\n"
"buffer_load_dwordx4 v251,s[20:23],s74 offen lds\n"
"v_mov_b32 v176,0\n"
"v_mov_b32 v177,0\n"
"v_mov_b32 v180,0\n"
"v_mov_b32 v181,0\n"
"s_mov_b32 s90,0xcccccccc\n"
"s_add_u32 s74,s86,10240\n"
"buffer_load_dwordx4 v[172:175],v252,s[32:35],s74 offen offset:1024 sc1\n"
"s_mov_b32 s91,0xcccccccc\n"
"s_mov_b32 s92,0xaaaaaaaa\n"
"s_mov_b32 s93,0xaaaaaaaa\n"
"v_mov_b32 v208,0x3f80\n"
"v_cndmask_b32 v208,v208,1.0,s[92:93]\n"
"s_addk_i32 m0,4096\n"
"s_add_u32 s74,s83,368768\n"
"buffer_load_dwordx4 v251,s[20:23],s74 offen lds\n"
"v_bfe_u32 v209,v254,2,2\n"
"v_lshrrev_b32 v210,4,v254\n"
"v_cmp_eq_u32 vcc,v209,v210\n"
"s_nop 1\n"
"v_cndmask_b32 v208,0,v208,vcc\n"
"v_cndmask_b32 v178,v208,0,s[90:91]\n"
"v_cndmask_b32 v179,0,v208,s[90:91]\n"
"s_waitcnt lgkmcnt(0)\n"
"s_setprio 3\n"
"s_barrier\n"
"ds_read_b64_tr_b16 v[208:209],v248\n"
"ds_read_b64_tr_b16 v[210:211],v248 offset:2048\n"
"ds_read_b64_tr_b16 v[212:213],v248 offset:4096\n"
"ds_read_b64_tr_b16 v[214:215],v248 offset:6144\n"
"ds_read_b64_tr_b16 v[216:217],v248 offset:8192\n"
"ds_read_b64_tr_b16 v[218:219],v248 offset:10240\n"
"ds_read_b64_tr_b16 v[220:221],v248 offset:12288\n"
"ds_read_b64_tr_b16 v[222:223],v248 offset:14336\n"
"ds_read_b64_tr_b16 v[224:225],v248 offset:16384\n"
"ds_read_b64_tr_b16 v[226:227],v248 offset:18432\n"
"ds_read_b64_tr_b16 v[228:229],v248 offset:20480\n"
"ds_read_b64_tr_b16 v[230:231],v248 offset:22528\n"
"s_setprio 1\n"
"s_waitcnt lgkmcnt(8)\n"
"v_mfma_f32_16x16x32_bf16 v[0:3],v[208:211],v[120:123],v[0:3]\n"
"ds_read_b64_tr_b16 v[232:233],v249\n"
"v_mfma_f32_16x16x32_bf16 v[0:3],v[212:215],v[128:131],v[0:3]\n"
"ds_read_b64_tr_b16 v[234:235],v249 offset:2048\n"
"v_mfma_f32_16x16x32_bf16 v[4:7],v[208:211],v[124:127],v[4:7]\n"
"ds_read_b64_tr_b16 v[236:237],v249 offset:4096\n"
"v_mfma_f32_16x16x32_bf16 v[4:7],v[212:215],v[132:135],v[4:7]\n"
"ds_read_b64_tr_b16 v[238:239],v249 offset:6144\n"
"s_waitcnt lgkmcnt(8)\n"
"v_mfma_f32_16x16x32_bf16 v[4:7],v[216:219],v[140:143],v[4:7]\n"
"ds_read_b64_tr_b16 v[208:209],v249 offset:8192\n"
"v_mfma_f32_16x16x32_bf16 v[4:7],v[220:223],v[196:199],v[4:7]\n"
"ds_read_b64_tr_b16 v[210:211],v249 offset:10240\n"
"v_mfma_f32_16x16x32_bf16 v[0:3],v[216:219],v[136:139],v[0:3]\n"
"ds_read_b64_tr_b16 v[212:213],v249 offset:12288\n"
"v_mfma_f32_16x16x32_bf16 v[0:3],v[220:223],v[192:195],v[0:3]\n"
"ds_read_b64_tr_b16 v[214:215],v249 offset:14336\n"
"s_waitcnt lgkmcnt(8)\n"
"v_mfma_f32_16x16x32_bf16 v[0:3],v[224:227],v[200:203],v[0:3]\n"
"ds_read_b64_tr_b16 v[216:217],v249 offset:16384\n"
"v_mfma_f32_16x16x32_bf16 v[0:3],v[228:231],v[144:147],v[0:3]\n"
"ds_read_b64_tr_b16 v[218:219],v249 offset:18432\n"
"v_mfma_f32_16x16x32_bf16 v[4:7],v[224:227],v[204:207],v[4:7]\n"
"ds_read_b64_tr_b16 v[220:221],v249 offset:20480\n"
"v_mfma_f32_16x16x32_bf16 v[4:7],v[228:231],v[148:151],v[4:7]\n"
"ds_read_b64_tr_b16 v[222:223],v249 offset:22528\n"
"s_waitcnt lgkmcnt(8)\n"
"v_mfma_f32_16x16x32_bf16 v[8:11],v[232:235],v[120:123],v[8:11]\n"
"ds_read_b64_tr_b16 v[224:225],v250\n"
"v_mfma_f32_16x16x32_bf16 v[8:11],v[236:239],v[128:131],v[8:11]\n"
"ds_read_b64_tr_b16 v[226:227],v250 offset:2048\n"
"v_mfma_f32_16x16x32_bf16 v[12:15],v[232:235],v[124:127],v[12:15]\n"
"ds_read_b64_tr_b16 v[228:229],v250 offset:4096\n"
"v_mfma_f32_16x16x32_bf16 v[12:15],v[236:239],v[132:135],v[12:15]\n"
"ds_read_b64_tr_b16 v[230:231],v250 offset:6144\n"
"s_waitcnt lgkmcnt(8)\n"
"v_mfma_f32_16x16x32_bf16 v[12:15],v[208:211],v[140:143],v[12:15]\n"
"ds_read_b64_tr_b16 v[232:233],v250 offset:8192\n"
"v_mfma_f32_16x16x32_bf16 v[12:15],v[212:215],v[196:199],v[12:15]\n"
"ds_read_b64_tr_b16 v[234:235],v250 offset:10240\n"
"v_mfma_f32_16x16x32_bf16 v[8:11],v[208:211],v[136:139],v[8:11]\n"
"ds_read_b64_tr_b16 v[236:237],v250 offset:12288\n"
"v_mfma_f32_16x16x32_bf16 v[8:11],v[212:215],v[192:195],v[8:11]\n"
"ds_read_b64_tr_b16 v[238:239],v250 offset:14336\n"
"s_waitcnt lgkmcnt(8)\n"
"v_mfma_f32_16x16x32_bf16 v[8:11],v[216:219],v[200:203],v[8:11]\n"
"ds_read_b64_tr_b16 v[208:209],v250 offset:16384\n"
"v_mfma_f32_16x16x32_bf16 v[8:11],v[220:223],v[144:147],v[8:11]\n"
"ds_read_b64_tr_b16 v[210:211],v250 offset:18432\n"
"v_mfma_f32_16x16x32_bf16 v[12:15],v[216:219],v[204:207],v[12:15]\n"
"ds_read_b64_tr_b16 v[212:213],v250 offset:20480\n"
"v_mfma_f32_16x16x32_bf16 v[12:15],v[220:223],v[148:151],v[12:15]\n"
"ds_read_b64_tr_b16 v[214:215],v250 offset:22528\n"
"s_waitcnt lgkmcnt(8)\n"
"v_mfma_f32_16x16x32_bf16 v[16:19],v[224:227],v[120:123],v[16:19]\n"
"ds_read_b64_tr_b16 v[216:217],v253\n"
"v_mfma_f32_16x16x32_bf16 v[16:19],v[228:231],v[128:131],v[16:19]\n"
"ds_read_b64_tr_b16 v[218:219],v253 offset:2048\n"
"v_mfma_f32_16x16x32_bf16 v[20:23],v[224:227],v[124:127],v[20:23]\n"
"ds_read_b64_tr_b16 v[220:221],v253 offset:4096\n"
"v_mfma_f32_16x16x32_bf16 v[20:23],v[228:231],v[132:135],v[20:23]\n"
"ds_read_b64_tr_b16 v[222:223],v253 offset:6144\n"
"s_waitcnt lgkmcnt(8)\n"
"v_mfma_f32_16x16x32_bf16 v[20:23],v[232:235],v[140:143],v[20:23]\n"
"ds_read_b64_tr_b16 v[224:225],v253 offset:8192\n"
"v_mfma_f32_16x16x32_bf16 v[20:23],v[236:239],v[196:199],v[20:23]\n"
"ds_read_b64_tr_b16 v[226:227],v253 offset:10240\n"
"v_mfma_f32_16x16x32_bf16 v[16:19],v[232:235],v[136:139],v[16:19]\n"
"ds_read_b64_tr_b16 v[228:229],v253 offset:12288\n"
"v_mfma_f32_16x16x32_bf16 v[16:19],v[236:239],v[192:195],v[16:19]\n"
"ds_read_b64_tr_b16 v[230:231],v253 offset:14336\n"
"s_waitcnt lgkmcnt(8)\n"
"v_mfma_f32_16x16x32_bf16 v[16:19],v[208:211],v[200:203],v[16:19]\n"
"ds_read_b64_tr_b16 v[232:233],v253 offset:16384\n"
"v_mfma_f32_16x16x32_bf16 v[16:19],v[212:215],v[144:147],v[16:19]\n"
"ds_read_b64_tr_b16 v[234:235],v253 offset:18432\n"
"v_mfma_f32_16x16x32_bf16 v[20:23],v[208:211],v[204:207],v[20:23]\n"
"ds_read_b64_tr_b16 v[236:237],v253 offset:20480\n"
"v_mfma_f32_16x16x32_bf16 v[20:23],v[212:215],v[148:151],v[20:23]\n"
"ds_read_b64_tr_b16 v[238:239],v253 offset:22528\n"
"s_waitcnt lgkmcnt(8)\n"
"v_mfma_f32_16x16x32_bf16 v[24:27],v[216:219],v[120:123],v[24:27]\n"
"ds_read_b64_tr_b16 v[208:209],v248 offset:24576\n"
"v_mfma_f32_16x16x32_bf16 v[24:27],v[220:223],v[128:131],v[24:27]\n"
"ds_read_b64_tr_b16 v[210:211],v248 offset:26624\n"
"v_mfma_f32_16x16x32_bf16 v[28:31],v[216:219],v[124:127],v[28:31]\n"
"ds_read_b64_tr_b16 v[212:213],v248 offset:28672\n"
"v_mfma_f32_16x16x32_bf16 v[28:31],v[220:223],v[132:135],v[28:31]\n"
"ds_read_b64_tr_b16 v[214:215],v248 offset:30720\n"
"s_waitcnt lgkmcnt(8)\n"
"v_mfma_f32_16x16x32_bf16 v[28:31],v[224:227],v[140:143],v[28:31]\n"
"ds_read_b64_tr_b16 v[216:217],v248 offset:32768\n"
"v_mfma_f32_16x16x32_bf16 v[28:31],v[228:231],v[196:199],v[28:31]\n"
"ds_read_b64_tr_b16 v[218:219],v248 offset:34816\n"
"s_setprio 2\n"
"v_mfma_f32_16x16x32_bf16 v[24:27],v[224:227],v[136:139],v[24:27]\n"
"ds_read_b64_tr_b16 v[220:221],v249 offset:24576\n"
"v_mfma_f32_16x16x32_bf16 v[24:27],v[228:231],v[192:195],v[24:27]\n"
"ds_read_b64_tr_b16 v[222:223],v249 offset:26624\n"
"s_waitcnt lgkmcnt(8)\n"
"v_mfma_f32_16x16x32_bf16 v[24:27],v[232:235],v[200:203],v[24:27]\n"
"ds_read_b64_tr_b16 v[224:225],v249 offset:28672\n"
"v_mfma_f32_16x16x32_bf16 v[24:27],v[236:239],v[144:147],v[24:27]\n"
"ds_read_b64_tr_b16 v[226:227],v249 offset:30720\n"
"v_mfma_f32_16x16x32_bf16 v[28:31],v[232:235],v[204:207],v[28:31]\n"
"ds_read_b64_tr_b16 v[228:229],v249 offset:32768\n"
"v_mfma_f32_16x16x32_bf16 v[28:31],v[236:239],v[148:151],v[28:31]\n"
"ds_read_b64_tr_b16 v[230:231],v249 offset:34816\n"
"s_waitcnt vmcnt(1)\n"
"s_waitcnt lgkmcnt(8)\n"
"v_mfma_f32_16x16x32_bf16 v[0:3],v[208:211],v[152:155],v[0:3]\n"
"ds_read_b64_tr_b16 v[232:233],v250 offset:24576\n"
"v_mfma_f32_16x16x32_bf16 v[0:3],v[212:215],v[160:163],v[0:3]\n"
"ds_read_b64_tr_b16 v[234:235],v250 offset:26624\n"
"v_mfma_f32_16x16x32_bf16 v[4:7],v[208:211],v[156:159],v[4:7]\n"
"ds_read_b64_tr_b16 v[236:237],v250 offset:28672\n"
"v_mfma_f32_16x16x32_bf16 v[4:7],v[212:215],v[164:167],v[4:7]\n"
"ds_read_b64_tr_b16 v[238:239],v250 offset:30720\n"
"s_waitcnt lgkmcnt(8)\n"
"v_mfma_f32_16x16x32_bf16 v[4:7],v[216:219],v[172:175],v[4:7]\n"
"ds_read_b64_tr_b16 v[208:209],v250 offset:32768\n"
"v_mfma_f32_16x16x32_bf16 v[0:3],v[216:219],v[168:171],v[0:3]\n"
"ds_read_b64_tr_b16 v[210:211],v250 offset:34816\n"
"v_mfma_f32_16x16x32_bf16 v[8:11],v[220:223],v[152:155],v[8:11]\n"
"ds_read_b64_tr_b16 v[212:213],v253 offset:24576\n"
"s_waitcnt lgkmcnt(7)\n"
"v_mfma_f32_16x16x32_bf16 v[8:11],v[224:227],v[160:163],v[8:11]\n"
"ds_read_b64_tr_b16 v[214:215],v253 offset:26624\n"
"v_mfma_f32_16x16x32_bf16 v[12:15],v[220:223],v[156:159],v[12:15]\n"
"ds_read_b64_tr_b16 v[216:217],v253 offset:28672\n"
"v_mfma_f32_16x16x32_bf16 v[12:15],v[224:227],v[164:167],v[12:15]\n"
"ds_read_b64_tr_b16 v[218:219],v253 offset:30720\n"
"v_mfma_f32_16x16x32_bf16 v[12:15],v[228:231],v[172:175],v[12:15]\n"
"ds_read_b64_tr_b16 v[220:221],v253 offset:32768\n"
"v_mfma_f32_16x16x32_bf16 v[8:11],v[228:231],v[168:171],v[8:11]\n"
"ds_read_b64_tr_b16 v[222:223],v253 offset:34816\n"
"s_waitcnt vmcnt(0)\n"
"s_waitcnt lgkmcnt(8)\n"
"v_mfma_f32_16x16x32_bf16 v[16:19],v[232:235],v[152:155],v[16:19]\n"
"v_mfma_f32_16x16x32_bf16 v[16:19],v[236:239],v[160:163],v[16:19]\n"
"s_barrier\n"
"v_mfma_f32_16x16x32_bf16 v[20:23],v[232:235],v[156:159],v[20:23]\n"
"v_mfma_f32_16x16x32_bf16 v[20:23],v[236:239],v[164:167],v[20:23]\n"
"s_waitcnt lgkmcnt(4)\n"
"v_mfma_f32_16x16x32_bf16 v[20:23],v[208:211],v[172:175],v[20:23]\n"
"v_mfma_f32_16x16x32_bf16 v[16:19],v[208:211],v[168:171],v[16:19]\n"
"v_mfma_f32_16x16x32_bf16 v[24:27],v[212:215],v[152:155],v[24:27]\n"
"s_waitcnt lgkmcnt(0)\n"
"v_mfma_f32_16x16x32_bf16 v[24:27],v[216:219],v[160:163],v[24:27]\n"
"v_mfma_f32_16x16x32_bf16 v[28:31],v[212:215],v[156:159],v[28:31]\n"
"v_mfma_f32_16x16x32_bf16 v[28:31],v[216:219],v[164:167],v[28:31]\n"
"v_mfma_f32_16x16x32_bf16 v[28:31],v[220:223],v[172:175],v[28:31]\n"
"v_mfma_f32_16x16x32_bf16 v[24:27],v[220:223],v[168:171],v[24:27]\n"
"s_setprio 0\n"
"v_add_u32 v248,4294893568,v248\n"
"v_add_u32 v249,4294893568,v249\n"
"v_add_u32 v250,4294893568,v250\n"
"v_add_u32 v253,4294893568,v253\n"
"s_setprio 3\n"
"ds_read_b128 v[220:223],v247\n"
"ds_read_b128 v[224:227],v247 offset:1024\n"
"ds_read_b128 v[228:231],v247 offset:2048\n"
"ds_read_b128 v[232:235],v247 offset:3072\n"
"s_setprio 0\n"
"s_add_u32 m0,s73,73728\n"
"s_add_u32 s74,s83,73984\n"
"buffer_load_dwordx4 v251,s[20:23],s74 offen lds\n"
"s_addk_i32 m0,4096\n"
"s_add_u32 s74,s82,147712\n"
"buffer_load_dwordx4 v251,s[20:23],s74 offen lds\n"
"s_addk_i32 m0,4096\n"
"s_add_u32 s74,s83,147712\n"
"buffer_load_dwordx4 v251,s[20:23],s74 offen lds\n"
"s_addk_i32 m0,4096\n"
"s_add_u32 s74,s82,221440\n"
"buffer_load_dwordx4 v251,s[20:23],s74 offen lds\n"
"s_addk_i32 m0,4096\n"
"s_add_u32 s74,s83,221440\n"
"buffer_load_dwordx4 v251,s[20:23],s74 offen lds\n"
"s_addk_i32 m0,4096\n"
"s_add_u32 s74,s82,295168\n"
"buffer_load_dwordx4 v251,s[20:23],s74 offen lds\n"
"s_waitcnt lgkmcnt(3)\n"
"v_lshlrev_b32 v236,16,v220\n"
"v_and_b32 v237,0xffff0000,v220\n"
"v_add_f32 v0,v0,v236\n"
"v_add_f32 v1,v1,v237\n"
"v_lshlrev_b32 v238,16,v221\n"
"v_and_b32 v239,0xffff0000,v221\n"
"v_add_f32 v2,v2,v238\n"
"v_add_f32 v3,v3,v239\n"
"v_lshlrev_b32 v236,16,v222\n"
"v_and_b32 v237,0xffff0000,v222\n"
"v_add_f32 v8,v8,v236\n"
"v_add_f32 v9,v9,v237\n"
"v_lshlrev_b32 v238,16,v223\n"
"v_and_b32 v239,0xffff0000,v223\n"
"v_add_f32 v10,v10,v238\n"
"v_add_f32 v11,v11,v239\n"
"v_cvt_pk_bf16_f32 v0,v0,v1\n"
"v_cvt_pk_bf16_f32 v1,v2,v3\n"
"v_cvt_pk_bf16_f32 v2,v8,v9\n"
"v_cvt_pk_bf16_f32 v3,v10,v11\n"
"s_add_u32 s78,s9,0\n"
"buffer_store_dwordx4 v[0:3],v252,s[32:35],s78 offen sc1\n"
"s_addk_i32 m0,4096\n"
"s_add_u32 s74,s83,295168\n"
"buffer_load_dwordx4 v251,s[20:23],s74 offen lds\n"
"s_addk_i32 m0,4096\n"
"s_add_u32 s74,s82,368896\n"
"buffer_load_dwordx4 v251,s[20:23],s74 offen lds\n"
"s_addk_i32 m0,4096\n"
"s_add_u32 s74,s83,368896\n"
"buffer_load_dwordx4 v251,s[20:23],s74 offen lds\n"
"s_waitcnt lgkmcnt(2)\n"
"v_lshlrev_b32 v236,16,v224\n"
"v_and_b32 v237,0xffff0000,v224\n"
"v_add_f32 v4,v4,v236\n"
"v_add_f32 v5,v5,v237\n"
"v_lshlrev_b32 v238,16,v225\n"
"v_and_b32 v239,0xffff0000,v225\n"
"v_add_f32 v6,v6,v238\n"
"v_add_f32 v7,v7,v239\n"
"v_lshlrev_b32 v236,16,v226\n"
"v_and_b32 v237,0xffff0000,v226\n"
"v_add_f32 v12,v12,v236\n"
"v_add_f32 v13,v13,v237\n"
"v_lshlrev_b32 v238,16,v227\n"
"v_and_b32 v239,0xffff0000,v227\n"
"v_add_f32 v14,v14,v238\n"
"v_add_f32 v15,v15,v239\n"
"v_cvt_pk_bf16_f32 v4,v4,v5\n"
"v_cvt_pk_bf16_f32 v5,v6,v7\n"
"v_cvt_pk_bf16_f32 v6,v12,v13\n"
"v_cvt_pk_bf16_f32 v7,v14,v15\n"
"s_add_u32 s78,s9,18432\n"
"buffer_store_dwordx4 v[4:7],v252,s[32:35],s78 offen sc1\n"
"s_waitcnt lgkmcnt(1)\n"
"v_lshlrev_b32 v236,16,v228\n"
"v_and_b32 v237,0xffff0000,v228\n"
"v_add_f32 v16,v16,v236\n"
"v_add_f32 v17,v17,v237\n"
"v_lshlrev_b32 v238,16,v229\n"
"v_and_b32 v239,0xffff0000,v229\n"
"v_add_f32 v18,v18,v238\n"
"v_add_f32 v19,v19,v239\n"
"v_lshlrev_b32 v236,16,v230\n"
"v_and_b32 v237,0xffff0000,v230\n"
"v_add_f32 v24,v24,v236\n"
"v_add_f32 v25,v25,v237\n"
"v_lshlrev_b32 v238,16,v231\n"
"v_and_b32 v239,0xffff0000,v231\n"
"v_add_f32 v26,v26,v238\n"
"v_add_f32 v27,v27,v239\n"
"v_cvt_pk_bf16_f32 v16,v16,v17\n"
"v_cvt_pk_bf16_f32 v17,v18,v19\n"
"v_cvt_pk_bf16_f32 v18,v24,v25\n"
"v_cvt_pk_bf16_f32 v19,v26,v27\n"
"s_add_u32 s78,s9,1024\n"
"buffer_store_dwordx4 v[16:19],v252,s[32:35],s78 offen sc1\n"
"s_waitcnt lgkmcnt(0)\n"
"s_setprio 3\n"
"s_barrier\n"
"ds_read_b64_tr_b16 v[208:209],v248\n"
"ds_read_b64_tr_b16 v[210:211],v248 offset:2048\n"
"ds_read_b64_tr_b16 v[212:213],v248 offset:4096\n"
"ds_read_b64_tr_b16 v[214:215],v248 offset:6144\n"
"ds_read_b64_tr_b16 v[216:217],v248 offset:8192\n"
"ds_read_b64_tr_b16 v[218:219],v248 offset:10240\n"
"ds_read_b64_tr_b16 v[220:221],v248 offset:12288\n"
"ds_read_b64_tr_b16 v[222:223],v248 offset:14336\n"
"ds_read_b64_tr_b16 v[224:225],v248 offset:16384\n"
"ds_read_b64_tr_b16 v[226:227],v248 offset:18432\n"
"ds_read_b64_tr_b16 v[228:229],v248 offset:20480\n"
"ds_read_b64_tr_b16 v[230:231],v248 offset:22528\n"
"v_lshlrev_b32 v236,16,v232\n"
"v_and_b32 v237,0xffff0000,v232\n"
"v_add_f32 v20,v20,v236\n"
"v_add_f32 v21,v21,v237\n"
"v_lshlrev_b32 v238,16,v233\n"
"v_and_b32 v239,0xffff0000,v233\n"
"v_add_f32 v22,v22,v238\n"
"v_add_f32 v23,v23,v239\n"
"v_lshlrev_b32 v236,16,v234\n"
"v_and_b32 v237,0xffff0000,v234\n"
"v_add_f32 v28,v28,v236\n"
"v_add_f32 v29,v29,v237\n"
"v_lshlrev_b32 v238,16,v235\n"
"v_and_b32 v239,0xffff0000,v235\n"
"v_add_f32 v30,v30,v238\n"
"v_add_f32 v31,v31,v239\n"
"v_cvt_pk_bf16_f32 v20,v20,v21\n"
"v_cvt_pk_bf16_f32 v21,v22,v23\n"
"v_cvt_pk_bf16_f32 v22,v28,v29\n"
"v_cvt_pk_bf16_f32 v23,v30,v31\n"
"s_add_u32 s78,s9,19456\n"
"buffer_store_dwordx4 v[20:23],v252,s[32:35],s78 offen sc1\n"
"s_setprio 1\n"
"s_waitcnt lgkmcnt(8)\n"
"v_mfma_f32_16x16x32_bf16 v[32:35],v[208:211],v[120:123],v[32:35]\n"
"ds_read_b64_tr_b16 v[232:233],v248 offset:24576\n"
"v_mfma_f32_16x16x32_bf16 v[32:35],v[212:215],v[128:131],v[32:35]\n"
"ds_read_b64_tr_b16 v[234:235],v248 offset:26624\n"
"v_mfma_f32_16x16x32_bf16 v[36:39],v[208:211],v[124:127],v[36:39]\n"
"ds_read_b64_tr_b16 v[236:237],v248 offset:28672\n"
"v_mfma_f32_16x16x32_bf16 v[36:39],v[212:215],v[132:135],v[36:39]\n"
"ds_read_b64_tr_b16 v[238:239],v248 offset:30720\n"
"s_waitcnt lgkmcnt(8)\n"
"v_mfma_f32_16x16x32_bf16 v[36:39],v[216:219],v[140:143],v[36:39]\n"
"ds_read_b64_tr_b16 v[208:209],v248 offset:32768\n"
"v_mfma_f32_16x16x32_bf16 v[36:39],v[220:223],v[196:199],v[36:39]\n"
"ds_read_b64_tr_b16 v[210:211],v248 offset:34816\n"
"v_mfma_f32_16x16x32_bf16 v[32:35],v[216:219],v[136:139],v[32:35]\n"
"ds_read_b64_tr_b16 v[212:213],v249\n"
"v_mfma_f32_16x16x32_bf16 v[32:35],v[220:223],v[192:195],v[32:35]\n"
"ds_read_b64_tr_b16 v[214:215],v249 offset:2048\n"
"s_waitcnt lgkmcnt(8)\n"
"v_mfma_f32_16x16x32_bf16 v[32:35],v[224:227],v[200:203],v[32:35]\n"
"ds_read_b64_tr_b16 v[216:217],v249 offset:4096\n"
"v_mfma_f32_16x16x32_bf16 v[32:35],v[228:231],v[144:147],v[32:35]\n"
"ds_read_b64_tr_b16 v[218:219],v249 offset:6144\n"
"v_mfma_f32_16x16x32_bf16 v[36:39],v[224:227],v[204:207],v[36:39]\n"
"ds_read_b64_tr_b16 v[220:221],v249 offset:8192\n"
"v_mfma_f32_16x16x32_bf16 v[36:39],v[228:231],v[148:151],v[36:39]\n"
"ds_read_b64_tr_b16 v[222:223],v249 offset:10240\n"
"s_waitcnt lgkmcnt(8)\n"
"v_mfma_f32_16x16x32_bf16 v[36:39],v[232:235],v[156:159],v[36:39]\n"
"ds_read_b64_tr_b16 v[224:225],v249 offset:12288\n"
"v_mfma_f32_16x16x32_bf16 v[36:39],v[236:239],v[164:167],v[36:39]\n"
"ds_read_b64_tr_b16 v[226:227],v249 offset:14336\n"
"v_mfma_f32_16x16x32_bf16 v[32:35],v[232:235],v[152:155],v[32:35]\n"
"ds_read_b64_tr_b16 v[228:229],v249 offset:16384\n"
"v_mfma_f32_16x16x32_bf16 v[32:35],v[236:239],v[160:163],v[32:35]\n"
"ds_read_b64_tr_b16 v[230:231],v249 offset:18432\n"
"s_waitcnt lgkmcnt(8)\n"
"v_mfma_f32_16x16x32_bf16 v[32:35],v[208:211],v[168:171],v[32:35]\n"
"ds_read_b64_tr_b16 v[232:233],v249 offset:20480\n"
"v_mfma_f32_16x16x32_bf16 v[36:39],v[208:211],v[172:175],v[36:39]\n"
"ds_read_b64_tr_b16 v[234:235],v249 offset:22528\n"
"v_mfma_f32_16x16x32_bf16 v[40:43],v[212:215],v[120:123],v[40:43]\n"
"ds_read_b64_tr_b16 v[236:237],v249 offset:24576\n"
"s_waitcnt lgkmcnt(7)\n"
"v_mfma_f32_16x16x32_bf16 v[40:43],v[216:219],v[128:131],v[40:43]\n"
"ds_read_b64_tr_b16 v[238:239],v249 offset:26624\n"
"v_mfma_f32_16x16x32_bf16 v[44:47],v[212:215],v[124:127],v[44:47]\n"
"ds_read_b64_tr_b16 v[208:209],v249 offset:28672\n"
"v_mfma_f32_16x16x32_bf16 v[44:47],v[216:219],v[132:135],v[44:47]\n"
"ds_read_b64_tr_b16 v[210:211],v249 offset:30720\n"
"v_mfma_f32_16x16x32_bf16 v[44:47],v[220:223],v[140:143],v[44:47]\n"
"ds_read_b64_tr_b16 v[212:213],v249 offset:32768\n"
"s_waitcnt lgkmcnt(7)\n"
"v_mfma_f32_16x16x32_bf16 v[44:47],v[224:227],v[196:199],v[44:47]\n"
"ds_read_b64_tr_b16 v[214:215],v249 offset:34816\n"
"v_mfma_f32_16x16x32_bf16 v[40:43],v[220:223],v[136:139],v[40:43]\n"
"ds_read_b64_tr_b16 v[216:217],v250\n"
"v_mfma_f32_16x16x32_bf16 v[40:43],v[224:227],v[192:195],v[40:43]\n"
"ds_read_b64_tr_b16 v[218:219],v250 offset:2048\n"
"v_mfma_f32_16x16x32_bf16 v[40:43],v[228:231],v[200:203],v[40:43]\n"
"ds_read_b64_tr_b16 v[220:221],v250 offset:4096\n"
"s_waitcnt lgkmcnt(7)\n"
"v_mfma_f32_16x16x32_bf16 v[40:43],v[232:235],v[144:147],v[40:43]\n"
"ds_read_b64_tr_b16 v[222:223],v250 offset:6144\n"
"v_mfma_f32_16x16x32_bf16 v[44:47],v[228:231],v[204:207],v[44:47]\n"
"ds_read_b64_tr_b16 v[224:225],v250 offset:8192\n"
"v_mfma_f32_16x16x32_bf16 v[44:47],v[232:235],v[148:151],v[44:47]\n"
"ds_read_b64_tr_b16 v[226:227],v250 offset:10240\n"
"v_mfma_f32_16x16x32_bf16 v[44:47],v[236:239],v[156:159],v[44:47]\n"
"ds_read_b64_tr_b16 v[228:229],v250 offset:12288\n"
"s_waitcnt lgkmcnt(7)\n"
"v_mfma_f32_16x16x32_bf16 v[44:47],v[208:211],v[164:167],v[44:47]\n"
"ds_read_b64_tr_b16 v[230:231],v250 offset:14336\n"
"v_mfma_f32_16x16x32_bf16 v[40:43],v[236:239],v[152:155],v[40:43]\n"
"ds_read_b64_tr_b16 v[232:233],v250 offset:16384\n"
"v_mfma_f32_16x16x32_bf16 v[40:43],v[208:211],v[160:163],v[40:43]\n"
"ds_read_b64_tr_b16 v[234:235],v250 offset:18432\n"
"v_mfma_f32_16x16x32_bf16 v[40:43],v[212:215],v[168:171],v[40:43]\n"
"ds_read_b64_tr_b16 v[236:237],v250 offset:20480\n"
"v_mfma_f32_16x16x32_bf16 v[44:47],v[212:215],v[172:175],v[44:47]\n"
"ds_read_b64_tr_b16 v[238:239],v250 offset:22528\n"
"s_waitcnt lgkmcnt(8)\n"
"v_mfma_f32_16x16x32_bf16 v[48:51],v[216:219],v[120:123],v[48:51]\n"
"ds_read_b64_tr_b16 v[208:209],v250 offset:24576\n"
"s_waitcnt vmcnt(0)\n"
"v_mov_b32 v240,4\n"
"s_mov_b64 exec,1\n"
"buffer_store_dword v240,off,s[32:35],s88 sc1\n"
"s_mov_b64 exec,-1\n"
"v_mfma_f32_16x16x32_bf16 v[48:51],v[220:223],v[128:131],v[48:51]\n"
"ds_read_b64_tr_b16 v[210:211],v250 offset:26624\n"
"v_mfma_f32_16x16x32_bf16 v[52:55],v[216:219],v[124:127],v[52:55]\n"
"ds_read_b64_tr_b16 v[212:213],v250 offset:28672\n"
"v_mfma_f32_16x16x32_bf16 v[52:55],v[220:223],v[132:135],v[52:55]\n"
"ds_read_b64_tr_b16 v[214:215],v250 offset:30720\n"
"s_waitcnt lgkmcnt(8)\n"
"v_mfma_f32_16x16x32_bf16 v[52:55],v[224:227],v[140:143],v[52:55]\n"
"ds_read_b64_tr_b16 v[216:217],v250 offset:32768\n"
"v_mfma_f32_16x16x32_bf16 v[52:55],v[228:231],v[196:199],v[52:55]\n"
"ds_read_b64_tr_b16 v[218:219],v250 offset:34816\n"
"s_setprio 2\n"
"v_mfma_f32_16x16x32_bf16 v[48:51],v[224:227],v[136:139],v[48:51]\n"
"ds_read_b64_tr_b16 v[220:221],v253\n"
"v_mfma_f32_16x16x32_bf16 v[48:51],v[228:231],v[192:195],v[48:51]\n"
"ds_read_b64_tr_b16 v[222:223],v253 offset:2048\n"
"s_waitcnt lgkmcnt(8)\n"
"v_mfma_f32_16x16x32_bf16 v[48:51],v[232:235],v[200:203],v[48:51]\n"
"ds_read_b64_tr_b16 v[224:225],v253 offset:4096\n"
"v_mfma_f32_16x16x32_bf16 v[48:51],v[236:239],v[144:147],v[48:51]\n"
"ds_read_b64_tr_b16 v[226:227],v253 offset:6144\n"
"v_mfma_f32_16x16x32_bf16 v[52:55],v[232:235],v[204:207],v[52:55]\n"
"ds_read_b64_tr_b16 v[228:229],v253 offset:8192\n"
"v_mfma_f32_16x16x32_bf16 v[52:55],v[236:239],v[148:151],v[52:55]\n"
"ds_read_b64_tr_b16 v[230:231],v253 offset:10240\n"
"s_waitcnt lgkmcnt(8)\n"
"v_mfma_f32_16x16x32_bf16 v[52:55],v[208:211],v[156:159],v[52:55]\n"
"ds_read_b64_tr_b16 v[232:233],v253 offset:12288\n"
"v_mfma_f32_16x16x32_bf16 v[52:55],v[212:215],v[164:167],v[52:55]\n"
"ds_read_b64_tr_b16 v[234:235],v253 offset:14336\n"
"v_mfma_f32_16x16x32_bf16 v[48:51],v[208:211],v[152:155],v[48:51]\n"
"ds_read_b64_tr_b16 v[236:237],v253 offset:16384\n"
"v_mfma_f32_16x16x32_bf16 v[48:51],v[212:215],v[160:163],v[48:51]\n"
"ds_read_b64_tr_b16 v[238:239],v253 offset:18432\n"
"s_waitcnt lgkmcnt(8)\n"
"v_mfma_f32_16x16x32_bf16 v[48:51],v[216:219],v[168:171],v[48:51]\n"
"ds_read_b64_tr_b16 v[208:209],v253 offset:20480\n"
"v_mfma_f32_16x16x32_bf16 v[52:55],v[216:219],v[172:175],v[52:55]\n"
"ds_read_b64_tr_b16 v[210:211],v253 offset:22528\n"
"v_mfma_f32_16x16x32_bf16 v[56:59],v[220:223],v[120:123],v[56:59]\n"
"ds_read_b64_tr_b16 v[212:213],v253 offset:24576\n"
"s_waitcnt lgkmcnt(7)\n"
"v_mfma_f32_16x16x32_bf16 v[56:59],v[224:227],v[128:131],v[56:59]\n"
"ds_read_b64_tr_b16 v[214:215],v253 offset:26624\n"
"v_mfma_f32_16x16x32_bf16 v[60:63],v[220:223],v[124:127],v[60:63]\n"
"ds_read_b64_tr_b16 v[216:217],v253 offset:28672\n"
"v_mfma_f32_16x16x32_bf16 v[60:63],v[224:227],v[132:135],v[60:63]\n"
"ds_read_b64_tr_b16 v[218:219],v253 offset:30720\n"
"v_mfma_f32_16x16x32_bf16 v[60:63],v[228:231],v[140:143],v[60:63]\n"
"ds_read_b64_tr_b16 v[220:221],v253 offset:32768\n"
"s_waitcnt lgkmcnt(7)\n"
"v_mfma_f32_16x16x32_bf16 v[60:63],v[232:235],v[196:199],v[60:63]\n"
"ds_read_b64_tr_b16 v[222:223],v253 offset:34816\n"
"v_mfma_f32_16x16x32_bf16 v[56:59],v[228:231],v[136:139],v[56:59]\n"
"v_mfma_f32_16x16x32_bf16 v[56:59],v[232:235],v[192:195],v[56:59]\n"
"s_barrier\n"
"v_mfma_f32_16x16x32_bf16 v[56:59],v[236:239],v[200:203],v[56:59]\n"
"s_waitcnt lgkmcnt(4)\n"
"v_mfma_f32_16x16x32_bf16 v[56:59],v[208:211],v[144:147],v[56:59]\n"
"v_mfma_f32_16x16x32_bf16 v[60:63],v[236:239],v[204:207],v[60:63]\n"
"v_mov_b32 v255,2\n"
"v_mfma_f32_16x16x32_bf16 v[60:63],v[208:211],v[148:151],v[60:63]\n"
"v_cndmask_b32 v255,v255,2,s[96:97]\n"
"v_mfma_f32_16x16x32_bf16 v[60:63],v[212:215],v[156:159],v[60:63]\n"
"buffer_load_dword v254,v190,s[32:35],s95 offen sc1\n"
"s_waitcnt lgkmcnt(0)\n"
"v_mfma_f32_16x16x32_bf16 v[60:63],v[216:219],v[164:167],v[60:63]\n"
"v_mfma_f32_16x16x32_bf16 v[56:59],v[212:215],v[152:155],v[56:59]\n"
"v_mfma_f32_16x16x32_bf16 v[56:59],v[216:219],v[160:163],v[56:59]\n"
"v_mfma_f32_16x16x32_bf16 v[56:59],v[220:223],v[168:171],v[56:59]\n"
"v_mfma_f32_16x16x32_bf16 v[60:63],v[220:223],v[172:175],v[60:63]\n"
"s_setprio 0\n"
"v_add_u32 v248,73728,v248\n"
"v_add_u32 v249,73728,v249\n"
"v_add_u32 v250,73728,v250\n"
"v_add_u32 v253,73728,v253\n"
"s_setprio 3\n"
"ds_read_b128 v[220:223],v247\n"
"ds_read_b128 v[224:227],v247 offset:1024\n"
"ds_read_b128 v[228:231],v247 offset:2048\n"
"ds_read_b128 v[232:235],v247 offset:3072\n"
"s_setprio 0\n"
"s_cmp_lg_u32 s71,s69\n"
"s_cbranch_scc1 .Lk_mail0\n"
"s_setprio 3\n"
"ds_write_b128 v252,v[0:3]\n"
"ds_write_b128 v252,v[4:7] offset:2048\n"
"ds_write_b128 v252,v[16:19] offset:1024\n"
"ds_write_b128 v252,v[20:23] offset:3072\n"
"s_setprio 0\n"
".Lk_mail0:\n"
"s_waitcnt lgkmcnt(3)\n"
"v_lshlrev_b32 v236,16,v220\n"
"v_and_b32 v237,0xffff0000,v220\n"
"v_add_f32 v32,v32,v236\n"
"v_add_f32 v33,v33,v237\n"
"v_lshlrev_b32 v238,16,v221\n"
"v_and_b32 v239,0xffff0000,v221\n"
"v_add_f32 v34,v34,v238\n"
"v_add_f32 v35,v35,v239\n"
"v_lshlrev_b32 v236,16,v222\n"
"v_and_b32 v237,0xffff0000,v222\n"
"v_add_f32 v40,v40,v236\n"
"v_add_f32 v41,v41,v237\n"
"v_lshlrev_b32 v238,16,v223\n"
"v_and_b32 v239,0xffff0000,v223\n"
"v_add_f32 v42,v42,v238\n"
"v_add_f32 v43,v43,v239\n"
"v_cvt_pk_bf16_f32 v32,v32,v33\n"
"v_cvt_pk_bf16_f32 v33,v34,v35\n"
"v_cvt_pk_bf16_f32 v34,v40,v41\n"
"v_cvt_pk_bf16_f32 v35,v42,v43\n"
"s_add_u32 s78,s9,2048\n"
"buffer_store_dwordx4 v[32:35],v252,s[32:35],s78 offen sc1\n"
"s_waitcnt lgkmcnt(2)\n"
"v_lshlrev_b32 v236,16,v224\n"
"v_and_b32 v237,0xffff0000,v224\n"
"v_add_f32 v36,v36,v236\n"
"v_add_f32 v37,v37,v237\n"
"v_lshlrev_b32 v238,16,v225\n"
"v_and_b32 v239,0xffff0000,v225\n"
"v_add_f32 v38,v38,v238\n"
"v_add_f32 v39,v39,v239\n"
"v_lshlrev_b32 v236,16,v226\n"
"v_and_b32 v237,0xffff0000,v226\n"
"v_add_f32 v44,v44,v236\n"
"v_add_f32 v45,v45,v237\n"
"v_lshlrev_b32 v238,16,v227\n"
"v_and_b32 v239,0xffff0000,v227\n"
"v_add_f32 v46,v46,v238\n"
"v_add_f32 v47,v47,v239\n"
"v_cvt_pk_bf16_f32 v36,v36,v37\n"
"v_cvt_pk_bf16_f32 v37,v38,v39\n"
"v_cvt_pk_bf16_f32 v38,v44,v45\n"
"v_cvt_pk_bf16_f32 v39,v46,v47\n"
"s_add_u32 s78,s9,20480\n"
"buffer_store_dwordx4 v[36:39],v252,s[32:35],s78 offen sc1\n"
"s_waitcnt lgkmcnt(1)\n"
"v_lshlrev_b32 v236,16,v228\n"
"v_and_b32 v237,0xffff0000,v228\n"
"v_add_f32 v48,v48,v236\n"
"v_add_f32 v49,v49,v237\n"
"v_lshlrev_b32 v238,16,v229\n"
"v_and_b32 v239,0xffff0000,v229\n"
".Lk_padm12:\n"
"s_cbranch_execz .Lk_hop_retm\n"
"v_add_f32 v50,v50,v238\n"
"v_add_f32 v51,v51,v239\n"
"v_lshlrev_b32 v236,16,v230\n"
"v_and_b32 v237,0xffff0000,v230\n"
"v_add_f32 v56,v56,v236\n"
"v_add_f32 v57,v57,v237\n"
"v_lshlrev_b32 v238,16,v231\n"
"v_and_b32 v239,0xffff0000,v231\n"
"v_add_f32 v58,v58,v238\n"
"v_add_f32 v59,v59,v239\n"
"v_cvt_pk_bf16_f32 v48,v48,v49\n"
"v_cvt_pk_bf16_f32 v49,v50,v51\n"
"v_cvt_pk_bf16_f32 v50,v56,v57\n"
"v_cvt_pk_bf16_f32 v51,v58,v59\n"
"s_add_u32 s78,s9,3072\n"
"buffer_store_dwordx4 v[48:51],v252,s[32:35],s78 offen sc1\n"
"s_waitcnt lgkmcnt(0)\n"
"v_lshlrev_b32 v236,16,v232\n"
"v_and_b32 v237,0xffff0000,v232\n"
"v_add_f32 v52,v52,v236\n"
"v_add_f32 v53,v53,v237\n"
"v_lshlrev_b32 v238,16,v233\n"
"v_and_b32 v239,0xffff0000,v233\n"
"v_add_f32 v54,v54,v238\n"
"v_add_f32 v55,v55,v239\n"
"v_lshlrev_b32 v236,16,v234\n"
"v_and_b32 v237,0xffff0000,v234\n"
"v_add_f32 v60,v60,v236\n"
"v_add_f32 v61,v61,v237\n"
"v_lshlrev_b32 v238,16,v235\n"
"v_and_b32 v239,0xffff0000,v235\n"
"v_add_f32 v62,v62,v238\n"
"v_add_f32 v63,v63,v239\n"
"v_cvt_pk_bf16_f32 v52,v52,v53\n"
"v_cvt_pk_bf16_f32 v53,v54,v55\n"
"v_cvt_pk_bf16_f32 v54,v60,v61\n"
"v_cvt_pk_bf16_f32 v55,v62,v63\n"
"s_add_u32 s78,s9,21504\n"
"buffer_store_dwordx4 v[52:55],v252,s[32:35],s78 offen sc1\n"
"s_cmp_lg_u32 s71,s69\n"
"s_cbranch_scc1 .Lk_mail1\n"
"s_setprio 3\n"
"ds_write_b128 v252,v[32:35] offset:4096\n"
"ds_write_b128 v252,v[36:39] offset:6144\n"
"ds_write_b128 v252,v[48:51] offset:5120\n"
"ds_write_b128 v252,v[52:55] offset:7168\n"
"s_setprio 0\n"
".Lk_mail1:\n"
"s_add_u32 s81,s69,1\n"
"s_and_b32 s81,s81,3\n"
"s_mul_i32 s81,s81,2359296\n"
"s_add_u32 s81,s81,s80\n"
"s_add_u32 s82,s69,2\n"
"s_and_b32 s82,s82,3\n"
"s_mul_i32 s82,s82,2359296\n"
"s_add_u32 s82,s82,s80\n"
"s_add_u32 s83,s69,3\n"
"s_and_b32 s83,s83,3\n"
"s_mul_i32 s83,s83,2359296\n"
"s_add_u32 s83,s83,s80\n"
"s_waitcnt lgkmcnt(0)\n"
"s_waitcnt vmcnt(4)\n"
"v_cmp_gt_u32 vcc,v255,v254\n"
"s_nop 1\n"
"s_cbranch_vccz .Lk_rv21_ok\n"
"s_mov_b32 s89,0x100000\n"
".Lk_rv21_spin:\n"
"s_sleep 2\n"
"buffer_load_dword v254,v190,s[32:35],s95 offen sc1\n"
"s_waitcnt vmcnt(0)\n"
"v_cmp_gt_u32 vcc,v255,v254\n"
"s_nop 1\n"
"s_cbranch_vccz .Lk_rv21_ok\n"
"s_sub_u32 s89,s89,1\n"
"s_cmp_lg_u32 s89,0\n"
"s_cbranch_scc1 .Lk_rv21_spin\n"
".Lk_rv21_ok:\n"
"s_add_u32 s77,s80,9439232\n"
"buffer_load_dwordx4 v[16:19],v252,s[32:35],s77 offen sc1\n"
"s_add_u32 s77,s80,11798528\n"
"buffer_load_dwordx4 v[20:23],v252,s[32:35],s77 offen sc1\n"
"s_add_u32 s77,s80,14157824\n"
"buffer_load_dwordx4 v[24:27],v252,s[32:35],s77 offen sc1\n"
"s_add_u32 s77,s80,16517120\n"
"buffer_load_dwordx4 v[28:31],v252,s[32:35],s77 offen sc1\n"
"s_waitcnt lgkmcnt(0)\n"
"s_setprio 3\n"
"s_barrier\n"
"ds_read_b64_tr_b16 v[208:209],v248\n"
"ds_read_b64_tr_b16 v[210:211],v248 offset:2048\n"
"ds_read_b64_tr_b16 v[212:213],v248 offset:4096\n"
"ds_read_b64_tr_b16 v[214:215],v248 offset:6144\n"
"ds_read_b64_tr_b16 v[216:217],v248 offset:8192\n"
"ds_read_b64_tr_b16 v[218:219],v248 offset:10240\n"
"ds_read_b64_tr_b16 v[220:221],v248 offset:12288\n"
"ds_read_b64_tr_b16 v[222:223],v248 offset:14336\n"
"ds_read_b64_tr_b16 v[224:225],v248 offset:16384\n"
"ds_read_b64_tr_b16 v[226:227],v248 offset:18432\n"
"ds_read_b64_tr_b16 v[228:229],v248 offset:20480\n"
"ds_read_b64_tr_b16 v[230:231],v248 offset:22528\n"
"buffer_load_dwordx2 v[104:105],v188,s[12:15],s84 offen offset:128\n"
"buffer_load_dwordx2 v[106:107],v188,s[12:15],s84 offen offset:1280\n"
"global_load_dwordx2 v[108:109],v189,s[4:5] offset:128\n"
"global_load_dwordx2 v[110:111],v189,s[6:7] offset:128\n"
"s_setprio 1\n"
"s_waitcnt lgkmcnt(8)\n"
"v_mfma_f32_16x16x32_bf16 v[64:67],v[208:211],v[120:123],v[64:67]\n"
"ds_read_b64_tr_b16 v[232:233],v248 offset:24576\n"
"v_mfma_f32_16x16x32_bf16 v[64:67],v[212:215],v[128:131],v[64:67]\n"
"ds_read_b64_tr_b16 v[234:235],v248 offset:26624\n"
"v_mfma_f32_16x16x32_bf16 v[68:71],v[208:211],v[124:127],v[68:71]\n"
"ds_read_b64_tr_b16 v[236:237],v248 offset:28672\n"
"v_mfma_f32_16x16x32_bf16 v[68:71],v[212:215],v[132:135],v[68:71]\n"
"ds_read_b64_tr_b16 v[238:239],v248 offset:30720\n"
"s_waitcnt lgkmcnt(8)\n"
"v_mfma_f32_16x16x32_bf16 v[68:71],v[216:219],v[140:143],v[68:71]\n"
"ds_read_b64_tr_b16 v[208:209],v248 offset:32768\n"
"v_mfma_f32_16x16x32_bf16 v[68:71],v[220:223],v[196:199],v[68:71]\n"
"ds_read_b64_tr_b16 v[210:211],v248 offset:34816\n"
"v_mfma_f32_16x16x32_bf16 v[64:67],v[216:219],v[136:139],v[64:67]\n"
"ds_read_b64_tr_b16 v[212:213],v249\n"
"v_mfma_f32_16x16x32_bf16 v[64:67],v[220:223],v[192:195],v[64:67]\n"
"ds_read_b64_tr_b16 v[214:215],v249 offset:2048\n"
"s_waitcnt lgkmcnt(8)\n"
"v_mfma_f32_16x16x32_bf16 v[64:67],v[224:227],v[200:203],v[64:67]\n"
"ds_read_b64_tr_b16 v[216:217],v249 offset:4096\n"
"v_mfma_f32_16x16x32_bf16 v[64:67],v[228:231],v[144:147],v[64:67]\n"
"ds_read_b64_tr_b16 v[218:219],v249 offset:6144\n"
"v_mfma_f32_16x16x32_bf16 v[68:71],v[224:227],v[204:207],v[68:71]\n"
"ds_read_b64_tr_b16 v[220:221],v249 offset:8192\n"
"v_mfma_f32_16x16x32_bf16 v[68:71],v[228:231],v[148:151],v[68:71]\n"
"ds_read_b64_tr_b16 v[222:223],v249 offset:10240\n"
"s_waitcnt lgkmcnt(8)\n"
"v_mfma_f32_16x16x32_bf16 v[68:71],v[232:235],v[156:159],v[68:71]\n"
"ds_read_b64_tr_b16 v[224:225],v249 offset:12288\n"
"v_mfma_f32_16x16x32_bf16 v[68:71],v[236:239],v[164:167],v[68:71]\n"
"ds_read_b64_tr_b16 v[226:227],v249 offset:14336\n"
"v_mfma_f32_16x16x32_bf16 v[64:67],v[232:235],v[152:155],v[64:67]\n"
"ds_read_b64_tr_b16 v[228:229],v249 offset:16384\n"
"v_mfma_f32_16x16x32_bf16 v[64:67],v[236:239],v[160:163],v[64:67]\n"
"ds_read_b64_tr_b16 v[230:231],v249 offset:18432\n"
"s_waitcnt lgkmcnt(8)\n"
"v_mfma_f32_16x16x32_bf16 v[64:67],v[208:211],v[168:171],v[64:67]\n"
"ds_read_b64_tr_b16 v[232:233],v249 offset:20480\n"
"v_mfma_f32_16x16x32_bf16 v[68:71],v[208:211],v[172:175],v[68:71]\n"
"ds_read_b64_tr_b16 v[234:235],v249 offset:22528\n"
"s_barrier\n"
"v_mfma_f32_16x16x32_bf16 v[72:75],v[212:215],v[120:123],v[72:75]\n"
"ds_read_b64_tr_b16 v[236:237],v249 offset:24576\n"
"ds_read_b128 v[0:3],v247\n"
"s_waitcnt lgkmcnt(8)\n"
"v_mfma_f32_16x16x32_bf16 v[72:75],v[216:219],v[128:131],v[72:75]\n"
"ds_read_b64_tr_b16 v[238:239],v249 offset:26624\n"
"ds_read_b128 v[4:7],v247 offset:1024\n"
"v_mfma_f32_16x16x32_bf16 v[76:79],v[212:215],v[124:127],v[76:79]\n"
"ds_read_b64_tr_b16 v[208:209],v249 offset:28672\n"
"ds_read_b128 v[8:11],v247 offset:2048\n"
"v_mfma_f32_16x16x32_bf16 v[76:79],v[216:219],v[132:135],v[76:79]\n"
"ds_read_b64_tr_b16 v[210:211],v249 offset:30720\n"
"ds_read_b128 v[12:15],v247 offset:3072\n"
"v_mfma_f32_16x16x32_bf16 v[76:79],v[220:223],v[140:143],v[76:79]\n"
"ds_read_b64_tr_b16 v[212:213],v249 offset:32768\n"
"s_waitcnt lgkmcnt(11)\n"
"v_mfma_f32_16x16x32_bf16 v[76:79],v[224:227],v[196:199],v[76:79]\n"
"ds_read_b64_tr_b16 v[214:215],v249 offset:34816\n"
"v_mfma_f32_16x16x32_bf16 v[72:75],v[220:223],v[136:139],v[72:75]\n"
"ds_read_b64_tr_b16 v[216:217],v250\n"
"v_mfma_f32_16x16x32_bf16 v[72:75],v[224:227],v[192:195],v[72:75]\n"
"ds_read_b64_tr_b16 v[218:219],v250 offset:2048\n"
"v_mfma_f32_16x16x32_bf16 v[72:75],v[228:231],v[200:203],v[72:75]\n"
"ds_read_b64_tr_b16 v[220:221],v250 offset:4096\n"
"s_waitcnt lgkmcnt(10)\n"
"v_mfma_f32_16x16x32_bf16 v[72:75],v[232:235],v[144:147],v[72:75]\n"
"ds_read_b64_tr_b16 v[222:223],v250 offset:6144\n"
"v_mfma_f32_16x16x32_bf16 v[76:79],v[228:231],v[204:207],v[76:79]\n"
"ds_read_b64_tr_b16 v[224:225],v250 offset:8192\n"
"v_mfma_f32_16x16x32_bf16 v[76:79],v[232:235],v[148:151],v[76:79]\n"
"ds_read_b64_tr_b16 v[226:227],v250 offset:10240\n"
"v_mfma_f32_16x16x32_bf16 v[76:79],v[236:239],v[156:159],v[76:79]\n"
"ds_read_b64_tr_b16 v[228:229],v250 offset:12288\n"
"s_waitcnt lgkmcnt(7)\n"
"v_mfma_f32_16x16x32_bf16 v[76:79],v[208:211],v[164:167],v[76:79]\n"
"ds_read_b64_tr_b16 v[230:231],v250 offset:14336\n"
"v_mfma_f32_16x16x32_bf16 v[72:75],v[236:239],v[152:155],v[72:75]\n"
"ds_read_b64_tr_b16 v[232:233],v250 offset:16384\n"
"v_mfma_f32_16x16x32_bf16 v[72:75],v[208:211],v[160:163],v[72:75]\n"
"ds_read_b64_tr_b16 v[234:235],v250 offset:18432\n"
"v_mfma_f32_16x16x32_bf16 v[72:75],v[212:215],v[168:171],v[72:75]\n"
"ds_read_b64_tr_b16 v[236:237],v250 offset:20480\n"
"v_mfma_f32_16x16x32_bf16 v[76:79],v[212:215],v[172:175],v[76:79]\n"
"ds_read_b64_tr_b16 v[238:239],v250 offset:22528\n"
"v_mfma_f32_16x16x32_bf16 v[64:67],v[178:181],v[0:3],v[64:67]\n"
"v_mfma_f32_16x16x32_bf16 v[72:75],v[176:179],v[0:3],v[72:75]\n"
"v_mfma_f32_16x16x32_bf16 v[68:71],v[178:181],v[4:7],v[68:71]\n"
"v_mfma_f32_16x16x32_bf16 v[76:79],v[176:179],v[4:7],v[76:79]\n"
"v_mfma_f32_16x16x32_bf16 v[80:83],v[178:181],v[8:11],v[80:83]\n"
"v_mfma_f32_16x16x32_bf16 v[88:91],v[176:179],v[8:11],v[88:91]\n"
"v_mfma_f32_16x16x32_bf16 v[84:87],v[178:181],v[12:15],v[84:87]\n"
"v_mfma_f32_16x16x32_bf16 v[92:95],v[176:179],v[12:15],v[92:95]\n"
"s_waitcnt lgkmcnt(8)\n"
"v_mfma_f32_16x16x32_bf16 v[80:83],v[216:219],v[120:123],v[80:83]\n"
"ds_read_b64_tr_b16 v[208:209],v250 offset:24576\n"
"v_mfma_f32_16x16x32_bf16 v[80:83],v[220:223],v[128:131],v[80:83]\n"
"ds_read_b64_tr_b16 v[210:211],v250 offset:26624\n"
"v_mfma_f32_16x16x32_bf16 v[84:87],v[216:219],v[124:127],v[84:87]\n"
"ds_read_b64_tr_b16 v[212:213],v250 offset:28672\n"
"v_mfma_f32_16x16x32_bf16 v[84:87],v[220:223],v[132:135],v[84:87]\n"
"ds_read_b64_tr_b16 v[214:215],v250 offset:30720\n"
"s_waitcnt lgkmcnt(8)\n"
"v_mfma_f32_16x16x32_bf16 v[84:87],v[224:227],v[140:143],v[84:87]\n"
"ds_read_b64_tr_b16 v[216:217],v250 offset:32768\n"
"v_cvt_pk_bf16_f32 v64,v64,v65\n"
"v_mfma_f32_16x16x32_bf16 v[84:87],v[228:231],v[196:199],v[84:87]\n"
"ds_read_b64_tr_b16 v[218:219],v250 offset:34816\n"
"s_setprio 2\n"
"v_mfma_f32_16x16x32_bf16 v[80:83],v[224:227],v[136:139],v[80:83]\n"
"ds_read_b64_tr_b16 v[220:221],v253\n"
"v_cvt_pk_bf16_f32 v65,v66,v67\n"
"v_mfma_f32_16x16x32_bf16 v[80:83],v[228:231],v[192:195],v[80:83]\n"
"ds_read_b64_tr_b16 v[222:223],v253 offset:2048\n"
"s_waitcnt lgkmcnt(8)\n"
"v_mfma_f32_16x16x32_bf16 v[80:83],v[232:235],v[200:203],v[80:83]\n"
"ds_read_b64_tr_b16 v[224:225],v253 offset:4096\n"
"v_cvt_pk_bf16_f32 v66,v72,v73\n"
"v_mfma_f32_16x16x32_bf16 v[80:83],v[236:239],v[144:147],v[80:83]\n"
"ds_read_b64_tr_b16 v[226:227],v253 offset:6144\n"
"v_cvt_pk_bf16_f32 v67,v74,v75\n"
"v_mfma_f32_16x16x32_bf16 v[84:87],v[232:235],v[204:207],v[84:87]\n"
"ds_read_b64_tr_b16 v[228:229],v253 offset:8192\n"
"s_add_u32 s78,s9,4096\n"
"v_mfma_f32_16x16x32_bf16 v[84:87],v[236:239],v[148:151],v[84:87]\n"
"ds_read_b64_tr_b16 v[230:231],v253 offset:10240\n"
"s_waitcnt lgkmcnt(8)\n"
"v_mfma_f32_16x16x32_bf16 v[84:87],v[208:211],v[156:159],v[84:87]\n"
"ds_read_b64_tr_b16 v[232:233],v253 offset:12288\n"
"buffer_store_dwordx4 v[64:67],v252,s[32:35],s78 offen sc1\n"
"v_mfma_f32_16x16x32_bf16 v[84:87],v[212:215],v[164:167],v[84:87]\n"
"ds_read_b64_tr_b16 v[234:235],v253 offset:14336\n"
"v_cvt_pk_bf16_f32 v68,v68,v69\n"
"v_mfma_f32_16x16x32_bf16 v[80:83],v[208:211],v[152:155],v[80:83]\n"
"ds_read_b64_tr_b16 v[236:237],v253 offset:16384\n"
"v_cvt_pk_bf16_f32 v69,v70,v71\n"
"v_mfma_f32_16x16x32_bf16 v[80:83],v[212:215],v[160:163],v[80:83]\n"
"ds_read_b64_tr_b16 v[238:239],v253 offset:18432\n"
"s_waitcnt lgkmcnt(8)\n"
"v_mfma_f32_16x16x32_bf16 v[80:83],v[216:219],v[168:171],v[80:83]\n"
"ds_read_b64_tr_b16 v[208:209],v253 offset:20480\n"
"v_cvt_pk_bf16_f32 v70,v76,v77\n"
"v_mfma_f32_16x16x32_bf16 v[84:87],v[216:219],v[172:175],v[84:87]\n"
"ds_read_b64_tr_b16 v[210:211],v253 offset:22528\n"
"v_cvt_pk_bf16_f32 v71,v78,v79\n"
"v_mfma_f32_16x16x32_bf16 v[88:91],v[220:223],v[120:123],v[88:91]\n"
"ds_read_b64_tr_b16 v[212:213],v253 offset:24576\n"
"s_waitcnt lgkmcnt(7)\n"
"v_mfma_f32_16x16x32_bf16 v[88:91],v[224:227],v[128:131],v[88:91]\n"
"ds_read_b64_tr_b16 v[214:215],v253 offset:26624\n"
"s_add_u32 s78,s9,22528\n"
"v_mfma_f32_16x16x32_bf16 v[92:95],v[220:223],v[124:127],v[92:95]\n"
"ds_read_b64_tr_b16 v[216:217],v253 offset:28672\n"
"buffer_store_dwordx4 v[68:71],v252,s[32:35],s78 offen sc1\n"
"v_mfma_f32_16x16x32_bf16 v[92:95],v[224:227],v[132:135],v[92:95]\n"
"ds_read_b64_tr_b16 v[218:219],v253 offset:30720\n"
"v_mfma_f32_16x16x32_bf16 v[92:95],v[228:231],v[140:143],v[92:95]\n"
"ds_read_b64_tr_b16 v[220:221],v253 offset:32768\n"
"s_waitcnt lgkmcnt(7)\n"
"v_mfma_f32_16x16x32_bf16 v[92:95],v[232:235],v[196:199],v[92:95]\n"
"ds_read_b64_tr_b16 v[222:223],v253 offset:34816\n"
"v_mfma_f32_16x16x32_bf16 v[88:91],v[228:231],v[136:139],v[88:91]\n"
"v_mfma_f32_16x16x32_bf16 v[88:91],v[232:235],v[192:195],v[88:91]\n"
"v_mfma_f32_16x16x32_bf16 v[88:91],v[236:239],v[200:203],v[88:91]\n"
"s_waitcnt lgkmcnt(4)\n"
"v_mfma_f32_16x16x32_bf16 v[88:91],v[208:211],v[144:147],v[88:91]\n"
"v_mfma_f32_16x16x32_bf16 v[92:95],v[236:239],v[204:207],v[92:95]\n"
"v_mfma_f32_16x16x32_bf16 v[92:95],v[208:211],v[148:151],v[92:95]\n"
"v_mfma_f32_16x16x32_bf16 v[92:95],v[212:215],v[156:159],v[92:95]\n"
"s_waitcnt lgkmcnt(0)\n"
"v_mfma_f32_16x16x32_bf16 v[92:95],v[216:219],v[164:167],v[92:95]\n"
"v_mfma_f32_16x16x32_bf16 v[88:91],v[212:215],v[152:155],v[88:91]\n"
"v_mfma_f32_16x16x32_bf16 v[88:91],v[216:219],v[160:163],v[88:91]\n"
"v_mfma_f32_16x16x32_bf16 v[88:91],v[220:223],v[168:171],v[88:91]\n"
"v_mfma_f32_16x16x32_bf16 v[92:95],v[220:223],v[172:175],v[92:95]\n"
"s_setprio 0\n"
"v_add_u32 v248,4294893568,v248\n"
"v_add_u32 v249,4294893568,v249\n"
"v_add_u32 v250,4294893568,v250\n"
"v_add_u32 v253,4294893568,v253\n"
"v_cvt_pk_bf16_f32 v84,v84,v85\n"
"v_cvt_pk_bf16_f32 v85,v86,v87\n"
"s_nop 0\n"
"v_cvt_pk_bf16_f32 v86,v92,v93\n"
"v_cvt_pk_bf16_f32 v87,v94,v95\n"
"s_add_u32 s78,s9,23552\n"
"buffer_store_dwordx4 v[84:87],v252,s[32:35],s78 offen sc1\n"
"v_cvt_pk_bf16_f32 v80,v80,v81\n"
"v_cvt_pk_bf16_f32 v81,v82,v83\n"
"v_cvt_pk_bf16_f32 v82,v88,v89\n"
"v_cvt_pk_bf16_f32 v83,v90,v91\n"
"s_add_u32 s78,s9,5120\n"
"buffer_store_dwordx4 v[80:83],v252,s[32:35],s78 offen sc1\n"
"s_cmp_lg_u32 s71,s69\n"
"s_cbranch_scc1 .Lk_mail2\n"
"s_setprio 3\n"
"ds_write_b128 v252,v[64:67] offset:8192\n"
"ds_write_b128 v252,v[68:71] offset:10240\n"
"ds_write_b128 v252,v[80:83] offset:9216\n"
"ds_write_b128 v252,v[84:87] offset:11264\n"
"s_setprio 0\n"
".Lk_mail2:\n"
"s_waitcnt lgkmcnt(0)\n"
"s_barrier\n"
"s_setprio 0\n"
"s_add_u32 s77,s81,2048\n"
"buffer_load_dwordx4 v[32:35],v252,s[32:35],s77 offen sc1\n"
"s_add_u32 s77,s82,2048\n"
"buffer_load_dwordx4 v[36:39],v252,s[32:35],s77 offen sc1\n"
"s_add_u32 s77,s83,2048\n"
"buffer_load_dwordx4 v[40:43],v252,s[32:35],s77 offen sc1\n"
"s_waitcnt vmcnt(7)\n"
"v_mul_f32_e64 v184,-v184,v185\n"
"v_mul_f32_e64 v186,-v186,v187\n"
"v_lshlrev_b32 v188,1,v188\n"
"s_lshl_b32 s84,s84,1\n"
"v_lshlrev_b32 v48,16,v104\n"
"v_and_b32 v49,0xffff0000,v104\n"
"v_lshlrev_b32 v50,16,v105\n"
"v_and_b32 v51,0xffff0000,v105\n"
"v_lshlrev_b32 v52,16,v106\n"
"v_and_b32 v53,0xffff0000,v106\n"
"v_lshlrev_b32 v54,16,v107\n"
"v_and_b32 v55,0xffff0000,v107\n"
"v_lshlrev_b32 v56,16,v108\n"
"v_and_b32 v57,0xffff0000,v108\n"
"v_lshlrev_b32 v58,16,v109\n"
"v_and_b32 v59,0xffff0000,v109\n"
"v_lshlrev_b32 v60,16,v110\n"
"v_and_b32 v61,0xffff0000,v110\n"
"v_lshlrev_b32 v62,16,v111\n"
"v_and_b32 v63,0xffff0000,v111\n"
"v_add_f32 v104,v48,v60\n"
"v_add_f32 v105,v49,v61\n"
"v_add_f32 v106,v50,v62\n"
"v_add_f32 v107,v51,v63\n"
"v_fma_f32 v48,v48,v185,v184\n"
"v_fma_f32 v49,v49,v185,v184\n"
"v_fma_f32 v50,v50,v185,v184\n"
"v_fma_f32 v51,v51,v185,v184\n"
"v_fmac_f32 v104,v48,v56\n"
"v_fmac_f32 v105,v49,v57\n"
"v_fmac_f32 v106,v50,v58\n"
"v_fmac_f32 v107,v51,v59\n"
"v_add_f32 v108,v52,v60\n"
"v_add_f32 v109,v53,v61\n"
"v_add_f32 v110,v54,v62\n"
"v_add_f32 v111,v55,v63\n"
"v_fma_f32 v52,v52,v187,v186\n"
"v_fma_f32 v53,v53,v187,v186\n"
"v_fma_f32 v54,v54,v187,v186\n"
"v_fma_f32 v55,v55,v187,v186\n"
"v_fmac_f32 v108,v52,v56\n"
"v_fmac_f32 v109,v53,v57\n"
"v_fmac_f32 v110,v54,v58\n"
"v_fmac_f32 v111,v55,v59\n"
"v_mfma_f32_16x16x32_bf16 v[208:211],v[178:181],v[16:19],0\n"
"v_mfma_f32_16x16x32_bf16 v[212:215],v[176:179],v[16:19],0\n"
"v_mfma_f32_16x16x32_bf16 v[208:211],v[178:181],v[20:23],v[208:211]\n"
"v_mfma_f32_16x16x32_bf16 v[212:215],v[176:179],v[20:23],v[212:215]\n"
"v_mfma_f32_16x16x32_bf16 v[208:211],v[178:181],v[24:27],v[208:211]\n"
"v_mfma_f32_16x16x32_bf16 v[212:215],v[176:179],v[24:27],v[212:215]\n"
"v_mfma_f32_16x16x32_bf16 v[208:211],v[178:181],v[28:31],v[208:211]\n"
"v_mfma_f32_16x16x32_bf16 v[212:215],v[176:179],v[28:31],v[212:215]\n"
"ds_read_b128 v[44:47],v191 offset:4096\n"
"s_waitcnt lgkmcnt(0)\n"
"v_mfma_f32_16x16x32_bf16 v[208:211],v[178:181],v[44:47],v[208:211]\n"
"v_mfma_f32_16x16x32_bf16 v[212:215],v[176:179],v[44:47],v[212:215]\n"
"s_waitcnt vmcnt(0)\n"
"v_max_u32 v236,v32,v33\n"
"v_max3_u32 v236,v236,v34,v35\n"
"v_max3_u32 v236,v236,v36,v37\n"
"v_max3_u32 v236,v236,v38,v39\n"
"v_max3_u32 v236,v236,v40,v41\n"
"v_max3_u32 v236,v236,v42,v43\n"
"v_cmp_eq_u32 vcc,-1,v236\n"
"s_mov_b32 s89,0x10000f\n"
"s_nop 0\n"
"s_cbranch_vccz .Lk_fm1_ok\n"
"s_waitcnt vmcnt(0)\n"
"v_mov_b32 v242,5\n"
"s_mov_b64 exec,1\n"
"buffer_store_dword v242,off,s[32:35],s88 sc1\n"
"s_mov_b64 exec,-1\n"
"v_mov_b32 v255,5\n"
"v_cndmask_b32 v255,v255,0,s[96:97]\n"
".Lk_fm1_spin:\n"
"s_sleep 2\n"
"s_and_b32 s75,s89,15\n"
"s_cbranch_scc1 .Lk_fm1_nw\n"
"buffer_load_dword v254,v190,s[32:35],s95 offen sc1\n"
"s_waitcnt vmcnt(0)\n"
".Lk_fm1_nw:\n"
"s_add_u32 s77,s81,2048\n"
"buffer_load_dwordx4 v[32:35],v252,s[32:35],s77 offen sc1\n"
"s_add_u32 s77,s82,2048\n"
"buffer_load_dwordx4 v[36:39],v252,s[32:35],s77 offen sc1\n"
"s_add_u32 s77,s83,2048\n"
"buffer_load_dwordx4 v[40:43],v252,s[32:35],s77 offen sc1\n"
"s_waitcnt vmcnt(0)\n"
"v_max_u32 v236,v32,v33\n"
"v_max3_u32 v236,v236,v34,v35\n"
"v_max3_u32 v236,v236,v36,v37\n"
"v_max3_u32 v236,v236,v38,v39\n"
"v_max3_u32 v236,v236,v40,v41\n"
"v_max3_u32 v236,v236,v42,v43\n"
"v_cmp_eq_u32 vcc,-1,v236\n"
"s_sub_u32 s89,s89,1\n"
"s_cbranch_scc1 .Lk_fm1_ok\n"
"s_cbranch_vccz .Lk_fm1_ok\n"
"s_cmp_lg_u32 s75,0\n"
"s_cbranch_scc1 .Lk_fm1_spin\n"
"v_cmp_gt_u32 vcc,v255,v254\n"
"s_nop 1\n"
"s_cbranch_vccnz .Lk_fm1_spin\n"
".Lk_fm1_ok:\n"
"v_mfma_f32_16x16x32_bf16 v[208:211],v[178:181],v[32:35],v[208:211]\n"
"v_mfma_f32_16x16x32_bf16 v[212:215],v[176:179],v[32:35],v[212:215]\n"
"v_mfma_f32_16x16x32_bf16 v[208:211],v[178:181],v[36:39],v[208:211]\n"
"v_mfma_f32_16x16x32_bf16 v[212:215],v[176:179],v[36:39],v[212:215]\n"
"v_mfma_f32_16x16x32_bf16 v[208:211],v[178:181],v[40:43],v[208:211]\n"
"v_mfma_f32_16x16x32_bf16 v[212:215],v[176:179],v[40:43],v[212:215]\n"
"s_nop 7\n"
"v_permlane32_swap_b32 v208,v212\n"
"v_permlane32_swap_b32 v209,v213\n"
"v_permlane32_swap_b32 v210,v214\n"
"v_permlane32_swap_b32 v211,v215\n"
"s_mov_b64 vcc,s[90:91]\n"
"v_cndmask_b32_dpp v232,v208,v212,vcc quad_perm:[2,3,0,1] row_mask:0xf bank_mask:0xf\n"
"v_cndmask_b32_dpp v233,v209,v213,vcc quad_perm:[2,3,0,1] row_mask:0xf bank_mask:0xf\n"
"v_cndmask_b32_dpp v234,v210,v214,vcc quad_perm:[2,3,0,1] row_mask:0xf bank_mask:0xf\n"
"v_cndmask_b32_dpp v235,v211,v215,vcc quad_perm:[2,3,0,1] row_mask:0xf bank_mask:0xf\n"
"s_not_b64 vcc,vcc\n"
"v_cndmask_b32_dpp v208,v212,v208,vcc quad_perm:[2,3,0,1] row_mask:0xf bank_mask:0xf\n"
"v_cndmask_b32_dpp v209,v213,v209,vcc quad_perm:[2,3,0,1] row_mask:0xf bank_mask:0xf\n"
"v_cndmask_b32_dpp v210,v214,v210,vcc quad_perm:[2,3,0,1] row_mask:0xf bank_mask:0xf\n"
"v_cndmask_b32_dpp v211,v215,v211,vcc quad_perm:[2,3,0,1] row_mask:0xf bank_mask:0xf\n"
"v_permlane16_swap_b32 v208,v232\n"
"v_permlane16_swap_b32 v209,v233\n"
"v_permlane16_swap_b32 v210,v234\n"
"v_permlane16_swap_b32 v211,v235\n"
"s_mov_b64 vcc,s[92:93]\n"
"v_cndmask_b32_dpp v212,v208,v232,vcc quad_perm:[1,0,3,2] row_mask:0xf bank_mask:0xf\n"
"v_cndmask_b32_dpp v213,v209,v233,vcc quad_perm:[1,0,3,2] row_mask:0xf bank_mask:0xf\n"
"v_cndmask_b32_dpp v214,v210,v234,vcc quad_perm:[1,0,3,2] row_mask:0xf bank_mask:0xf\n"
"v_cndmask_b32_dpp v215,v211,v235,vcc quad_perm:[1,0,3,2] row_mask:0xf bank_mask:0xf\n"
"s_not_b64 vcc,vcc\n"
"v_cndmask_b32_dpp v208,v232,v208,vcc quad_perm:[1,0,3,2] row_mask:0xf bank_mask:0xf\n"
"v_cndmask_b32_dpp v209,v233,v209,vcc quad_perm:[1,0,3,2] row_mask:0xf bank_mask:0xf\n"
"v_cndmask_b32_dpp v210,v234,v210,vcc quad_perm:[1,0,3,2] row_mask:0xf bank_mask:0xf\n"
"v_cndmask_b32_dpp v211,v235,v211,vcc quad_perm:[1,0,3,2] row_mask:0xf bank_mask:0xf\n"
"v_fmac_f32 v104,s79,v208\n"
"v_fmac_f32 v105,s79,v209\n"
"v_fmac_f32 v106,s79,v210\n"
"v_fmac_f32 v107,s79,v211\n"
"v_fmac_f32 v108,s79,v212\n"
"v_fmac_f32 v109,s79,v213\n"
"v_fmac_f32 v110,s79,v214\n"
"v_fmac_f32 v111,s79,v215\n"
"v_mov_b32 v242,5\n"
"s_mov_b64 exec,1\n"
"buffer_store_dword v242,off,s[32:35],s88 sc1\n"
"s_mov_b64 exec,-1\n"
"buffer_store_dwordx4 v[104:107],v188,s[28:31],s84 offen offset:256 sc1\n"
"buffer_store_dwordx4 v[108:111],v188,s[28:31],s84 offen offset:2560 sc1\n"
"s_endpgm\n"
".Lk_mlp_end:\n"
".Lk_ssm:\n"
"s_cmp_lt_u32 s71,2\n"
"s_cselect_b64 s[42:43],s[42:43],s[44:45]\n"
"s_mov_b32 s16,s42\n"
"s_and_b32 s17,s43,0xffff\n"
"s_mov_b32 s18,663552\n"
"s_mov_b32 s19,0x20000\n"
"s_mov_b32 s24,s46\n"
"s_and_b32 s25,s47,0xffff\n"
"s_mov_b32 s26,663552\n"
"s_mov_b32 s27,0x20000\n"
"s_mov_b32 s28,s48\n"
"s_and_b32 s29,s49,0xffff\n"
"s_mov_b32 s30,663552\n"
"s_mov_b32 s31,0x20000\n"
"s_mov_b32 s20,s58\n"
"s_and_b32 s21,s59,0xffff\n"
"s_mov_b32 s22,5308416\n"
"s_mov_b32 s23,0x20000\n"
"v_and_b32 v188,255,v255\n"
"v_min_u32 v188,143,v188\n"
"s_lshl_b32 s75,s69,7\n"
"s_lshl_b32 s76,s69,4\n"
"s_add_u32 s76,s76,512\n"
"s_mul_i32 s92,s76,1152\n"
"s_sub_u32 s76,s76,128\n"
"v_mov_b32 v241,s75\n"
"v_mov_b32 v242,s76\n"
"v_cmp_lt_u32 vcc,127,v188\n"
"s_nop 1\n"
"v_cndmask_b32 v241,v241,v242,vcc\n"
"v_add_lshl_u32 v241,v241,v188,1\n"
"v_mov_b32 v195,v241\n"
"s_mov_b64 s[4:5],s[38:39]\n"
"s_mov_b64 s[6:7],s[40:41]\n"
"s_mov_b64 s[82:83],s[50:51]\n"
"s_mul_i32 s80,s75,1152\n"
"s_and_b32 s76,s71,1\n"
"s_lshl_b32 s76,s76,3\n"
"v_lshrrev_b32 v241,3,v254\n"
"v_add_u32 v241,s76,v241\n"
"v_mul_u32_u24 v250,1152,v241\n"
"s_and_b32 s78,s71,1\n"
"s_lshl_b32 s78,s78,2\n"
"v_lshrrev_b32 v241,4,v254\n"
"v_add_u32 v241,s78,v241\n"
"v_and_b32 v242,7,v254\n"
"v_xor_b32 v241,v241,v242\n"
"v_lshl_add_u32 v250,v241,4,v250\n"
"s_lshl_b32 s76,s11,3\n"
"v_lshrrev_b32 v241,3,v254\n"
"v_add_u32 v241,s76,v241\n"
"v_mul_u32_u24 v251,1152,v241\n"
"s_and_b32 s78,s71,1\n"
"s_lshl_b32 s78,s78,2\n"
"v_lshrrev_b32 v241,4,v254\n"
"v_add_u32 v241,s78,v241\n"
"v_and_b32 v242,7,v254\n"
"v_xor_b32 v241,v241,v242\n"
"v_lshl_add_u32 v251,v241,4,v251\n"
"s_lshl_b32 s81,s69,8\n"
"v_and_b32 v241,15,v254\n"
"v_lshrrev_b32 v242,4,v254\n"
"v_bfe_u32 v243,v254,1,3\n"
"v_xor_b32 v242,v242,v243\n"
"v_lshl_add_u32 v241,v241,3,v242\n"
"v_lshlrev_b32 v248,4,v241\n"
"v_xor_b32 v249,64,v248\n"
"s_mul_i32 s78,s72,36864\n"
"v_add_u32 v248,s78,v248\n"
"v_add_u32 v249,s78,v249\n"
"s_lshl_b32 s76,s69,4\n"
"s_add_u32 s76,s76,s68\n"
"s_add_u32 s76,s76,64\n"
"s_lshl_b32 s76,s76,2\n"
"s_add_u32 s76,s76,s71\n"
"s_mul_i32 s87,s76,36864\n"
"s_lshl_b32 s76,s72,10\n"
"s_add_u32 s87,s87,s76\n"
"s_add_u32 s87,s87,20971520\n"
"s_lshl_b32 s76,s11,6\n"
"s_add_u32 s88,s88,s76\n"
"s_lshl_b32 s76,s71,9\n"
"s_add_u32 s76,s76,143360\n"
"v_lshlrev_b32 v253,3,v254\n"
"v_add_u32 v253,s76,v253\n"
"v_and_b32 v255,15,v254\n"
"v_lshlrev_b32 v255,3,v255\n"
"v_add_u32 v255,110592,v255\n"
"v_mov_b32 v208,0x3f803f80\n"
"s_mul_i32 s75,s71,18432\n"
"s_add_u32 s75,s75,73728\n"
"s_lshl_b32 s76,s89,10\n"
"s_add_u32 s76,s76,s75\n"
"v_add_u32 v220,s76,v252\n"
"v_and_b32 v240,15,v254\n"
"v_lshrrev_b32 v241,4,v254\n"
"v_bfe_u32 v242,v254,1,3\n"
"v_xor_b32 v241,v241,v242\n"
"v_lshl_add_u32 v240,v240,3,v241\n"
"v_lshlrev_b32 v240,4,v240\n"
"v_xor_b32 v241,64,v240\n"
"v_add_u32 v174,s75,v240\n"
"v_add_u32 v175,s75,v241\n"
"s_cmp_eq_u32 s72,1\n"
"s_cbranch_scc1 .Lk_hop_skips\n"
"s_getpc_b64 s[96:97]\n"
".Lk_hop_pcas:\n"
"s_add_u32 s96,s96,.Lk_hop_backas-.Lk_hop_pcas\n"
"s_addc_u32 s97,s97,0\n"
"s_mov_b64 exec,0\n"
"s_cmp_eq_u32 s71,0\n"
"s_cbranch_scc1 .Lk_pads0\n"
"s_cmp_eq_u32 s71,1\n"
"s_cbranch_scc1 .Lk_pads1\n"
"s_cmp_eq_u32 s71,2\n"
"s_cbranch_scc1 .Lk_pads2\n"
"s_branch .Lk_pads3\n"
".Lk_hop_rets:\n"
"s_mov_b64 exec,-1\n"
"s_setpc_b64 s[96:97]\n"
".Lk_hop_backas:\n"
".Lk_hop_skips:\n"
"s_setprio 2\n"
"v_mov_b32 v210,0\n"
"v_mov_b32 v211,0\n"
"v_mov_b32 v212,0\n"
"v_mov_b32 v213,0\n"
"s_waitcnt vmcnt(8)\n"
"v_dot2c_f32_bf16 v210,v0,v208\n"
"v_dot2c_f32_bf16 v212,v0,v0\n"
"v_dot2c_f32_bf16 v211,v1,v208\n"
"v_dot2c_f32_bf16 v213,v1,v1\n"
"v_dot2c_f32_bf16 v210,v2,v208\n"
"v_dot2c_f32_bf16 v212,v2,v2\n"
"v_dot2c_f32_bf16 v211,v3,v208\n"
"v_dot2c_f32_bf16 v213,v3,v3\n"
"s_waitcnt vmcnt(7)\n"
"v_dot2c_f32_bf16 v210,v4,v208\n"
"v_dot2c_f32_bf16 v212,v4,v4\n"
"v_dot2c_f32_bf16 v211,v5,v208\n"
"v_dot2c_f32_bf16 v213,v5,v5\n"
"v_dot2c_f32_bf16 v210,v6,v208\n"
"v_dot2c_f32_bf16 v212,v6,v6\n"
"v_dot2c_f32_bf16 v211,v7,v208\n"
"v_dot2c_f32_bf16 v213,v7,v7\n"
"s_waitcnt vmcnt(6)\n"
"v_dot2c_f32_bf16 v210,v8,v208\n"
"v_dot2c_f32_bf16 v212,v8,v8\n"
"v_dot2c_f32_bf16 v211,v9,v208\n"
"v_dot2c_f32_bf16 v213,v9,v9\n"
"v_dot2c_f32_bf16 v210,v10,v208\n"
"v_dot2c_f32_bf16 v212,v10,v10\n"
"v_dot2c_f32_bf16 v211,v11,v208\n"
"v_dot2c_f32_bf16 v213,v11,v11\n"
"s_waitcnt vmcnt(5)\n"
"v_dot2c_f32_bf16 v210,v12,v208\n"
"v_dot2c_f32_bf16 v212,v12,v12\n"
"v_dot2c_f32_bf16 v211,v13,v208\n"
"v_dot2c_f32_bf16 v213,v13,v13\n"
"v_dot2c_f32_bf16 v210,v14,v208\n"
"v_dot2c_f32_bf16 v212,v14,v14\n"
"v_dot2c_f32_bf16 v211,v15,v208\n"
"v_dot2c_f32_bf16 v213,v15,v15\n"
"s_waitcnt vmcnt(4)\n"
"v_dot2c_f32_bf16 v210,v16,v208\n"
"v_dot2c_f32_bf16 v212,v16,v16\n"
"v_dot2c_f32_bf16 v211,v17,v208\n"
"v_dot2c_f32_bf16 v213,v17,v17\n"
"v_dot2c_f32_bf16 v210,v18,v208\n"
"v_dot2c_f32_bf16 v212,v18,v18\n"
"v_dot2c_f32_bf16 v211,v19,v208\n"
"v_dot2c_f32_bf16 v213,v19,v19\n"
"s_waitcnt vmcnt(3)\n"
"v_dot2c_f32_bf16 v210,v20,v208\n"
"v_dot2c_f32_bf16 v212,v20,v20\n"
"v_dot2c_f32_bf16 v211,v21,v208\n"
"v_dot2c_f32_bf16 v213,v21,v21\n"
"v_dot2c_f32_bf16 v210,v22,v208\n"
"v_dot2c_f32_bf16 v212,v22,v22\n"
"v_dot2c_f32_bf16 v211,v23,v208\n"
"v_dot2c_f32_bf16 v213,v23,v23\n"
"s_waitcnt vmcnt(2)\n"
"v_dot2c_f32_bf16 v210,v24,v208\n"
"v_dot2c_f32_bf16 v212,v24,v24\n"
"v_dot2c_f32_bf16 v211,v25,v208\n"
"v_dot2c_f32_bf16 v213,v25,v25\n"
"v_dot2c_f32_bf16 v210,v26,v208\n"
"v_dot2c_f32_bf16 v212,v26,v26\n"
"v_dot2c_f32_bf16 v211,v27,v208\n"
"v_dot2c_f32_bf16 v213,v27,v27\n"
"s_waitcnt vmcnt(1)\n"
"buffer_load_dwordx4 v[72:75],v247,s[12:15],s95 offen\n"
"v_dot2c_f32_bf16 v210,v28,v208\n"
"v_dot2c_f32_bf16 v212,v28,v28\n"
"v_dot2c_f32_bf16 v211,v29,v208\n"
"v_dot2c_f32_bf16 v213,v29,v29\n"
"v_dot2c_f32_bf16 v210,v30,v208\n"
"v_dot2c_f32_bf16 v212,v30,v30\n"
"v_dot2c_f32_bf16 v211,v31,v208\n"
"v_dot2c_f32_bf16 v213,v31,v31\n"
"s_waitcnt vmcnt(1)\n"
"buffer_load_dwordx4 v[76:79],v247,s[12:15],s95 offen offset:128\n"
"v_dot2c_f32_bf16 v210,v32,v208\n"
"v_dot2c_f32_bf16 v212,v32,v32\n"
"v_dot2c_f32_bf16 v211,v33,v208\n"
"v_dot2c_f32_bf16 v213,v33,v33\n"
"v_dot2c_f32_bf16 v210,v34,v208\n"
"v_dot2c_f32_bf16 v212,v34,v34\n"
"v_dot2c_f32_bf16 v211,v35,v208\n"
"v_dot2c_f32_bf16 v213,v35,v35\n"
"buffer_load_dwordx4 v[80:83],v247,s[12:15],s95 offen offset:256\n"
"buffer_load_dwordx4 v[84:87],v247,s[12:15],s95 offen offset:384\n"
"buffer_load_dwordx4 v[88:91],v247,s[12:15],s95 offen offset:512\n"
".Lk_pads0:\n"
"s_cbranch_execz .Lk_pads4\n"
"buffer_load_dwordx4 v[92:95],v247,s[12:15],s95 offen offset:640\n"
"buffer_load_dwordx4 v[96:99],v247,s[12:15],s95 offen offset:768\n"
"buffer_load_dwordx4 v[100:103],v247,s[12:15],s95 offen offset:896\n"
"buffer_load_dwordx4 v[104:107],v247,s[12:15],s95 offen offset:1024\n"
"v_add_f32 v210,v210,v211\n"
"v_add_f32 v212,v212,v213\n"
"s_nop 0\n"
"v_add_f32_dpp v210,v210,v210 quad_perm:[1,0,3,2] row_mask:0xf bank_mask:0xf\n"
"v_add_f32_dpp v212,v212,v212 quad_perm:[1,0,3,2] row_mask:0xf bank_mask:0xf\n"
"s_nop 0\n"
"v_add_f32_dpp v210,v210,v210 quad_perm:[2,3,0,1] row_mask:0xf bank_mask:0xf\n"
"v_add_f32_dpp v212,v212,v212 quad_perm:[2,3,0,1] row_mask:0xf bank_mask:0xf\n"
"s_nop 0\n"
"v_add_f32_dpp v210,v210,v210 row_half_mirror row_mask:0xf bank_mask:0xf\n"
"v_add_f32_dpp v212,v212,v212 row_half_mirror row_mask:0xf bank_mask:0xf\n"
"v_mul_f32 v218,0x3ae38e39,v210\n"
"v_mul_f32 v212,0x3ae38e39,v212\n"
"v_fma_f32 v212,-v218,v218,v212\n"
"v_max_f32 v212,0,v212\n"
"v_add_f32 v219,0x3727c5ac,v212\n"
"v_rsq_f32 v219,v219\n"
"s_nop 0\n"
"v_cvt_pk_bf16_f32 v214,v219,0\n"
"v_lshlrev_b32 v215,16,v214\n"
"v_mul_f32_e64 v216,-v218,v215\n"
"v_dot2_f32_bf16 v240,v0,v214,v216\n"
"v_dot2_f32_bf16 v241,v0,v215,v216\n"
"v_dot2_f32_bf16 v242,v1,v214,v216\n"
"v_dot2_f32_bf16 v243,v1,v215,v216\n"
"v_dot2_f32_bf16 v244,v2,v214,v216\n"
"v_dot2_f32_bf16 v245,v2,v215,v216\n"
"v_cvt_pk_bf16_f32 v0,v240,v241\n"
"v_dot2_f32_bf16 v240,v3,v214,v216\n"
"v_dot2_f32_bf16 v241,v3,v215,v216\n"
"v_cvt_pk_bf16_f32 v1,v242,v243\n"
"v_dot2_f32_bf16 v242,v4,v214,v216\n"
"v_dot2_f32_bf16 v243,v4,v215,v216\n"
"v_cvt_pk_bf16_f32 v2,v244,v245\n"
"v_dot2_f32_bf16 v244,v5,v214,v216\n"
"v_dot2_f32_bf16 v245,v5,v215,v216\n"
"v_cvt_pk_bf16_f32 v3,v240,v241\n"
"ds_write_b128 v220,v[0:3]\n"
"v_dot2_f32_bf16 v240,v6,v214,v216\n"
"v_dot2_f32_bf16 v241,v6,v215,v216\n"
"v_cvt_pk_bf16_f32 v4,v242,v243\n"
"v_dot2_f32_bf16 v242,v7,v214,v216\n"
"v_dot2_f32_bf16 v243,v7,v215,v216\n"
"v_cvt_pk_bf16_f32 v5,v244,v245\n"
"v_dot2_f32_bf16 v244,v8,v214,v216\n"
"v_dot2_f32_bf16 v245,v8,v215,v216\n"
"v_cvt_pk_bf16_f32 v6,v240,v241\n"
"v_dot2_f32_bf16 v240,v9,v214,v216\n"
"v_dot2_f32_bf16 v241,v9,v215,v216\n"
"v_cvt_pk_bf16_f32 v7,v242,v243\n"
"ds_write_b128 v220,v[4:7] offset:2048\n"
"v_dot2_f32_bf16 v242,v10,v214,v216\n"
"v_dot2_f32_bf16 v243,v10,v215,v216\n"
"v_cvt_pk_bf16_f32 v8,v244,v245\n"
"v_dot2_f32_bf16 v244,v11,v214,v216\n"
"v_dot2_f32_bf16 v245,v11,v215,v216\n"
"v_cvt_pk_bf16_f32 v9,v240,v241\n"
"v_dot2_f32_bf16 v240,v12,v214,v216\n"
"v_dot2_f32_bf16 v241,v12,v215,v216\n"
"v_cvt_pk_bf16_f32 v10,v242,v243\n"
"v_dot2_f32_bf16 v242,v13,v214,v216\n"
"v_dot2_f32_bf16 v243,v13,v215,v216\n"
"v_cvt_pk_bf16_f32 v11,v244,v245\n"
"ds_write_b128 v220,v[8:11] offset:4096\n"
"v_dot2_f32_bf16 v244,v14,v214,v216\n"
"v_dot2_f32_bf16 v245,v14,v215,v216\n"
"v_cvt_pk_bf16_f32 v12,v240,v241\n"
"v_dot2_f32_bf16 v240,v15,v214,v216\n"
"v_dot2_f32_bf16 v241,v15,v215,v216\n"
"v_cvt_pk_bf16_f32 v13,v242,v243\n"
"v_dot2_f32_bf16 v242,v16,v214,v216\n"
"v_dot2_f32_bf16 v243,v16,v215,v216\n"
"v_cvt_pk_bf16_f32 v14,v244,v245\n"
"v_dot2_f32_bf16 v244,v17,v214,v216\n"
"v_dot2_f32_bf16 v245,v17,v215,v216\n"
"v_cvt_pk_bf16_f32 v15,v240,v241\n"
"ds_write_b128 v220,v[12:15] offset:6144\n"
"v_dot2_f32_bf16 v240,v18,v214,v216\n"
"v_dot2_f32_bf16 v241,v18,v215,v216\n"
"v_cvt_pk_bf16_f32 v16,v242,v243\n"
"v_dot2_f32_bf16 v242,v19,v214,v216\n"
"v_dot2_f32_bf16 v243,v19,v215,v216\n"
"v_cvt_pk_bf16_f32 v17,v244,v245\n"
"v_dot2_f32_bf16 v244,v20,v214,v216\n"
"v_dot2_f32_bf16 v245,v20,v215,v216\n"
"v_cvt_pk_bf16_f32 v18,v240,v241\n"
"v_dot2_f32_bf16 v240,v21,v214,v216\n"
"v_dot2_f32_bf16 v241,v21,v215,v216\n"
"v_cvt_pk_bf16_f32 v19,v242,v243\n"
"ds_write_b128 v220,v[16:19] offset:8192\n"
"v_dot2_f32_bf16 v242,v22,v214,v216\n"
"v_dot2_f32_bf16 v243,v22,v215,v216\n"
"v_cvt_pk_bf16_f32 v20,v244,v245\n"
"v_dot2_f32_bf16 v244,v23,v214,v216\n"
"v_dot2_f32_bf16 v245,v23,v215,v216\n"
"v_cvt_pk_bf16_f32 v21,v240,v241\n"
"v_dot2_f32_bf16 v240,v24,v214,v216\n"
"v_dot2_f32_bf16 v241,v24,v215,v216\n"
"v_cvt_pk_bf16_f32 v22,v242,v243\n"
"v_dot2_f32_bf16 v242,v25,v214,v216\n"
"v_dot2_f32_bf16 v243,v25,v215,v216\n"
"v_cvt_pk_bf16_f32 v23,v244,v245\n"
"ds_write_b128 v220,v[20:23] offset:10240\n"
"v_dot2_f32_bf16 v244,v26,v214,v216\n"
"v_dot2_f32_bf16 v245,v26,v215,v216\n"
"v_cvt_pk_bf16_f32 v24,v240,v241\n"
"v_dot2_f32_bf16 v240,v27,v214,v216\n"
"v_dot2_f32_bf16 v241,v27,v215,v216\n"
"v_cvt_pk_bf16_f32 v25,v242,v243\n"
"v_dot2_f32_bf16 v242,v28,v214,v216\n"
"v_dot2_f32_bf16 v243,v28,v215,v216\n"
"v_cvt_pk_bf16_f32 v26,v244,v245\n"
"v_dot2_f32_bf16 v244,v29,v214,v216\n"
"v_dot2_f32_bf16 v245,v29,v215,v216\n"
"v_cvt_pk_bf16_f32 v27,v240,v241\n"
"ds_write_b128 v220,v[24:27] offset:12288\n"
"v_dot2_f32_bf16 v240,v30,v214,v216\n"
"v_dot2_f32_bf16 v241,v30,v215,v216\n"
"v_cvt_pk_bf16_f32 v28,v242,v243\n"
"v_dot2_f32_bf16 v242,v31,v214,v216\n"
"v_dot2_f32_bf16 v243,v31,v215,v216\n"
"v_cvt_pk_bf16_f32 v29,v244,v245\n"
"v_dot2_f32_bf16 v244,v32,v214,v216\n"
"v_dot2_f32_bf16 v245,v32,v215,v216\n"
"v_cvt_pk_bf16_f32 v30,v240,v241\n"
"v_dot2_f32_bf16 v240,v33,v214,v216\n"
"v_dot2_f32_bf16 v241,v33,v215,v216\n"
"v_cvt_pk_bf16_f32 v31,v242,v243\n"
"ds_write_b128 v220,v[28:31] offset:14336\n"
"v_dot2_f32_bf16 v242,v34,v214,v216\n"
"v_dot2_f32_bf16 v243,v34,v215,v216\n"
"v_cvt_pk_bf16_f32 v32,v244,v245\n"
"v_dot2_f32_bf16 v244,v35,v214,v216\n"
"v_dot2_f32_bf16 v245,v35,v215,v216\n"
"v_cvt_pk_bf16_f32 v33,v240,v241\n"
"v_cvt_pk_bf16_f32 v34,v242,v243\n"
"s_nop 0\n"
"v_cvt_pk_bf16_f32 v35,v244,v245\n"
"ds_write_b128 v220,v[32:35] offset:16384\n"
"s_cmp_eq_u32 s72,1\n"
"s_cbranch_scc1 .Lk_ssm_g1\n"
".Lk_ssm_code:\n"
"s_setprio 0\n"
"global_load_ushort v192,v195,s[4:5]\n"
"global_load_ushort v193,v195,s[6:7]\n"
"global_load_ushort v194,v195,s[82:83]\n"
"v_mov_b32 v210,0\n"
"v_mov_b32 v211,0\n"
"v_mov_b32 v212,0\n"
"v_mov_b32 v213,0\n"
"s_waitcnt vmcnt(11)\n"
"s_add_u32 m0,s73,0\n"
"s_add_u32 s74,s80,0\n"
"buffer_load_dwordx4 v250,s[16:19],s74 offen lds\n"
"v_dot2c_f32_bf16 v210,v72,v208\n"
"v_dot2c_f32_bf16 v212,v72,v72\n"
"v_dot2c_f32_bf16 v211,v73,v208\n"
"v_dot2c_f32_bf16 v213,v73,v73\n"
"v_dot2c_f32_bf16 v210,v74,v208\n"
"v_dot2c_f32_bf16 v212,v74,v74\n"
"v_dot2c_f32_bf16 v211,v75,v208\n"
"v_dot2c_f32_bf16 v213,v75,v75\n"
"s_waitcnt vmcnt(11)\n"
"s_addk_i32 m0,4096\n"
"s_addk_i32 s74,128\n"
"buffer_load_dwordx4 v250,s[16:19],s74 offen lds\n"
"v_dot2c_f32_bf16 v210,v76,v208\n"
"v_dot2c_f32_bf16 v212,v76,v76\n"
"v_dot2c_f32_bf16 v211,v77,v208\n"
"v_dot2c_f32_bf16 v213,v77,v77\n"
"v_dot2c_f32_bf16 v210,v78,v208\n"
"v_dot2c_f32_bf16 v212,v78,v78\n"
"v_dot2c_f32_bf16 v211,v79,v208\n"
"v_dot2c_f32_bf16 v213,v79,v79\n"
"s_waitcnt vmcnt(11)\n"
"s_addk_i32 m0,4096\n"
"s_addk_i32 s74,128\n"
"buffer_load_dwordx4 v250,s[16:19],s74 offen lds\n"
"v_dot2c_f32_bf16 v210,v80,v208\n"
"v_dot2c_f32_bf16 v212,v80,v80\n"
"v_dot2c_f32_bf16 v211,v81,v208\n"
"v_dot2c_f32_bf16 v213,v81,v81\n"
"v_dot2c_f32_bf16 v210,v82,v208\n"
"v_dot2c_f32_bf16 v212,v82,v82\n"
"v_dot2c_f32_bf16 v211,v83,v208\n"
"v_dot2c_f32_bf16 v213,v83,v83\n"
"s_waitcnt vmcnt(11)\n"
"s_addk_i32 m0,4096\n"
"s_addk_i32 s74,128\n"
"buffer_load_dwordx4 v250,s[16:19],s74 offen lds\n"
"v_dot2c_f32_bf16 v210,v84,v208\n"
"v_dot2c_f32_bf16 v212,v84,v84\n"
"v_dot2c_f32_bf16 v211,v85,v208\n"
"v_dot2c_f32_bf16 v213,v85,v85\n"
"v_dot2c_f32_bf16 v210,v86,v208\n"
"v_dot2c_f32_bf16 v212,v86,v86\n"
"v_dot2c_f32_bf16 v211,v87,v208\n"
"v_dot2c_f32_bf16 v213,v87,v87\n"
"s_waitcnt vmcnt(11)\n"
"s_addk_i32 m0,4096\n"
"s_addk_i32 s74,128\n"
"buffer_load_dwordx4 v250,s[16:19],s74 offen lds\n"
"v_dot2c_f32_bf16 v210,v88,v208\n"
"v_dot2c_f32_bf16 v212,v88,v88\n"
"v_dot2c_f32_bf16 v211,v89,v208\n"
"v_dot2c_f32_bf16 v213,v89,v89\n"
"v_dot2c_f32_bf16 v210,v90,v208\n"
"v_dot2c_f32_bf16 v212,v90,v90\n"
"v_dot2c_f32_bf16 v211,v91,v208\n"
"v_dot2c_f32_bf16 v213,v91,v91\n"
"s_waitcnt vmcnt(11)\n"
"s_addk_i32 m0,4096\n"
"s_addk_i32 s74,128\n"
"buffer_load_dwordx4 v250,s[16:19],s74 offen lds\n"
"v_dot2c_f32_bf16 v210,v92,v208\n"
"v_dot2c_f32_bf16 v212,v92,v92\n"
"v_dot2c_f32_bf16 v211,v93,v208\n"
"v_dot2c_f32_bf16 v213,v93,v93\n"
"v_dot2c_f32_bf16 v210,v94,v208\n"
"v_dot2c_f32_bf16 v212,v94,v94\n"
"v_dot2c_f32_bf16 v211,v95,v208\n"
"v_dot2c_f32_bf16 v213,v95,v95\n"
"s_waitcnt vmcnt(11)\n"
"s_addk_i32 m0,4096\n"
"s_addk_i32 s74,128\n"
"buffer_load_dwordx4 v250,s[16:19],s74 offen lds\n"
"v_dot2c_f32_bf16 v210,v96,v208\n"
"v_dot2c_f32_bf16 v212,v96,v96\n"
"v_dot2c_f32_bf16 v211,v97,v208\n"
"v_dot2c_f32_bf16 v213,v97,v97\n"
"v_dot2c_f32_bf16 v210,v98,v208\n"
"v_dot2c_f32_bf16 v212,v98,v98\n"
"v_dot2c_f32_bf16 v211,v99,v208\n"
"v_dot2c_f32_bf16 v213,v99,v99\n"
"s_waitcnt vmcnt(11)\n"
"s_addk_i32 m0,4096\n"
"s_addk_i32 s74,128\n"
"buffer_load_dwordx4 v250,s[16:19],s74 offen lds\n"
"v_dot2c_f32_bf16 v210,v100,v208\n"
"v_dot2c_f32_bf16 v212,v100,v100\n"
"v_dot2c_f32_bf16 v211,v101,v208\n"
"v_dot2c_f32_bf16 v213,v101,v101\n"
"v_dot2c_f32_bf16 v210,v102,v208\n"
"v_dot2c_f32_bf16 v212,v102,v102\n"
"v_dot2c_f32_bf16 v211,v103,v208\n"
"v_dot2c_f32_bf16 v213,v103,v103\n"
"s_waitcnt vmcnt(11)\n"
"s_addk_i32 m0,4096\n"
"s_addk_i32 s74,128\n"
"buffer_load_dwordx4 v250,s[16:19],s74 offen lds\n"
"v_dot2c_f32_bf16 v210,v104,v208\n"
"v_dot2c_f32_bf16 v212,v104,v104\n"
"v_dot2c_f32_bf16 v211,v105,v208\n"
"v_dot2c_f32_bf16 v213,v105,v105\n"
"v_dot2c_f32_bf16 v210,v106,v208\n"
"v_dot2c_f32_bf16 v212,v106,v106\n"
"v_dot2c_f32_bf16 v211,v107,v208\n"
"v_dot2c_f32_bf16 v213,v107,v107\n"
"s_nop 1\n"
"v_add_f32 v210,v210,v211\n"
"v_add_f32 v212,v212,v213\n"
"s_nop 0\n"
"v_add_f32_dpp v210,v210,v210 quad_perm:[1,0,3,2] row_mask:0xf bank_mask:0xf\n"
"v_add_f32_dpp v212,v212,v212 quad_perm:[1,0,3,2] row_mask:0xf bank_mask:0xf\n"
"s_nop 0\n"
"v_add_f32_dpp v210,v210,v210 quad_perm:[2,3,0,1] row_mask:0xf bank_mask:0xf\n"
"v_add_f32_dpp v212,v212,v212 quad_perm:[2,3,0,1] row_mask:0xf bank_mask:0xf\n"
"s_nop 0\n"
"v_add_f32_dpp v210,v210,v210 row_half_mirror row_mask:0xf bank_mask:0xf\n"
"v_add_f32_dpp v212,v212,v212 row_half_mirror row_mask:0xf bank_mask:0xf\n"
"v_mul_f32 v218,0x3ae38e39,v210\n"
"v_mul_f32 v212,0x3ae38e39,v212\n"
"v_fma_f32 v212,-v218,v218,v212\n"
"v_max_f32 v212,0,v212\n"
"v_add_f32 v219,0x3727c5ac,v212\n"
"v_rsq_f32 v219,v219\n"
"s_nop 0\n"
"v_cvt_pk_bf16_f32 v214,v219,0\n"
"v_lshlrev_b32 v215,16,v214\n"
"v_mul_f32_e64 v216,-v218,v215\n"
"s_waitcnt lgkmcnt(0)\n"
"s_barrier\n"
"v_dot2_f32_bf16 v240,v72,v214,v216\n"
"v_dot2_f32_bf16 v241,v72,v215,v216\n"
"v_dot2_f32_bf16 v242,v73,v214,v216\n"
"v_dot2_f32_bf16 v243,v73,v215,v216\n"
"v_dot2_f32_bf16 v244,v74,v214,v216\n"
"v_dot2_f32_bf16 v245,v74,v215,v216\n"
"v_cvt_pk_bf16_f32 v72,v240,v241\n"
"v_dot2_f32_bf16 v240,v75,v214,v216\n"
"v_dot2_f32_bf16 v241,v75,v215,v216\n"
"v_cvt_pk_bf16_f32 v73,v242,v243\n"
"v_dot2_f32_bf16 v242,v76,v214,v216\n"
"v_dot2_f32_bf16 v243,v76,v215,v216\n"
"v_cvt_pk_bf16_f32 v74,v244,v245\n"
"v_dot2_f32_bf16 v244,v77,v214,v216\n"
"v_dot2_f32_bf16 v245,v77,v215,v216\n"
"v_cvt_pk_bf16_f32 v75,v240,v241\n"
"ds_read_b128 v[0:3],v174\n"
"ds_read_b128 v[4:7],v175\n"
"v_dot2_f32_bf16 v240,v78,v214,v216\n"
"v_dot2_f32_bf16 v241,v78,v215,v216\n"
"v_cvt_pk_bf16_f32 v76,v242,v243\n"
"v_dot2_f32_bf16 v242,v79,v214,v216\n"
"v_dot2_f32_bf16 v243,v79,v215,v216\n"
"v_cvt_pk_bf16_f32 v77,v244,v245\n"
"v_dot2_f32_bf16 v244,v80,v214,v216\n"
"v_dot2_f32_bf16 v245,v80,v215,v216\n"
"v_cvt_pk_bf16_f32 v78,v240,v241\n"
"v_dot2_f32_bf16 v240,v81,v214,v216\n"
"v_dot2_f32_bf16 v241,v81,v215,v216\n"
"v_cvt_pk_bf16_f32 v79,v242,v243\n"
"ds_read_b128 v[8:11],v174 offset:2048\n"
"ds_read_b128 v[12:15],v175 offset:2048\n"
"v_dot2_f32_bf16 v242,v82,v214,v216\n"
"v_dot2_f32_bf16 v243,v82,v215,v216\n"
"v_cvt_pk_bf16_f32 v80,v244,v245\n"
"v_dot2_f32_bf16 v244,v83,v214,v216\n"
"v_dot2_f32_bf16 v245,v83,v215,v216\n"
"v_cvt_pk_bf16_f32 v81,v240,v241\n"
"v_dot2_f32_bf16 v240,v84,v214,v216\n"
"v_dot2_f32_bf16 v241,v84,v215,v216\n"
"v_cvt_pk_bf16_f32 v82,v242,v243\n"
"v_dot2_f32_bf16 v242,v85,v214,v216\n"
"v_dot2_f32_bf16 v243,v85,v215,v216\n"
"v_cvt_pk_bf16_f32 v83,v244,v245\n"
"ds_read_b128 v[16:19],v174 offset:4096\n"
"ds_read_b128 v[20:23],v175 offset:4096\n"
"v_dot2_f32_bf16 v244,v86,v214,v216\n"
"v_dot2_f32_bf16 v245,v86,v215,v216\n"
"v_cvt_pk_bf16_f32 v84,v240,v241\n"
"v_dot2_f32_bf16 v240,v87,v214,v216\n"
"v_dot2_f32_bf16 v241,v87,v215,v216\n"
"v_cvt_pk_bf16_f32 v85,v242,v243\n"
"v_dot2_f32_bf16 v242,v88,v214,v216\n"
"v_dot2_f32_bf16 v243,v88,v215,v216\n"
"v_cvt_pk_bf16_f32 v86,v244,v245\n"
"v_dot2_f32_bf16 v244,v89,v214,v216\n"
"v_dot2_f32_bf16 v245,v89,v215,v216\n"
"v_cvt_pk_bf16_f32 v87,v240,v241\n"
"ds_read_b128 v[24:27],v174 offset:6144\n"
"ds_read_b128 v[28:31],v175 offset:6144\n"
"v_dot2_f32_bf16 v240,v90,v214,v216\n"
"v_dot2_f32_bf16 v241,v90,v215,v216\n"
"v_cvt_pk_bf16_f32 v88,v242,v243\n"
"v_dot2_f32_bf16 v242,v91,v214,v216\n"
"v_dot2_f32_bf16 v243,v91,v215,v216\n"
"v_cvt_pk_bf16_f32 v89,v244,v245\n"
"v_dot2_f32_bf16 v244,v92,v214,v216\n"
"v_dot2_f32_bf16 v245,v92,v215,v216\n"
"v_cvt_pk_bf16_f32 v90,v240,v241\n"
"v_dot2_f32_bf16 v240,v93,v214,v216\n"
"v_dot2_f32_bf16 v241,v93,v215,v216\n"
"v_cvt_pk_bf16_f32 v91,v242,v243\n"
"ds_read_b128 v[32:35],v174 offset:8192\n"
"ds_read_b128 v[36:39],v175 offset:8192\n"
"v_dot2_f32_bf16 v242,v94,v214,v216\n"
"v_dot2_f32_bf16 v243,v94,v215,v216\n"
"v_cvt_pk_bf16_f32 v92,v244,v245\n"
"v_dot2_f32_bf16 v244,v95,v214,v216\n"
"v_dot2_f32_bf16 v245,v95,v215,v216\n"
"v_cvt_pk_bf16_f32 v93,v240,v241\n"
"v_dot2_f32_bf16 v240,v96,v214,v216\n"
"v_dot2_f32_bf16 v241,v96,v215,v216\n"
"v_cvt_pk_bf16_f32 v94,v242,v243\n"
"v_dot2_f32_bf16 v242,v97,v214,v216\n"
"v_dot2_f32_bf16 v243,v97,v215,v216\n"
"v_cvt_pk_bf16_f32 v95,v244,v245\n"
"ds_read_b128 v[40:43],v174 offset:10240\n"
"ds_read_b128 v[44:47],v175 offset:10240\n"
"v_dot2_f32_bf16 v244,v98,v214,v216\n"
"v_dot2_f32_bf16 v245,v98,v215,v216\n"
"v_cvt_pk_bf16_f32 v96,v240,v241\n"
"v_dot2_f32_bf16 v240,v99,v214,v216\n"
"v_dot2_f32_bf16 v241,v99,v215,v216\n"
"v_cvt_pk_bf16_f32 v97,v242,v243\n"
"v_dot2_f32_bf16 v242,v100,v214,v216\n"
"v_dot2_f32_bf16 v243,v100,v215,v216\n"
"v_cvt_pk_bf16_f32 v98,v244,v245\n"
"v_dot2_f32_bf16 v244,v101,v214,v216\n"
"v_dot2_f32_bf16 v245,v101,v215,v216\n"
"v_cvt_pk_bf16_f32 v99,v240,v241\n"
"ds_read_b128 v[48:51],v174 offset:12288\n"
"ds_read_b128 v[52:55],v175 offset:12288\n"
"v_dot2_f32_bf16 v240,v102,v214,v216\n"
"v_dot2_f32_bf16 v241,v102,v215,v216\n"
"v_cvt_pk_bf16_f32 v100,v242,v243\n"
"v_dot2_f32_bf16 v242,v103,v214,v216\n"
"v_dot2_f32_bf16 v243,v103,v215,v216\n"
"v_cvt_pk_bf16_f32 v101,v244,v245\n"
"v_dot2_f32_bf16 v244,v104,v214,v216\n"
"v_dot2_f32_bf16 v245,v104,v215,v216\n"
"v_cvt_pk_bf16_f32 v102,v240,v241\n"
"v_dot2_f32_bf16 v240,v105,v214,v216\n"
"v_dot2_f32_bf16 v241,v105,v215,v216\n"
"v_cvt_pk_bf16_f32 v103,v242,v243\n"
"ds_read_b128 v[56:59],v174 offset:14336\n"
"ds_read_b128 v[60:63],v175 offset:14336\n"
"v_dot2_f32_bf16 v242,v106,v214,v216\n"
"v_dot2_f32_bf16 v243,v106,v215,v216\n"
"v_cvt_pk_bf16_f32 v104,v244,v245\n"
"v_dot2_f32_bf16 v244,v107,v214,v216\n"
"v_dot2_f32_bf16 v245,v107,v215,v216\n"
"v_cvt_pk_bf16_f32 v105,v240,v241\n"
"v_cvt_pk_bf16_f32 v106,v242,v243\n"
"s_nop 0\n"
"v_cvt_pk_bf16_f32 v107,v244,v245\n"
"ds_read_b128 v[64:67],v174 offset:16384\n"
"ds_read_b128 v[68:71],v175 offset:16384\n"
"s_waitcnt lgkmcnt(0)\n"
"s_barrier\n"
"s_lshl_b32 s75,s68,1\n"
"s_and_b32 s76,s71,1\n"
"s_add_u32 s75,s75,s76\n"
"s_mul_i32 s76,s75,3456\n"
"s_add_u32 s76,s76,s80\n"
"s_add_u32 s76,s76,36864\n"
"s_mul_i32 s75,s75,640\n"
"s_add_u32 s75,s75,s92\n"
"s_sub_u32 s75,s75,3456\n"
"v_mov_b32 v246,s76\n"
"v_mov_b32 v247,s75\n"
"v_cmp_lt_u32 vcc,26,v254\n"
"s_nop 1\n"
"v_cndmask_b32 v246,v246,v247,vcc\n"
"v_lshl_add_u32 v246,v254,7,v246\n"
"s_mov_b32 m0,163072\n"
"s_bfm_b64 exec,32,0\n"
"buffer_load_dword v246,s[16:19],0 offen lds\n"
"s_mov_b64 exec,-1\n"
"s_lshl_b32 s75,s68,2\n"
"s_add_u32 s75,s75,s71\n"
"v_lshlrev_b32 v246,7,v254\n"
"s_lshl_b32 s75,s75,2\n"
"s_add_u32 s75,s75,s69\n"
"s_xor_b32 s75,s75,0\n"
"s_mul_i32 s75,s75,2688\n"
"s_mov_b32 m0,163072\n"
"s_bfm_b64 exec,21,0\n"
"buffer_load_dword v246,s[28:31],s75 offen lds\n"
"s_mov_b64 exec,-1\n"
"ds_write_b128 v220,v[72:75]\n"
"ds_write_b128 v220,v[76:79] offset:2048\n"
"ds_write_b128 v220,v[80:83] offset:4096\n"
"ds_write_b128 v220,v[84:87] offset:6144\n"
"ds_write_b128 v220,v[88:91] offset:8192\n"
"ds_write_b128 v220,v[92:95] offset:10240\n"
"ds_write_b128 v220,v[96:99] offset:12288\n"
"ds_write_b128 v220,v[100:103] offset:14336\n"
"ds_write_b128 v220,v[104:107] offset:16384\n"
"s_waitcnt lgkmcnt(0)\n"
"s_waitcnt vmcnt(2)\n"
"s_barrier\n"
"s_setprio 3\n"
"ds_read_b128 v[72:75],v174\n"
"ds_read_b128 v[76:79],v175\n"
"ds_read_b128 v[80:83],v174 offset:2048\n"
"ds_read_b128 v[84:87],v175 offset:2048\n"
"ds_read_b128 v[88:91],v174 offset:4096\n"
"ds_read_b128 v[92:95],v175 offset:4096\n"
"ds_read_b128 v[96:99],v174 offset:6144\n"
"ds_read_b128 v[100:103],v175 offset:6144\n"
"ds_read_b128 v[104:107],v174 offset:8192\n"
"ds_read_b128 v[108:111],v175 offset:8192\n"
"ds_read_b128 v[112:115],v174 offset:10240\n"
"ds_read_b128 v[116:119],v175 offset:10240\n"
"ds_read_b128 v[120:123],v174 offset:12288\n"
"ds_read_b128 v[124:127],v175 offset:12288\n"
"ds_read_b128 v[128:131],v174 offset:14336\n"
"ds_read_b128 v[132:135],v175 offset:14336\n"
"ds_read_b128 v[136:139],v174 offset:16384\n"
"ds_read_b128 v[140:143],v175 offset:16384\n"
"ds_read_b128 v[208:211],v248\n"
"ds_read_b128 v[212:215],v249\n"
"ds_read_b128 v[216:219],v248 offset:4096\n"
"ds_read_b128 v[220:223],v249 offset:4096\n"
"ds_read_b128 v[224:227],v248 offset:8192\n"
"ds_read_b128 v[228:231],v249 offset:8192\n"
"s_waitcnt lgkmcnt(6)\n"
"s_setprio 1\n"
"s_waitcnt lgkmcnt(4)\n"
"v_mfma_f32_16x16x32_bf16 v[192:195],v[0:3],v[208:211],0\n"
"ds_read_b128 v[232:235],v248 offset:12288\n"
"v_mfma_f32_16x16x32_bf16 v[192:195],v[4:7],v[212:215],v[192:195]\n"
"v_mfma_f32_16x16x32_bf16 v[196:199],v[72:75],v[208:211],0\n"
"ds_read_b128 v[236:239],v249 offset:12288\n"
"v_mfma_f32_16x16x32_bf16 v[196:199],v[76:79],v[212:215],v[196:199]\n"
"s_waitcnt lgkmcnt(4)\n"
"v_mfma_f32_16x16x32_bf16 v[196:199],v[80:83],v[216:219],v[196:199]\n"
"ds_read_b128 v[208:211],v248 offset:16384\n"
"v_mfma_f32_16x16x32_bf16 v[196:199],v[84:87],v[220:223],v[196:199]\n"
"v_mfma_f32_16x16x32_bf16 v[192:195],v[8:11],v[216:219],v[192:195]\n"
"ds_read_b128 v[212:215],v249 offset:16384\n"
"v_mfma_f32_16x16x32_bf16 v[192:195],v[12:15],v[220:223],v[192:195]\n"
"s_waitcnt lgkmcnt(4)\n"
"v_mfma_f32_16x16x32_bf16 v[192:195],v[16:19],v[224:227],v[192:195]\n"
"ds_read_b128 v[216:219],v248 offset:20480\n"
"v_mfma_f32_16x16x32_bf16 v[192:195],v[20:23],v[228:231],v[192:195]\n"
"v_mfma_f32_16x16x32_bf16 v[196:199],v[88:91],v[224:227],v[196:199]\n"
"ds_read_b128 v[220:223],v249 offset:20480\n"
"v_mfma_f32_16x16x32_bf16 v[196:199],v[92:95],v[228:231],v[196:199]\n"
"s_waitcnt lgkmcnt(4)\n"
"v_mfma_f32_16x16x32_bf16 v[196:199],v[96:99],v[232:235],v[196:199]\n"
"ds_read_b128 v[224:227],v248 offset:24576\n"
"v_mfma_f32_16x16x32_bf16 v[196:199],v[100:103],v[236:239],v[196:199]\n"
"v_mfma_f32_16x16x32_bf16 v[192:195],v[24:27],v[232:235],v[192:195]\n"
"ds_read_b128 v[228:231],v249 offset:24576\n"
"v_mfma_f32_16x16x32_bf16 v[192:195],v[28:31],v[236:239],v[192:195]\n"
"s_waitcnt lgkmcnt(4)\n"
"v_mfma_f32_16x16x32_bf16 v[192:195],v[32:35],v[208:211],v[192:195]\n"
"ds_read_b128 v[232:235],v248 offset:28672\n"
"v_mfma_f32_16x16x32_bf16 v[192:195],v[36:39],v[212:215],v[192:195]\n"
"v_mfma_f32_16x16x32_bf16 v[196:199],v[104:107],v[208:211],v[196:199]\n"
"ds_read_b128 v[236:239],v249 offset:28672\n"
"v_mfma_f32_16x16x32_bf16 v[196:199],v[108:111],v[212:215],v[196:199]\n"
"s_waitcnt lgkmcnt(4)\n"
"v_mfma_f32_16x16x32_bf16 v[196:199],v[112:115],v[216:219],v[196:199]\n"
"ds_read_b128 v[208:211],v248 offset:32768\n"
"v_mfma_f32_16x16x32_bf16 v[196:199],v[116:119],v[220:223],v[196:199]\n"
"s_barrier\n"
"v_mfma_f32_16x16x32_bf16 v[192:195],v[40:43],v[216:219],v[192:195]\n"
"ds_read_b128 v[212:215],v249 offset:32768\n"
"v_mfma_f32_16x16x32_bf16 v[192:195],v[44:47],v[220:223],v[192:195]\n"
"s_waitcnt lgkmcnt(4)\n"
"v_mfma_f32_16x16x32_bf16 v[192:195],v[48:51],v[224:227],v[192:195]\n"
"ds_read_b128 v[216:219],v248 offset:2048\n"
"v_mfma_f32_16x16x32_bf16 v[192:195],v[52:55],v[228:231],v[192:195]\n"
"v_mfma_f32_16x16x32_bf16 v[196:199],v[120:123],v[224:227],v[196:199]\n"
"ds_read_b128 v[220:223],v249 offset:2048\n"
"v_mfma_f32_16x16x32_bf16 v[196:199],v[124:127],v[228:231],v[196:199]\n"
"s_waitcnt lgkmcnt(4)\n"
"v_mfma_f32_16x16x32_bf16 v[196:199],v[128:131],v[232:235],v[196:199]\n"
"ds_read_b128 v[224:227],v248 offset:6144\n"
"v_mfma_f32_16x16x32_bf16 v[196:199],v[132:135],v[236:239],v[196:199]\n"
"v_mfma_f32_16x16x32_bf16 v[192:195],v[56:59],v[232:235],v[192:195]\n"
"ds_read_b128 v[228:231],v249 offset:6144\n"
"v_mfma_f32_16x16x32_bf16 v[192:195],v[60:63],v[236:239],v[192:195]\n"
"s_waitcnt lgkmcnt(4)\n"
"v_mfma_f32_16x16x32_bf16 v[192:195],v[64:67],v[208:211],v[192:195]\n"
"ds_read_b128 v[232:235],v248 offset:10240\n"
"v_mfma_f32_16x16x32_bf16 v[192:195],v[68:71],v[212:215],v[192:195]\n"
"v_mfma_f32_16x16x32_bf16 v[196:199],v[136:139],v[208:211],v[196:199]\n"
"ds_read_b128 v[236:239],v249 offset:10240\n"
"v_mfma_f32_16x16x32_bf16 v[196:199],v[140:143],v[212:215],v[196:199]\n"
"s_waitcnt lgkmcnt(4)\n"
"v_mfma_f32_16x16x32_bf16 v[200:203],v[0:3],v[216:219],0\n"
"ds_read_b128 v[208:211],v248 offset:14336\n"
"v_mfma_f32_16x16x32_bf16 v[200:203],v[4:7],v[220:223],v[200:203]\n"
"v_mfma_f32_16x16x32_bf16 v[204:207],v[72:75],v[216:219],0\n"
"ds_read_b128 v[212:215],v249 offset:14336\n"
"v_mfma_f32_16x16x32_bf16 v[204:207],v[76:79],v[220:223],v[204:207]\n"
"s_waitcnt lgkmcnt(4)\n"
"v_mfma_f32_16x16x32_bf16 v[204:207],v[80:83],v[224:227],v[204:207]\n"
"ds_read_b128 v[216:219],v248 offset:18432\n"
"v_mfma_f32_16x16x32_bf16 v[204:207],v[84:87],v[228:231],v[204:207]\n"
"v_mfma_f32_16x16x32_bf16 v[200:203],v[8:11],v[224:227],v[200:203]\n"
"ds_read_b128 v[220:223],v249 offset:18432\n"
"v_mfma_f32_16x16x32_bf16 v[200:203],v[12:15],v[228:231],v[200:203]\n"
"s_waitcnt lgkmcnt(4)\n"
"v_mfma_f32_16x16x32_bf16 v[200:203],v[16:19],v[232:235],v[200:203]\n"
"ds_read_b128 v[224:227],v248 offset:22528\n"
"v_mfma_f32_16x16x32_bf16 v[200:203],v[20:23],v[236:239],v[200:203]\n"
"v_mfma_f32_16x16x32_bf16 v[204:207],v[88:91],v[232:235],v[204:207]\n"
"ds_read_b128 v[228:231],v249 offset:22528\n"
"v_mfma_f32_16x16x32_bf16 v[204:207],v[92:95],v[236:239],v[204:207]\n"
"s_waitcnt lgkmcnt(4)\n"
"v_mfma_f32_16x16x32_bf16 v[204:207],v[96:99],v[208:211],v[204:207]\n"
"ds_read_b128 v[232:235],v248 offset:26624\n"
"v_mfma_f32_16x16x32_bf16 v[204:207],v[100:103],v[212:215],v[204:207]\n"
"v_mfma_f32_16x16x32_bf16 v[200:203],v[24:27],v[208:211],v[200:203]\n"
"ds_read_b128 v[236:239],v249 offset:26624\n"
"v_mfma_f32_16x16x32_bf16 v[200:203],v[28:31],v[212:215],v[200:203]\n"
"s_waitcnt lgkmcnt(4)\n"
"v_mfma_f32_16x16x32_bf16 v[200:203],v[32:35],v[216:219],v[200:203]\n"
"ds_read_b128 v[208:211],v248 offset:30720\n"
"v_mfma_f32_16x16x32_bf16 v[200:203],v[36:39],v[220:223],v[200:203]\n"
"v_mfma_f32_16x16x32_bf16 v[204:207],v[104:107],v[216:219],v[204:207]\n"
"ds_read_b128 v[212:215],v249 offset:30720\n"
"v_mfma_f32_16x16x32_bf16 v[204:207],v[108:111],v[220:223],v[204:207]\n"
"s_waitcnt lgkmcnt(4)\n"
"v_mfma_f32_16x16x32_bf16 v[204:207],v[112:115],v[224:227],v[204:207]\n"
"ds_read_b128 v[216:219],v248 offset:34816\n"
"v_mfma_f32_16x16x32_bf16 v[204:207],v[116:119],v[228:231],v[204:207]\n"
"v_mfma_f32_16x16x32_bf16 v[200:203],v[40:43],v[224:227],v[200:203]\n"
"ds_read_b128 v[220:223],v249 offset:34816\n"
"v_mfma_f32_16x16x32_bf16 v[200:203],v[44:47],v[228:231],v[200:203]\n"
"s_waitcnt lgkmcnt(4)\n"
"v_mfma_f32_16x16x32_bf16 v[200:203],v[48:51],v[232:235],v[200:203]\n"
"v_mfma_f32_16x16x32_bf16 v[200:203],v[52:55],v[236:239],v[200:203]\n"
"v_mfma_f32_16x16x32_bf16 v[204:207],v[120:123],v[232:235],v[204:207]\n"
"v_mfma_f32_16x16x32_bf16 v[204:207],v[124:127],v[236:239],v[204:207]\n"
"s_setprio 2\n"
"s_waitcnt lgkmcnt(2)\n"
"v_mfma_f32_16x16x32_bf16 v[204:207],v[128:131],v[208:211],v[204:207]\n"
"s_waitcnt lgkmcnt(0)\n"
"v_mfma_f32_16x16x32_bf16 v[204:207],v[132:135],v[212:215],v[204:207]\n"
"s_barrier\n"
"v_mfma_f32_16x16x32_bf16 v[200:203],v[56:59],v[208:211],v[200:203]\n"
".Lk_pads2:\n"
"s_cbranch_execz .Lk_pads6\n"
"v_mfma_f32_16x16x32_bf16 v[200:203],v[60:63],v[212:215],v[200:203]\n"
"v_mfma_f32_16x16x32_bf16 v[200:203],v[64:67],v[216:219],v[200:203]\n"
"v_mfma_f32_16x16x32_bf16 v[200:203],v[68:71],v[220:223],v[200:203]\n"
"v_mfma_f32_16x16x32_bf16 v[204:207],v[136:139],v[216:219],v[204:207]\n"
"v_mfma_f32_16x16x32_bf16 v[204:207],v[140:143],v[220:223],v[204:207]\n"
"s_setprio 0\n"
"v_add_u32 v248,73728,v248\n"
"v_add_u32 v249,73728,v249\n"
"s_setprio 3\n"
"ds_read_b64 v[210:211],v255\n"
"ds_read_b64 v[208:209],v255 offset:51200\n"
"s_setprio 0\n"
"s_waitcnt lgkmcnt(0)\n"
"v_mul_f32 v216,v210,v192\n"
"v_mul_f32 v217,v210,v200\n"
"v_fma_f32 v216,-v211,v200,v216\n"
"v_fmac_f32 v217,v211,v192\n"
"s_add_u32 m0,s73,0\n"
"s_add_u32 s74,s80,55296\n"
"buffer_load_dwordx4 v250,s[16:19],s74 offen lds\n"
"v_mul_f32 v218,v210,v193\n"
"v_mul_f32 v219,v210,v201\n"
"v_fma_f32 v218,-v211,v201,v218\n"
"v_fmac_f32 v219,v211,v193\n"
"v_mul_f32 v220,v210,v194\n"
"v_mul_f32 v221,v210,v202\n"
"v_fma_f32 v220,-v211,v202,v220\n"
"v_fmac_f32 v221,v211,v194\n"
"s_addk_i32 m0,4096\n"
"s_addk_i32 s74,128\n"
"buffer_load_dwordx4 v250,s[16:19],s74 offen lds\n"
"v_mul_f32 v222,v210,v195\n"
"v_mul_f32 v223,v210,v203\n"
"v_fma_f32 v222,-v211,v203,v222\n"
"v_fmac_f32 v223,v211,v195\n"
"v_mul_f32 v224,v210,v196\n"
"v_mul_f32 v225,v210,v204\n"
"v_fma_f32 v224,-v211,v204,v224\n"
"v_fmac_f32 v225,v211,v196\n"
"s_addk_i32 m0,4096\n"
"s_addk_i32 s74,128\n"
"buffer_load_dwordx4 v250,s[16:19],s74 offen lds\n"
"v_mul_f32 v226,v210,v197\n"
"v_mul_f32 v227,v210,v205\n"
"v_fma_f32 v226,-v211,v205,v226\n"
"v_fmac_f32 v227,v211,v197\n"
"v_mul_f32 v228,v210,v198\n"
"v_mul_f32 v229,v210,v206\n"
"v_fma_f32 v228,-v211,v206,v228\n"
"v_fmac_f32 v229,v211,v198\n"
"s_addk_i32 m0,4096\n"
"s_addk_i32 s74,128\n"
"buffer_load_dwordx4 v250,s[16:19],s74 offen lds\n"
"v_mul_f32 v230,v210,v199\n"
"v_mul_f32 v231,v210,v207\n"
"v_fma_f32 v230,-v211,v207,v230\n"
"v_fmac_f32 v231,v211,v199\n"
"v_cvt_pk_bf16_f32 v144,v216,v217\n"
"v_cvt_pk_bf16_f32 v145,v218,v219\n"
"v_cvt_pk_bf16_f32 v146,v220,v221\n"
"v_cvt_pk_bf16_f32 v147,v222,v223\n"
"v_cvt_pk_bf16_f32 v148,v224,v225\n"
"s_addk_i32 m0,4096\n"
"s_addk_i32 s74,128\n"
"buffer_load_dwordx4 v250,s[16:19],s74 offen lds\n"
"v_cvt_pk_bf16_f32 v149,v226,v227\n"
"v_cvt_pk_bf16_f32 v150,v228,v229\n"
"v_cvt_pk_bf16_f32 v151,v230,v231\n"
"v_fma_f32 v218,v208,v216,v218\n"
"v_fma_f32 v219,v208,v217,v219\n"
"v_fma_f32 v218,-v209,v217,v218\n"
"v_fmac_f32 v219,v209,v216\n"
"v_fma_f32 v220,v208,v218,v220\n"
"s_addk_i32 m0,4096\n"
"s_addk_i32 s74,128\n"
"buffer_load_dwordx4 v250,s[16:19],s74 offen lds\n"
"v_fma_f32 v221,v208,v219,v221\n"
"v_fma_f32 v220,-v209,v219,v220\n"
"v_fmac_f32 v221,v209,v218\n"
"v_fma_f32 v222,v208,v220,v222\n"
"v_fma_f32 v223,v208,v221,v223\n"
"v_fma_f32 v222,-v209,v221,v222\n"
"v_fmac_f32 v223,v209,v220\n"
"v_fma_f32 v224,v208,v222,v224\n"
"s_addk_i32 m0,4096\n"
"s_addk_i32 s74,128\n"
"buffer_load_dwordx4 v250,s[16:19],s74 offen lds\n"
"v_fma_f32 v225,v208,v223,v225\n"
"v_fma_f32 v224,-v209,v223,v224\n"
"v_fmac_f32 v225,v209,v222\n"
"v_fma_f32 v226,v208,v224,v226\n"
"v_fma_f32 v227,v208,v225,v227\n"
"v_fma_f32 v226,-v209,v225,v226\n"
"v_fmac_f32 v227,v209,v224\n"
"v_fma_f32 v228,v208,v226,v228\n"
"s_addk_i32 m0,4096\n"
"s_addk_i32 s74,128\n"
"buffer_load_dwordx4 v250,s[16:19],s74 offen lds\n"
"v_fma_f32 v229,v208,v227,v229\n"
"v_fma_f32 v228,-v209,v227,v228\n"
"v_fmac_f32 v229,v209,v226\n"
"v_fma_f32 v230,v208,v228,v230\n"
"v_fma_f32 v231,v208,v229,v231\n"
"v_fma_f32 v230,-v209,v229,v230\n"
"v_fmac_f32 v231,v209,v228\n"
"s_setprio 3\n"
"ds_write_b64 v253,v[230:231]\n"
"s_setprio 0\n"
"s_addk_i32 m0,4096\n"
"s_addk_i32 s74,128\n"
"buffer_load_dwordx4 v250,s[16:19],s74 offen lds\n"
"s_nop 0\n"
"s_lshl_b32 s75,s68,2\n"
"s_add_u32 s75,s75,s71\n"
"v_lshlrev_b32 v246,7,v254\n"
"s_lshl_b32 s75,s75,2\n"
"s_add_u32 s75,s75,s69\n"
"s_xor_b32 s75,s75,0\n"
"s_mul_i32 s75,s75,2688\n"
"s_mov_b32 m0,163072\n"
"s_bfm_b64 exec,21,0\n"
"buffer_load_dword v246,s[24:27],s75 offen lds\n"
"s_mov_b64 exec,-1\n"
"s_waitcnt lgkmcnt(0)\n"
"s_barrier\n"
"s_setprio 3\n"
"ds_read_b128 v[208:211],v248\n"
"ds_read_b128 v[212:215],v249\n"
"ds_read_b128 v[216:219],v248 offset:4096\n"
"ds_read_b128 v[220:223],v249 offset:4096\n"
"ds_read_b128 v[224:227],v248 offset:8192\n"
"ds_read_b128 v[228:231],v249 offset:8192\n"
"s_setprio 1\n"
"s_waitcnt lgkmcnt(4)\n"
"v_mfma_f32_16x16x32_bf16 v[192:195],v[0:3],v[208:211],0\n"
"ds_read_b128 v[232:235],v248 offset:12288\n"
"v_mfma_f32_16x16x32_bf16 v[192:195],v[4:7],v[212:215],v[192:195]\n"
"v_mfma_f32_16x16x32_bf16 v[196:199],v[72:75],v[208:211],0\n"
"ds_read_b128 v[236:239],v249 offset:12288\n"
"v_mfma_f32_16x16x32_bf16 v[196:199],v[76:79],v[212:215],v[196:199]\n"
"s_waitcnt lgkmcnt(4)\n"
"v_mfma_f32_16x16x32_bf16 v[196:199],v[80:83],v[216:219],v[196:199]\n"
"ds_read_b128 v[208:211],v248 offset:16384\n"
"v_mfma_f32_16x16x32_bf16 v[196:199],v[84:87],v[220:223],v[196:199]\n"
"v_mfma_f32_16x16x32_bf16 v[192:195],v[8:11],v[216:219],v[192:195]\n"
"ds_read_b128 v[212:215],v249 offset:16384\n"
"v_mfma_f32_16x16x32_bf16 v[192:195],v[12:15],v[220:223],v[192:195]\n"
"s_waitcnt lgkmcnt(4)\n"
"v_mfma_f32_16x16x32_bf16 v[192:195],v[16:19],v[224:227],v[192:195]\n"
"ds_read_b128 v[216:219],v248 offset:20480\n"
"v_mfma_f32_16x16x32_bf16 v[192:195],v[20:23],v[228:231],v[192:195]\n"
"v_mfma_f32_16x16x32_bf16 v[196:199],v[88:91],v[224:227],v[196:199]\n"
"ds_read_b128 v[220:223],v249 offset:20480\n"
"v_mfma_f32_16x16x32_bf16 v[196:199],v[92:95],v[228:231],v[196:199]\n"
"s_waitcnt lgkmcnt(4)\n"
"v_mfma_f32_16x16x32_bf16 v[196:199],v[96:99],v[232:235],v[196:199]\n"
"ds_read_b128 v[224:227],v248 offset:24576\n"
"v_mfma_f32_16x16x32_bf16 v[196:199],v[100:103],v[236:239],v[196:199]\n"
"v_mfma_f32_16x16x32_bf16 v[192:195],v[24:27],v[232:235],v[192:195]\n"
"ds_read_b128 v[228:231],v249 offset:24576\n"
"v_mfma_f32_16x16x32_bf16 v[192:195],v[28:31],v[236:239],v[192:195]\n"
"s_waitcnt lgkmcnt(4)\n"
"v_mfma_f32_16x16x32_bf16 v[192:195],v[32:35],v[208:211],v[192:195]\n"
"ds_read_b128 v[232:235],v248 offset:28672\n"
"v_mfma_f32_16x16x32_bf16 v[192:195],v[36:39],v[212:215],v[192:195]\n"
"v_mfma_f32_16x16x32_bf16 v[196:199],v[104:107],v[208:211],v[196:199]\n"
"ds_read_b128 v[236:239],v249 offset:28672\n"
"v_mfma_f32_16x16x32_bf16 v[196:199],v[108:111],v[212:215],v[196:199]\n"
"s_waitcnt lgkmcnt(4)\n"
"v_mfma_f32_16x16x32_bf16 v[196:199],v[112:115],v[216:219],v[196:199]\n"
"ds_read_b128 v[208:211],v248 offset:32768\n"
"v_mfma_f32_16x16x32_bf16 v[196:199],v[116:119],v[220:223],v[196:199]\n"
"v_mfma_f32_16x16x32_bf16 v[192:195],v[40:43],v[216:219],v[192:195]\n"
"ds_read_b128 v[212:215],v249 offset:32768\n"
"v_mfma_f32_16x16x32_bf16 v[192:195],v[44:47],v[220:223],v[192:195]\n"
"s_waitcnt lgkmcnt(4)\n"
"v_mfma_f32_16x16x32_bf16 v[192:195],v[48:51],v[224:227],v[192:195]\n"
"ds_read_b128 v[216:219],v248 offset:2048\n"
"v_mfma_f32_16x16x32_bf16 v[192:195],v[52:55],v[228:231],v[192:195]\n"
"v_mfma_f32_16x16x32_bf16 v[196:199],v[120:123],v[224:227],v[196:199]\n"
"ds_read_b128 v[220:223],v249 offset:2048\n"
"v_mfma_f32_16x16x32_bf16 v[196:199],v[124:127],v[228:231],v[196:199]\n"
"s_waitcnt lgkmcnt(4)\n"
"v_mfma_f32_16x16x32_bf16 v[196:199],v[128:131],v[232:235],v[196:199]\n"
"ds_read_b128 v[224:227],v248 offset:6144\n"
"v_mfma_f32_16x16x32_bf16 v[196:199],v[132:135],v[236:239],v[196:199]\n"
"v_mfma_f32_16x16x32_bf16 v[192:195],v[56:59],v[232:235],v[192:195]\n"
"ds_read_b128 v[228:231],v249 offset:6144\n"
"v_mfma_f32_16x16x32_bf16 v[192:195],v[60:63],v[236:239],v[192:195]\n"
"s_waitcnt lgkmcnt(4)\n"
"v_mfma_f32_16x16x32_bf16 v[192:195],v[64:67],v[208:211],v[192:195]\n"
"ds_read_b128 v[232:235],v248 offset:10240\n"
"v_mfma_f32_16x16x32_bf16 v[192:195],v[68:71],v[212:215],v[192:195]\n"
"v_mfma_f32_16x16x32_bf16 v[196:199],v[136:139],v[208:211],v[196:199]\n"
"ds_read_b128 v[236:239],v249 offset:10240\n"
"v_mfma_f32_16x16x32_bf16 v[196:199],v[140:143],v[212:215],v[196:199]\n"
"s_waitcnt lgkmcnt(4)\n"
"v_mfma_f32_16x16x32_bf16 v[200:203],v[0:3],v[216:219],0\n"
"ds_read_b128 v[208:211],v248 offset:14336\n"
"v_mfma_f32_16x16x32_bf16 v[200:203],v[4:7],v[220:223],v[200:203]\n"
"v_mfma_f32_16x16x32_bf16 v[204:207],v[72:75],v[216:219],0\n"
"ds_read_b128 v[212:215],v249 offset:14336\n"
"v_mfma_f32_16x16x32_bf16 v[204:207],v[76:79],v[220:223],v[204:207]\n"
"s_waitcnt lgkmcnt(4)\n"
"v_mfma_f32_16x16x32_bf16 v[204:207],v[80:83],v[224:227],v[204:207]\n"
"ds_read_b128 v[216:219],v248 offset:18432\n"
"v_mfma_f32_16x16x32_bf16 v[204:207],v[84:87],v[228:231],v[204:207]\n"
"v_mfma_f32_16x16x32_bf16 v[200:203],v[8:11],v[224:227],v[200:203]\n"
"ds_read_b128 v[220:223],v249 offset:18432\n"
"v_mfma_f32_16x16x32_bf16 v[200:203],v[12:15],v[228:231],v[200:203]\n"
"s_waitcnt lgkmcnt(4)\n"
"v_mfma_f32_16x16x32_bf16 v[200:203],v[16:19],v[232:235],v[200:203]\n"
"ds_read_b128 v[224:227],v248 offset:22528\n"
"v_mfma_f32_16x16x32_bf16 v[200:203],v[20:23],v[236:239],v[200:203]\n"
"v_mfma_f32_16x16x32_bf16 v[204:207],v[88:91],v[232:235],v[204:207]\n"
"ds_read_b128 v[228:231],v249 offset:22528\n"
"v_mfma_f32_16x16x32_bf16 v[204:207],v[92:95],v[236:239],v[204:207]\n"
"s_waitcnt lgkmcnt(4)\n"
"v_mfma_f32_16x16x32_bf16 v[204:207],v[96:99],v[208:211],v[204:207]\n"
"ds_read_b128 v[232:235],v248 offset:26624\n"
"v_mfma_f32_16x16x32_bf16 v[204:207],v[100:103],v[212:215],v[204:207]\n"
"v_mfma_f32_16x16x32_bf16 v[200:203],v[24:27],v[208:211],v[200:203]\n"
"ds_read_b128 v[236:239],v249 offset:26624\n"
"v_mfma_f32_16x16x32_bf16 v[200:203],v[28:31],v[212:215],v[200:203]\n"
"s_waitcnt lgkmcnt(4)\n"
"v_mfma_f32_16x16x32_bf16 v[200:203],v[32:35],v[216:219],v[200:203]\n"
"ds_read_b128 v[208:211],v248 offset:30720\n"
"v_mfma_f32_16x16x32_bf16 v[200:203],v[36:39],v[220:223],v[200:203]\n"
"v_mfma_f32_16x16x32_bf16 v[204:207],v[104:107],v[216:219],v[204:207]\n"
"ds_read_b128 v[212:215],v249 offset:30720\n"
"v_mfma_f32_16x16x32_bf16 v[204:207],v[108:111],v[220:223],v[204:207]\n"
"s_waitcnt lgkmcnt(4)\n"
"v_mfma_f32_16x16x32_bf16 v[204:207],v[112:115],v[224:227],v[204:207]\n"
"ds_read_b128 v[216:219],v248 offset:34816\n"
"v_mfma_f32_16x16x32_bf16 v[204:207],v[116:119],v[228:231],v[204:207]\n"
"v_mfma_f32_16x16x32_bf16 v[200:203],v[40:43],v[224:227],v[200:203]\n"
"ds_read_b128 v[220:223],v249 offset:34816\n"
"v_mfma_f32_16x16x32_bf16 v[200:203],v[44:47],v[228:231],v[200:203]\n"
"s_waitcnt lgkmcnt(4)\n"
"v_mfma_f32_16x16x32_bf16 v[200:203],v[48:51],v[232:235],v[200:203]\n"
"v_mfma_f32_16x16x32_bf16 v[200:203],v[52:55],v[236:239],v[200:203]\n"
"v_mfma_f32_16x16x32_bf16 v[204:207],v[120:123],v[232:235],v[204:207]\n"
"v_mfma_f32_16x16x32_bf16 v[204:207],v[124:127],v[236:239],v[204:207]\n"
"s_setprio 2\n"
"s_waitcnt lgkmcnt(2)\n"
"v_mfma_f32_16x16x32_bf16 v[204:207],v[128:131],v[208:211],v[204:207]\n"
"s_waitcnt lgkmcnt(0)\n"
"v_mfma_f32_16x16x32_bf16 v[204:207],v[132:135],v[212:215],v[204:207]\n"
"s_waitcnt vmcnt(1)\n"
"s_barrier\n"
"v_mfma_f32_16x16x32_bf16 v[200:203],v[56:59],v[208:211],v[200:203]\n"
"v_mfma_f32_16x16x32_bf16 v[200:203],v[60:63],v[212:215],v[200:203]\n"
"v_mfma_f32_16x16x32_bf16 v[200:203],v[64:67],v[216:219],v[200:203]\n"
"v_mfma_f32_16x16x32_bf16 v[200:203],v[68:71],v[220:223],v[200:203]\n"
"v_mfma_f32_16x16x32_bf16 v[204:207],v[136:139],v[216:219],v[204:207]\n"
"v_mfma_f32_16x16x32_bf16 v[204:207],v[140:143],v[220:223],v[204:207]\n"
"s_setprio 0\n"
"v_add_u32 v248,4294930432,v248\n"
"v_add_u32 v249,4294930432,v249\n"
"s_setprio 3\n"
"ds_read_b64 v[210:211],v255 offset:256\n"
"ds_read_b64 v[208:209],v255 offset:51456\n"
"s_setprio 0\n"
"s_waitcnt lgkmcnt(0)\n"
"v_mul_f32 v216,v210,v192\n"
"v_mul_f32 v217,v210,v200\n"
"v_fma_f32 v216,-v211,v200,v216\n"
"v_fmac_f32 v217,v211,v192\n"
"s_add_u32 m0,s73,73728\n"
"s_add_u32 s74,s80,92160\n"
"buffer_load_dwordx4 v250,s[16:19],s74 offen lds\n"
"v_mul_f32 v218,v210,v193\n"
"v_mul_f32 v219,v210,v201\n"
"v_fma_f32 v218,-v211,v201,v218\n"
"v_fmac_f32 v219,v211,v193\n"
"v_mul_f32 v220,v210,v194\n"
"v_mul_f32 v221,v210,v202\n"
"v_fma_f32 v220,-v211,v202,v220\n"
"v_fmac_f32 v221,v211,v194\n"
"s_addk_i32 m0,4096\n"
"s_addk_i32 s74,128\n"
"buffer_load_dwordx4 v250,s[16:19],s74 offen lds\n"
"v_mul_f32 v222,v210,v195\n"
"v_mul_f32 v223,v210,v203\n"
"v_fma_f32 v222,-v211,v203,v222\n"
"v_fmac_f32 v223,v211,v195\n"
"v_mul_f32 v224,v210,v196\n"
"v_mul_f32 v225,v210,v204\n"
"v_fma_f32 v224,-v211,v204,v224\n"
"v_fmac_f32 v225,v211,v196\n"
"s_addk_i32 m0,4096\n"
"s_addk_i32 s74,128\n"
"buffer_load_dwordx4 v250,s[16:19],s74 offen lds\n"
"v_mul_f32 v226,v210,v197\n"
"v_mul_f32 v227,v210,v205\n"
"v_fma_f32 v226,-v211,v205,v226\n"
"v_fmac_f32 v227,v211,v197\n"
"v_mul_f32 v228,v210,v198\n"
"v_mul_f32 v229,v210,v206\n"
"v_fma_f32 v228,-v211,v206,v228\n"
"v_fmac_f32 v229,v211,v198\n"
"s_addk_i32 m0,4096\n"
"s_addk_i32 s74,128\n"
"buffer_load_dwordx4 v250,s[16:19],s74 offen lds\n"
"v_mul_f32 v230,v210,v199\n"
"v_mul_f32 v231,v210,v207\n"
"v_fma_f32 v230,-v211,v207,v230\n"
"v_fmac_f32 v231,v211,v199\n"
"v_cvt_pk_bf16_f32 v152,v216,v217\n"
"v_cvt_pk_bf16_f32 v153,v218,v219\n"
"v_cvt_pk_bf16_f32 v154,v220,v221\n"
"v_cvt_pk_bf16_f32 v155,v222,v223\n"
"v_cvt_pk_bf16_f32 v156,v224,v225\n"
"s_addk_i32 m0,4096\n"
"s_addk_i32 s74,128\n"
"buffer_load_dwordx4 v250,s[16:19],s74 offen lds\n"
"v_cvt_pk_bf16_f32 v157,v226,v227\n"
"v_cvt_pk_bf16_f32 v158,v228,v229\n"
"v_cvt_pk_bf16_f32 v159,v230,v231\n"
"v_fma_f32 v218,v208,v216,v218\n"
"v_fma_f32 v219,v208,v217,v219\n"
"v_fma_f32 v218,-v209,v217,v218\n"
"v_fmac_f32 v219,v209,v216\n"
"v_fma_f32 v220,v208,v218,v220\n"
"s_addk_i32 m0,4096\n"
"s_addk_i32 s74,128\n"
"buffer_load_dwordx4 v250,s[16:19],s74 offen lds\n"
"v_fma_f32 v221,v208,v219,v221\n"
"v_fma_f32 v220,-v209,v219,v220\n"
"v_fmac_f32 v221,v209,v218\n"
"v_fma_f32 v222,v208,v220,v222\n"
"v_fma_f32 v223,v208,v221,v223\n"
"v_fma_f32 v222,-v209,v221,v222\n"
"v_fmac_f32 v223,v209,v220\n"
"v_fma_f32 v224,v208,v222,v224\n"
"s_addk_i32 m0,4096\n"
"s_addk_i32 s74,128\n"
"buffer_load_dwordx4 v250,s[16:19],s74 offen lds\n"
"v_fma_f32 v225,v208,v223,v225\n"
"v_fma_f32 v224,-v209,v223,v224\n"
"v_fmac_f32 v225,v209,v222\n"
"v_fma_f32 v226,v208,v224,v226\n"
"v_fma_f32 v227,v208,v225,v227\n"
"v_fma_f32 v226,-v209,v225,v226\n"
"v_fmac_f32 v227,v209,v224\n"
"v_fma_f32 v228,v208,v226,v228\n"
"s_addk_i32 m0,4096\n"
"s_addk_i32 s74,128\n"
"buffer_load_dwordx4 v250,s[16:19],s74 offen lds\n"
"v_fma_f32 v229,v208,v227,v229\n"
"v_fma_f32 v228,-v209,v227,v228\n"
"v_fmac_f32 v229,v209,v226\n"
"v_fma_f32 v230,v208,v228,v230\n"
"v_fma_f32 v231,v208,v229,v231\n"
"v_fma_f32 v230,-v209,v229,v230\n"
"v_fmac_f32 v231,v209,v228\n"
"s_setprio 3\n"
"ds_write_b64 v253,v[230:231] offset:4096\n"
"s_setprio 0\n"
"s_addk_i32 m0,4096\n"
"s_addk_i32 s74,128\n"
"buffer_load_dwordx4 v250,s[16:19],s74 offen lds\n"
"s_nop 0\n"
"s_waitcnt lgkmcnt(0)\n"
"s_barrier\n"
"s_setprio 3\n"
"ds_read_b128 v[208:211],v248\n"
"ds_read_b128 v[212:215],v249\n"
"ds_read_b128 v[216:219],v248 offset:4096\n"
"ds_read_b128 v[220:223],v249 offset:4096\n"
"ds_read_b128 v[224:227],v248 offset:8192\n"
"ds_read_b128 v[228:231],v249 offset:8192\n"
"s_setprio 1\n"
"s_waitcnt lgkmcnt(4)\n"
"v_mfma_f32_16x16x32_bf16 v[192:195],v[0:3],v[208:211],0\n"
"ds_read_b128 v[232:235],v248 offset:12288\n"
"v_mfma_f32_16x16x32_bf16 v[192:195],v[4:7],v[212:215],v[192:195]\n"
"v_mfma_f32_16x16x32_bf16 v[196:199],v[72:75],v[208:211],0\n"
"ds_read_b128 v[236:239],v249 offset:12288\n"
"v_mfma_f32_16x16x32_bf16 v[196:199],v[76:79],v[212:215],v[196:199]\n"
"s_waitcnt lgkmcnt(4)\n"
"v_mfma_f32_16x16x32_bf16 v[196:199],v[80:83],v[216:219],v[196:199]\n"
"ds_read_b128 v[208:211],v248 offset:16384\n"
"v_mfma_f32_16x16x32_bf16 v[196:199],v[84:87],v[220:223],v[196:199]\n"
"v_mfma_f32_16x16x32_bf16 v[192:195],v[8:11],v[216:219],v[192:195]\n"
"ds_read_b128 v[212:215],v249 offset:16384\n"
"v_mfma_f32_16x16x32_bf16 v[192:195],v[12:15],v[220:223],v[192:195]\n"
"s_waitcnt lgkmcnt(4)\n"
"v_mfma_f32_16x16x32_bf16 v[192:195],v[16:19],v[224:227],v[192:195]\n"
"ds_read_b128 v[216:219],v248 offset:20480\n"
"v_mfma_f32_16x16x32_bf16 v[192:195],v[20:23],v[228:231],v[192:195]\n"
"v_mfma_f32_16x16x32_bf16 v[196:199],v[88:91],v[224:227],v[196:199]\n"
"ds_read_b128 v[220:223],v249 offset:20480\n"
"v_mfma_f32_16x16x32_bf16 v[196:199],v[92:95],v[228:231],v[196:199]\n"
"s_waitcnt lgkmcnt(4)\n"
"v_mfma_f32_16x16x32_bf16 v[196:199],v[96:99],v[232:235],v[196:199]\n"
"ds_read_b128 v[224:227],v248 offset:24576\n"
"v_mfma_f32_16x16x32_bf16 v[196:199],v[100:103],v[236:239],v[196:199]\n"
"v_mfma_f32_16x16x32_bf16 v[192:195],v[24:27],v[232:235],v[192:195]\n"
"ds_read_b128 v[228:231],v249 offset:24576\n"
"v_mfma_f32_16x16x32_bf16 v[192:195],v[28:31],v[236:239],v[192:195]\n"
"s_waitcnt lgkmcnt(4)\n"
"v_mfma_f32_16x16x32_bf16 v[192:195],v[32:35],v[208:211],v[192:195]\n"
"ds_read_b128 v[232:235],v248 offset:28672\n"
"v_mfma_f32_16x16x32_bf16 v[192:195],v[36:39],v[212:215],v[192:195]\n"
"v_mfma_f32_16x16x32_bf16 v[196:199],v[104:107],v[208:211],v[196:199]\n"
"ds_read_b128 v[236:239],v249 offset:28672\n"
"v_mfma_f32_16x16x32_bf16 v[196:199],v[108:111],v[212:215],v[196:199]\n"
"s_waitcnt lgkmcnt(4)\n"
"v_mfma_f32_16x16x32_bf16 v[196:199],v[112:115],v[216:219],v[196:199]\n"
"ds_read_b128 v[208:211],v248 offset:32768\n"
"v_mfma_f32_16x16x32_bf16 v[196:199],v[116:119],v[220:223],v[196:199]\n"
"v_mfma_f32_16x16x32_bf16 v[192:195],v[40:43],v[216:219],v[192:195]\n"
"ds_read_b128 v[212:215],v249 offset:32768\n"
"v_mfma_f32_16x16x32_bf16 v[192:195],v[44:47],v[220:223],v[192:195]\n"
"s_waitcnt lgkmcnt(4)\n"
"v_mfma_f32_16x16x32_bf16 v[192:195],v[48:51],v[224:227],v[192:195]\n"
"ds_read_b128 v[216:219],v248 offset:2048\n"
"v_mfma_f32_16x16x32_bf16 v[192:195],v[52:55],v[228:231],v[192:195]\n"
"v_mfma_f32_16x16x32_bf16 v[196:199],v[120:123],v[224:227],v[196:199]\n"
"ds_read_b128 v[220:223],v249 offset:2048\n"
"v_mfma_f32_16x16x32_bf16 v[196:199],v[124:127],v[228:231],v[196:199]\n"
"s_waitcnt lgkmcnt(4)\n"
"v_mfma_f32_16x16x32_bf16 v[196:199],v[128:131],v[232:235],v[196:199]\n"
"ds_read_b128 v[224:227],v248 offset:6144\n"
"v_mfma_f32_16x16x32_bf16 v[196:199],v[132:135],v[236:239],v[196:199]\n"
"v_mfma_f32_16x16x32_bf16 v[192:195],v[56:59],v[232:235],v[192:195]\n"
"ds_read_b128 v[228:231],v249 offset:6144\n"
"v_mfma_f32_16x16x32_bf16 v[192:195],v[60:63],v[236:239],v[192:195]\n"
"s_waitcnt lgkmcnt(4)\n"
"v_mfma_f32_16x16x32_bf16 v[192:195],v[64:67],v[208:211],v[192:195]\n"
"ds_read_b128 v[232:235],v248 offset:10240\n"
"v_mfma_f32_16x16x32_bf16 v[192:195],v[68:71],v[212:215],v[192:195]\n"
"v_mfma_f32_16x16x32_bf16 v[196:199],v[136:139],v[208:211],v[196:199]\n"
"ds_read_b128 v[236:239],v249 offset:10240\n"
"v_mfma_f32_16x16x32_bf16 v[196:199],v[140:143],v[212:215],v[196:199]\n"
"s_waitcnt lgkmcnt(4)\n"
"v_mfma_f32_16x16x32_bf16 v[200:203],v[0:3],v[216:219],0\n"
"ds_read_b128 v[208:211],v248 offset:14336\n"
"v_mfma_f32_16x16x32_bf16 v[200:203],v[4:7],v[220:223],v[200:203]\n"
"v_mfma_f32_16x16x32_bf16 v[204:207],v[72:75],v[216:219],0\n"
"ds_read_b128 v[212:215],v249 offset:14336\n"
"v_mfma_f32_16x16x32_bf16 v[204:207],v[76:79],v[220:223],v[204:207]\n"
"s_waitcnt lgkmcnt(4)\n"
"v_mfma_f32_16x16x32_bf16 v[204:207],v[80:83],v[224:227],v[204:207]\n"
"ds_read_b128 v[216:219],v248 offset:18432\n"
"v_mfma_f32_16x16x32_bf16 v[204:207],v[84:87],v[228:231],v[204:207]\n"
"v_mfma_f32_16x16x32_bf16 v[200:203],v[8:11],v[224:227],v[200:203]\n"
"ds_read_b128 v[220:223],v249 offset:18432\n"
"v_mfma_f32_16x16x32_bf16 v[200:203],v[12:15],v[228:231],v[200:203]\n"
"s_waitcnt lgkmcnt(4)\n"
"v_mfma_f32_16x16x32_bf16 v[200:203],v[16:19],v[232:235],v[200:203]\n"
"ds_read_b128 v[224:227],v248 offset:22528\n"
"v_mfma_f32_16x16x32_bf16 v[200:203],v[20:23],v[236:239],v[200:203]\n"
"v_mfma_f32_16x16x32_bf16 v[204:207],v[88:91],v[232:235],v[204:207]\n"
"ds_read_b128 v[228:231],v249 offset:22528\n"
"v_mfma_f32_16x16x32_bf16 v[204:207],v[92:95],v[236:239],v[204:207]\n"
"s_waitcnt lgkmcnt(4)\n"
"v_mfma_f32_16x16x32_bf16 v[204:207],v[96:99],v[208:211],v[204:207]\n"
"ds_read_b128 v[232:235],v248 offset:26624\n"
"v_mfma_f32_16x16x32_bf16 v[204:207],v[100:103],v[212:215],v[204:207]\n"
"v_mfma_f32_16x16x32_bf16 v[200:203],v[24:27],v[208:211],v[200:203]\n"
"ds_read_b128 v[236:239],v249 offset:26624\n"
"v_mfma_f32_16x16x32_bf16 v[200:203],v[28:31],v[212:215],v[200:203]\n"
"s_waitcnt lgkmcnt(4)\n"
"v_mfma_f32_16x16x32_bf16 v[200:203],v[32:35],v[216:219],v[200:203]\n"
"ds_read_b128 v[208:211],v248 offset:30720\n"
"v_mfma_f32_16x16x32_bf16 v[200:203],v[36:39],v[220:223],v[200:203]\n"
"v_mfma_f32_16x16x32_bf16 v[204:207],v[104:107],v[216:219],v[204:207]\n"
"ds_read_b128 v[212:215],v249 offset:30720\n"
"v_mfma_f32_16x16x32_bf16 v[204:207],v[108:111],v[220:223],v[204:207]\n"
"s_waitcnt lgkmcnt(4)\n"
"v_mfma_f32_16x16x32_bf16 v[204:207],v[112:115],v[224:227],v[204:207]\n"
"ds_read_b128 v[216:219],v248 offset:34816\n"
"v_mfma_f32_16x16x32_bf16 v[204:207],v[116:119],v[228:231],v[204:207]\n"
"v_mfma_f32_16x16x32_bf16 v[200:203],v[40:43],v[224:227],v[200:203]\n"
"ds_read_b128 v[220:223],v249 offset:34816\n"
"v_mfma_f32_16x16x32_bf16 v[200:203],v[44:47],v[228:231],v[200:203]\n"
"s_waitcnt lgkmcnt(4)\n"
"v_mfma_f32_16x16x32_bf16 v[200:203],v[48:51],v[232:235],v[200:203]\n"
"v_mfma_f32_16x16x32_bf16 v[200:203],v[52:55],v[236:239],v[200:203]\n"
"v_mfma_f32_16x16x32_bf16 v[204:207],v[120:123],v[232:235],v[204:207]\n"
"v_mfma_f32_16x16x32_bf16 v[204:207],v[124:127],v[236:239],v[204:207]\n"
"s_setprio 2\n"
"s_waitcnt lgkmcnt(2)\n"
"v_mfma_f32_16x16x32_bf16 v[204:207],v[128:131],v[208:211],v[204:207]\n"
"s_waitcnt lgkmcnt(0)\n"
"v_mfma_f32_16x16x32_bf16 v[204:207],v[132:135],v[212:215],v[204:207]\n"
"s_waitcnt vmcnt(0)\n"
"s_barrier\n"
"v_mfma_f32_16x16x32_bf16 v[200:203],v[56:59],v[208:211],v[200:203]\n"
"v_mfma_f32_16x16x32_bf16 v[200:203],v[60:63],v[212:215],v[200:203]\n"
"v_mfma_f32_16x16x32_bf16 v[200:203],v[64:67],v[216:219],v[200:203]\n"
"v_mfma_f32_16x16x32_bf16 v[200:203],v[68:71],v[220:223],v[200:203]\n"
"v_mfma_f32_16x16x32_bf16 v[204:207],v[136:139],v[216:219],v[204:207]\n"
"v_mfma_f32_16x16x32_bf16 v[204:207],v[140:143],v[220:223],v[204:207]\n"
"s_setprio 0\n"
"v_add_u32 v248,4294930432,v248\n"
"v_add_u32 v249,4294930432,v249\n"
"s_setprio 3\n"
"ds_read_b64 v[210:211],v255 offset:512\n"
"ds_read_b64 v[208:209],v255 offset:51712\n"
"s_setprio 0\n"
"s_waitcnt lgkmcnt(0)\n"
"v_mul_f32 v216,v210,v192\n"
"v_mul_f32 v217,v210,v200\n"
"v_fma_f32 v216,-v211,v200,v216\n"
"v_fmac_f32 v217,v211,v192\n"
"s_add_u32 m0,s73,36864\n"
"s_add_u32 s74,s80,129024\n"
"buffer_load_dwordx4 v250,s[16:19],s74 offen lds\n"
"v_mul_f32 v218,v210,v193\n"
"v_mul_f32 v219,v210,v201\n"
"v_fma_f32 v218,-v211,v201,v218\n"
"v_fmac_f32 v219,v211,v193\n"
"v_mul_f32 v220,v210,v194\n"
"v_mul_f32 v221,v210,v202\n"
"v_fma_f32 v220,-v211,v202,v220\n"
"v_fmac_f32 v221,v211,v194\n"
"s_addk_i32 m0,4096\n"
"s_addk_i32 s74,128\n"
"buffer_load_dwordx4 v250,s[16:19],s74 offen lds\n"
"v_mul_f32 v222,v210,v195\n"
"v_mul_f32 v223,v210,v203\n"
"v_fma_f32 v222,-v211,v203,v222\n"
"v_fmac_f32 v223,v211,v195\n"
"v_mul_f32 v224,v210,v196\n"
"v_mul_f32 v225,v210,v204\n"
"v_fma_f32 v224,-v211,v204,v224\n"
"v_fmac_f32 v225,v211,v196\n"
"s_addk_i32 m0,4096\n"
"s_addk_i32 s74,128\n"
"buffer_load_dwordx4 v250,s[16:19],s74 offen lds\n"
"v_mul_f32 v226,v210,v197\n"
"v_mul_f32 v227,v210,v205\n"
"v_fma_f32 v226,-v211,v205,v226\n"
"v_fmac_f32 v227,v211,v197\n"
"v_mul_f32 v228,v210,v198\n"
"v_mul_f32 v229,v210,v206\n"
"v_fma_f32 v228,-v211,v206,v228\n"
"v_fmac_f32 v229,v211,v198\n"
"s_addk_i32 m0,4096\n"
"s_addk_i32 s74,128\n"
"buffer_load_dwordx4 v250,s[16:19],s74 offen lds\n"
"v_mul_f32 v230,v210,v199\n"
"v_mul_f32 v231,v210,v207\n"
"v_fma_f32 v230,-v211,v207,v230\n"
"v_fmac_f32 v231,v211,v199\n"
"v_cvt_pk_bf16_f32 v160,v216,v217\n"
"v_cvt_pk_bf16_f32 v161,v218,v219\n"
"v_cvt_pk_bf16_f32 v162,v220,v221\n"
"v_cvt_pk_bf16_f32 v163,v222,v223\n"
"v_cvt_pk_bf16_f32 v164,v224,v225\n"
"s_addk_i32 m0,4096\n"
"s_addk_i32 s74,128\n"
"buffer_load_dwordx4 v250,s[16:19],s74 offen lds\n"
"v_cvt_pk_bf16_f32 v165,v226,v227\n"
"v_cvt_pk_bf16_f32 v166,v228,v229\n"
"v_cvt_pk_bf16_f32 v167,v230,v231\n"
"v_fma_f32 v218,v208,v216,v218\n"
"v_fma_f32 v219,v208,v217,v219\n"
"v_fma_f32 v218,-v209,v217,v218\n"
"v_fmac_f32 v219,v209,v216\n"
"v_fma_f32 v220,v208,v218,v220\n"
"s_addk_i32 m0,4096\n"
"s_addk_i32 s74,128\n"
"buffer_load_dwordx4 v250,s[16:19],s74 offen lds\n"
"v_fma_f32 v221,v208,v219,v221\n"
"v_fma_f32 v220,-v209,v219,v220\n"
"v_fmac_f32 v221,v209,v218\n"
"v_fma_f32 v222,v208,v220,v222\n"
"v_fma_f32 v223,v208,v221,v223\n"
"v_fma_f32 v222,-v209,v221,v222\n"
"v_fmac_f32 v223,v209,v220\n"
"v_fma_f32 v224,v208,v222,v224\n"
"s_addk_i32 m0,4096\n"
"s_addk_i32 s74,128\n"
"buffer_load_dwordx4 v250,s[16:19],s74 offen lds\n"
"v_fma_f32 v225,v208,v223,v225\n"
"v_fma_f32 v224,-v209,v223,v224\n"
"v_fmac_f32 v225,v209,v222\n"
"v_fma_f32 v226,v208,v224,v226\n"
"v_fma_f32 v227,v208,v225,v227\n"
"v_fma_f32 v226,-v209,v225,v226\n"
"v_fmac_f32 v227,v209,v224\n"
"v_fma_f32 v228,v208,v226,v228\n"
"s_addk_i32 m0,4096\n"
"s_addk_i32 s74,128\n"
"buffer_load_dwordx4 v250,s[16:19],s74 offen lds\n"
"v_fma_f32 v229,v208,v227,v229\n"
"v_fma_f32 v228,-v209,v227,v228\n"
"v_fmac_f32 v229,v209,v226\n"
"v_fma_f32 v230,v208,v228,v230\n"
"v_fma_f32 v231,v208,v229,v231\n"
"v_fma_f32 v230,-v209,v229,v230\n"
"v_fmac_f32 v231,v209,v228\n"
"s_setprio 3\n"
"ds_write_b64 v253,v[230:231] offset:8192\n"
"s_setprio 0\n"
"s_addk_i32 m0,4096\n"
"s_addk_i32 s74,128\n"
"buffer_load_dwordx4 v250,s[16:19],s74 offen lds\n"
"s_nop 0\n"
"s_waitcnt lgkmcnt(0)\n"
"s_barrier\n"
"s_setprio 3\n"
"ds_read_b128 v[208:211],v248\n"
"ds_read_b128 v[212:215],v249\n"
"ds_read_b128 v[216:219],v248 offset:4096\n"
"ds_read_b128 v[220:223],v249 offset:4096\n"
"ds_read_b128 v[224:227],v248 offset:8192\n"
"ds_read_b128 v[228:231],v249 offset:8192\n"
"s_setprio 1\n"
"s_waitcnt lgkmcnt(4)\n"
"v_mfma_f32_16x16x32_bf16 v[192:195],v[0:3],v[208:211],0\n"
"ds_read_b128 v[232:235],v248 offset:12288\n"
"v_mfma_f32_16x16x32_bf16 v[192:195],v[4:7],v[212:215],v[192:195]\n"
"v_mfma_f32_16x16x32_bf16 v[196:199],v[72:75],v[208:211],0\n"
"ds_read_b128 v[236:239],v249 offset:12288\n"
"v_mfma_f32_16x16x32_bf16 v[196:199],v[76:79],v[212:215],v[196:199]\n"
"s_waitcnt lgkmcnt(4)\n"
"v_mfma_f32_16x16x32_bf16 v[196:199],v[80:83],v[216:219],v[196:199]\n"
"ds_read_b128 v[208:211],v248 offset:16384\n"
"v_mfma_f32_16x16x32_bf16 v[196:199],v[84:87],v[220:223],v[196:199]\n"
"v_mfma_f32_16x16x32_bf16 v[192:195],v[8:11],v[216:219],v[192:195]\n"
".Lk_pads4:\n"
"s_cbranch_execz .Lk_hop_rets\n"
"ds_read_b128 v[212:215],v249 offset:16384\n"
"v_mfma_f32_16x16x32_bf16 v[192:195],v[12:15],v[220:223],v[192:195]\n"
"s_waitcnt lgkmcnt(4)\n"
"v_mfma_f32_16x16x32_bf16 v[192:195],v[16:19],v[224:227],v[192:195]\n"
"ds_read_b128 v[216:219],v248 offset:20480\n"
"v_mfma_f32_16x16x32_bf16 v[192:195],v[20:23],v[228:231],v[192:195]\n"
"v_mfma_f32_16x16x32_bf16 v[196:199],v[88:91],v[224:227],v[196:199]\n"
"ds_read_b128 v[220:223],v249 offset:20480\n"
"v_mfma_f32_16x16x32_bf16 v[196:199],v[92:95],v[228:231],v[196:199]\n"
"s_waitcnt lgkmcnt(4)\n"
"v_mfma_f32_16x16x32_bf16 v[196:199],v[96:99],v[232:235],v[196:199]\n"
"ds_read_b128 v[224:227],v248 offset:24576\n"
"v_mfma_f32_16x16x32_bf16 v[196:199],v[100:103],v[236:239],v[196:199]\n"
"v_mfma_f32_16x16x32_bf16 v[192:195],v[24:27],v[232:235],v[192:195]\n"
"ds_read_b128 v[228:231],v249 offset:24576\n"
"v_mfma_f32_16x16x32_bf16 v[192:195],v[28:31],v[236:239],v[192:195]\n"
"s_waitcnt lgkmcnt(4)\n"
"v_mfma_f32_16x16x32_bf16 v[192:195],v[32:35],v[208:211],v[192:195]\n"
"ds_read_b128 v[232:235],v248 offset:28672\n"
"v_mfma_f32_16x16x32_bf16 v[192:195],v[36:39],v[212:215],v[192:195]\n"
"v_mfma_f32_16x16x32_bf16 v[196:199],v[104:107],v[208:211],v[196:199]\n"
"ds_read_b128 v[236:239],v249 offset:28672\n"
"v_mfma_f32_16x16x32_bf16 v[196:199],v[108:111],v[212:215],v[196:199]\n"
"s_waitcnt lgkmcnt(4)\n"
"v_mfma_f32_16x16x32_bf16 v[196:199],v[112:115],v[216:219],v[196:199]\n"
"ds_read_b128 v[208:211],v248 offset:32768\n"
"v_mfma_f32_16x16x32_bf16 v[196:199],v[116:119],v[220:223],v[196:199]\n"
"v_mfma_f32_16x16x32_bf16 v[192:195],v[40:43],v[216:219],v[192:195]\n"
"ds_read_b128 v[212:215],v249 offset:32768\n"
"v_mfma_f32_16x16x32_bf16 v[192:195],v[44:47],v[220:223],v[192:195]\n"
"s_waitcnt lgkmcnt(4)\n"
"v_mfma_f32_16x16x32_bf16 v[192:195],v[48:51],v[224:227],v[192:195]\n"
"ds_read_b128 v[216:219],v248 offset:2048\n"
"v_mfma_f32_16x16x32_bf16 v[192:195],v[52:55],v[228:231],v[192:195]\n"
"v_mfma_f32_16x16x32_bf16 v[196:199],v[120:123],v[224:227],v[196:199]\n"
"ds_read_b128 v[220:223],v249 offset:2048\n"
"v_mfma_f32_16x16x32_bf16 v[196:199],v[124:127],v[228:231],v[196:199]\n"
"s_waitcnt lgkmcnt(4)\n"
"v_mfma_f32_16x16x32_bf16 v[196:199],v[128:131],v[232:235],v[196:199]\n"
"ds_read_b128 v[224:227],v248 offset:6144\n"
"v_mfma_f32_16x16x32_bf16 v[196:199],v[132:135],v[236:239],v[196:199]\n"
"v_mfma_f32_16x16x32_bf16 v[192:195],v[56:59],v[232:235],v[192:195]\n"
"ds_read_b128 v[228:231],v249 offset:6144\n"
"v_mfma_f32_16x16x32_bf16 v[192:195],v[60:63],v[236:239],v[192:195]\n"
"s_waitcnt lgkmcnt(4)\n"
"v_mfma_f32_16x16x32_bf16 v[192:195],v[64:67],v[208:211],v[192:195]\n"
"ds_read_b128 v[232:235],v248 offset:10240\n"
"v_mfma_f32_16x16x32_bf16 v[192:195],v[68:71],v[212:215],v[192:195]\n"
"v_mfma_f32_16x16x32_bf16 v[196:199],v[136:139],v[208:211],v[196:199]\n"
"ds_read_b128 v[236:239],v249 offset:10240\n"
"v_mfma_f32_16x16x32_bf16 v[196:199],v[140:143],v[212:215],v[196:199]\n"
"s_waitcnt lgkmcnt(4)\n"
"v_mfma_f32_16x16x32_bf16 v[200:203],v[0:3],v[216:219],0\n"
"ds_read_b128 v[208:211],v248 offset:14336\n"
"v_mfma_f32_16x16x32_bf16 v[200:203],v[4:7],v[220:223],v[200:203]\n"
"v_mfma_f32_16x16x32_bf16 v[204:207],v[72:75],v[216:219],0\n"
"ds_read_b128 v[212:215],v249 offset:14336\n"
"v_mfma_f32_16x16x32_bf16 v[204:207],v[76:79],v[220:223],v[204:207]\n"
"s_waitcnt lgkmcnt(4)\n"
"v_mfma_f32_16x16x32_bf16 v[204:207],v[80:83],v[224:227],v[204:207]\n"
"ds_read_b128 v[216:219],v248 offset:18432\n"
"v_mfma_f32_16x16x32_bf16 v[204:207],v[84:87],v[228:231],v[204:207]\n"
"v_mfma_f32_16x16x32_bf16 v[200:203],v[8:11],v[224:227],v[200:203]\n"
"ds_read_b128 v[220:223],v249 offset:18432\n"
"v_mfma_f32_16x16x32_bf16 v[200:203],v[12:15],v[228:231],v[200:203]\n"
"s_waitcnt lgkmcnt(4)\n"
"v_mfma_f32_16x16x32_bf16 v[200:203],v[16:19],v[232:235],v[200:203]\n"
"ds_read_b128 v[224:227],v248 offset:22528\n"
"v_mfma_f32_16x16x32_bf16 v[200:203],v[20:23],v[236:239],v[200:203]\n"
"v_mfma_f32_16x16x32_bf16 v[204:207],v[88:91],v[232:235],v[204:207]\n"
"ds_read_b128 v[228:231],v249 offset:22528\n"
"v_mfma_f32_16x16x32_bf16 v[204:207],v[92:95],v[236:239],v[204:207]\n"
"s_waitcnt lgkmcnt(4)\n"
"v_mfma_f32_16x16x32_bf16 v[204:207],v[96:99],v[208:211],v[204:207]\n"
"ds_read_b128 v[232:235],v248 offset:26624\n"
"v_mfma_f32_16x16x32_bf16 v[204:207],v[100:103],v[212:215],v[204:207]\n"
"v_mfma_f32_16x16x32_bf16 v[200:203],v[24:27],v[208:211],v[200:203]\n"
"ds_read_b128 v[236:239],v249 offset:26624\n"
"v_mfma_f32_16x16x32_bf16 v[200:203],v[28:31],v[212:215],v[200:203]\n"
"s_waitcnt lgkmcnt(4)\n"
"v_mfma_f32_16x16x32_bf16 v[200:203],v[32:35],v[216:219],v[200:203]\n"
"ds_read_b128 v[208:211],v248 offset:30720\n"
"v_mfma_f32_16x16x32_bf16 v[200:203],v[36:39],v[220:223],v[200:203]\n"
"v_mfma_f32_16x16x32_bf16 v[204:207],v[104:107],v[216:219],v[204:207]\n"
"ds_read_b128 v[212:215],v249 offset:30720\n"
"v_mfma_f32_16x16x32_bf16 v[204:207],v[108:111],v[220:223],v[204:207]\n"
"s_waitcnt lgkmcnt(4)\n"
"v_mfma_f32_16x16x32_bf16 v[204:207],v[112:115],v[224:227],v[204:207]\n"
"ds_read_b128 v[216:219],v248 offset:34816\n"
"v_mfma_f32_16x16x32_bf16 v[204:207],v[116:119],v[228:231],v[204:207]\n"
"v_mfma_f32_16x16x32_bf16 v[200:203],v[40:43],v[224:227],v[200:203]\n"
"ds_read_b128 v[220:223],v249 offset:34816\n"
"v_mfma_f32_16x16x32_bf16 v[200:203],v[44:47],v[228:231],v[200:203]\n"
"s_waitcnt lgkmcnt(4)\n"
"v_mfma_f32_16x16x32_bf16 v[200:203],v[48:51],v[232:235],v[200:203]\n"
"v_mfma_f32_16x16x32_bf16 v[200:203],v[52:55],v[236:239],v[200:203]\n"
"v_mfma_f32_16x16x32_bf16 v[204:207],v[120:123],v[232:235],v[204:207]\n"
"v_mfma_f32_16x16x32_bf16 v[204:207],v[124:127],v[236:239],v[204:207]\n"
"s_setprio 2\n"
"s_waitcnt lgkmcnt(2)\n"
"v_mfma_f32_16x16x32_bf16 v[204:207],v[128:131],v[208:211],v[204:207]\n"
"s_waitcnt lgkmcnt(0)\n"
"v_mfma_f32_16x16x32_bf16 v[204:207],v[132:135],v[212:215],v[204:207]\n"
"s_waitcnt vmcnt(0)\n"
"s_barrier\n"
"v_mfma_f32_16x16x32_bf16 v[200:203],v[56:59],v[208:211],v[200:203]\n"
"v_mfma_f32_16x16x32_bf16 v[200:203],v[60:63],v[212:215],v[200:203]\n"
"v_mfma_f32_16x16x32_bf16 v[200:203],v[64:67],v[216:219],v[200:203]\n"
"v_mfma_f32_16x16x32_bf16 v[200:203],v[68:71],v[220:223],v[200:203]\n"
"v_mfma_f32_16x16x32_bf16 v[204:207],v[136:139],v[216:219],v[204:207]\n"
"v_mfma_f32_16x16x32_bf16 v[204:207],v[140:143],v[220:223],v[204:207]\n"
"s_setprio 0\n"
"v_add_u32 v248,73728,v248\n"
"v_add_u32 v249,73728,v249\n"
"s_setprio 3\n"
"ds_read_b64 v[210:211],v255 offset:768\n"
"ds_read_b64 v[208:209],v255 offset:51968\n"
"s_setprio 0\n"
"s_waitcnt lgkmcnt(0)\n"
"v_mul_f32 v216,v210,v192\n"
"v_mul_f32 v217,v210,v200\n"
"v_fma_f32 v216,-v211,v200,v216\n"
"v_fmac_f32 v217,v211,v192\n"
"v_lshrrev_b32 v240,1,v254\n"
"v_lshrrev_b32 v241,4,v254\n"
"v_xor_b32 v241,v241,v254\n"
"v_and_b32 v241,1,v241\n"
"v_mul_u32_u24 v240,1152,v240\n"
"v_lshl_add_u32 v242,v241,4,v240\n"
"s_cmp_lt_u32 s71,2\n"
"s_cselect_b64 s[96:97],s[24:25],s[28:29]\n"
"s_mov_b64 s[12:13],s[96:97]\n"
"s_mov_b32 s14,663552\n"
"s_and_b32 s75,s71,1\n"
"s_mul_i32 s75,s75,331776\n"
"s_lshl_b32 s76,s69,5\n"
"s_add_u32 s86,s75,s76\n"
"s_mul_i32 s85,s71,9216\n"
"s_add_u32 m0,s85,0\n"
"s_add_u32 s74,s86,1024\n"
"buffer_load_dwordx4 v242,s[12:15],s74 offen lds\n"
"v_mul_f32 v218,v210,v193\n"
"v_mul_f32 v219,v210,v201\n"
"v_fma_f32 v218,-v211,v201,v218\n"
"v_fmac_f32 v219,v211,v193\n"
"v_mul_f32 v220,v210,v194\n"
"v_mul_f32 v221,v210,v202\n"
"v_fma_f32 v220,-v211,v202,v220\n"
"v_fmac_f32 v221,v211,v194\n"
"s_addk_i32 m0,1024\n"
"s_add_u32 s74,s86,37888\n"
"buffer_load_dwordx4 v242,s[12:15],s74 offen lds\n"
"v_mul_f32 v222,v210,v195\n"
"v_mul_f32 v223,v210,v203\n"
"v_fma_f32 v222,-v211,v203,v222\n"
"v_fmac_f32 v223,v211,v195\n"
"v_mul_f32 v224,v210,v196\n"
"v_mul_f32 v225,v210,v204\n"
"v_fma_f32 v224,-v211,v204,v224\n"
"v_fmac_f32 v225,v211,v196\n"
"s_addk_i32 m0,1024\n"
"s_add_u32 s74,s86,74752\n"
"buffer_load_dwordx4 v242,s[12:15],s74 offen lds\n"
"v_mul_f32 v226,v210,v197\n"
"v_mul_f32 v227,v210,v205\n"
"v_fma_f32 v226,-v211,v205,v226\n"
"v_fmac_f32 v227,v211,v197\n"
"v_mul_f32 v228,v210,v198\n"
"v_mul_f32 v229,v210,v206\n"
"v_fma_f32 v228,-v211,v206,v228\n"
"v_fmac_f32 v229,v211,v198\n"
"s_addk_i32 m0,1024\n"
"s_add_u32 s74,s86,111616\n"
"buffer_load_dwordx4 v242,s[12:15],s74 offen lds\n"
"v_mul_f32 v230,v210,v199\n"
"v_mul_f32 v231,v210,v207\n"
"v_fma_f32 v230,-v211,v207,v230\n"
"v_fmac_f32 v231,v211,v199\n"
"v_cvt_pk_bf16_f32 v168,v216,v217\n"
"v_cvt_pk_bf16_f32 v169,v218,v219\n"
"v_cvt_pk_bf16_f32 v170,v220,v221\n"
"v_cvt_pk_bf16_f32 v171,v222,v223\n"
"v_cvt_pk_bf16_f32 v172,v224,v225\n"
"s_addk_i32 m0,1024\n"
"s_add_u32 s74,s86,148480\n"
"buffer_load_dwordx4 v242,s[12:15],s74 offen lds\n"
"v_cvt_pk_bf16_f32 v173,v226,v227\n"
"v_cvt_pk_bf16_f32 v174,v228,v229\n"
"v_cvt_pk_bf16_f32 v175,v230,v231\n"
"v_fma_f32 v218,v208,v216,v218\n"
"v_fma_f32 v219,v208,v217,v219\n"
"v_fma_f32 v218,-v209,v217,v218\n"
"v_fmac_f32 v219,v209,v216\n"
"v_fma_f32 v220,v208,v218,v220\n"
"s_addk_i32 m0,1024\n"
"s_add_u32 s74,s86,185344\n"
"buffer_load_dwordx4 v242,s[12:15],s74 offen lds\n"
"v_fma_f32 v221,v208,v219,v221\n"
"v_fma_f32 v220,-v209,v219,v220\n"
"v_fmac_f32 v221,v209,v218\n"
"v_fma_f32 v222,v208,v220,v222\n"
"v_fma_f32 v223,v208,v221,v223\n"
"v_fma_f32 v222,-v209,v221,v222\n"
"v_fmac_f32 v223,v209,v220\n"
"v_fma_f32 v224,v208,v222,v224\n"
"s_addk_i32 m0,1024\n"
"s_add_u32 s74,s86,222208\n"
"buffer_load_dwordx4 v242,s[12:15],s74 offen lds\n"
"v_fma_f32 v225,v208,v223,v225\n"
"v_fma_f32 v224,-v209,v223,v224\n"
"v_fmac_f32 v225,v209,v222\n"
"v_fma_f32 v226,v208,v224,v226\n"
"v_fma_f32 v227,v208,v225,v227\n"
"v_fma_f32 v226,-v209,v225,v226\n"
"v_fmac_f32 v227,v209,v224\n"
"v_fma_f32 v228,v208,v226,v228\n"
"s_addk_i32 m0,1024\n"
"s_add_u32 s74,s86,259072\n"
"buffer_load_dwordx4 v242,s[12:15],s74 offen lds\n"
"v_fma_f32 v229,v208,v227,v229\n"
"v_fma_f32 v228,-v209,v227,v228\n"
"v_fmac_f32 v229,v209,v226\n"
"v_fma_f32 v230,v208,v228,v230\n"
"v_fma_f32 v231,v208,v229,v231\n"
"v_fma_f32 v230,-v209,v229,v230\n"
"v_fmac_f32 v231,v209,v228\n"
"s_setprio 3\n"
"ds_write_b64 v253,v[230:231] offset:12288\n"
"s_setprio 0\n"
"s_addk_i32 m0,1024\n"
"s_add_u32 s74,s86,295936\n"
"buffer_load_dwordx4 v242,s[12:15],s74 offen lds\n"
"s_nop 0\n"
"s_waitcnt lgkmcnt(0)\n"
"s_barrier\n"
"s_setprio 3\n"
"ds_read_b128 v[208:211],v248\n"
"ds_read_b128 v[212:215],v249\n"
"ds_read_b128 v[216:219],v248 offset:4096\n"
"ds_read_b128 v[220:223],v249 offset:4096\n"
"ds_read_b128 v[224:227],v248 offset:8192\n"
"ds_read_b128 v[228:231],v249 offset:8192\n"
"s_setprio 1\n"
"s_waitcnt lgkmcnt(4)\n"
"v_mfma_f32_16x16x32_bf16 v[192:195],v[0:3],v[208:211],0\n"
"ds_read_b128 v[232:235],v248 offset:12288\n"
"v_mfma_f32_16x16x32_bf16 v[192:195],v[4:7],v[212:215],v[192:195]\n"
"v_mfma_f32_16x16x32_bf16 v[196:199],v[72:75],v[208:211],0\n"
"ds_read_b128 v[236:239],v249 offset:12288\n"
"v_mfma_f32_16x16x32_bf16 v[196:199],v[76:79],v[212:215],v[196:199]\n"
"s_waitcnt lgkmcnt(4)\n"
"v_mfma_f32_16x16x32_bf16 v[196:199],v[80:83],v[216:219],v[196:199]\n"
"ds_read_b128 v[208:211],v248 offset:16384\n"
"v_mfma_f32_16x16x32_bf16 v[196:199],v[84:87],v[220:223],v[196:199]\n"
"v_mfma_f32_16x16x32_bf16 v[192:195],v[8:11],v[216:219],v[192:195]\n"
"ds_read_b128 v[212:215],v249 offset:16384\n"
"v_mfma_f32_16x16x32_bf16 v[192:195],v[12:15],v[220:223],v[192:195]\n"
"s_waitcnt lgkmcnt(4)\n"
"v_mfma_f32_16x16x32_bf16 v[192:195],v[16:19],v[224:227],v[192:195]\n"
"ds_read_b128 v[216:219],v248 offset:20480\n"
"v_mfma_f32_16x16x32_bf16 v[192:195],v[20:23],v[228:231],v[192:195]\n"
"v_mfma_f32_16x16x32_bf16 v[196:199],v[88:91],v[224:227],v[196:199]\n"
"ds_read_b128 v[220:223],v249 offset:20480\n"
"v_mfma_f32_16x16x32_bf16 v[196:199],v[92:95],v[228:231],v[196:199]\n"
"s_waitcnt lgkmcnt(4)\n"
"v_mfma_f32_16x16x32_bf16 v[196:199],v[96:99],v[232:235],v[196:199]\n"
"ds_read_b128 v[224:227],v248 offset:24576\n"
"v_mfma_f32_16x16x32_bf16 v[196:199],v[100:103],v[236:239],v[196:199]\n"
"v_mfma_f32_16x16x32_bf16 v[192:195],v[24:27],v[232:235],v[192:195]\n"
"ds_read_b128 v[228:231],v249 offset:24576\n"
"v_mfma_f32_16x16x32_bf16 v[192:195],v[28:31],v[236:239],v[192:195]\n"
"s_waitcnt lgkmcnt(4)\n"
"v_mfma_f32_16x16x32_bf16 v[192:195],v[32:35],v[208:211],v[192:195]\n"
"ds_read_b128 v[232:235],v248 offset:28672\n"
"v_mfma_f32_16x16x32_bf16 v[192:195],v[36:39],v[212:215],v[192:195]\n"
"v_mfma_f32_16x16x32_bf16 v[196:199],v[104:107],v[208:211],v[196:199]\n"
"ds_read_b128 v[236:239],v249 offset:28672\n"
"v_mfma_f32_16x16x32_bf16 v[196:199],v[108:111],v[212:215],v[196:199]\n"
"s_waitcnt lgkmcnt(4)\n"
"v_mfma_f32_16x16x32_bf16 v[196:199],v[112:115],v[216:219],v[196:199]\n"
"ds_read_b128 v[208:211],v248 offset:32768\n"
"v_mfma_f32_16x16x32_bf16 v[196:199],v[116:119],v[220:223],v[196:199]\n"
"v_mfma_f32_16x16x32_bf16 v[192:195],v[40:43],v[216:219],v[192:195]\n"
"ds_read_b128 v[212:215],v249 offset:32768\n"
"v_mfma_f32_16x16x32_bf16 v[192:195],v[44:47],v[220:223],v[192:195]\n"
"s_waitcnt lgkmcnt(4)\n"
"v_mfma_f32_16x16x32_bf16 v[192:195],v[48:51],v[224:227],v[192:195]\n"
"ds_read_b128 v[216:219],v248 offset:2048\n"
"v_mfma_f32_16x16x32_bf16 v[192:195],v[52:55],v[228:231],v[192:195]\n"
"v_mfma_f32_16x16x32_bf16 v[196:199],v[120:123],v[224:227],v[196:199]\n"
"ds_read_b128 v[220:223],v249 offset:2048\n"
"v_mfma_f32_16x16x32_bf16 v[196:199],v[124:127],v[228:231],v[196:199]\n"
"s_waitcnt lgkmcnt(4)\n"
"v_mfma_f32_16x16x32_bf16 v[196:199],v[128:131],v[232:235],v[196:199]\n"
"ds_read_b128 v[224:227],v248 offset:6144\n"
"v_mfma_f32_16x16x32_bf16 v[196:199],v[132:135],v[236:239],v[196:199]\n"
"v_mfma_f32_16x16x32_bf16 v[192:195],v[56:59],v[232:235],v[192:195]\n"
"ds_read_b128 v[228:231],v249 offset:6144\n"
"v_mfma_f32_16x16x32_bf16 v[192:195],v[60:63],v[236:239],v[192:195]\n"
"s_waitcnt lgkmcnt(4)\n"
"v_mfma_f32_16x16x32_bf16 v[192:195],v[64:67],v[208:211],v[192:195]\n"
"ds_read_b128 v[232:235],v248 offset:10240\n"
"v_mfma_f32_16x16x32_bf16 v[192:195],v[68:71],v[212:215],v[192:195]\n"
"v_mfma_f32_16x16x32_bf16 v[196:199],v[136:139],v[208:211],v[196:199]\n"
"ds_read_b128 v[236:239],v249 offset:10240\n"
"v_mfma_f32_16x16x32_bf16 v[196:199],v[140:143],v[212:215],v[196:199]\n"
"s_waitcnt lgkmcnt(4)\n"
"v_mfma_f32_16x16x32_bf16 v[200:203],v[0:3],v[216:219],0\n"
"ds_read_b128 v[208:211],v248 offset:14336\n"
"v_mfma_f32_16x16x32_bf16 v[200:203],v[4:7],v[220:223],v[200:203]\n"
"v_mfma_f32_16x16x32_bf16 v[204:207],v[72:75],v[216:219],0\n"
"ds_read_b128 v[212:215],v249 offset:14336\n"
"v_mfma_f32_16x16x32_bf16 v[204:207],v[76:79],v[220:223],v[204:207]\n"
"s_waitcnt lgkmcnt(4)\n"
"v_mfma_f32_16x16x32_bf16 v[204:207],v[80:83],v[224:227],v[204:207]\n"
"ds_read_b128 v[216:219],v248 offset:18432\n"
"v_mfma_f32_16x16x32_bf16 v[204:207],v[84:87],v[228:231],v[204:207]\n"
"v_mfma_f32_16x16x32_bf16 v[200:203],v[8:11],v[224:227],v[200:203]\n"
"ds_read_b128 v[220:223],v249 offset:18432\n"
"v_mfma_f32_16x16x32_bf16 v[200:203],v[12:15],v[228:231],v[200:203]\n"
"s_waitcnt lgkmcnt(4)\n"
"v_mfma_f32_16x16x32_bf16 v[200:203],v[16:19],v[232:235],v[200:203]\n"
"ds_read_b128 v[224:227],v248 offset:22528\n"
"v_mfma_f32_16x16x32_bf16 v[200:203],v[20:23],v[236:239],v[200:203]\n"
"v_mfma_f32_16x16x32_bf16 v[204:207],v[88:91],v[232:235],v[204:207]\n"
"ds_read_b128 v[228:231],v249 offset:22528\n"
"v_mfma_f32_16x16x32_bf16 v[204:207],v[92:95],v[236:239],v[204:207]\n"
"s_waitcnt lgkmcnt(4)\n"
"v_mfma_f32_16x16x32_bf16 v[204:207],v[96:99],v[208:211],v[204:207]\n"
"ds_read_b128 v[232:235],v248 offset:26624\n"
"v_mfma_f32_16x16x32_bf16 v[204:207],v[100:103],v[212:215],v[204:207]\n"
"v_mfma_f32_16x16x32_bf16 v[200:203],v[24:27],v[208:211],v[200:203]\n"
"ds_read_b128 v[236:239],v249 offset:26624\n"
"v_mfma_f32_16x16x32_bf16 v[200:203],v[28:31],v[212:215],v[200:203]\n"
"s_waitcnt lgkmcnt(4)\n"
"v_mfma_f32_16x16x32_bf16 v[200:203],v[32:35],v[216:219],v[200:203]\n"
"ds_read_b128 v[208:211],v248 offset:30720\n"
"v_mfma_f32_16x16x32_bf16 v[200:203],v[36:39],v[220:223],v[200:203]\n"
"v_mfma_f32_16x16x32_bf16 v[204:207],v[104:107],v[216:219],v[204:207]\n"
"ds_read_b128 v[212:215],v249 offset:30720\n"
"v_mfma_f32_16x16x32_bf16 v[204:207],v[108:111],v[220:223],v[204:207]\n"
"s_waitcnt lgkmcnt(4)\n"
"v_mfma_f32_16x16x32_bf16 v[204:207],v[112:115],v[224:227],v[204:207]\n"
"ds_read_b128 v[216:219],v248 offset:34816\n"
"v_mfma_f32_16x16x32_bf16 v[204:207],v[116:119],v[228:231],v[204:207]\n"
"v_mfma_f32_16x16x32_bf16 v[200:203],v[40:43],v[224:227],v[200:203]\n"
"ds_read_b128 v[220:223],v249 offset:34816\n"
"v_mfma_f32_16x16x32_bf16 v[200:203],v[44:47],v[228:231],v[200:203]\n"
"s_waitcnt lgkmcnt(4)\n"
"v_mfma_f32_16x16x32_bf16 v[200:203],v[48:51],v[232:235],v[200:203]\n"
"v_mfma_f32_16x16x32_bf16 v[200:203],v[52:55],v[236:239],v[200:203]\n"
"v_mfma_f32_16x16x32_bf16 v[204:207],v[120:123],v[232:235],v[204:207]\n"
"v_mfma_f32_16x16x32_bf16 v[204:207],v[124:127],v[236:239],v[204:207]\n"
"s_setprio 2\n"
"s_waitcnt lgkmcnt(2)\n"
"v_mfma_f32_16x16x32_bf16 v[204:207],v[128:131],v[208:211],v[204:207]\n"
"s_waitcnt lgkmcnt(0)\n"
"v_mfma_f32_16x16x32_bf16 v[204:207],v[132:135],v[212:215],v[204:207]\n"
"s_barrier\n"
"v_mfma_f32_16x16x32_bf16 v[200:203],v[56:59],v[208:211],v[200:203]\n"
"v_mfma_f32_16x16x32_bf16 v[200:203],v[60:63],v[212:215],v[200:203]\n"
"v_mfma_f32_16x16x32_bf16 v[200:203],v[64:67],v[216:219],v[200:203]\n"
"v_mfma_f32_16x16x32_bf16 v[200:203],v[68:71],v[220:223],v[200:203]\n"
"v_mfma_f32_16x16x32_bf16 v[204:207],v[136:139],v[216:219],v[204:207]\n"
"v_mfma_f32_16x16x32_bf16 v[204:207],v[140:143],v[220:223],v[204:207]\n"
"s_setprio 0\n"
"s_setprio 3\n"
"ds_read_b64 v[210:211],v255 offset:1024\n"
"ds_read_b64 v[208:209],v255 offset:52224\n"
"s_setprio 0\n"
"s_waitcnt lgkmcnt(0)\n"
"v_mul_f32 v216,v210,v192\n"
"v_mul_f32 v217,v210,v200\n"
"v_fma_f32 v216,-v211,v200,v216\n"
"v_fmac_f32 v217,v211,v192\n"
"v_mul_f32 v218,v210,v193\n"
"v_mul_f32 v219,v210,v201\n"
"v_fma_f32 v218,-v211,v201,v218\n"
"v_fmac_f32 v219,v211,v193\n"
"v_mul_f32 v220,v210,v194\n"
"v_mul_f32 v221,v210,v202\n"
"v_fma_f32 v220,-v211,v202,v220\n"
"v_fmac_f32 v221,v211,v194\n"
"v_mul_f32 v222,v210,v195\n"
"v_mul_f32 v223,v210,v203\n"
"v_fma_f32 v222,-v211,v203,v222\n"
"s_add_u32 m0,s73,36864\n"
"s_add_u32 s74,s81,0\n"
"buffer_load_dwordx4 v251,s[24:27],s74 offen lds\n"
"v_fmac_f32 v223,v211,v195\n"
"v_mul_f32 v224,v210,v196\n"
"v_mul_f32 v225,v210,v204\n"
"v_fma_f32 v224,-v211,v204,v224\n"
"v_fmac_f32 v225,v211,v196\n"
"v_mul_f32 v226,v210,v197\n"
"v_mul_f32 v227,v210,v205\n"
"v_fma_f32 v226,-v211,v205,v226\n"
"v_fmac_f32 v227,v211,v197\n"
"v_mul_f32 v228,v210,v198\n"
"v_mul_f32 v229,v210,v206\n"
"v_fma_f32 v228,-v211,v206,v228\n"
"v_fmac_f32 v229,v211,v198\n"
"v_mul_f32 v230,v210,v199\n"
"v_mul_f32 v231,v210,v207\n"
"v_fma_f32 v230,-v211,v207,v230\n"
"v_fmac_f32 v231,v211,v199\n"
"v_cvt_pk_bf16_f32 v176,v216,v217\n"
"s_addk_i32 m0,8192\n"
"s_addk_i32 s74,128\n"
"buffer_load_dwordx4 v251,s[24:27],s74 offen lds\n"
"v_cvt_pk_bf16_f32 v177,v218,v219\n"
"v_cvt_pk_bf16_f32 v178,v220,v221\n"
"v_cvt_pk_bf16_f32 v179,v222,v223\n"
"v_cvt_pk_bf16_f32 v180,v224,v225\n"
"v_cvt_pk_bf16_f32 v181,v226,v227\n"
"v_cvt_pk_bf16_f32 v182,v228,v229\n"
"v_cvt_pk_bf16_f32 v183,v230,v231\n"
"v_fma_f32 v218,v208,v216,v218\n"
"v_fma_f32 v219,v208,v217,v219\n"
"v_fma_f32 v218,-v209,v217,v218\n"
"v_fmac_f32 v219,v209,v216\n"
"v_fma_f32 v220,v208,v218,v220\n"
"v_fma_f32 v221,v208,v219,v221\n"
"v_fma_f32 v220,-v209,v219,v220\n"
"v_fmac_f32 v221,v209,v218\n"
"v_fma_f32 v222,v208,v220,v222\n"
"v_fma_f32 v223,v208,v221,v223\n"
"v_fma_f32 v222,-v209,v221,v222\n"
"s_addk_i32 m0,8192\n"
"s_addk_i32 s74,-128\n"
"buffer_load_dwordx4 v251,s[28:31],s74 offen lds\n"
"v_fmac_f32 v223,v209,v220\n"
"v_fma_f32 v224,v208,v222,v224\n"
"v_fma_f32 v225,v208,v223,v225\n"
"v_fma_f32 v224,-v209,v223,v224\n"
"v_fmac_f32 v225,v209,v222\n"
"v_fma_f32 v226,v208,v224,v226\n"
"v_fma_f32 v227,v208,v225,v227\n"
"v_fma_f32 v226,-v209,v225,v226\n"
"v_fmac_f32 v227,v209,v224\n"
"v_fma_f32 v228,v208,v226,v228\n"
"v_fma_f32 v229,v208,v227,v229\n"
"v_fma_f32 v228,-v209,v227,v228\n"
"v_fmac_f32 v229,v209,v226\n"
"v_fma_f32 v230,v208,v228,v230\n"
"v_fma_f32 v231,v208,v229,v231\n"
"v_fma_f32 v230,-v209,v229,v230\n"
"v_fmac_f32 v231,v209,v228\n"
"s_setprio 3\n"
"ds_write_b64 v253,v[230:231] offset:16384\n"
"s_setprio 0\n"
"s_addk_i32 m0,8192\n"
"s_addk_i32 s74,128\n"
"buffer_load_dwordx4 v251,s[28:31],s74 offen lds\n"
"s_nop 0\n"
"s_waitcnt lgkmcnt(0)\n"
"s_setprio 0\n"
"s_barrier\n"
"v_lshlrev_b32 v0,16,v144\n"
"v_and_b32 v1,0xffff0000,v144\n"
"v_lshlrev_b32 v2,16,v145\n"
"v_and_b32 v3,0xffff0000,v145\n"
"v_lshlrev_b32 v4,16,v146\n"
"v_and_b32 v5,0xffff0000,v146\n"
"v_lshlrev_b32 v6,16,v147\n"
"v_and_b32 v7,0xffff0000,v147\n"
"v_lshlrev_b32 v8,16,v148\n"
"v_and_b32 v9,0xffff0000,v148\n"
"v_lshlrev_b32 v10,16,v149\n"
"v_and_b32 v11,0xffff0000,v149\n"
"v_lshlrev_b32 v12,16,v150\n"
"v_and_b32 v13,0xffff0000,v150\n"
"v_lshlrev_b32 v14,16,v151\n"
"v_and_b32 v15,0xffff0000,v151\n"
"v_lshlrev_b32 v16,16,v152\n"
"v_and_b32 v17,0xffff0000,v152\n"
"v_lshlrev_b32 v18,16,v153\n"
"v_and_b32 v19,0xffff0000,v153\n"
"v_lshlrev_b32 v20,16,v154\n"
"v_and_b32 v21,0xffff0000,v154\n"
"v_lshlrev_b32 v22,16,v155\n"
"v_and_b32 v23,0xffff0000,v155\n"
"v_lshlrev_b32 v24,16,v156\n"
"v_and_b32 v25,0xffff0000,v156\n"
"v_lshlrev_b32 v26,16,v157\n"
"v_and_b32 v27,0xffff0000,v157\n"
"v_lshlrev_b32 v28,16,v158\n"
"v_and_b32 v29,0xffff0000,v158\n"
"v_lshlrev_b32 v30,16,v159\n"
"v_and_b32 v31,0xffff0000,v159\n"
"v_lshlrev_b32 v32,16,v160\n"
"v_and_b32 v33,0xffff0000,v160\n"
"v_lshlrev_b32 v34,16,v161\n"
"v_and_b32 v35,0xffff0000,v161\n"
"v_lshlrev_b32 v36,16,v162\n"
"v_and_b32 v37,0xffff0000,v162\n"
"v_lshlrev_b32 v38,16,v163\n"
"v_and_b32 v39,0xffff0000,v163\n"
"v_lshlrev_b32 v40,16,v164\n"
"v_and_b32 v41,0xffff0000,v164\n"
"v_lshlrev_b32 v42,16,v165\n"
"v_and_b32 v43,0xffff0000,v165\n"
"v_lshlrev_b32 v44,16,v166\n"
"v_and_b32 v45,0xffff0000,v166\n"
"v_lshlrev_b32 v46,16,v167\n"
"v_and_b32 v47,0xffff0000,v167\n"
"v_lshlrev_b32 v48,16,v168\n"
"v_and_b32 v49,0xffff0000,v168\n"
"v_lshlrev_b32 v50,16,v169\n"
"v_and_b32 v51,0xffff0000,v169\n"
"v_lshlrev_b32 v52,16,v170\n"
"v_and_b32 v53,0xffff0000,v170\n"
"v_lshlrev_b32 v54,16,v171\n"
"v_and_b32 v55,0xffff0000,v171\n"
"v_lshlrev_b32 v56,16,v172\n"
"v_and_b32 v57,0xffff0000,v172\n"
"v_lshlrev_b32 v58,16,v173\n"
"v_and_b32 v59,0xffff0000,v173\n"
"v_lshlrev_b32 v60,16,v174\n"
"v_and_b32 v61,0xffff0000,v174\n"
"v_lshlrev_b32 v62,16,v175\n"
"v_and_b32 v63,0xffff0000,v175\n"
"v_lshlrev_b32 v224,16,v176\n"
"v_and_b32 v225,0xffff0000,v176\n"
"v_lshlrev_b32 v226,16,v177\n"
"v_and_b32 v227,0xffff0000,v177\n"
"v_lshlrev_b32 v228,16,v178\n"
"v_and_b32 v229,0xffff0000,v178\n"
"v_lshlrev_b32 v230,16,v179\n"
"v_and_b32 v231,0xffff0000,v179\n"
"v_lshlrev_b32 v232,16,v180\n"
"v_and_b32 v233,0xffff0000,v180\n"
"v_lshlrev_b32 v234,16,v181\n"
"v_and_b32 v235,0xffff0000,v181\n"
"v_lshlrev_b32 v236,16,v182\n"
"v_and_b32 v237,0xffff0000,v182\n"
"v_lshlrev_b32 v238,16,v183\n"
"v_and_b32 v239,0xffff0000,v183\n"
"v_and_b32 v242,15,v254\n"
"v_bfe_u32 v241,v254,4,1\n"
"v_lshlrev_b32 v241,3,v241\n"
"v_lshl_add_u32 v240,v242,7,v241\n"
"v_lshl_add_u32 v242,v242,5,v241\n"
"v_add_u32 v240,36864,v240\n"
"v_lshrrev_b32 v241,5,v254\n"
"v_bfe_u32 v243,v254,3,1\n"
"v_xor_b32 v243,v243,v241\n"
"v_lshl_add_u32 v148,v243,4,v242\n"
"v_add_u32 v148,0,v148\n"
"v_bfe_u32 v243,v254,1,3\n"
"v_xor_b32 v241,v241,v243\n"
"v_lshl_add_u32 v144,v241,4,v240\n"
"v_xor_b32 v145,32,v144\n"
"v_xor_b32 v146,64,v144\n"
"v_xor_b32 v147,96,v144\n"
"v_lshrrev_b32 v241,4,v254\n"
"v_bfe_u32 v242,v254,2,2\n"
"v_xor_b32 v242,v242,v241\n"
"v_and_b32 v240,15,v254\n"
"v_lshl_add_u32 v240,v240,2,v242\n"
"v_lshlrev_b32 v240,3,v240\n"
"s_mul_i32 s75,s71,18432\n"
"s_add_u32 s75,s75,69632\n"
"v_add_u32 v240,s75,v240\n"
"v_xor_b32 v241,v241,v254\n"
"v_lshlrev_b32 v241,3,v241\n"
"v_add_u32 v241,s75,v241\n"
"s_waitcnt lgkmcnt(0)\n"
"s_setprio 0\n"
"s_barrier\n"
"ds_read_b64 v[208:209],v255 offset:51200\n"
"ds_read_b64 v[210:211],v253\n"
"ds_read_b64 v[214:215],v255 offset:51456\n"
"ds_read_b64 v[216:217],v253 offset:4096\n"
"s_waitcnt lgkmcnt(2)\n"
"v_xor_b32 v212,0x80000000,v209\n"
"v_fmac_f32 v0,v208,v210\n"
"v_fmac_f32 v1,v208,v211\n"
"v_fmac_f32 v0,v212,v211\n"
"v_fmac_f32 v1,v209,v210\n"
"v_fmac_f32 v2,v208,v0\n"
"v_fmac_f32 v3,v208,v1\n"
"v_fmac_f32 v2,v212,v1\n"
"v_fmac_f32 v3,v209,v0\n"
"v_fmac_f32 v4,v208,v2\n"
"v_fmac_f32 v5,v208,v3\n"
"v_fmac_f32 v4,v212,v3\n"
"v_fmac_f32 v5,v209,v2\n"
"v_fmac_f32 v6,v208,v4\n"
"v_fmac_f32 v7,v208,v5\n"
"v_fmac_f32 v6,v212,v5\n"
"v_fmac_f32 v7,v209,v4\n"
"v_cvt_pk_bf16_f32 v64,v0,v2\n"
"v_cvt_pk_bf16_f32 v65,v4,v6\n"
"ds_write_b64 v240,v[64:65]\n"
"v_cvt_pk_bf16_f32 v66,-v1,-v3\n"
"v_cvt_pk_bf16_f32 v67,-v5,-v7\n"
"ds_write_b64 v240,v[66:67] offset:512\n"
"v_fmac_f32 v8,v208,v6\n"
"v_fmac_f32 v9,v208,v7\n"
"v_fmac_f32 v8,v212,v7\n"
"v_fmac_f32 v9,v209,v6\n"
"v_fmac_f32 v10,v208,v8\n"
"v_fmac_f32 v11,v208,v9\n"
"v_fmac_f32 v10,v212,v9\n"
"v_fmac_f32 v11,v209,v8\n"
"v_fmac_f32 v12,v208,v10\n"
"v_fmac_f32 v13,v208,v11\n"
"v_fmac_f32 v12,v212,v11\n"
"v_fmac_f32 v13,v209,v10\n"
"v_fmac_f32 v14,v208,v12\n"
"v_fmac_f32 v15,v208,v13\n"
"v_fmac_f32 v14,v212,v13\n"
"v_fmac_f32 v15,v209,v12\n"
"v_cvt_pk_bf16_f32 v68,v8,v10\n"
"v_cvt_pk_bf16_f32 v69,v12,v14\n"
"ds_write_b64 v240,v[68:69] offset:1024\n"
"v_cvt_pk_bf16_f32 v70,-v9,-v11\n"
"v_cvt_pk_bf16_f32 v71,-v13,-v15\n"
"ds_write_b64 v240,v[70:71] offset:1536\n"
"ds_read_b64_tr_b16 v[72:73],v241\n"
"ds_read_b64_tr_b16 v[74:75],v241 offset:512\n"
"ds_read_b64_tr_b16 v[76:77],v241 offset:1024\n"
"ds_read_b64_tr_b16 v[78:79],v241 offset:1536\n"
"ds_read_b64 v[208:209],v255 offset:51712\n"
"ds_read_b64 v[210:211],v253 offset:8192\n"
"s_waitcnt lgkmcnt(10)\n"
"v_xor_b32 v218,0x80000000,v215\n"
"v_fmac_f32 v16,v214,v216\n"
"v_fmac_f32 v17,v214,v217\n"
"v_fmac_f32 v16,v218,v217\n"
"v_fmac_f32 v17,v215,v216\n"
"v_fmac_f32 v18,v214,v16\n"
"v_fmac_f32 v19,v214,v17\n"
"v_fmac_f32 v18,v218,v17\n"
".Lk_pads6:\n"
"s_cbranch_execz .Lk_hop_rets\n"
"v_fmac_f32 v19,v215,v16\n"
"v_fmac_f32 v20,v214,v18\n"
"v_fmac_f32 v21,v214,v19\n"
"v_fmac_f32 v20,v218,v19\n"
"v_fmac_f32 v21,v215,v18\n"
"v_fmac_f32 v22,v214,v20\n"
"v_fmac_f32 v23,v214,v21\n"
"v_fmac_f32 v22,v218,v21\n"
"v_fmac_f32 v23,v215,v20\n"
"v_cvt_pk_bf16_f32 v64,v16,v18\n"
"v_cvt_pk_bf16_f32 v65,v20,v22\n"
"ds_write_b64 v240,v[64:65] offset:4096\n"
"v_cvt_pk_bf16_f32 v66,-v17,-v19\n"
"v_cvt_pk_bf16_f32 v67,-v21,-v23\n"
"ds_write_b64 v240,v[66:67] offset:4608\n"
"v_fmac_f32 v24,v214,v22\n"
"v_fmac_f32 v25,v214,v23\n"
"v_fmac_f32 v24,v218,v23\n"
"v_fmac_f32 v25,v215,v22\n"
"v_fmac_f32 v26,v214,v24\n"
"v_fmac_f32 v27,v214,v25\n"
"v_fmac_f32 v26,v218,v25\n"
"v_fmac_f32 v27,v215,v24\n"
"v_fmac_f32 v28,v214,v26\n"
"v_fmac_f32 v29,v214,v27\n"
"v_fmac_f32 v28,v218,v27\n"
"v_fmac_f32 v29,v215,v26\n"
"v_fmac_f32 v30,v214,v28\n"
"v_fmac_f32 v31,v214,v29\n"
"v_fmac_f32 v30,v218,v29\n"
"v_fmac_f32 v31,v215,v28\n"
"v_cvt_pk_bf16_f32 v68,v24,v26\n"
"v_cvt_pk_bf16_f32 v69,v28,v30\n"
"ds_write_b64 v240,v[68:69] offset:5120\n"
"v_cvt_pk_bf16_f32 v70,-v25,-v27\n"
"v_cvt_pk_bf16_f32 v71,-v29,-v31\n"
"ds_write_b64 v240,v[70:71] offset:5632\n"
"ds_read_b64_tr_b16 v[88:89],v241 offset:4096\n"
"ds_read_b64_tr_b16 v[90:91],v241 offset:4608\n"
"ds_read_b64_tr_b16 v[92:93],v241 offset:5120\n"
"ds_read_b64_tr_b16 v[94:95],v241 offset:5632\n"
"ds_read_b64 v[214:215],v255 offset:51968\n"
"ds_read_b64 v[216:217],v253 offset:12288\n"
"s_waitcnt lgkmcnt(10)\n"
"v_xor_b32 v212,0x80000000,v209\n"
"v_fmac_f32 v32,v208,v210\n"
"v_fmac_f32 v33,v208,v211\n"
"v_fmac_f32 v32,v212,v211\n"
"v_fmac_f32 v33,v209,v210\n"
"v_fmac_f32 v34,v208,v32\n"
"v_fmac_f32 v35,v208,v33\n"
"v_fmac_f32 v34,v212,v33\n"
"v_fmac_f32 v35,v209,v32\n"
"v_fmac_f32 v36,v208,v34\n"
"v_fmac_f32 v37,v208,v35\n"
"v_fmac_f32 v36,v212,v35\n"
"v_fmac_f32 v37,v209,v34\n"
"v_fmac_f32 v38,v208,v36\n"
"v_fmac_f32 v39,v208,v37\n"
"v_fmac_f32 v38,v212,v37\n"
"v_fmac_f32 v39,v209,v36\n"
"v_cvt_pk_bf16_f32 v64,v32,v34\n"
"v_cvt_pk_bf16_f32 v65,v36,v38\n"
"ds_write_b64 v240,v[64:65] offset:8192\n"
"v_cvt_pk_bf16_f32 v66,-v33,-v35\n"
"v_cvt_pk_bf16_f32 v67,-v37,-v39\n"
"ds_write_b64 v240,v[66:67] offset:8704\n"
"v_fmac_f32 v40,v208,v38\n"
"v_fmac_f32 v41,v208,v39\n"
"v_fmac_f32 v40,v212,v39\n"
"v_fmac_f32 v41,v209,v38\n"
"v_fmac_f32 v42,v208,v40\n"
"v_fmac_f32 v43,v208,v41\n"
"v_fmac_f32 v42,v212,v41\n"
"v_fmac_f32 v43,v209,v40\n"
"v_fmac_f32 v44,v208,v42\n"
"v_fmac_f32 v45,v208,v43\n"
"v_fmac_f32 v44,v212,v43\n"
"v_fmac_f32 v45,v209,v42\n"
"v_fmac_f32 v46,v208,v44\n"
"v_fmac_f32 v47,v208,v45\n"
"v_fmac_f32 v46,v212,v45\n"
"v_fmac_f32 v47,v209,v44\n"
"v_cvt_pk_bf16_f32 v68,v40,v42\n"
"v_cvt_pk_bf16_f32 v69,v44,v46\n"
"ds_write_b64 v240,v[68:69] offset:9216\n"
"v_cvt_pk_bf16_f32 v70,-v41,-v43\n"
"v_cvt_pk_bf16_f32 v71,-v45,-v47\n"
"ds_write_b64 v240,v[70:71] offset:9728\n"
"ds_read_b64_tr_b16 v[104:105],v241 offset:8192\n"
"ds_read_b64_tr_b16 v[106:107],v241 offset:8704\n"
"ds_read_b64_tr_b16 v[108:109],v241 offset:9216\n"
"ds_read_b64_tr_b16 v[110:111],v241 offset:9728\n"
"ds_read_b64 v[208:209],v255 offset:52224\n"
"ds_read_b64 v[210:211],v253 offset:16384\n"
"s_waitcnt lgkmcnt(10)\n"
"v_xor_b32 v218,0x80000000,v215\n"
"v_fmac_f32 v48,v214,v216\n"
"v_fmac_f32 v49,v214,v217\n"
"v_fmac_f32 v48,v218,v217\n"
"v_fmac_f32 v49,v215,v216\n"
"v_fmac_f32 v50,v214,v48\n"
"v_fmac_f32 v51,v214,v49\n"
"v_fmac_f32 v50,v218,v49\n"
"v_fmac_f32 v51,v215,v48\n"
"v_fmac_f32 v52,v214,v50\n"
"v_fmac_f32 v53,v214,v51\n"
"v_fmac_f32 v52,v218,v51\n"
"v_fmac_f32 v53,v215,v50\n"
"v_fmac_f32 v54,v214,v52\n"
"v_fmac_f32 v55,v214,v53\n"
"v_fmac_f32 v54,v218,v53\n"
"v_fmac_f32 v55,v215,v52\n"
"v_cvt_pk_bf16_f32 v64,v48,v50\n"
"v_cvt_pk_bf16_f32 v65,v52,v54\n"
"ds_write_b64 v240,v[64:65] offset:12288\n"
"v_cvt_pk_bf16_f32 v66,-v49,-v51\n"
"v_cvt_pk_bf16_f32 v67,-v53,-v55\n"
"ds_write_b64 v240,v[66:67] offset:12800\n"
"v_fmac_f32 v56,v214,v54\n"
"v_fmac_f32 v57,v214,v55\n"
"v_fmac_f32 v56,v218,v55\n"
"v_fmac_f32 v57,v215,v54\n"
"v_fmac_f32 v58,v214,v56\n"
"v_fmac_f32 v59,v214,v57\n"
"v_fmac_f32 v58,v218,v57\n"
"v_fmac_f32 v59,v215,v56\n"
"v_fmac_f32 v60,v214,v58\n"
"v_fmac_f32 v61,v214,v59\n"
"v_fmac_f32 v60,v218,v59\n"
"v_fmac_f32 v61,v215,v58\n"
"v_fmac_f32 v62,v214,v60\n"
"v_fmac_f32 v63,v214,v61\n"
"v_fmac_f32 v62,v218,v61\n"
"v_fmac_f32 v63,v215,v60\n"
"v_cvt_pk_bf16_f32 v68,v56,v58\n"
"v_cvt_pk_bf16_f32 v69,v60,v62\n"
"ds_write_b64 v240,v[68:69] offset:13312\n"
"v_cvt_pk_bf16_f32 v70,-v57,-v59\n"
"v_cvt_pk_bf16_f32 v71,-v61,-v63\n"
"ds_write_b64 v240,v[70:71] offset:13824\n"
"ds_read_b64_tr_b16 v[120:121],v241 offset:12288\n"
"ds_read_b64_tr_b16 v[122:123],v241 offset:12800\n"
"ds_read_b64_tr_b16 v[124:125],v241 offset:13312\n"
"ds_read_b64_tr_b16 v[126:127],v241 offset:13824\n"
"s_waitcnt lgkmcnt(8)\n"
"v_xor_b32 v212,0x80000000,v209\n"
"v_fmac_f32 v224,v208,v210\n"
"v_fmac_f32 v225,v208,v211\n"
"v_fmac_f32 v224,v212,v211\n"
"v_fmac_f32 v225,v209,v210\n"
"v_fmac_f32 v226,v208,v224\n"
"v_fmac_f32 v227,v208,v225\n"
"v_fmac_f32 v226,v212,v225\n"
"v_fmac_f32 v227,v209,v224\n"
"v_fmac_f32 v228,v208,v226\n"
"v_fmac_f32 v229,v208,v227\n"
"v_fmac_f32 v228,v212,v227\n"
"v_fmac_f32 v229,v209,v226\n"
"v_fmac_f32 v230,v208,v228\n"
"v_fmac_f32 v231,v208,v229\n"
"v_fmac_f32 v230,v212,v229\n"
"v_fmac_f32 v231,v209,v228\n"
"v_cvt_pk_bf16_f32 v64,v224,v226\n"
"v_cvt_pk_bf16_f32 v65,v228,v230\n"
"ds_write_b64 v240,v[64:65] offset:16384\n"
"v_cvt_pk_bf16_f32 v66,-v225,-v227\n"
"v_cvt_pk_bf16_f32 v67,-v229,-v231\n"
"ds_write_b64 v240,v[66:67] offset:16896\n"
"v_fmac_f32 v232,v208,v230\n"
"v_fmac_f32 v233,v208,v231\n"
"v_fmac_f32 v232,v212,v231\n"
"v_fmac_f32 v233,v209,v230\n"
"v_fmac_f32 v234,v208,v232\n"
"v_fmac_f32 v235,v208,v233\n"
"v_fmac_f32 v234,v212,v233\n"
"v_fmac_f32 v235,v209,v232\n"
"v_fmac_f32 v236,v208,v234\n"
"v_fmac_f32 v237,v208,v235\n"
"v_fmac_f32 v236,v212,v235\n"
"v_fmac_f32 v237,v209,v234\n"
"v_fmac_f32 v238,v208,v236\n"
"v_fmac_f32 v239,v208,v237\n"
"v_fmac_f32 v238,v212,v237\n"
"v_fmac_f32 v239,v209,v236\n"
"v_cvt_pk_bf16_f32 v68,v232,v234\n"
"v_cvt_pk_bf16_f32 v69,v236,v238\n"
"ds_write_b64 v240,v[68:69] offset:17408\n"
"v_cvt_pk_bf16_f32 v70,-v233,-v235\n"
"v_cvt_pk_bf16_f32 v71,-v237,-v239\n"
"ds_write_b64 v240,v[70:71] offset:17920\n"
"ds_read_b64_tr_b16 v[136:137],v241 offset:16384\n"
"ds_read_b64_tr_b16 v[138:139],v241 offset:16896\n"
"ds_read_b64_tr_b16 v[140:141],v241 offset:17408\n"
"ds_read_b64_tr_b16 v[142:143],v241 offset:17920\n"
"s_waitcnt lgkmcnt(0)\n"
"s_waitcnt vmcnt(0)\n"
"s_barrier\n"
"ds_read_b64_tr_b16 v[80:81],v241 offset:2048\n"
"ds_read_b64_tr_b16 v[82:83],v241 offset:2560\n"
"ds_read_b64_tr_b16 v[84:85],v241 offset:3072\n"
"ds_read_b64_tr_b16 v[86:87],v241 offset:3584\n"
"ds_read_b64_tr_b16 v[96:97],v241 offset:6144\n"
"ds_read_b64_tr_b16 v[98:99],v241 offset:6656\n"
"ds_read_b64_tr_b16 v[100:101],v241 offset:7168\n"
"ds_read_b64_tr_b16 v[102:103],v241 offset:7680\n"
"ds_read_b64_tr_b16 v[112:113],v241 offset:10240\n"
"ds_read_b64_tr_b16 v[114:115],v241 offset:10752\n"
"ds_read_b64_tr_b16 v[116:117],v241 offset:11264\n"
"ds_read_b64_tr_b16 v[118:119],v241 offset:11776\n"
"ds_read_b64_tr_b16 v[128:129],v241 offset:14336\n"
"ds_read_b64_tr_b16 v[130:131],v241 offset:14848\n"
"ds_read_b64_tr_b16 v[132:133],v241 offset:15360\n"
"ds_read_b64_tr_b16 v[134:135],v241 offset:15872\n"
"ds_read_b64 v[208:209],v144\n"
"ds_read_b64 v[210:211],v144 offset:16384\n"
"ds_read_b64 v[212:213],v145\n"
"ds_read_b64 v[214:215],v145 offset:16384\n"
"ds_read_b64 v[216:217],v146\n"
"ds_read_b64 v[218:219],v146 offset:16384\n"
"ds_read_b64 v[220:221],v147\n"
"ds_read_b64 v[222:223],v147 offset:16384\n"
"ds_read_b64 v[224:225],v144 offset:8192\n"
"ds_read_b64 v[226:227],v144 offset:24576\n"
"ds_read_b64 v[228:229],v145 offset:8192\n"
"ds_read_b64 v[230:231],v145 offset:24576\n"
"s_waitcnt lgkmcnt(12)\n"
"s_barrier\n"
"s_setprio 1\n"
"s_waitcnt lgkmcnt(8)\n"
"v_mfma_f32_16x16x32_bf16 v[0:3],v[208:211],v[72:75],0\n"
"ds_read_b64 v[232:233],v146 offset:8192\n"
"v_mfma_f32_16x16x32_bf16 v[0:3],v[212:215],v[80:83],v[0:3]\n"
"ds_read_b64 v[234:235],v146 offset:24576\n"
"v_mfma_f32_16x16x32_bf16 v[8:11],v[208:211],v[76:79],0\n"
"ds_read_b64 v[236:237],v147 offset:8192\n"
"v_mfma_f32_16x16x32_bf16 v[8:11],v[212:215],v[84:87],v[8:11]\n"
"ds_read_b64 v[238:239],v147 offset:24576\n"
"s_waitcnt lgkmcnt(8)\n"
"v_mfma_f32_16x16x32_bf16 v[8:11],v[216:219],v[92:95],v[8:11]\n"
"ds_read_b64 v[208:209],v148\n"
"v_mfma_f32_16x16x32_bf16 v[8:11],v[220:223],v[100:103],v[8:11]\n"
"ds_read_b64 v[210:211],v148 offset:18432\n"
"s_setprio 2\n"
"v_mfma_f32_16x16x32_bf16 v[0:3],v[216:219],v[88:91],v[0:3]\n"
"ds_read_b64 v[212:213],v144 offset:2048\n"
"v_mfma_f32_16x16x32_bf16 v[0:3],v[220:223],v[96:99],v[0:3]\n"
"ds_read_b64 v[214:215],v144 offset:18432\n"
"s_waitcnt lgkmcnt(8)\n"
"v_mfma_f32_16x16x32_bf16 v[0:3],v[224:227],v[104:107],v[0:3]\n"
"ds_read_b64 v[216:217],v145 offset:2048\n"
"v_mfma_f32_16x16x32_bf16 v[0:3],v[228:231],v[112:115],v[0:3]\n"
"ds_read_b64 v[218:219],v145 offset:18432\n"
"v_mfma_f32_16x16x32_bf16 v[8:11],v[224:227],v[108:111],v[8:11]\n"
"ds_read_b64 v[220:221],v146 offset:2048\n"
"v_mfma_f32_16x16x32_bf16 v[8:11],v[228:231],v[116:119],v[8:11]\n"
"ds_read_b64 v[222:223],v146 offset:18432\n"
"s_waitcnt lgkmcnt(8)\n"
"v_mfma_f32_16x16x32_bf16 v[8:11],v[232:235],v[124:127],v[8:11]\n"
"ds_read_b64 v[224:225],v147 offset:2048\n"
"v_mfma_f32_16x16x32_bf16 v[8:11],v[236:239],v[132:135],v[8:11]\n"
"ds_read_b64 v[226:227],v147 offset:18432\n"
"v_mfma_f32_16x16x32_bf16 v[0:3],v[232:235],v[120:123],v[0:3]\n"
"ds_read_b64 v[228:229],v144 offset:10240\n"
"v_mfma_f32_16x16x32_bf16 v[0:3],v[236:239],v[128:131],v[0:3]\n"
"ds_read_b64 v[230:231],v144 offset:26624\n"
"s_waitcnt lgkmcnt(8)\n"
"v_mfma_f32_16x16x32_bf16 v[0:3],v[208:211],v[136:139],v[0:3]\n"
"ds_read_b64 v[232:233],v145 offset:10240\n"
"v_mfma_f32_16x16x32_bf16 v[8:11],v[208:211],v[140:143],v[8:11]\n"
"ds_read_b64 v[234:235],v145 offset:26624\n"
"v_mfma_f32_16x16x32_bf16 v[4:7],v[212:215],v[72:75],0\n"
"ds_read_b64 v[236:237],v146 offset:10240\n"
"s_waitcnt lgkmcnt(7)\n"
"v_mfma_f32_16x16x32_bf16 v[4:7],v[216:219],v[80:83],v[4:7]\n"
"ds_read_b64 v[238:239],v146 offset:26624\n"
"v_mfma_f32_16x16x32_bf16 v[12:15],v[212:215],v[76:79],0\n"
"ds_read_b64 v[208:209],v147 offset:10240\n"
"v_mfma_f32_16x16x32_bf16 v[12:15],v[216:219],v[84:87],v[12:15]\n"
"ds_read_b64 v[210:211],v147 offset:26624\n"
"v_mfma_f32_16x16x32_bf16 v[12:15],v[220:223],v[92:95],v[12:15]\n"
"ds_read_b64 v[212:213],v148 offset:512\n"
"s_waitcnt lgkmcnt(7)\n"
"v_mfma_f32_16x16x32_bf16 v[12:15],v[224:227],v[100:103],v[12:15]\n"
"ds_read_b64 v[214:215],v148 offset:18944\n"
"v_mfma_f32_16x16x32_bf16 v[4:7],v[220:223],v[88:91],v[4:7]\n"
"s_barrier\n"
"v_mfma_f32_16x16x32_bf16 v[4:7],v[224:227],v[96:99],v[4:7]\n"
"v_mfma_f32_16x16x32_bf16 v[4:7],v[228:231],v[104:107],v[4:7]\n"
"s_waitcnt lgkmcnt(4)\n"
"v_mfma_f32_16x16x32_bf16 v[4:7],v[232:235],v[112:115],v[4:7]\n"
"v_mfma_f32_16x16x32_bf16 v[12:15],v[228:231],v[108:111],v[12:15]\n"
"v_mfma_f32_16x16x32_bf16 v[12:15],v[232:235],v[116:119],v[12:15]\n"
"v_mfma_f32_16x16x32_bf16 v[12:15],v[236:239],v[124:127],v[12:15]\n"
"s_waitcnt lgkmcnt(0)\n"
"v_mfma_f32_16x16x32_bf16 v[12:15],v[208:211],v[132:135],v[12:15]\n"
"v_mfma_f32_16x16x32_bf16 v[4:7],v[236:239],v[120:123],v[4:7]\n"
"v_mfma_f32_16x16x32_bf16 v[4:7],v[208:211],v[128:131],v[4:7]\n"
"v_mfma_f32_16x16x32_bf16 v[4:7],v[212:215],v[136:139],v[4:7]\n"
"v_mfma_f32_16x16x32_bf16 v[12:15],v[212:215],v[140:143],v[12:15]\n"
"s_setprio 0\n"
"v_add_u32 v144,32768,v144\n"
"v_add_u32 v145,32768,v145\n"
"v_add_u32 v146,32768,v146\n"
"v_add_u32 v147,32768,v147\n"
"s_add_u32 m0,s73,102400\n"
"s_add_u32 s74,s81,442368\n"
"buffer_load_dwordx4 v251,s[24:27],s74 offen lds\n"
"s_addk_i32 m0,8192\n"
"s_addk_i32 s74,128\n"
"buffer_load_dwordx4 v251,s[24:27],s74 offen lds\n"
"s_addk_i32 m0,8192\n"
"s_addk_i32 s74,-128\n"
"buffer_load_dwordx4 v251,s[28:31],s74 offen lds\n"
"s_addk_i32 m0,8192\n"
"s_addk_i32 s74,128\n"
"buffer_load_dwordx4 v251,s[28:31],s74 offen lds\n"
"v_cvt_pk_bf16_f32 v16,v0,v1\n"
"v_cvt_pk_bf16_f32 v17,v2,v3\n"
"v_cvt_pk_bf16_f32 v18,v4,v5\n"
"v_cvt_pk_bf16_f32 v19,v6,v7\n"
"s_add_u32 s78,s87,0\n"
"buffer_store_dwordx4 v[16:19],v252,s[32:35],s78 offen sc1\n"
"v_cvt_pk_bf16_f32 v20,v8,v9\n"
"v_cvt_pk_bf16_f32 v21,v10,v11\n"
"v_cvt_pk_bf16_f32 v22,v12,v13\n"
"v_cvt_pk_bf16_f32 v23,v14,v15\n"
"s_add_u32 s78,s87,18432\n"
"buffer_store_dwordx4 v[20:23],v252,s[32:35],s78 offen sc1\n"
"s_setprio 3\n"
"s_barrier\n"
"ds_read_b64 v[208:209],v144\n"
"ds_read_b64 v[210:211],v144 offset:16384\n"
"ds_read_b64 v[212:213],v145\n"
"ds_read_b64 v[214:215],v145 offset:16384\n"
"ds_read_b64 v[216:217],v146\n"
"ds_read_b64 v[218:219],v146 offset:16384\n"
"ds_read_b64 v[220:221],v147\n"
"ds_read_b64 v[222:223],v147 offset:16384\n"
"ds_read_b64 v[224:225],v144 offset:8192\n"
"ds_read_b64 v[226:227],v144 offset:24576\n"
"ds_read_b64 v[228:229],v145 offset:8192\n"
"ds_read_b64 v[230:231],v145 offset:24576\n"
"s_setprio 1\n"
"s_waitcnt lgkmcnt(8)\n"
"v_mfma_f32_16x16x32_bf16 v[0:3],v[208:211],v[72:75],0\n"
"ds_read_b64 v[232:233],v146 offset:8192\n"
"v_mfma_f32_16x16x32_bf16 v[0:3],v[212:215],v[80:83],v[0:3]\n"
"ds_read_b64 v[234:235],v146 offset:24576\n"
"v_mfma_f32_16x16x32_bf16 v[8:11],v[208:211],v[76:79],0\n"
"ds_read_b64 v[236:237],v147 offset:8192\n"
"v_mfma_f32_16x16x32_bf16 v[8:11],v[212:215],v[84:87],v[8:11]\n"
"ds_read_b64 v[238:239],v147 offset:24576\n"
"s_waitcnt lgkmcnt(8)\n"
"v_mfma_f32_16x16x32_bf16 v[8:11],v[216:219],v[92:95],v[8:11]\n"
"ds_read_b64 v[208:209],v148 offset:6144\n"
"v_mfma_f32_16x16x32_bf16 v[8:11],v[220:223],v[100:103],v[8:11]\n"
"ds_read_b64 v[210:211],v148 offset:24576\n"
"s_setprio 2\n"
"v_mfma_f32_16x16x32_bf16 v[0:3],v[216:219],v[88:91],v[0:3]\n"
"ds_read_b64 v[212:213],v144 offset:2048\n"
"v_mfma_f32_16x16x32_bf16 v[0:3],v[220:223],v[96:99],v[0:3]\n"
"ds_read_b64 v[214:215],v144 offset:18432\n"
"s_waitcnt lgkmcnt(8)\n"
"v_mfma_f32_16x16x32_bf16 v[0:3],v[224:227],v[104:107],v[0:3]\n"
"ds_read_b64 v[216:217],v145 offset:2048\n"
"v_mfma_f32_16x16x32_bf16 v[0:3],v[228:231],v[112:115],v[0:3]\n"
"ds_read_b64 v[218:219],v145 offset:18432\n"
"v_mfma_f32_16x16x32_bf16 v[8:11],v[224:227],v[108:111],v[8:11]\n"
"ds_read_b64 v[220:221],v146 offset:2048\n"
"v_mfma_f32_16x16x32_bf16 v[8:11],v[228:231],v[116:119],v[8:11]\n"
"ds_read_b64 v[222:223],v146 offset:18432\n"
"s_waitcnt lgkmcnt(8)\n"
"v_mfma_f32_16x16x32_bf16 v[8:11],v[232:235],v[124:127],v[8:11]\n"
"ds_read_b64 v[224:225],v147 offset:2048\n"
"v_mfma_f32_16x16x32_bf16 v[8:11],v[236:239],v[132:135],v[8:11]\n"
"ds_read_b64 v[226:227],v147 offset:18432\n"
"v_mfma_f32_16x16x32_bf16 v[0:3],v[232:235],v[120:123],v[0:3]\n"
"ds_read_b64 v[228:229],v144 offset:10240\n"
"v_mfma_f32_16x16x32_bf16 v[0:3],v[236:239],v[128:131],v[0:3]\n"
"ds_read_b64 v[230:231],v144 offset:26624\n"
"s_waitcnt lgkmcnt(8)\n"
"v_mfma_f32_16x16x32_bf16 v[0:3],v[208:211],v[136:139],v[0:3]\n"
"ds_read_b64 v[232:233],v145 offset:10240\n"
"v_mfma_f32_16x16x32_bf16 v[8:11],v[208:211],v[140:143],v[8:11]\n"
"ds_read_b64 v[234:235],v145 offset:26624\n"
"v_mfma_f32_16x16x32_bf16 v[4:7],v[212:215],v[72:75],0\n"
"ds_read_b64 v[236:237],v146 offset:10240\n"
"s_waitcnt lgkmcnt(7)\n"
"v_mfma_f32_16x16x32_bf16 v[4:7],v[216:219],v[80:83],v[4:7]\n"
"ds_read_b64 v[238:239],v146 offset:26624\n"
"v_mfma_f32_16x16x32_bf16 v[12:15],v[212:215],v[76:79],0\n"
"ds_read_b64 v[208:209],v147 offset:10240\n"
"v_mfma_f32_16x16x32_bf16 v[12:15],v[216:219],v[84:87],v[12:15]\n"
"ds_read_b64 v[210:211],v147 offset:26624\n"
"v_mfma_f32_16x16x32_bf16 v[12:15],v[220:223],v[92:95],v[12:15]\n"
"ds_read_b64 v[212:213],v148 offset:6656\n"
"s_waitcnt lgkmcnt(7)\n"
"v_mfma_f32_16x16x32_bf16 v[12:15],v[224:227],v[100:103],v[12:15]\n"
"ds_read_b64 v[214:215],v148 offset:25088\n"
"v_mfma_f32_16x16x32_bf16 v[4:7],v[220:223],v[88:91],v[4:7]\n"
"s_barrier\n"
"v_mfma_f32_16x16x32_bf16 v[4:7],v[224:227],v[96:99],v[4:7]\n"
"v_mfma_f32_16x16x32_bf16 v[4:7],v[228:231],v[104:107],v[4:7]\n"
"s_waitcnt lgkmcnt(4)\n"
"v_mfma_f32_16x16x32_bf16 v[4:7],v[232:235],v[112:115],v[4:7]\n"
"v_mfma_f32_16x16x32_bf16 v[12:15],v[228:231],v[108:111],v[12:15]\n"
"v_mfma_f32_16x16x32_bf16 v[12:15],v[232:235],v[116:119],v[12:15]\n"
"v_mfma_f32_16x16x32_bf16 v[12:15],v[236:239],v[124:127],v[12:15]\n"
"s_waitcnt lgkmcnt(0)\n"
"v_mfma_f32_16x16x32_bf16 v[12:15],v[208:211],v[132:135],v[12:15]\n"
"v_mfma_f32_16x16x32_bf16 v[4:7],v[236:239],v[120:123],v[4:7]\n"
"v_mfma_f32_16x16x32_bf16 v[4:7],v[208:211],v[128:131],v[4:7]\n"
"v_mfma_f32_16x16x32_bf16 v[4:7],v[212:215],v[136:139],v[4:7]\n"
"v_mfma_f32_16x16x32_bf16 v[12:15],v[212:215],v[140:143],v[12:15]\n"
"s_setprio 0\n"
"v_add_u32 v144,32768,v144\n"
"v_add_u32 v145,32768,v145\n"
"v_add_u32 v146,32768,v146\n"
"v_add_u32 v147,32768,v147\n"
"s_add_u32 m0,s73,36864\n"
"s_add_u32 s74,s81,73728\n"
"buffer_load_dwordx4 v251,s[24:27],s74 offen lds\n"
"s_addk_i32 m0,8192\n"
"s_addk_i32 s74,128\n"
"buffer_load_dwordx4 v251,s[24:27],s74 offen lds\n"
"s_addk_i32 m0,8192\n"
"s_addk_i32 s74,-128\n"
"buffer_load_dwordx4 v251,s[28:31],s74 offen lds\n"
"s_addk_i32 m0,8192\n"
"s_addk_i32 s74,128\n"
"buffer_load_dwordx4 v251,s[28:31],s74 offen lds\n"
"v_cvt_pk_bf16_f32 v16,v0,v1\n"
"v_cvt_pk_bf16_f32 v17,v2,v3\n"
"v_cvt_pk_bf16_f32 v18,v4,v5\n"
"v_cvt_pk_bf16_f32 v19,v6,v7\n"
"s_add_u32 s78,s87,6144\n"
"buffer_store_dwordx4 v[16:19],v252,s[32:35],s78 offen sc1\n"
"v_cvt_pk_bf16_f32 v20,v8,v9\n"
"v_cvt_pk_bf16_f32 v21,v10,v11\n"
"v_cvt_pk_bf16_f32 v22,v12,v13\n"
"v_cvt_pk_bf16_f32 v23,v14,v15\n"
"s_add_u32 s78,s87,24576\n"
"buffer_store_dwordx4 v[20:23],v252,s[32:35],s78 offen sc1\n"
"s_waitcnt vmcnt(8)\n"
"s_setprio 3\n"
"s_barrier\n"
"ds_read_b64 v[208:209],v144\n"
"ds_read_b64 v[210:211],v144 offset:16384\n"
"ds_read_b64 v[212:213],v145\n"
"ds_read_b64 v[214:215],v145 offset:16384\n"
"ds_read_b64 v[216:217],v146\n"
"ds_read_b64 v[218:219],v146 offset:16384\n"
"ds_read_b64 v[220:221],v147\n"
"ds_read_b64 v[222:223],v147 offset:16384\n"
"ds_read_b64 v[224:225],v144 offset:8192\n"
"ds_read_b64 v[226:227],v144 offset:24576\n"
"ds_read_b64 v[228:229],v145 offset:8192\n"
"ds_read_b64 v[230:231],v145 offset:24576\n"
"s_setprio 1\n"
"s_waitcnt lgkmcnt(8)\n"
"v_mfma_f32_16x16x32_bf16 v[0:3],v[208:211],v[72:75],0\n"
"ds_read_b64 v[232:233],v146 offset:8192\n"
"v_mfma_f32_16x16x32_bf16 v[0:3],v[212:215],v[80:83],v[0:3]\n"
"ds_read_b64 v[234:235],v146 offset:24576\n"
"v_mfma_f32_16x16x32_bf16 v[8:11],v[208:211],v[76:79],0\n"
"ds_read_b64 v[236:237],v147 offset:8192\n"
"v_mfma_f32_16x16x32_bf16 v[8:11],v[212:215],v[84:87],v[8:11]\n"
"ds_read_b64 v[238:239],v147 offset:24576\n"
"s_waitcnt lgkmcnt(8)\n"
"v_mfma_f32_16x16x32_bf16 v[8:11],v[216:219],v[92:95],v[8:11]\n"
"ds_read_b64 v[208:209],v148 offset:12288\n"
"v_mfma_f32_16x16x32_bf16 v[8:11],v[220:223],v[100:103],v[8:11]\n"
"ds_read_b64 v[210:211],v148 offset:30720\n"
"s_setprio 2\n"
"v_mfma_f32_16x16x32_bf16 v[0:3],v[216:219],v[88:91],v[0:3]\n"
"ds_read_b64 v[212:213],v144 offset:2048\n"
"v_mfma_f32_16x16x32_bf16 v[0:3],v[220:223],v[96:99],v[0:3]\n"
"ds_read_b64 v[214:215],v144 offset:18432\n"
"s_waitcnt lgkmcnt(8)\n"
"v_mfma_f32_16x16x32_bf16 v[0:3],v[224:227],v[104:107],v[0:3]\n"
"ds_read_b64 v[216:217],v145 offset:2048\n"
"v_mfma_f32_16x16x32_bf16 v[0:3],v[228:231],v[112:115],v[0:3]\n"
"ds_read_b64 v[218:219],v145 offset:18432\n"
"v_mfma_f32_16x16x32_bf16 v[8:11],v[224:227],v[108:111],v[8:11]\n"
"ds_read_b64 v[220:221],v146 offset:2048\n"
"v_mfma_f32_16x16x32_bf16 v[8:11],v[228:231],v[116:119],v[8:11]\n"
"ds_read_b64 v[222:223],v146 offset:18432\n"
"s_waitcnt lgkmcnt(8)\n"
"v_mfma_f32_16x16x32_bf16 v[8:11],v[232:235],v[124:127],v[8:11]\n"
"ds_read_b64 v[224:225],v147 offset:2048\n"
"v_mfma_f32_16x16x32_bf16 v[8:11],v[236:239],v[132:135],v[8:11]\n"
"ds_read_b64 v[226:227],v147 offset:18432\n"
"v_mfma_f32_16x16x32_bf16 v[0:3],v[232:235],v[120:123],v[0:3]\n"
"ds_read_b64 v[228:229],v144 offset:10240\n"
"v_mfma_f32_16x16x32_bf16 v[0:3],v[236:239],v[128:131],v[0:3]\n"
"ds_read_b64 v[230:231],v144 offset:26624\n"
"s_waitcnt lgkmcnt(8)\n"
"v_mfma_f32_16x16x32_bf16 v[0:3],v[208:211],v[136:139],v[0:3]\n"
"ds_read_b64 v[232:233],v145 offset:10240\n"
"v_mfma_f32_16x16x32_bf16 v[8:11],v[208:211],v[140:143],v[8:11]\n"
"ds_read_b64 v[234:235],v145 offset:26624\n"
"v_mfma_f32_16x16x32_bf16 v[4:7],v[212:215],v[72:75],0\n"
"ds_read_b64 v[236:237],v146 offset:10240\n"
"s_waitcnt lgkmcnt(7)\n"
"v_mfma_f32_16x16x32_bf16 v[4:7],v[216:219],v[80:83],v[4:7]\n"
"ds_read_b64 v[238:239],v146 offset:26624\n"
"v_mfma_f32_16x16x32_bf16 v[12:15],v[212:215],v[76:79],0\n"
"ds_read_b64 v[208:209],v147 offset:10240\n"
"v_mfma_f32_16x16x32_bf16 v[12:15],v[216:219],v[84:87],v[12:15]\n"
"ds_read_b64 v[210:211],v147 offset:26624\n"
"v_mfma_f32_16x16x32_bf16 v[12:15],v[220:223],v[92:95],v[12:15]\n"
"ds_read_b64 v[212:213],v148 offset:12800\n"
"s_waitcnt lgkmcnt(7)\n"
"v_mfma_f32_16x16x32_bf16 v[12:15],v[224:227],v[100:103],v[12:15]\n"
"ds_read_b64 v[214:215],v148 offset:31232\n"
"v_mfma_f32_16x16x32_bf16 v[4:7],v[220:223],v[88:91],v[4:7]\n"
"s_barrier\n"
"v_mfma_f32_16x16x32_bf16 v[4:7],v[224:227],v[96:99],v[4:7]\n"
"v_mfma_f32_16x16x32_bf16 v[4:7],v[228:231],v[104:107],v[4:7]\n"
"s_waitcnt lgkmcnt(4)\n"
"v_mfma_f32_16x16x32_bf16 v[4:7],v[232:235],v[112:115],v[4:7]\n"
"v_mfma_f32_16x16x32_bf16 v[12:15],v[228:231],v[108:111],v[12:15]\n"
"v_mfma_f32_16x16x32_bf16 v[12:15],v[232:235],v[116:119],v[12:15]\n"
"v_mfma_f32_16x16x32_bf16 v[12:15],v[236:239],v[124:127],v[12:15]\n"
"s_waitcnt lgkmcnt(0)\n"
"v_mfma_f32_16x16x32_bf16 v[12:15],v[208:211],v[132:135],v[12:15]\n"
"v_mfma_f32_16x16x32_bf16 v[4:7],v[236:239],v[120:123],v[4:7]\n"
"v_mfma_f32_16x16x32_bf16 v[4:7],v[208:211],v[128:131],v[4:7]\n"
"v_mfma_f32_16x16x32_bf16 v[4:7],v[212:215],v[136:139],v[4:7]\n"
"v_mfma_f32_16x16x32_bf16 v[12:15],v[212:215],v[140:143],v[12:15]\n"
"s_setprio 0\n"
"v_add_u32 v144,4294901760,v144\n"
"v_add_u32 v145,4294901760,v145\n"
"v_add_u32 v146,4294901760,v146\n"
"v_add_u32 v147,4294901760,v147\n"
"s_addk_i32 m0,8192\n"
"s_add_u32 s74,s81,294912\n"
"buffer_load_dwordx4 v251,s[24:27],s74 offen lds\n"
"s_addk_i32 m0,8192\n"
"s_addk_i32 s74,128\n"
"buffer_load_dwordx4 v251,s[24:27],s74 offen lds\n"
"s_addk_i32 m0,8192\n"
"s_addk_i32 s74,-128\n"
"buffer_load_dwordx4 v251,s[28:31],s74 offen lds\n"
"s_addk_i32 m0,8192\n"
"s_addk_i32 s74,128\n"
"buffer_load_dwordx4 v251,s[28:31],s74 offen lds\n"
"v_cvt_pk_bf16_f32 v16,v0,v1\n"
"v_cvt_pk_bf16_f32 v17,v2,v3\n"
"v_cvt_pk_bf16_f32 v18,v4,v5\n"
"v_cvt_pk_bf16_f32 v19,v6,v7\n"
"s_add_u32 s78,s87,12288\n"
"buffer_store_dwordx4 v[16:19],v252,s[32:35],s78 offen sc1\n"
"v_cvt_pk_bf16_f32 v20,v8,v9\n"
"v_cvt_pk_bf16_f32 v21,v10,v11\n"
"v_cvt_pk_bf16_f32 v22,v12,v13\n"
"v_cvt_pk_bf16_f32 v23,v14,v15\n"
"s_add_u32 s78,s87,30720\n"
"buffer_store_dwordx4 v[20:23],v252,s[32:35],s78 offen sc1\n"
"s_waitcnt vmcnt(8)\n"
"s_setprio 3\n"
"s_barrier\n"
"ds_read_b64 v[208:209],v144\n"
"ds_read_b64 v[210:211],v144 offset:16384\n"
"ds_read_b64 v[212:213],v145\n"
"ds_read_b64 v[214:215],v145 offset:16384\n"
"ds_read_b64 v[216:217],v146\n"
"ds_read_b64 v[218:219],v146 offset:16384\n"
"ds_read_b64 v[220:221],v147\n"
"ds_read_b64 v[222:223],v147 offset:16384\n"
"ds_read_b64 v[224:225],v144 offset:8192\n"
"ds_read_b64 v[226:227],v144 offset:24576\n"
"ds_read_b64 v[228:229],v145 offset:8192\n"
"ds_read_b64 v[230:231],v145 offset:24576\n"
"s_setprio 1\n"
"s_waitcnt lgkmcnt(8)\n"
"v_mfma_f32_16x16x32_bf16 v[0:3],v[208:211],v[72:75],0\n"
"ds_read_b64 v[232:233],v146 offset:8192\n"
"v_mfma_f32_16x16x32_bf16 v[0:3],v[212:215],v[80:83],v[0:3]\n"
"ds_read_b64 v[234:235],v146 offset:24576\n"
"v_mfma_f32_16x16x32_bf16 v[8:11],v[208:211],v[76:79],0\n"
"ds_read_b64 v[236:237],v147 offset:8192\n"
"v_mfma_f32_16x16x32_bf16 v[8:11],v[212:215],v[84:87],v[8:11]\n"
"ds_read_b64 v[238:239],v147 offset:24576\n"
"s_waitcnt lgkmcnt(8)\n"
"v_mfma_f32_16x16x32_bf16 v[8:11],v[216:219],v[92:95],v[8:11]\n"
"ds_read_b64 v[208:209],v148 offset:2048\n"
"v_mfma_f32_16x16x32_bf16 v[8:11],v[220:223],v[100:103],v[8:11]\n"
"ds_read_b64 v[210:211],v148 offset:20480\n"
"s_setprio 2\n"
"v_mfma_f32_16x16x32_bf16 v[0:3],v[216:219],v[88:91],v[0:3]\n"
"ds_read_b64 v[212:213],v144 offset:2048\n"
"v_mfma_f32_16x16x32_bf16 v[0:3],v[220:223],v[96:99],v[0:3]\n"
"ds_read_b64 v[214:215],v144 offset:18432\n"
"s_waitcnt lgkmcnt(8)\n"
"v_mfma_f32_16x16x32_bf16 v[0:3],v[224:227],v[104:107],v[0:3]\n"
"ds_read_b64 v[216:217],v145 offset:2048\n"
"v_mfma_f32_16x16x32_bf16 v[0:3],v[228:231],v[112:115],v[0:3]\n"
"ds_read_b64 v[218:219],v145 offset:18432\n"
"v_mfma_f32_16x16x32_bf16 v[8:11],v[224:227],v[108:111],v[8:11]\n"
"ds_read_b64 v[220:221],v146 offset:2048\n"
"v_mfma_f32_16x16x32_bf16 v[8:11],v[228:231],v[116:119],v[8:11]\n"
"ds_read_b64 v[222:223],v146 offset:18432\n"
"s_waitcnt lgkmcnt(8)\n"
"v_mfma_f32_16x16x32_bf16 v[8:11],v[232:235],v[124:127],v[8:11]\n"
"ds_read_b64 v[224:225],v147 offset:2048\n"
"v_mfma_f32_16x16x32_bf16 v[8:11],v[236:239],v[132:135],v[8:11]\n"
"ds_read_b64 v[226:227],v147 offset:18432\n"
"v_mfma_f32_16x16x32_bf16 v[0:3],v[232:235],v[120:123],v[0:3]\n"
"ds_read_b64 v[228:229],v144 offset:10240\n"
"v_mfma_f32_16x16x32_bf16 v[0:3],v[236:239],v[128:131],v[0:3]\n"
"ds_read_b64 v[230:231],v144 offset:26624\n"
".Lk_pads8:\n"
"s_cbranch_execz .Lk_hop_rets\n"
"s_waitcnt lgkmcnt(8)\n"
"v_mfma_f32_16x16x32_bf16 v[0:3],v[208:211],v[136:139],v[0:3]\n"
"ds_read_b64 v[232:233],v145 offset:10240\n"
"v_mfma_f32_16x16x32_bf16 v[8:11],v[208:211],v[140:143],v[8:11]\n"
"ds_read_b64 v[234:235],v145 offset:26624\n"
"v_mfma_f32_16x16x32_bf16 v[4:7],v[212:215],v[72:75],0\n"
"ds_read_b64 v[236:237],v146 offset:10240\n"
"s_waitcnt lgkmcnt(7)\n"
"v_mfma_f32_16x16x32_bf16 v[4:7],v[216:219],v[80:83],v[4:7]\n"
"ds_read_b64 v[238:239],v146 offset:26624\n"
"v_mfma_f32_16x16x32_bf16 v[12:15],v[212:215],v[76:79],0\n"
"ds_read_b64 v[208:209],v147 offset:10240\n"
"v_mfma_f32_16x16x32_bf16 v[12:15],v[216:219],v[84:87],v[12:15]\n"
"ds_read_b64 v[210:211],v147 offset:26624\n"
"v_mfma_f32_16x16x32_bf16 v[12:15],v[220:223],v[92:95],v[12:15]\n"
"ds_read_b64 v[212:213],v148 offset:2560\n"
"s_waitcnt lgkmcnt(7)\n"
"v_mfma_f32_16x16x32_bf16 v[12:15],v[224:227],v[100:103],v[12:15]\n"
"ds_read_b64 v[214:215],v148 offset:20992\n"
"v_mfma_f32_16x16x32_bf16 v[4:7],v[220:223],v[88:91],v[4:7]\n"
"s_barrier\n"
"v_mfma_f32_16x16x32_bf16 v[4:7],v[224:227],v[96:99],v[4:7]\n"
"v_mfma_f32_16x16x32_bf16 v[4:7],v[228:231],v[104:107],v[4:7]\n"
"s_waitcnt lgkmcnt(4)\n"
"v_mfma_f32_16x16x32_bf16 v[4:7],v[232:235],v[112:115],v[4:7]\n"
"v_mfma_f32_16x16x32_bf16 v[12:15],v[228:231],v[108:111],v[12:15]\n"
"v_mfma_f32_16x16x32_bf16 v[12:15],v[232:235],v[116:119],v[12:15]\n"
"v_mfma_f32_16x16x32_bf16 v[12:15],v[236:239],v[124:127],v[12:15]\n"
"s_waitcnt lgkmcnt(0)\n"
"v_mfma_f32_16x16x32_bf16 v[12:15],v[208:211],v[132:135],v[12:15]\n"
"v_mfma_f32_16x16x32_bf16 v[4:7],v[236:239],v[120:123],v[4:7]\n"
"v_mfma_f32_16x16x32_bf16 v[4:7],v[208:211],v[128:131],v[4:7]\n"
"v_mfma_f32_16x16x32_bf16 v[4:7],v[212:215],v[136:139],v[4:7]\n"
"v_mfma_f32_16x16x32_bf16 v[12:15],v[212:215],v[140:143],v[12:15]\n"
"s_setprio 0\n"
"v_add_u32 v144,32768,v144\n"
"v_add_u32 v145,32768,v145\n"
"v_add_u32 v146,32768,v146\n"
"v_add_u32 v147,32768,v147\n"
"s_addk_i32 m0,8192\n"
"s_add_u32 s74,s81,516096\n"
"buffer_load_dwordx4 v251,s[24:27],s74 offen lds\n"
"s_addk_i32 m0,8192\n"
"s_addk_i32 s74,128\n"
"buffer_load_dwordx4 v251,s[24:27],s74 offen lds\n"
"s_addk_i32 m0,8192\n"
"s_addk_i32 s74,-128\n"
"buffer_load_dwordx4 v251,s[28:31],s74 offen lds\n"
"s_addk_i32 m0,8192\n"
"s_addk_i32 s74,128\n"
"buffer_load_dwordx4 v251,s[28:31],s74 offen lds\n"
"v_cvt_pk_bf16_f32 v16,v0,v1\n"
"v_cvt_pk_bf16_f32 v17,v2,v3\n"
"v_cvt_pk_bf16_f32 v18,v4,v5\n"
"v_cvt_pk_bf16_f32 v19,v6,v7\n"
"s_add_u32 s78,s87,2048\n"
"buffer_store_dwordx4 v[16:19],v252,s[32:35],s78 offen sc1\n"
"v_cvt_pk_bf16_f32 v20,v8,v9\n"
"v_cvt_pk_bf16_f32 v21,v10,v11\n"
"v_cvt_pk_bf16_f32 v22,v12,v13\n"
"v_cvt_pk_bf16_f32 v23,v14,v15\n"
"s_add_u32 s78,s87,20480\n"
"buffer_store_dwordx4 v[20:23],v252,s[32:35],s78 offen sc1\n"
"s_waitcnt vmcnt(8)\n"
"s_setprio 3\n"
"s_barrier\n"
"ds_read_b64 v[208:209],v144\n"
"ds_read_b64 v[210:211],v144 offset:16384\n"
"ds_read_b64 v[212:213],v145\n"
"ds_read_b64 v[214:215],v145 offset:16384\n"
"ds_read_b64 v[216:217],v146\n"
"ds_read_b64 v[218:219],v146 offset:16384\n"
"ds_read_b64 v[220:221],v147\n"
"ds_read_b64 v[222:223],v147 offset:16384\n"
"ds_read_b64 v[224:225],v144 offset:8192\n"
"ds_read_b64 v[226:227],v144 offset:24576\n"
"ds_read_b64 v[228:229],v145 offset:8192\n"
"ds_read_b64 v[230:231],v145 offset:24576\n"
"s_waitcnt vmcnt(6)\n"
"v_mov_b32 v240,1\n"
"s_mov_b64 exec,1\n"
"buffer_store_dword v240,off,s[32:35],s88 sc1\n"
"s_mov_b64 exec,-1\n"
"s_setprio 1\n"
"s_waitcnt lgkmcnt(8)\n"
"v_mfma_f32_16x16x32_bf16 v[0:3],v[208:211],v[72:75],0\n"
"ds_read_b64 v[232:233],v146 offset:8192\n"
"v_mfma_f32_16x16x32_bf16 v[0:3],v[212:215],v[80:83],v[0:3]\n"
"ds_read_b64 v[234:235],v146 offset:24576\n"
"v_mfma_f32_16x16x32_bf16 v[8:11],v[208:211],v[76:79],0\n"
"ds_read_b64 v[236:237],v147 offset:8192\n"
"v_mfma_f32_16x16x32_bf16 v[8:11],v[212:215],v[84:87],v[8:11]\n"
"ds_read_b64 v[238:239],v147 offset:24576\n"
"s_waitcnt lgkmcnt(8)\n"
"v_mfma_f32_16x16x32_bf16 v[8:11],v[216:219],v[92:95],v[8:11]\n"
"ds_read_b64 v[208:209],v148 offset:8192\n"
"v_mfma_f32_16x16x32_bf16 v[8:11],v[220:223],v[100:103],v[8:11]\n"
"ds_read_b64 v[210:211],v148 offset:26624\n"
"s_setprio 2\n"
"v_mfma_f32_16x16x32_bf16 v[0:3],v[216:219],v[88:91],v[0:3]\n"
"ds_read_b64 v[212:213],v144 offset:2048\n"
"v_mfma_f32_16x16x32_bf16 v[0:3],v[220:223],v[96:99],v[0:3]\n"
"ds_read_b64 v[214:215],v144 offset:18432\n"
"s_waitcnt lgkmcnt(8)\n"
"v_mfma_f32_16x16x32_bf16 v[0:3],v[224:227],v[104:107],v[0:3]\n"
"ds_read_b64 v[216:217],v145 offset:2048\n"
"v_mfma_f32_16x16x32_bf16 v[0:3],v[228:231],v[112:115],v[0:3]\n"
"ds_read_b64 v[218:219],v145 offset:18432\n"
"v_mfma_f32_16x16x32_bf16 v[8:11],v[224:227],v[108:111],v[8:11]\n"
"ds_read_b64 v[220:221],v146 offset:2048\n"
"v_mfma_f32_16x16x32_bf16 v[8:11],v[228:231],v[116:119],v[8:11]\n"
"ds_read_b64 v[222:223],v146 offset:18432\n"
"s_waitcnt lgkmcnt(8)\n"
"v_mfma_f32_16x16x32_bf16 v[8:11],v[232:235],v[124:127],v[8:11]\n"
"ds_read_b64 v[224:225],v147 offset:2048\n"
"v_mfma_f32_16x16x32_bf16 v[8:11],v[236:239],v[132:135],v[8:11]\n"
"ds_read_b64 v[226:227],v147 offset:18432\n"
"v_mfma_f32_16x16x32_bf16 v[0:3],v[232:235],v[120:123],v[0:3]\n"
"ds_read_b64 v[228:229],v144 offset:10240\n"
"v_mfma_f32_16x16x32_bf16 v[0:3],v[236:239],v[128:131],v[0:3]\n"
"ds_read_b64 v[230:231],v144 offset:26624\n"
"s_waitcnt lgkmcnt(8)\n"
"v_mfma_f32_16x16x32_bf16 v[0:3],v[208:211],v[136:139],v[0:3]\n"
"ds_read_b64 v[232:233],v145 offset:10240\n"
"v_mfma_f32_16x16x32_bf16 v[8:11],v[208:211],v[140:143],v[8:11]\n"
"ds_read_b64 v[234:235],v145 offset:26624\n"
"v_mfma_f32_16x16x32_bf16 v[4:7],v[212:215],v[72:75],0\n"
"ds_read_b64 v[236:237],v146 offset:10240\n"
"s_waitcnt lgkmcnt(7)\n"
"v_mfma_f32_16x16x32_bf16 v[4:7],v[216:219],v[80:83],v[4:7]\n"
"ds_read_b64 v[238:239],v146 offset:26624\n"
"v_mfma_f32_16x16x32_bf16 v[12:15],v[212:215],v[76:79],0\n"
"ds_read_b64 v[208:209],v147 offset:10240\n"
"v_mfma_f32_16x16x32_bf16 v[12:15],v[216:219],v[84:87],v[12:15]\n"
"ds_read_b64 v[210:211],v147 offset:26624\n"
"v_mfma_f32_16x16x32_bf16 v[12:15],v[220:223],v[92:95],v[12:15]\n"
"ds_read_b64 v[212:213],v148 offset:8704\n"
"s_waitcnt lgkmcnt(7)\n"
"v_mfma_f32_16x16x32_bf16 v[12:15],v[224:227],v[100:103],v[12:15]\n"
"ds_read_b64 v[214:215],v148 offset:27136\n"
"v_mfma_f32_16x16x32_bf16 v[4:7],v[220:223],v[88:91],v[4:7]\n"
"s_barrier\n"
"v_mfma_f32_16x16x32_bf16 v[4:7],v[224:227],v[96:99],v[4:7]\n"
"v_mfma_f32_16x16x32_bf16 v[4:7],v[228:231],v[104:107],v[4:7]\n"
"s_waitcnt lgkmcnt(4)\n"
"v_mfma_f32_16x16x32_bf16 v[4:7],v[232:235],v[112:115],v[4:7]\n"
"v_mfma_f32_16x16x32_bf16 v[12:15],v[228:231],v[108:111],v[12:15]\n"
"v_mfma_f32_16x16x32_bf16 v[12:15],v[232:235],v[116:119],v[12:15]\n"
"v_mfma_f32_16x16x32_bf16 v[12:15],v[236:239],v[124:127],v[12:15]\n"
"s_waitcnt lgkmcnt(0)\n"
"v_mfma_f32_16x16x32_bf16 v[12:15],v[208:211],v[132:135],v[12:15]\n"
"v_mfma_f32_16x16x32_bf16 v[4:7],v[236:239],v[120:123],v[4:7]\n"
"v_mfma_f32_16x16x32_bf16 v[4:7],v[208:211],v[128:131],v[4:7]\n"
"v_mfma_f32_16x16x32_bf16 v[4:7],v[212:215],v[136:139],v[4:7]\n"
"v_mfma_f32_16x16x32_bf16 v[12:15],v[212:215],v[140:143],v[12:15]\n"
"s_setprio 0\n"
"v_add_u32 v144,32768,v144\n"
"v_add_u32 v145,32768,v145\n"
"v_add_u32 v146,32768,v146\n"
"v_add_u32 v147,32768,v147\n"
"s_add_u32 m0,s73,36864\n"
"s_add_u32 s74,s81,147456\n"
"buffer_load_dwordx4 v251,s[24:27],s74 offen lds\n"
"s_addk_i32 m0,8192\n"
"s_addk_i32 s74,128\n"
"buffer_load_dwordx4 v251,s[24:27],s74 offen lds\n"
"s_addk_i32 m0,8192\n"
"s_addk_i32 s74,-128\n"
"buffer_load_dwordx4 v251,s[28:31],s74 offen lds\n"
"s_addk_i32 m0,8192\n"
"s_addk_i32 s74,128\n"
"buffer_load_dwordx4 v251,s[28:31],s74 offen lds\n"
"v_cvt_pk_bf16_f32 v16,v0,v1\n"
"v_cvt_pk_bf16_f32 v17,v2,v3\n"
"v_cvt_pk_bf16_f32 v18,v4,v5\n"
"v_cvt_pk_bf16_f32 v19,v6,v7\n"
"s_add_u32 s78,s87,8192\n"
"buffer_store_dwordx4 v[16:19],v252,s[32:35],s78 offen sc1\n"
"v_cvt_pk_bf16_f32 v20,v8,v9\n"
"v_cvt_pk_bf16_f32 v21,v10,v11\n"
"v_cvt_pk_bf16_f32 v22,v12,v13\n"
"v_cvt_pk_bf16_f32 v23,v14,v15\n"
"s_add_u32 s78,s87,26624\n"
"buffer_store_dwordx4 v[20:23],v252,s[32:35],s78 offen sc1\n"
"s_waitcnt vmcnt(9)\n"
"s_setprio 3\n"
"s_barrier\n"
"ds_read_b64 v[208:209],v144\n"
"ds_read_b64 v[210:211],v144 offset:16384\n"
"ds_read_b64 v[212:213],v145\n"
"ds_read_b64 v[214:215],v145 offset:16384\n"
"ds_read_b64 v[216:217],v146\n"
"ds_read_b64 v[218:219],v146 offset:16384\n"
"ds_read_b64 v[220:221],v147\n"
"ds_read_b64 v[222:223],v147 offset:16384\n"
"ds_read_b64 v[224:225],v144 offset:8192\n"
"ds_read_b64 v[226:227],v144 offset:24576\n"
"ds_read_b64 v[228:229],v145 offset:8192\n"
"ds_read_b64 v[230:231],v145 offset:24576\n"
"s_setprio 1\n"
"s_waitcnt lgkmcnt(8)\n"
"v_mfma_f32_16x16x32_bf16 v[0:3],v[208:211],v[72:75],0\n"
"ds_read_b64 v[232:233],v146 offset:8192\n"
"v_mfma_f32_16x16x32_bf16 v[0:3],v[212:215],v[80:83],v[0:3]\n"
"ds_read_b64 v[234:235],v146 offset:24576\n"
"v_mfma_f32_16x16x32_bf16 v[8:11],v[208:211],v[76:79],0\n"
"ds_read_b64 v[236:237],v147 offset:8192\n"
"v_mfma_f32_16x16x32_bf16 v[8:11],v[212:215],v[84:87],v[8:11]\n"
"ds_read_b64 v[238:239],v147 offset:24576\n"
"s_waitcnt lgkmcnt(8)\n"
"v_mfma_f32_16x16x32_bf16 v[8:11],v[216:219],v[92:95],v[8:11]\n"
"ds_read_b64 v[208:209],v148 offset:14336\n"
"v_mfma_f32_16x16x32_bf16 v[8:11],v[220:223],v[100:103],v[8:11]\n"
"ds_read_b64 v[210:211],v148 offset:32768\n"
"s_setprio 2\n"
"v_mfma_f32_16x16x32_bf16 v[0:3],v[216:219],v[88:91],v[0:3]\n"
"ds_read_b64 v[212:213],v144 offset:2048\n"
"v_mfma_f32_16x16x32_bf16 v[0:3],v[220:223],v[96:99],v[0:3]\n"
"ds_read_b64 v[214:215],v144 offset:18432\n"
"s_waitcnt lgkmcnt(8)\n"
"v_mfma_f32_16x16x32_bf16 v[0:3],v[224:227],v[104:107],v[0:3]\n"
"ds_read_b64 v[216:217],v145 offset:2048\n"
"v_mfma_f32_16x16x32_bf16 v[0:3],v[228:231],v[112:115],v[0:3]\n"
"ds_read_b64 v[218:219],v145 offset:18432\n"
"v_mfma_f32_16x16x32_bf16 v[8:11],v[224:227],v[108:111],v[8:11]\n"
"ds_read_b64 v[220:221],v146 offset:2048\n"
"v_mfma_f32_16x16x32_bf16 v[8:11],v[228:231],v[116:119],v[8:11]\n"
"ds_read_b64 v[222:223],v146 offset:18432\n"
"s_waitcnt lgkmcnt(8)\n"
"v_mfma_f32_16x16x32_bf16 v[8:11],v[232:235],v[124:127],v[8:11]\n"
"ds_read_b64 v[224:225],v147 offset:2048\n"
"v_mfma_f32_16x16x32_bf16 v[8:11],v[236:239],v[132:135],v[8:11]\n"
"ds_read_b64 v[226:227],v147 offset:18432\n"
"v_mfma_f32_16x16x32_bf16 v[0:3],v[232:235],v[120:123],v[0:3]\n"
"ds_read_b64 v[228:229],v144 offset:10240\n"
"v_mfma_f32_16x16x32_bf16 v[0:3],v[236:239],v[128:131],v[0:3]\n"
"ds_read_b64 v[230:231],v144 offset:26624\n"
"s_waitcnt lgkmcnt(8)\n"
"v_mfma_f32_16x16x32_bf16 v[0:3],v[208:211],v[136:139],v[0:3]\n"
"ds_read_b64 v[232:233],v145 offset:10240\n"
"v_mfma_f32_16x16x32_bf16 v[8:11],v[208:211],v[140:143],v[8:11]\n"
"ds_read_b64 v[234:235],v145 offset:26624\n"
"v_mfma_f32_16x16x32_bf16 v[4:7],v[212:215],v[72:75],0\n"
"ds_read_b64 v[236:237],v146 offset:10240\n"
"s_waitcnt lgkmcnt(7)\n"
"v_mfma_f32_16x16x32_bf16 v[4:7],v[216:219],v[80:83],v[4:7]\n"
"ds_read_b64 v[238:239],v146 offset:26624\n"
"v_mfma_f32_16x16x32_bf16 v[12:15],v[212:215],v[76:79],0\n"
"ds_read_b64 v[208:209],v147 offset:10240\n"
"v_mfma_f32_16x16x32_bf16 v[12:15],v[216:219],v[84:87],v[12:15]\n"
"ds_read_b64 v[210:211],v147 offset:26624\n"
"v_mfma_f32_16x16x32_bf16 v[12:15],v[220:223],v[92:95],v[12:15]\n"
"ds_read_b64 v[212:213],v148 offset:14848\n"
"s_waitcnt lgkmcnt(7)\n"
"v_mfma_f32_16x16x32_bf16 v[12:15],v[224:227],v[100:103],v[12:15]\n"
"ds_read_b64 v[214:215],v148 offset:33280\n"
"v_mfma_f32_16x16x32_bf16 v[4:7],v[220:223],v[88:91],v[4:7]\n"
"s_barrier\n"
"v_mfma_f32_16x16x32_bf16 v[4:7],v[224:227],v[96:99],v[4:7]\n"
"v_mfma_f32_16x16x32_bf16 v[4:7],v[228:231],v[104:107],v[4:7]\n"
"s_waitcnt lgkmcnt(4)\n"
"v_mfma_f32_16x16x32_bf16 v[4:7],v[232:235],v[112:115],v[4:7]\n"
"v_mfma_f32_16x16x32_bf16 v[12:15],v[228:231],v[108:111],v[12:15]\n"
"v_mfma_f32_16x16x32_bf16 v[12:15],v[232:235],v[116:119],v[12:15]\n"
"v_mfma_f32_16x16x32_bf16 v[12:15],v[236:239],v[124:127],v[12:15]\n"
"s_waitcnt lgkmcnt(0)\n"
"v_mfma_f32_16x16x32_bf16 v[12:15],v[208:211],v[132:135],v[12:15]\n"
"v_mfma_f32_16x16x32_bf16 v[4:7],v[236:239],v[120:123],v[4:7]\n"
"v_mfma_f32_16x16x32_bf16 v[4:7],v[208:211],v[128:131],v[4:7]\n"
"v_mfma_f32_16x16x32_bf16 v[4:7],v[212:215],v[136:139],v[4:7]\n"
"v_mfma_f32_16x16x32_bf16 v[12:15],v[212:215],v[140:143],v[12:15]\n"
"s_setprio 0\n"
"v_add_u32 v144,4294901760,v144\n"
"v_add_u32 v145,4294901760,v145\n"
"v_add_u32 v146,4294901760,v146\n"
"v_add_u32 v147,4294901760,v147\n"
"s_addk_i32 m0,8192\n"
"s_add_u32 s74,s81,368640\n"
"buffer_load_dwordx4 v251,s[24:27],s74 offen lds\n"
"s_addk_i32 m0,8192\n"
"s_addk_i32 s74,128\n"
"buffer_load_dwordx4 v251,s[24:27],s74 offen lds\n"
"s_addk_i32 m0,8192\n"
"s_addk_i32 s74,-128\n"
"buffer_load_dwordx4 v251,s[28:31],s74 offen lds\n"
"s_addk_i32 m0,8192\n"
"s_addk_i32 s74,128\n"
"buffer_load_dwordx4 v251,s[28:31],s74 offen lds\n"
"v_cvt_pk_bf16_f32 v16,v0,v1\n"
"v_cvt_pk_bf16_f32 v17,v2,v3\n"
"v_cvt_pk_bf16_f32 v18,v4,v5\n"
"v_cvt_pk_bf16_f32 v19,v6,v7\n"
"s_add_u32 s78,s87,14336\n"
"buffer_store_dwordx4 v[16:19],v252,s[32:35],s78 offen sc1\n"
"v_cvt_pk_bf16_f32 v20,v8,v9\n"
"v_cvt_pk_bf16_f32 v21,v10,v11\n"
"v_cvt_pk_bf16_f32 v22,v12,v13\n"
"v_cvt_pk_bf16_f32 v23,v14,v15\n"
"s_add_u32 s78,s87,32768\n"
"buffer_store_dwordx4 v[20:23],v252,s[32:35],s78 offen sc1\n"
"s_waitcnt vmcnt(8)\n"
"s_setprio 3\n"
"s_barrier\n"
"ds_read_b64 v[208:209],v144\n"
"ds_read_b64 v[210:211],v144 offset:16384\n"
"ds_read_b64 v[212:213],v145\n"
"ds_read_b64 v[214:215],v145 offset:16384\n"
"ds_read_b64 v[216:217],v146\n"
"ds_read_b64 v[218:219],v146 offset:16384\n"
"ds_read_b64 v[220:221],v147\n"
"ds_read_b64 v[222:223],v147 offset:16384\n"
"ds_read_b64 v[224:225],v144 offset:8192\n"
"ds_read_b64 v[226:227],v144 offset:24576\n"
"ds_read_b64 v[228:229],v145 offset:8192\n"
"ds_read_b64 v[230:231],v145 offset:24576\n"
"s_setprio 1\n"
"s_waitcnt lgkmcnt(8)\n"
"v_mfma_f32_16x16x32_bf16 v[0:3],v[208:211],v[72:75],0\n"
"ds_read_b64 v[232:233],v146 offset:8192\n"
"v_mfma_f32_16x16x32_bf16 v[0:3],v[212:215],v[80:83],v[0:3]\n"
"ds_read_b64 v[234:235],v146 offset:24576\n"
"v_mfma_f32_16x16x32_bf16 v[8:11],v[208:211],v[76:79],0\n"
"ds_read_b64 v[236:237],v147 offset:8192\n"
"v_mfma_f32_16x16x32_bf16 v[8:11],v[212:215],v[84:87],v[8:11]\n"
"ds_read_b64 v[238:239],v147 offset:24576\n"
"s_waitcnt lgkmcnt(8)\n"
"v_mfma_f32_16x16x32_bf16 v[8:11],v[216:219],v[92:95],v[8:11]\n"
"ds_read_b64 v[208:209],v148 offset:4096\n"
"v_mfma_f32_16x16x32_bf16 v[8:11],v[220:223],v[100:103],v[8:11]\n"
"ds_read_b64 v[210:211],v148 offset:22528\n"
"s_setprio 2\n"
"v_mfma_f32_16x16x32_bf16 v[0:3],v[216:219],v[88:91],v[0:3]\n"
"ds_read_b64 v[212:213],v144 offset:2048\n"
"v_mfma_f32_16x16x32_bf16 v[0:3],v[220:223],v[96:99],v[0:3]\n"
"ds_read_b64 v[214:215],v144 offset:18432\n"
"s_waitcnt lgkmcnt(8)\n"
"v_mfma_f32_16x16x32_bf16 v[0:3],v[224:227],v[104:107],v[0:3]\n"
"ds_read_b64 v[216:217],v145 offset:2048\n"
"v_mfma_f32_16x16x32_bf16 v[0:3],v[228:231],v[112:115],v[0:3]\n"
"ds_read_b64 v[218:219],v145 offset:18432\n"
"v_mfma_f32_16x16x32_bf16 v[8:11],v[224:227],v[108:111],v[8:11]\n"
"ds_read_b64 v[220:221],v146 offset:2048\n"
"v_mfma_f32_16x16x32_bf16 v[8:11],v[228:231],v[116:119],v[8:11]\n"
"ds_read_b64 v[222:223],v146 offset:18432\n"
"s_waitcnt lgkmcnt(8)\n"
"v_mfma_f32_16x16x32_bf16 v[8:11],v[232:235],v[124:127],v[8:11]\n"
"ds_read_b64 v[224:225],v147 offset:2048\n"
"v_mfma_f32_16x16x32_bf16 v[8:11],v[236:239],v[132:135],v[8:11]\n"
"ds_read_b64 v[226:227],v147 offset:18432\n"
"v_mfma_f32_16x16x32_bf16 v[0:3],v[232:235],v[120:123],v[0:3]\n"
"ds_read_b64 v[228:229],v144 offset:10240\n"
"v_mfma_f32_16x16x32_bf16 v[0:3],v[236:239],v[128:131],v[0:3]\n"
"ds_read_b64 v[230:231],v144 offset:26624\n"
"s_waitcnt lgkmcnt(8)\n"
"v_mfma_f32_16x16x32_bf16 v[0:3],v[208:211],v[136:139],v[0:3]\n"
"ds_read_b64 v[232:233],v145 offset:10240\n"
"v_mfma_f32_16x16x32_bf16 v[8:11],v[208:211],v[140:143],v[8:11]\n"
"ds_read_b64 v[234:235],v145 offset:26624\n"
"v_mfma_f32_16x16x32_bf16 v[4:7],v[212:215],v[72:75],0\n"
"ds_read_b64 v[236:237],v146 offset:10240\n"
"s_waitcnt lgkmcnt(7)\n"
"v_mfma_f32_16x16x32_bf16 v[4:7],v[216:219],v[80:83],v[4:7]\n"
"ds_read_b64 v[238:239],v146 offset:26624\n"
"v_mfma_f32_16x16x32_bf16 v[12:15],v[212:215],v[76:79],0\n"
"ds_read_b64 v[208:209],v147 offset:10240\n"
"v_mfma_f32_16x16x32_bf16 v[12:15],v[216:219],v[84:87],v[12:15]\n"
"ds_read_b64 v[210:211],v147 offset:26624\n"
"v_mfma_f32_16x16x32_bf16 v[12:15],v[220:223],v[92:95],v[12:15]\n"
"ds_read_b64 v[212:213],v148 offset:4608\n"
"s_waitcnt lgkmcnt(7)\n"
"v_mfma_f32_16x16x32_bf16 v[12:15],v[224:227],v[100:103],v[12:15]\n"
"ds_read_b64 v[214:215],v148 offset:23040\n"
"v_mfma_f32_16x16x32_bf16 v[4:7],v[220:223],v[88:91],v[4:7]\n"
"s_barrier\n"
"v_mfma_f32_16x16x32_bf16 v[4:7],v[224:227],v[96:99],v[4:7]\n"
"v_mfma_f32_16x16x32_bf16 v[4:7],v[228:231],v[104:107],v[4:7]\n"
"s_waitcnt lgkmcnt(4)\n"
"v_mfma_f32_16x16x32_bf16 v[4:7],v[232:235],v[112:115],v[4:7]\n"
"v_mfma_f32_16x16x32_bf16 v[12:15],v[228:231],v[108:111],v[12:15]\n"
"v_mfma_f32_16x16x32_bf16 v[12:15],v[232:235],v[116:119],v[12:15]\n"
"v_mfma_f32_16x16x32_bf16 v[12:15],v[236:239],v[124:127],v[12:15]\n"
"s_waitcnt lgkmcnt(0)\n"
"v_mfma_f32_16x16x32_bf16 v[12:15],v[208:211],v[132:135],v[12:15]\n"
"v_mfma_f32_16x16x32_bf16 v[4:7],v[236:239],v[120:123],v[4:7]\n"
"v_mfma_f32_16x16x32_bf16 v[4:7],v[208:211],v[128:131],v[4:7]\n"
"v_mfma_f32_16x16x32_bf16 v[4:7],v[212:215],v[136:139],v[4:7]\n"
"v_mfma_f32_16x16x32_bf16 v[12:15],v[212:215],v[140:143],v[12:15]\n"
"s_setprio 0\n"
"v_add_u32 v144,32768,v144\n"
"v_add_u32 v145,32768,v145\n"
"v_add_u32 v146,32768,v146\n"
"v_add_u32 v147,32768,v147\n"
"s_addk_i32 m0,8192\n"
"s_add_u32 s74,s81,589824\n"
"buffer_load_dwordx4 v251,s[24:27],s74 offen lds\n"
"s_addk_i32 m0,8192\n"
"s_addk_i32 s74,128\n"
"buffer_load_dwordx4 v251,s[24:27],s74 offen lds\n"
"s_addk_i32 m0,8192\n"
"s_addk_i32 s74,-128\n"
"buffer_load_dwordx4 v251,s[28:31],s74 offen lds\n"
"s_addk_i32 m0,8192\n"
"s_addk_i32 s74,128\n"
"buffer_load_dwordx4 v251,s[28:31],s74 offen lds\n"
"v_cvt_pk_bf16_f32 v16,v0,v1\n"
"v_cvt_pk_bf16_f32 v17,v2,v3\n"
"v_cvt_pk_bf16_f32 v18,v4,v5\n"
"v_cvt_pk_bf16_f32 v19,v6,v7\n"
"s_add_u32 s78,s87,4096\n"
"buffer_store_dwordx4 v[16:19],v252,s[32:35],s78 offen sc1\n"
"v_cvt_pk_bf16_f32 v20,v8,v9\n"
"v_cvt_pk_bf16_f32 v21,v10,v11\n"
"v_cvt_pk_bf16_f32 v22,v12,v13\n"
"v_cvt_pk_bf16_f32 v23,v14,v15\n"
"s_add_u32 s78,s87,22528\n"
"buffer_store_dwordx4 v[20:23],v252,s[32:35],s78 offen sc1\n"
"s_waitcnt vmcnt(8)\n"
"s_setprio 3\n"
"s_barrier\n"
"ds_read_b64 v[208:209],v144\n"
"ds_read_b64 v[210:211],v144 offset:16384\n"
"ds_read_b64 v[212:213],v145\n"
"ds_read_b64 v[214:215],v145 offset:16384\n"
"ds_read_b64 v[216:217],v146\n"
"ds_read_b64 v[218:219],v146 offset:16384\n"
"ds_read_b64 v[220:221],v147\n"
"ds_read_b64 v[222:223],v147 offset:16384\n"
"ds_read_b64 v[224:225],v144 offset:8192\n"
"ds_read_b64 v[226:227],v144 offset:24576\n"
"ds_read_b64 v[228:229],v145 offset:8192\n"
"ds_read_b64 v[230:231],v145 offset:24576\n"
"s_waitcnt vmcnt(6)\n"
"v_mov_b32 v240,2\n"
"s_mov_b64 exec,1\n"
"buffer_store_dword v240,off,s[32:35],s88 sc1\n"
"s_mov_b64 exec,-1\n"
"s_setprio 1\n"
"s_waitcnt lgkmcnt(8)\n"
"v_mfma_f32_16x16x32_bf16 v[0:3],v[208:211],v[72:75],0\n"
"ds_read_b64 v[232:233],v146 offset:8192\n"
"v_mfma_f32_16x16x32_bf16 v[0:3],v[212:215],v[80:83],v[0:3]\n"
"ds_read_b64 v[234:235],v146 offset:24576\n"
"v_mfma_f32_16x16x32_bf16 v[8:11],v[208:211],v[76:79],0\n"
"ds_read_b64 v[236:237],v147 offset:8192\n"
"v_mfma_f32_16x16x32_bf16 v[8:11],v[212:215],v[84:87],v[8:11]\n"
"ds_read_b64 v[238:239],v147 offset:24576\n"
"s_waitcnt lgkmcnt(8)\n"
"v_mfma_f32_16x16x32_bf16 v[8:11],v[216:219],v[92:95],v[8:11]\n"
"ds_read_b64 v[208:209],v148 offset:10240\n"
"v_mfma_f32_16x16x32_bf16 v[8:11],v[220:223],v[100:103],v[8:11]\n"
"ds_read_b64 v[210:211],v148 offset:28672\n"
"s_setprio 2\n"
"v_mfma_f32_16x16x32_bf16 v[0:3],v[216:219],v[88:91],v[0:3]\n"
"ds_read_b64 v[212:213],v144 offset:2048\n"
"v_mfma_f32_16x16x32_bf16 v[0:3],v[220:223],v[96:99],v[0:3]\n"
"ds_read_b64 v[214:215],v144 offset:18432\n"
"s_waitcnt lgkmcnt(8)\n"
"v_mfma_f32_16x16x32_bf16 v[0:3],v[224:227],v[104:107],v[0:3]\n"
"ds_read_b64 v[216:217],v145 offset:2048\n"
"v_mfma_f32_16x16x32_bf16 v[0:3],v[228:231],v[112:115],v[0:3]\n"
"ds_read_b64 v[218:219],v145 offset:18432\n"
"v_mfma_f32_16x16x32_bf16 v[8:11],v[224:227],v[108:111],v[8:11]\n"
"ds_read_b64 v[220:221],v146 offset:2048\n"
"v_mfma_f32_16x16x32_bf16 v[8:11],v[228:231],v[116:119],v[8:11]\n"
"ds_read_b64 v[222:223],v146 offset:18432\n"
"s_waitcnt lgkmcnt(8)\n"
"v_mfma_f32_16x16x32_bf16 v[8:11],v[232:235],v[124:127],v[8:11]\n"
"ds_read_b64 v[224:225],v147 offset:2048\n"
"v_mfma_f32_16x16x32_bf16 v[8:11],v[236:239],v[132:135],v[8:11]\n"
"ds_read_b64 v[226:227],v147 offset:18432\n"
"v_mfma_f32_16x16x32_bf16 v[0:3],v[232:235],v[120:123],v[0:3]\n"
"ds_read_b64 v[228:229],v144 offset:10240\n"
"v_mfma_f32_16x16x32_bf16 v[0:3],v[236:239],v[128:131],v[0:3]\n"
"ds_read_b64 v[230:231],v144 offset:26624\n"
"s_waitcnt lgkmcnt(8)\n"
"v_mfma_f32_16x16x32_bf16 v[0:3],v[208:211],v[136:139],v[0:3]\n"
"ds_read_b64 v[232:233],v145 offset:10240\n"
"v_mfma_f32_16x16x32_bf16 v[8:11],v[208:211],v[140:143],v[8:11]\n"
"ds_read_b64 v[234:235],v145 offset:26624\n"
"v_mfma_f32_16x16x32_bf16 v[4:7],v[212:215],v[72:75],0\n"
"ds_read_b64 v[236:237],v146 offset:10240\n"
"s_waitcnt lgkmcnt(7)\n"
"v_mfma_f32_16x16x32_bf16 v[4:7],v[216:219],v[80:83],v[4:7]\n"
"ds_read_b64 v[238:239],v146 offset:26624\n"
"v_mfma_f32_16x16x32_bf16 v[12:15],v[212:215],v[76:79],0\n"
"ds_read_b64 v[208:209],v147 offset:10240\n"
"v_mfma_f32_16x16x32_bf16 v[12:15],v[216:219],v[84:87],v[12:15]\n"
"ds_read_b64 v[210:211],v147 offset:26624\n"
"v_mfma_f32_16x16x32_bf16 v[12:15],v[220:223],v[92:95],v[12:15]\n"
"ds_read_b64 v[212:213],v148 offset:10752\n"
"s_waitcnt lgkmcnt(7)\n"
"v_mfma_f32_16x16x32_bf16 v[12:15],v[224:227],v[100:103],v[12:15]\n"
"ds_read_b64 v[214:215],v148 offset:29184\n"
"v_mfma_f32_16x16x32_bf16 v[4:7],v[220:223],v[88:91],v[4:7]\n"
"s_barrier\n"
"v_mfma_f32_16x16x32_bf16 v[4:7],v[224:227],v[96:99],v[4:7]\n"
"v_mfma_f32_16x16x32_bf16 v[4:7],v[228:231],v[104:107],v[4:7]\n"
"s_waitcnt lgkmcnt(4)\n"
"v_mfma_f32_16x16x32_bf16 v[4:7],v[232:235],v[112:115],v[4:7]\n"
"v_mfma_f32_16x16x32_bf16 v[12:15],v[228:231],v[108:111],v[12:15]\n"
"v_mfma_f32_16x16x32_bf16 v[12:15],v[232:235],v[116:119],v[12:15]\n"
"v_mfma_f32_16x16x32_bf16 v[12:15],v[236:239],v[124:127],v[12:15]\n"
"s_waitcnt lgkmcnt(0)\n"
"v_mfma_f32_16x16x32_bf16 v[12:15],v[208:211],v[132:135],v[12:15]\n"
"v_mfma_f32_16x16x32_bf16 v[4:7],v[236:239],v[120:123],v[4:7]\n"
"v_mfma_f32_16x16x32_bf16 v[4:7],v[208:211],v[128:131],v[4:7]\n"
"v_mfma_f32_16x16x32_bf16 v[4:7],v[212:215],v[136:139],v[4:7]\n"
"v_mfma_f32_16x16x32_bf16 v[12:15],v[212:215],v[140:143],v[12:15]\n"
"s_setprio 0\n"
"v_add_u32 v144,32768,v144\n"
"v_add_u32 v145,32768,v145\n"
"v_add_u32 v146,32768,v146\n"
"v_add_u32 v147,32768,v147\n"
"v_cvt_pk_bf16_f32 v16,v0,v1\n"
"v_cvt_pk_bf16_f32 v17,v2,v3\n"
"v_cvt_pk_bf16_f32 v18,v4,v5\n"
"v_cvt_pk_bf16_f32 v19,v6,v7\n"
"s_add_u32 s78,s87,10240\n"
"buffer_store_dwordx4 v[16:19],v252,s[32:35],s78 offen sc1\n"
"v_cvt_pk_bf16_f32 v20,v8,v9\n"
"v_cvt_pk_bf16_f32 v21,v10,v11\n"
"v_cvt_pk_bf16_f32 v22,v12,v13\n"
"v_cvt_pk_bf16_f32 v23,v14,v15\n"
"s_add_u32 s78,s87,28672\n"
"buffer_store_dwordx4 v[20:23],v252,s[32:35],s78 offen sc1\n"
"s_waitcnt vmcnt(5)\n"
"s_setprio 3\n"
"s_barrier\n"
"ds_read_b64 v[208:209],v144\n"
"ds_read_b64 v[210:211],v144 offset:16384\n"
"ds_read_b64 v[212:213],v145\n"
"ds_read_b64 v[214:215],v145 offset:16384\n"
"ds_read_b64 v[216:217],v146\n"
"ds_read_b64 v[218:219],v146 offset:16384\n"
"ds_read_b64 v[220:221],v147\n"
"ds_read_b64 v[222:223],v147 offset:16384\n"
"ds_read_b64 v[224:225],v144 offset:8192\n"
"ds_read_b64 v[226:227],v144 offset:24576\n"
"ds_read_b64 v[228:229],v145 offset:8192\n"
"ds_read_b64 v[230:231],v145 offset:24576\n"
"s_setprio 1\n"
"s_waitcnt lgkmcnt(8)\n"
"v_mfma_f32_16x16x32_bf16 v[0:3],v[208:211],v[72:75],0\n"
"ds_read_b64 v[232:233],v146 offset:8192\n"
"v_mfma_f32_16x16x32_bf16 v[0:3],v[212:215],v[80:83],v[0:3]\n"
"ds_read_b64 v[234:235],v146 offset:24576\n"
"v_mfma_f32_16x16x32_bf16 v[8:11],v[208:211],v[76:79],0\n"
"ds_read_b64 v[236:237],v147 offset:8192\n"
"v_mfma_f32_16x16x32_bf16 v[8:11],v[212:215],v[84:87],v[8:11]\n"
"ds_read_b64 v[238:239],v147 offset:24576\n"
"s_waitcnt lgkmcnt(8)\n"
"v_mfma_f32_16x16x32_bf16 v[8:11],v[216:219],v[92:95],v[8:11]\n"
"ds_read_b64 v[208:209],v148 offset:16384\n"
"v_mfma_f32_16x16x32_bf16 v[8:11],v[220:223],v[100:103],v[8:11]\n"
"ds_read_b64 v[210:211],v148 offset:34816\n"
"s_setprio 2\n"
"v_mfma_f32_16x16x32_bf16 v[0:3],v[216:219],v[88:91],v[0:3]\n"
"ds_read_b64 v[212:213],v144 offset:2048\n"
"v_mfma_f32_16x16x32_bf16 v[0:3],v[220:223],v[96:99],v[0:3]\n"
"ds_read_b64 v[214:215],v144 offset:18432\n"
"s_waitcnt lgkmcnt(8)\n"
"v_mfma_f32_16x16x32_bf16 v[0:3],v[224:227],v[104:107],v[0:3]\n"
"ds_read_b64 v[216:217],v145 offset:2048\n"
"v_mfma_f32_16x16x32_bf16 v[0:3],v[228:231],v[112:115],v[0:3]\n"
"ds_read_b64 v[218:219],v145 offset:18432\n"
"v_mfma_f32_16x16x32_bf16 v[8:11],v[224:227],v[108:111],v[8:11]\n"
"ds_read_b64 v[220:221],v146 offset:2048\n"
"v_mfma_f32_16x16x32_bf16 v[8:11],v[228:231],v[116:119],v[8:11]\n"
".Lk_pads10:\n"
"s_cbranch_execz .Lk_hop_rets\n"
"ds_read_b64 v[222:223],v146 offset:18432\n"
"s_waitcnt lgkmcnt(8)\n"
"v_mfma_f32_16x16x32_bf16 v[8:11],v[232:235],v[124:127],v[8:11]\n"
"ds_read_b64 v[224:225],v147 offset:2048\n"
"v_mfma_f32_16x16x32_bf16 v[8:11],v[236:239],v[132:135],v[8:11]\n"
"ds_read_b64 v[226:227],v147 offset:18432\n"
"v_mfma_f32_16x16x32_bf16 v[0:3],v[232:235],v[120:123],v[0:3]\n"
"ds_read_b64 v[228:229],v144 offset:10240\n"
"v_mfma_f32_16x16x32_bf16 v[0:3],v[236:239],v[128:131],v[0:3]\n"
"ds_read_b64 v[230:231],v144 offset:26624\n"
"s_waitcnt lgkmcnt(8)\n"
"v_mfma_f32_16x16x32_bf16 v[0:3],v[208:211],v[136:139],v[0:3]\n"
"ds_read_b64 v[232:233],v145 offset:10240\n"
"v_mfma_f32_16x16x32_bf16 v[8:11],v[208:211],v[140:143],v[8:11]\n"
"ds_read_b64 v[234:235],v145 offset:26624\n"
"v_mfma_f32_16x16x32_bf16 v[4:7],v[212:215],v[72:75],0\n"
"ds_read_b64 v[236:237],v146 offset:10240\n"
"s_waitcnt lgkmcnt(7)\n"
"v_mfma_f32_16x16x32_bf16 v[4:7],v[216:219],v[80:83],v[4:7]\n"
"ds_read_b64 v[238:239],v146 offset:26624\n"
"v_mfma_f32_16x16x32_bf16 v[12:15],v[212:215],v[76:79],0\n"
"ds_read_b64 v[208:209],v147 offset:10240\n"
"v_mfma_f32_16x16x32_bf16 v[12:15],v[216:219],v[84:87],v[12:15]\n"
"ds_read_b64 v[210:211],v147 offset:26624\n"
"v_mfma_f32_16x16x32_bf16 v[12:15],v[220:223],v[92:95],v[12:15]\n"
"ds_read_b64 v[212:213],v148 offset:16896\n"
"s_waitcnt lgkmcnt(7)\n"
"v_mfma_f32_16x16x32_bf16 v[12:15],v[224:227],v[100:103],v[12:15]\n"
"ds_read_b64 v[214:215],v148 offset:35328\n"
"v_mfma_f32_16x16x32_bf16 v[4:7],v[220:223],v[88:91],v[4:7]\n"
"s_barrier\n"
"v_mfma_f32_16x16x32_bf16 v[4:7],v[224:227],v[96:99],v[4:7]\n"
"v_mfma_f32_16x16x32_bf16 v[4:7],v[228:231],v[104:107],v[4:7]\n"
"s_waitcnt lgkmcnt(4)\n"
"v_mfma_f32_16x16x32_bf16 v[4:7],v[232:235],v[112:115],v[4:7]\n"
"v_mfma_f32_16x16x32_bf16 v[12:15],v[228:231],v[108:111],v[12:15]\n"
"v_mfma_f32_16x16x32_bf16 v[12:15],v[232:235],v[116:119],v[12:15]\n"
"v_mfma_f32_16x16x32_bf16 v[12:15],v[236:239],v[124:127],v[12:15]\n"
"s_waitcnt lgkmcnt(0)\n"
"v_mfma_f32_16x16x32_bf16 v[12:15],v[208:211],v[132:135],v[12:15]\n"
"v_mfma_f32_16x16x32_bf16 v[4:7],v[236:239],v[120:123],v[4:7]\n"
"v_mfma_f32_16x16x32_bf16 v[4:7],v[208:211],v[128:131],v[4:7]\n"
"v_mfma_f32_16x16x32_bf16 v[4:7],v[212:215],v[136:139],v[4:7]\n"
"v_mfma_f32_16x16x32_bf16 v[12:15],v[212:215],v[140:143],v[12:15]\n"
"s_setprio 0\n"
"v_cvt_pk_bf16_f32 v16,v0,v1\n"
"v_cvt_pk_bf16_f32 v17,v2,v3\n"
"s_nop 3\n"
"v_cvt_pk_bf16_f32 v18,v4,v5\n"
"v_cvt_pk_bf16_f32 v19,v6,v7\n"
"s_add_u32 s78,s87,16384\n"
"buffer_store_dwordx4 v[16:19],v252,s[32:35],s78 offen sc1\n"
"v_cvt_pk_bf16_f32 v20,v8,v9\n"
"v_cvt_pk_bf16_f32 v21,v10,v11\n"
"v_cvt_pk_bf16_f32 v22,v12,v13\n"
"v_cvt_pk_bf16_f32 v23,v14,v15\n"
"s_add_u32 s78,s87,34816\n"
"buffer_store_dwordx4 v[20:23],v252,s[32:35],s78 offen sc1\n"
"s_waitcnt vmcnt(0)\n"
"v_mov_b32 v240,3\n"
"s_mov_b64 exec,1\n"
"buffer_store_dword v240,off,s[32:35],s88 sc1\n"
"s_mov_b64 exec,-1\n"
"s_endpgm\n"
".Lk_ssm_g1:\n"
".Lk_pads1:\n"
"s_cbranch_execz .Lk_pads5\n"
"s_add_u32 m0,s73,36864\n"
"s_add_u32 s74,s80,18432\n"
"buffer_load_dwordx4 v250,s[16:19],s74 offen lds\n"
"s_addk_i32 m0,4096\n"
"s_addk_i32 s74,128\n"
"buffer_load_dwordx4 v250,s[16:19],s74 offen lds\n"
"s_addk_i32 m0,4096\n"
"s_addk_i32 s74,128\n"
"buffer_load_dwordx4 v250,s[16:19],s74 offen lds\n"
"s_addk_i32 m0,4096\n"
"s_addk_i32 s74,128\n"
"buffer_load_dwordx4 v250,s[16:19],s74 offen lds\n"
"s_waitcnt lgkmcnt(0)\n"
"s_barrier\n"
"global_load_ushort v192,v195,s[4:5]\n"
"global_load_ushort v193,v195,s[6:7]\n"
"global_load_ushort v194,v195,s[82:83]\n"
"v_mov_b32 v210,0\n"
"v_mov_b32 v211,0\n"
"v_mov_b32 v212,0\n"
"v_mov_b32 v213,0\n"
"s_waitcnt vmcnt(15)\n"
"ds_read_b128 v[0:3],v174\n"
"ds_read_b128 v[4:7],v175\n"
"s_addk_i32 m0,4096\n"
"s_addk_i32 s74,128\n"
"buffer_load_dwordx4 v250,s[16:19],s74 offen lds\n"
"v_dot2c_f32_bf16 v210,v72,v208\n"
"v_dot2c_f32_bf16 v212,v72,v72\n"
"v_dot2c_f32_bf16 v211,v73,v208\n"
"v_dot2c_f32_bf16 v213,v73,v73\n"
"v_dot2c_f32_bf16 v210,v74,v208\n"
"v_dot2c_f32_bf16 v212,v74,v74\n"
"v_dot2c_f32_bf16 v211,v75,v208\n"
"v_dot2c_f32_bf16 v213,v75,v75\n"
"s_waitcnt vmcnt(15)\n"
"ds_read_b128 v[8:11],v174 offset:2048\n"
"ds_read_b128 v[12:15],v175 offset:2048\n"
"s_addk_i32 m0,4096\n"
"s_addk_i32 s74,128\n"
"buffer_load_dwordx4 v250,s[16:19],s74 offen lds\n"
"v_dot2c_f32_bf16 v210,v76,v208\n"
"v_dot2c_f32_bf16 v212,v76,v76\n"
"v_dot2c_f32_bf16 v211,v77,v208\n"
"v_dot2c_f32_bf16 v213,v77,v77\n"
"v_dot2c_f32_bf16 v210,v78,v208\n"
"v_dot2c_f32_bf16 v212,v78,v78\n"
"v_dot2c_f32_bf16 v211,v79,v208\n"
"v_dot2c_f32_bf16 v213,v79,v79\n"
"s_waitcnt vmcnt(15)\n"
"s_cmp_lg_u32 s11,4\n"
"s_cbranch_scc1 .Lk_hebs\n"
"s_add_u32 s96,s69,12\n"
"s_lshl_b32 s96,1,s96\n"
"s_lshl_b32 s97,1,s68\n"
"v_add_u32 v178,1,v176\n"
"s_mov_b64 exec,s[96:97]\n"
"buffer_store_dword v178,v177,s[32:35],0 offen sc1\n"
"s_mov_b64 exec,-1\n"
".Lk_hebs:\n"
"ds_read_b128 v[16:19],v174 offset:4096\n"
"ds_read_b128 v[20:23],v175 offset:4096\n"
"s_add_u32 m0,s73,61440\n"
"s_add_u32 s74,s80,19200\n"
"buffer_load_dwordx4 v250,s[16:19],s74 offen lds\n"
"v_dot2c_f32_bf16 v210,v80,v208\n"
"v_dot2c_f32_bf16 v212,v80,v80\n"
"v_dot2c_f32_bf16 v211,v81,v208\n"
"v_dot2c_f32_bf16 v213,v81,v81\n"
"v_dot2c_f32_bf16 v210,v82,v208\n"
"v_dot2c_f32_bf16 v212,v82,v82\n"
"v_dot2c_f32_bf16 v211,v83,v208\n"
"v_dot2c_f32_bf16 v213,v83,v83\n"
"s_waitcnt vmcnt(15)\n"
"ds_read_b128 v[24:27],v174 offset:6144\n"
"ds_read_b128 v[28:31],v175 offset:6144\n"
"s_addk_i32 m0,4096\n"
"s_addk_i32 s74,128\n"
"buffer_load_dwordx4 v250,s[16:19],s74 offen lds\n"
"v_dot2c_f32_bf16 v210,v84,v208\n"
"v_dot2c_f32_bf16 v212,v84,v84\n"
"v_dot2c_f32_bf16 v211,v85,v208\n"
"v_dot2c_f32_bf16 v213,v85,v85\n"
"v_dot2c_f32_bf16 v210,v86,v208\n"
"v_dot2c_f32_bf16 v212,v86,v86\n"
"v_dot2c_f32_bf16 v211,v87,v208\n"
"v_dot2c_f32_bf16 v213,v87,v87\n"
"s_waitcnt vmcnt(15)\n"
"ds_read_b128 v[32:35],v174 offset:8192\n"
"ds_read_b128 v[36:39],v175 offset:8192\n"
"s_addk_i32 m0,4096\n"
"s_addk_i32 s74,128\n"
"buffer_load_dwordx4 v250,s[16:19],s74 offen lds\n"
"v_dot2c_f32_bf16 v210,v88,v208\n"
"v_dot2c_f32_bf16 v212,v88,v88\n"
"v_dot2c_f32_bf16 v211,v89,v208\n"
"v_dot2c_f32_bf16 v213,v89,v89\n"
"v_dot2c_f32_bf16 v210,v90,v208\n"
"v_dot2c_f32_bf16 v212,v90,v90\n"
"v_dot2c_f32_bf16 v211,v91,v208\n"
"v_dot2c_f32_bf16 v213,v91,v91\n"
"s_waitcnt vmcnt(15)\n"
"ds_read_b128 v[40:43],v174 offset:10240\n"
"ds_read_b128 v[44:47],v175 offset:10240\n"
"v_dot2c_f32_bf16 v210,v92,v208\n"
"v_dot2c_f32_bf16 v212,v92,v92\n"
"v_dot2c_f32_bf16 v211,v93,v208\n"
"v_dot2c_f32_bf16 v213,v93,v93\n"
"v_dot2c_f32_bf16 v210,v94,v208\n"
"v_dot2c_f32_bf16 v212,v94,v94\n"
"v_dot2c_f32_bf16 v211,v95,v208\n"
"v_dot2c_f32_bf16 v213,v95,v95\n"
"s_waitcnt vmcnt(14)\n"
"ds_read_b128 v[48:51],v174 offset:12288\n"
"ds_read_b128 v[52:55],v175 offset:12288\n"
"v_dot2c_f32_bf16 v210,v96,v208\n"
"v_dot2c_f32_bf16 v212,v96,v96\n"
"v_dot2c_f32_bf16 v211,v97,v208\n"
"v_dot2c_f32_bf16 v213,v97,v97\n"
"v_dot2c_f32_bf16 v210,v98,v208\n"
"v_dot2c_f32_bf16 v212,v98,v98\n"
"v_dot2c_f32_bf16 v211,v99,v208\n"
"v_dot2c_f32_bf16 v213,v99,v99\n"
"s_waitcnt vmcnt(13)\n"
"ds_read_b128 v[56:59],v174 offset:14336\n"
"ds_read_b128 v[60:63],v175 offset:14336\n"
"v_dot2c_f32_bf16 v210,v100,v208\n"
"v_dot2c_f32_bf16 v212,v100,v100\n"
"v_dot2c_f32_bf16 v211,v101,v208\n"
"v_dot2c_f32_bf16 v213,v101,v101\n"
"v_dot2c_f32_bf16 v210,v102,v208\n"
"v_dot2c_f32_bf16 v212,v102,v102\n"
"v_dot2c_f32_bf16 v211,v103,v208\n"
"v_dot2c_f32_bf16 v213,v103,v103\n"
"s_waitcnt vmcnt(12)\n"
"ds_read_b128 v[64:67],v174 offset:16384\n"
"ds_read_b128 v[68:71],v175 offset:16384\n"
"v_dot2c_f32_bf16 v210,v104,v208\n"
"v_dot2c_f32_bf16 v212,v104,v104\n"
"v_dot2c_f32_bf16 v211,v105,v208\n"
"v_dot2c_f32_bf16 v213,v105,v105\n"
"v_dot2c_f32_bf16 v210,v106,v208\n"
"v_dot2c_f32_bf16 v212,v106,v106\n"
"v_dot2c_f32_bf16 v211,v107,v208\n"
"v_dot2c_f32_bf16 v213,v107,v107\n"
"s_nop 1\n"
"v_add_f32 v210,v210,v211\n"
"v_add_f32 v212,v212,v213\n"
"s_nop 0\n"
"v_add_f32_dpp v210,v210,v210 quad_perm:[1,0,3,2] row_mask:0xf bank_mask:0xf\n"
"v_add_f32_dpp v212,v212,v212 quad_perm:[1,0,3,2] row_mask:0xf bank_mask:0xf\n"
"s_nop 0\n"
"v_add_f32_dpp v210,v210,v210 quad_perm:[2,3,0,1] row_mask:0xf bank_mask:0xf\n"
"v_add_f32_dpp v212,v212,v212 quad_perm:[2,3,0,1] row_mask:0xf bank_mask:0xf\n"
"s_nop 0\n"
"v_add_f32_dpp v210,v210,v210 row_half_mirror row_mask:0xf bank_mask:0xf\n"
"v_add_f32_dpp v212,v212,v212 row_half_mirror row_mask:0xf bank_mask:0xf\n"
"v_mul_f32 v218,0x3ae38e39,v210\n"
"v_mul_f32 v212,0x3ae38e39,v212\n"
"v_fma_f32 v212,-v218,v218,v212\n"
"v_max_f32 v212,0,v212\n"
"v_add_f32 v219,0x3727c5ac,v212\n"
"v_rsq_f32 v219,v219\n"
"s_nop 0\n"
"v_cvt_pk_bf16_f32 v214,v219,0\n"
"v_lshlrev_b32 v215,16,v214\n"
"v_mul_f32_e64 v216,-v218,v215\n"
"v_dot2_f32_bf16 v240,v72,v214,v216\n"
"v_dot2_f32_bf16 v241,v72,v215,v216\n"
"v_dot2_f32_bf16 v242,v73,v214,v216\n"
"v_dot2_f32_bf16 v243,v73,v215,v216\n"
"v_dot2_f32_bf16 v244,v74,v214,v216\n"
"v_dot2_f32_bf16 v245,v74,v215,v216\n"
"v_cvt_pk_bf16_f32 v72,v240,v241\n"
"v_dot2_f32_bf16 v240,v75,v214,v216\n"
"v_dot2_f32_bf16 v241,v75,v215,v216\n"
"v_cvt_pk_bf16_f32 v73,v242,v243\n"
"v_dot2_f32_bf16 v242,v76,v214,v216\n"
"v_dot2_f32_bf16 v243,v76,v215,v216\n"
"v_cvt_pk_bf16_f32 v74,v244,v245\n"
"v_dot2_f32_bf16 v244,v77,v214,v216\n"
"v_dot2_f32_bf16 v245,v77,v215,v216\n"
"v_cvt_pk_bf16_f32 v75,v240,v241\n"
"s_waitcnt lgkmcnt(0)\n"
"s_barrier\n"
"ds_write_b128 v220,v[72:75]\n"
"v_dot2_f32_bf16 v240,v78,v214,v216\n"
"v_dot2_f32_bf16 v241,v78,v215,v216\n"
"v_cvt_pk_bf16_f32 v76,v242,v243\n"
"v_dot2_f32_bf16 v242,v79,v214,v216\n"
"v_dot2_f32_bf16 v243,v79,v215,v216\n"
"v_cvt_pk_bf16_f32 v77,v244,v245\n"
"v_dot2_f32_bf16 v244,v80,v214,v216\n"
"v_dot2_f32_bf16 v245,v80,v215,v216\n"
"v_cvt_pk_bf16_f32 v78,v240,v241\n"
"v_dot2_f32_bf16 v240,v81,v214,v216\n"
"v_dot2_f32_bf16 v241,v81,v215,v216\n"
"v_cvt_pk_bf16_f32 v79,v242,v243\n"
"ds_write_b128 v220,v[76:79] offset:2048\n"
"v_dot2_f32_bf16 v242,v82,v214,v216\n"
"v_dot2_f32_bf16 v243,v82,v215,v216\n"
"v_cvt_pk_bf16_f32 v80,v244,v245\n"
"v_dot2_f32_bf16 v244,v83,v214,v216\n"
"v_dot2_f32_bf16 v245,v83,v215,v216\n"
"v_cvt_pk_bf16_f32 v81,v240,v241\n"
"v_dot2_f32_bf16 v240,v84,v214,v216\n"
"v_dot2_f32_bf16 v241,v84,v215,v216\n"
"v_cvt_pk_bf16_f32 v82,v242,v243\n"
"v_dot2_f32_bf16 v242,v85,v214,v216\n"
"v_dot2_f32_bf16 v243,v85,v215,v216\n"
"v_cvt_pk_bf16_f32 v83,v244,v245\n"
"ds_write_b128 v220,v[80:83] offset:4096\n"
"v_dot2_f32_bf16 v244,v86,v214,v216\n"
"v_dot2_f32_bf16 v245,v86,v215,v216\n"
"v_cvt_pk_bf16_f32 v84,v240,v241\n"
"v_dot2_f32_bf16 v240,v87,v214,v216\n"
"v_dot2_f32_bf16 v241,v87,v215,v216\n"
"v_cvt_pk_bf16_f32 v85,v242,v243\n"
"v_dot2_f32_bf16 v242,v88,v214,v216\n"
"v_dot2_f32_bf16 v243,v88,v215,v216\n"
"v_cvt_pk_bf16_f32 v86,v244,v245\n"
"v_dot2_f32_bf16 v244,v89,v214,v216\n"
"v_dot2_f32_bf16 v245,v89,v215,v216\n"
"v_cvt_pk_bf16_f32 v87,v240,v241\n"
"ds_write_b128 v220,v[84:87] offset:6144\n"
"v_dot2_f32_bf16 v240,v90,v214,v216\n"
"v_dot2_f32_bf16 v241,v90,v215,v216\n"
"v_cvt_pk_bf16_f32 v88,v242,v243\n"
"v_dot2_f32_bf16 v242,v91,v214,v216\n"
"v_dot2_f32_bf16 v243,v91,v215,v216\n"
"v_cvt_pk_bf16_f32 v89,v244,v245\n"
"v_dot2_f32_bf16 v244,v92,v214,v216\n"
"v_dot2_f32_bf16 v245,v92,v215,v216\n"
"v_cvt_pk_bf16_f32 v90,v240,v241\n"
"v_dot2_f32_bf16 v240,v93,v214,v216\n"
"v_dot2_f32_bf16 v241,v93,v215,v216\n"
"v_cvt_pk_bf16_f32 v91,v242,v243\n"
"ds_write_b128 v220,v[88:91] offset:8192\n"
"v_dot2_f32_bf16 v242,v94,v214,v216\n"
"v_dot2_f32_bf16 v243,v94,v215,v216\n"
"v_cvt_pk_bf16_f32 v92,v244,v245\n"
"v_dot2_f32_bf16 v244,v95,v214,v216\n"
"v_dot2_f32_bf16 v245,v95,v215,v216\n"
"v_cvt_pk_bf16_f32 v93,v240,v241\n"
"v_dot2_f32_bf16 v240,v96,v214,v216\n"
"v_dot2_f32_bf16 v241,v96,v215,v216\n"
"v_cvt_pk_bf16_f32 v94,v242,v243\n"
"v_dot2_f32_bf16 v242,v97,v214,v216\n"
"v_dot2_f32_bf16 v243,v97,v215,v216\n"
"v_cvt_pk_bf16_f32 v95,v244,v245\n"
"ds_write_b128 v220,v[92:95] offset:10240\n"
"v_dot2_f32_bf16 v244,v98,v214,v216\n"
"v_dot2_f32_bf16 v245,v98,v215,v216\n"
"v_cvt_pk_bf16_f32 v96,v240,v241\n"
"v_dot2_f32_bf16 v240,v99,v214,v216\n"
"v_dot2_f32_bf16 v241,v99,v215,v216\n"
"v_cvt_pk_bf16_f32 v97,v242,v243\n"
"v_dot2_f32_bf16 v242,v100,v214,v216\n"
"v_dot2_f32_bf16 v243,v100,v215,v216\n"
"v_cvt_pk_bf16_f32 v98,v244,v245\n"
"v_dot2_f32_bf16 v244,v101,v214,v216\n"
"v_dot2_f32_bf16 v245,v101,v215,v216\n"
"v_cvt_pk_bf16_f32 v99,v240,v241\n"
"ds_write_b128 v220,v[96:99] offset:12288\n"
"v_dot2_f32_bf16 v240,v102,v214,v216\n"
"v_dot2_f32_bf16 v241,v102,v215,v216\n"
"v_cvt_pk_bf16_f32 v100,v242,v243\n"
"v_dot2_f32_bf16 v242,v103,v214,v216\n"
"v_dot2_f32_bf16 v243,v103,v215,v216\n"
"v_cvt_pk_bf16_f32 v101,v244,v245\n"
"v_dot2_f32_bf16 v244,v104,v214,v216\n"
"v_dot2_f32_bf16 v245,v104,v215,v216\n"
"v_cvt_pk_bf16_f32 v102,v240,v241\n"
"v_dot2_f32_bf16 v240,v105,v214,v216\n"
"v_dot2_f32_bf16 v241,v105,v215,v216\n"
"v_cvt_pk_bf16_f32 v103,v242,v243\n"
"ds_write_b128 v220,v[100:103] offset:14336\n"
"v_dot2_f32_bf16 v242,v106,v214,v216\n"
"v_dot2_f32_bf16 v243,v106,v215,v216\n"
"v_cvt_pk_bf16_f32 v104,v244,v245\n"
"v_dot2_f32_bf16 v244,v107,v214,v216\n"
"v_dot2_f32_bf16 v245,v107,v215,v216\n"
"v_cvt_pk_bf16_f32 v105,v240,v241\n"
"v_cvt_pk_bf16_f32 v106,v242,v243\n"
"s_nop 0\n"
"v_cvt_pk_bf16_f32 v107,v244,v245\n"
"ds_write_b128 v220,v[104:107] offset:16384\n"
"s_waitcnt lgkmcnt(0)\n"
"s_waitcnt vmcnt(0)\n"
"s_barrier\n"
"s_setprio 2\n"
"v_lshlrev_b32 v192,16,v192\n"
"v_lshlrev_b32 v193,16,v193\n"
"v_lshlrev_b32 v194,16,v194\n"
"v_mul_f32 v194,0x3fb8aa3b,v194\n"
"v_exp_f32 v194,v194\n"
"s_nop 0\n"
"v_cvt_pk_bf16_f32 v194,0,v194\n"
"v_mul_f32 v200,v194,v192\n"
"v_mul_f32 v201,v194,v193\n"
"v_mul_f32 v200,0x3fb8aa3b,v200\n"
"v_mul_f32 v201,0.15915494,v201\n"
"v_exp_f32 v200,v200\n"
"v_rndne_f32 v202,v201\n"
"v_sub_f32 v201,v201,v202\n"
"v_cos_f32 v196,v201\n"
"v_sin_f32 v197,v201\n"
"v_mul_f32 v202,v192,v192\n"
"v_fmac_f32 v202,v193,v193\n"
"v_mul_f32 v196,v196,v200\n"
"v_mul_f32 v197,v197,v200\n"
"v_rcp_f32 v202,v202\n"
"v_add_f32 v200,-1.0,v196\n"
"v_mul_f32 v202,0x3f6ff558,v202\n"
"v_mul_f32 v198,v200,v192\n"
"v_fmac_f32 v198,v197,v193\n"
"v_mul_f32 v199,v197,v192\n"
"v_fma_f32 v199,-v200,v193,v199\n"
"v_mul_f32 v198,v198,v202\n"
"v_mul_f32 v199,v199,v202\n"
"v_lshlrev_b32 v200,3,v188\n"
"v_add_u32 v200,110592,v200\n"
"ds_read_b128 v[72:75],v174\n"
"s_nop 2\n"
"ds_read_b128 v[76:79],v175\n"
"s_nop 2\n"
"ds_read_b128 v[80:83],v174 offset:2048\n"
"s_nop 2\n"
"ds_read_b128 v[84:87],v175 offset:2048\n"
"s_nop 2\n"
"ds_read_b128 v[88:91],v174 offset:4096\n"
"s_nop 2\n"
"ds_read_b128 v[92:95],v175 offset:4096\n"
"s_nop 2\n"
"ds_read_b128 v[96:99],v174 offset:6144\n"
"s_nop 2\n"
"ds_read_b128 v[100:103],v175 offset:6144\n"
"s_nop 2\n"
"ds_read_b128 v[104:107],v174 offset:8192\n"
"s_nop 2\n"
"ds_read_b128 v[108:111],v175 offset:8192\n"
"s_nop 2\n"
"ds_read_b128 v[112:115],v174 offset:10240\n"
"s_nop 2\n"
"ds_read_b128 v[116:119],v175 offset:10240\n"
"s_nop 2\n"
"ds_read_b128 v[120:123],v174 offset:12288\n"
"s_nop 2\n"
"ds_read_b128 v[124:127],v175 offset:12288\n"
"s_nop 2\n"
"ds_read_b128 v[128:131],v174 offset:14336\n"
"s_nop 2\n"
"ds_read_b128 v[132:135],v175 offset:14336\n"
"s_nop 2\n"
"ds_read_b128 v[136:139],v174 offset:16384\n"
"s_nop 2\n"
"ds_read_b128 v[140:143],v175 offset:16384\n"
"s_nop 2\n"
"s_waitcnt lgkmcnt(0)\n"
"s_barrier\n"
"ds_write_b64 v200,v[196:197] offset:51200\n"
"ds_write_b64 v200,v[198:199]\n"
"v_mul_f32 v240,v197,v197\n"
"v_mul_f32 v241,v196,v197\n"
"s_addk_i32 m0,4096\n"
"s_addk_i32 s74,17408\n"
"buffer_load_dwordx4 v250,s[16:19],s74 offen lds\n"
"v_fma_f32 v184,v196,v196,-v240\n"
"v_add_f32 v185,v241,v241\n"
"v_mul_f32 v240,v185,v185\n"
"s_addk_i32 m0,4096\n"
"s_addk_i32 s74,128\n"
"buffer_load_dwordx4 v250,s[16:19],s74 offen lds\n"
"v_mul_f32 v241,v184,v185\n"
"v_fma_f32 v184,v184,v184,-v240\n"
"v_add_f32 v185,v241,v241\n"
"s_addk_i32 m0,4096\n"
"s_addk_i32 s74,128\n"
"buffer_load_dwordx4 v250,s[16:19],s74 offen lds\n"
"v_mul_f32 v240,v185,v185\n"
"v_mul_f32 v241,v184,v185\n"
"v_fma_f32 v184,v184,v184,-v240\n"
"v_add_f32 v185,v241,v241\n"
"s_addk_i32 m0,4096\n"
"s_addk_i32 s74,128\n"
"buffer_load_dwordx4 v250,s[16:19],s74 offen lds\n"
"v_mul_f32 v240,v185,v185\n"
"v_mul_f32 v241,v184,v185\n"
"v_fma_f32 v186,v184,v184,-v240\n"
"s_addk_i32 m0,4096\n"
"s_addk_i32 s74,128\n"
"buffer_load_dwordx4 v250,s[16:19],s74 offen lds\n"
"v_add_f32 v187,v241,v241\n"
"v_mul_f32 v240,v187,v187\n"
"v_mul_f32 v241,v186,v187\n"
"s_addk_i32 m0,4096\n"
"s_addk_i32 s74,128\n"
"buffer_load_dwordx4 v250,s[16:19],s74 offen lds\n"
"v_fma_f32 v186,v186,v186,-v240\n"
"v_add_f32 v187,v241,v241\n"
"v_mul_f32 v240,v187,v187\n"
"v_mul_f32 v241,v186,v187\n"
"s_addk_i32 m0,4096\n"
"s_addk_i32 s74,128\n"
"buffer_load_dwordx4 v250,s[16:19],s74 offen lds\n"
"v_fma_f32 v186,v186,v186,-v240\n"
"v_add_f32 v187,v241,v241\n"
"v_mul_f32 v240,v187,v187\n"
"s_addk_i32 m0,4096\n"
"s_addk_i32 s74,128\n"
"buffer_load_dwordx4 v250,s[16:19],s74 offen lds\n"
"v_mul_f32 v241,v186,v187\n"
"v_fma_f32 v186,v186,v186,-v240\n"
"v_add_f32 v187,v241,v241\n"
"s_addk_i32 m0,4096\n"
"s_addk_i32 s74,128\n"
"buffer_load_dwordx4 v250,s[16:19],s74 offen lds\n"
"s_waitcnt lgkmcnt(0)\n"
"s_barrier\n"
"s_setprio 3\n"
"ds_read_b128 v[208:211],v248\n"
"ds_read_b128 v[212:215],v249\n"
"ds_read_b128 v[216:219],v248 offset:4096\n"
"ds_read_b128 v[220:223],v249 offset:4096\n"
"ds_read_b128 v[224:227],v248 offset:8192\n"
"ds_read_b128 v[228:231],v249 offset:8192\n"
"s_setprio 1\n"
"s_waitcnt lgkmcnt(4)\n"
"v_mfma_f32_16x16x32_bf16 v[192:195],v[0:3],v[208:211],0\n"
"ds_read_b128 v[232:235],v248 offset:12288\n"
"v_mfma_f32_16x16x32_bf16 v[192:195],v[4:7],v[212:215],v[192:195]\n"
"v_mfma_f32_16x16x32_bf16 v[196:199],v[72:75],v[208:211],0\n"
"ds_read_b128 v[236:239],v249 offset:12288\n"
"v_mfma_f32_16x16x32_bf16 v[196:199],v[76:79],v[212:215],v[196:199]\n"
"s_waitcnt lgkmcnt(4)\n"
"v_mfma_f32_16x16x32_bf16 v[196:199],v[80:83],v[216:219],v[196:199]\n"
"ds_read_b128 v[208:211],v248 offset:16384\n"
"v_mfma_f32_16x16x32_bf16 v[196:199],v[84:87],v[220:223],v[196:199]\n"
"v_mfma_f32_16x16x32_bf16 v[192:195],v[8:11],v[216:219],v[192:195]\n"
"ds_read_b128 v[212:215],v249 offset:16384\n"
"v_mfma_f32_16x16x32_bf16 v[192:195],v[12:15],v[220:223],v[192:195]\n"
"s_waitcnt lgkmcnt(4)\n"
"v_mfma_f32_16x16x32_bf16 v[192:195],v[16:19],v[224:227],v[192:195]\n"
"ds_read_b128 v[216:219],v248 offset:20480\n"
"v_mfma_f32_16x16x32_bf16 v[192:195],v[20:23],v[228:231],v[192:195]\n"
"v_mfma_f32_16x16x32_bf16 v[196:199],v[88:91],v[224:227],v[196:199]\n"
"ds_read_b128 v[220:223],v249 offset:20480\n"
"v_mfma_f32_16x16x32_bf16 v[196:199],v[92:95],v[228:231],v[196:199]\n"
"s_waitcnt lgkmcnt(4)\n"
"v_mfma_f32_16x16x32_bf16 v[196:199],v[96:99],v[232:235],v[196:199]\n"
"ds_read_b128 v[224:227],v248 offset:24576\n"
"v_mfma_f32_16x16x32_bf16 v[196:199],v[100:103],v[236:239],v[196:199]\n"
"v_mfma_f32_16x16x32_bf16 v[192:195],v[24:27],v[232:235],v[192:195]\n"
"ds_read_b128 v[228:231],v249 offset:24576\n"
"v_mfma_f32_16x16x32_bf16 v[192:195],v[28:31],v[236:239],v[192:195]\n"
"s_waitcnt lgkmcnt(4)\n"
"v_mfma_f32_16x16x32_bf16 v[192:195],v[32:35],v[208:211],v[192:195]\n"
"ds_read_b128 v[232:235],v248 offset:28672\n"
"v_mfma_f32_16x16x32_bf16 v[192:195],v[36:39],v[212:215],v[192:195]\n"
"v_mfma_f32_16x16x32_bf16 v[196:199],v[104:107],v[208:211],v[196:199]\n"
"ds_read_b128 v[236:239],v249 offset:28672\n"
"v_mfma_f32_16x16x32_bf16 v[196:199],v[108:111],v[212:215],v[196:199]\n"
"s_waitcnt lgkmcnt(4)\n"
"v_mfma_f32_16x16x32_bf16 v[196:199],v[112:115],v[216:219],v[196:199]\n"
"ds_read_b128 v[208:211],v248 offset:32768\n"
"v_mfma_f32_16x16x32_bf16 v[196:199],v[116:119],v[220:223],v[196:199]\n"
"v_mfma_f32_16x16x32_bf16 v[192:195],v[40:43],v[216:219],v[192:195]\n"
"ds_read_b128 v[212:215],v249 offset:32768\n"
"v_mfma_f32_16x16x32_bf16 v[192:195],v[44:47],v[220:223],v[192:195]\n"
"s_waitcnt lgkmcnt(4)\n"
"v_mfma_f32_16x16x32_bf16 v[192:195],v[48:51],v[224:227],v[192:195]\n"
"ds_read_b128 v[216:219],v248 offset:2048\n"
"v_mfma_f32_16x16x32_bf16 v[192:195],v[52:55],v[228:231],v[192:195]\n"
"v_mfma_f32_16x16x32_bf16 v[196:199],v[120:123],v[224:227],v[196:199]\n"
"ds_read_b128 v[220:223],v249 offset:2048\n"
"v_mfma_f32_16x16x32_bf16 v[196:199],v[124:127],v[228:231],v[196:199]\n"
"s_waitcnt lgkmcnt(4)\n"
"v_mfma_f32_16x16x32_bf16 v[196:199],v[128:131],v[232:235],v[196:199]\n"
"ds_read_b128 v[224:227],v248 offset:6144\n"
"v_mfma_f32_16x16x32_bf16 v[196:199],v[132:135],v[236:239],v[196:199]\n"
"v_mfma_f32_16x16x32_bf16 v[192:195],v[56:59],v[232:235],v[192:195]\n"
"ds_read_b128 v[228:231],v249 offset:6144\n"
"v_mfma_f32_16x16x32_bf16 v[192:195],v[60:63],v[236:239],v[192:195]\n"
"s_waitcnt lgkmcnt(4)\n"
"v_mfma_f32_16x16x32_bf16 v[192:195],v[64:67],v[208:211],v[192:195]\n"
"ds_read_b128 v[232:235],v248 offset:10240\n"
"v_mfma_f32_16x16x32_bf16 v[192:195],v[68:71],v[212:215],v[192:195]\n"
"v_mfma_f32_16x16x32_bf16 v[196:199],v[136:139],v[208:211],v[196:199]\n"
"ds_read_b128 v[236:239],v249 offset:10240\n"
"v_mfma_f32_16x16x32_bf16 v[196:199],v[140:143],v[212:215],v[196:199]\n"
"s_waitcnt lgkmcnt(4)\n"
"v_mfma_f32_16x16x32_bf16 v[200:203],v[0:3],v[216:219],0\n"
"ds_read_b128 v[208:211],v248 offset:14336\n"
"v_mfma_f32_16x16x32_bf16 v[200:203],v[4:7],v[220:223],v[200:203]\n"
"v_mfma_f32_16x16x32_bf16 v[204:207],v[72:75],v[216:219],0\n"
"ds_read_b128 v[212:215],v249 offset:14336\n"
"v_mfma_f32_16x16x32_bf16 v[204:207],v[76:79],v[220:223],v[204:207]\n"
"s_waitcnt lgkmcnt(4)\n"
"v_mfma_f32_16x16x32_bf16 v[204:207],v[80:83],v[224:227],v[204:207]\n"
"ds_read_b128 v[216:219],v248 offset:18432\n"
"v_mfma_f32_16x16x32_bf16 v[204:207],v[84:87],v[228:231],v[204:207]\n"
"v_mfma_f32_16x16x32_bf16 v[200:203],v[8:11],v[224:227],v[200:203]\n"
"ds_read_b128 v[220:223],v249 offset:18432\n"
"v_mfma_f32_16x16x32_bf16 v[200:203],v[12:15],v[228:231],v[200:203]\n"
"s_waitcnt lgkmcnt(4)\n"
"v_mfma_f32_16x16x32_bf16 v[200:203],v[16:19],v[232:235],v[200:203]\n"
"ds_read_b128 v[224:227],v248 offset:22528\n"
"v_mfma_f32_16x16x32_bf16 v[200:203],v[20:23],v[236:239],v[200:203]\n"
"v_mfma_f32_16x16x32_bf16 v[204:207],v[88:91],v[232:235],v[204:207]\n"
"ds_read_b128 v[228:231],v249 offset:22528\n"
"v_mfma_f32_16x16x32_bf16 v[204:207],v[92:95],v[236:239],v[204:207]\n"
"s_waitcnt lgkmcnt(4)\n"
"v_mfma_f32_16x16x32_bf16 v[204:207],v[96:99],v[208:211],v[204:207]\n"
"ds_read_b128 v[232:235],v248 offset:26624\n"
"v_mfma_f32_16x16x32_bf16 v[204:207],v[100:103],v[212:215],v[204:207]\n"
"v_mfma_f32_16x16x32_bf16 v[200:203],v[24:27],v[208:211],v[200:203]\n"
"ds_read_b128 v[236:239],v249 offset:26624\n"
"v_mfma_f32_16x16x32_bf16 v[200:203],v[28:31],v[212:215],v[200:203]\n"
"s_waitcnt lgkmcnt(4)\n"
"v_mfma_f32_16x16x32_bf16 v[200:203],v[32:35],v[216:219],v[200:203]\n"
"ds_read_b128 v[208:211],v248 offset:30720\n"
"v_mfma_f32_16x16x32_bf16 v[200:203],v[36:39],v[220:223],v[200:203]\n"
"v_mfma_f32_16x16x32_bf16 v[204:207],v[104:107],v[216:219],v[204:207]\n"
"ds_read_b128 v[212:215],v249 offset:30720\n"
"v_mfma_f32_16x16x32_bf16 v[204:207],v[108:111],v[220:223],v[204:207]\n"
"s_waitcnt lgkmcnt(4)\n"
"v_mfma_f32_16x16x32_bf16 v[204:207],v[112:115],v[224:227],v[204:207]\n"
"ds_read_b128 v[216:219],v248 offset:34816\n"
"v_mfma_f32_16x16x32_bf16 v[204:207],v[116:119],v[228:231],v[204:207]\n"
"v_mfma_f32_16x16x32_bf16 v[200:203],v[40:43],v[224:227],v[200:203]\n"
"ds_read_b128 v[220:223],v249 offset:34816\n"
"v_mfma_f32_16x16x32_bf16 v[200:203],v[44:47],v[228:231],v[200:203]\n"
"s_waitcnt lgkmcnt(4)\n"
"v_mfma_f32_16x16x32_bf16 v[200:203],v[48:51],v[232:235],v[200:203]\n"
"v_mfma_f32_16x16x32_bf16 v[200:203],v[52:55],v[236:239],v[200:203]\n"
"v_mfma_f32_16x16x32_bf16 v[204:207],v[120:123],v[232:235],v[204:207]\n"
"v_mfma_f32_16x16x32_bf16 v[204:207],v[124:127],v[236:239],v[204:207]\n"
"s_setprio 2\n"
"s_waitcnt lgkmcnt(2)\n"
"v_mfma_f32_16x16x32_bf16 v[204:207],v[128:131],v[208:211],v[204:207]\n"
"s_waitcnt lgkmcnt(0)\n"
"v_mfma_f32_16x16x32_bf16 v[204:207],v[132:135],v[212:215],v[204:207]\n"
"s_waitcnt vmcnt(0)\n"
"s_barrier\n"
"v_mfma_f32_16x16x32_bf16 v[200:203],v[56:59],v[208:211],v[200:203]\n"
"v_mfma_f32_16x16x32_bf16 v[200:203],v[60:63],v[212:215],v[200:203]\n"
"v_mfma_f32_16x16x32_bf16 v[200:203],v[64:67],v[216:219],v[200:203]\n"
"v_mfma_f32_16x16x32_bf16 v[200:203],v[68:71],v[220:223],v[200:203]\n"
"v_mfma_f32_16x16x32_bf16 v[204:207],v[136:139],v[216:219],v[204:207]\n"
"v_mfma_f32_16x16x32_bf16 v[204:207],v[140:143],v[220:223],v[204:207]\n"
"s_setprio 0\n"
"v_add_u32 v248,4294930432,v248\n"
"v_add_u32 v249,4294930432,v249\n"
"s_setprio 3\n"
"ds_read_b64 v[210:211],v255 offset:128\n"
"ds_read_b64 v[208:209],v255 offset:51328\n"
"s_setprio 0\n"
"s_waitcnt lgkmcnt(0)\n"
"v_mul_f32 v216,v210,v192\n"
"v_mul_f32 v217,v210,v200\n"
"v_fma_f32 v216,-v211,v200,v216\n"
"v_fmac_f32 v217,v211,v192\n"
"s_add_u32 m0,s73,36864\n"
"s_add_u32 s74,s80,73728\n"
"buffer_load_dwordx4 v250,s[16:19],s74 offen lds\n"
"v_mul_f32 v218,v210,v193\n"
"v_mul_f32 v219,v210,v201\n"
"v_fma_f32 v218,-v211,v201,v218\n"
"v_fmac_f32 v219,v211,v193\n"
"v_mul_f32 v220,v210,v194\n"
"v_mul_f32 v221,v210,v202\n"
"v_fma_f32 v220,-v211,v202,v220\n"
"v_fmac_f32 v221,v211,v194\n"
"s_addk_i32 m0,4096\n"
"s_addk_i32 s74,128\n"
"buffer_load_dwordx4 v250,s[16:19],s74 offen lds\n"
"v_mul_f32 v222,v210,v195\n"
"v_mul_f32 v223,v210,v203\n"
"v_fma_f32 v222,-v211,v203,v222\n"
"v_fmac_f32 v223,v211,v195\n"
"v_mul_f32 v224,v210,v196\n"
"v_mul_f32 v225,v210,v204\n"
"v_fma_f32 v224,-v211,v204,v224\n"
".Lk_pads3:\n"
"s_cbranch_execz .Lk_pads7\n"
"v_fmac_f32 v225,v211,v196\n"
"s_addk_i32 m0,4096\n"
"s_addk_i32 s74,128\n"
"buffer_load_dwordx4 v250,s[16:19],s74 offen lds\n"
"v_mul_f32 v226,v210,v197\n"
"v_mul_f32 v227,v210,v205\n"
"v_fma_f32 v226,-v211,v205,v226\n"
"v_fmac_f32 v227,v211,v197\n"
"v_mul_f32 v228,v210,v198\n"
"v_mul_f32 v229,v210,v206\n"
"v_fma_f32 v228,-v211,v206,v228\n"
"v_fmac_f32 v229,v211,v198\n"
"s_addk_i32 m0,4096\n"
"s_addk_i32 s74,128\n"
"buffer_load_dwordx4 v250,s[16:19],s74 offen lds\n"
"v_mul_f32 v230,v210,v199\n"
"v_mul_f32 v231,v210,v207\n"
"v_fma_f32 v230,-v211,v207,v230\n"
"v_fmac_f32 v231,v211,v199\n"
"v_cvt_pk_bf16_f32 v144,v216,v217\n"
"v_cvt_pk_bf16_f32 v145,v218,v219\n"
"v_cvt_pk_bf16_f32 v146,v220,v221\n"
"v_cvt_pk_bf16_f32 v147,v222,v223\n"
"v_cvt_pk_bf16_f32 v148,v224,v225\n"
"s_addk_i32 m0,4096\n"
"s_addk_i32 s74,128\n"
"buffer_load_dwordx4 v250,s[16:19],s74 offen lds\n"
"v_cvt_pk_bf16_f32 v149,v226,v227\n"
"v_cvt_pk_bf16_f32 v150,v228,v229\n"
"v_cvt_pk_bf16_f32 v151,v230,v231\n"
"v_fma_f32 v218,v208,v216,v218\n"
"v_fma_f32 v219,v208,v217,v219\n"
"v_fma_f32 v218,-v209,v217,v218\n"
"v_fmac_f32 v219,v209,v216\n"
"v_fma_f32 v220,v208,v218,v220\n"
"s_addk_i32 m0,4096\n"
"s_addk_i32 s74,128\n"
"buffer_load_dwordx4 v250,s[16:19],s74 offen lds\n"
"v_fma_f32 v221,v208,v219,v221\n"
"v_fma_f32 v220,-v209,v219,v220\n"
"v_fmac_f32 v221,v209,v218\n"
"v_fma_f32 v222,v208,v220,v222\n"
"v_fma_f32 v223,v208,v221,v223\n"
"v_fma_f32 v222,-v209,v221,v222\n"
"v_fmac_f32 v223,v209,v220\n"
"v_fma_f32 v224,v208,v222,v224\n"
"s_addk_i32 m0,4096\n"
"s_addk_i32 s74,128\n"
"buffer_load_dwordx4 v250,s[16:19],s74 offen lds\n"
"v_fma_f32 v225,v208,v223,v225\n"
"v_fma_f32 v224,-v209,v223,v224\n"
"v_fmac_f32 v225,v209,v222\n"
"v_fma_f32 v226,v208,v224,v226\n"
"v_fma_f32 v227,v208,v225,v227\n"
"v_fma_f32 v226,-v209,v225,v226\n"
"v_fmac_f32 v227,v209,v224\n"
"v_fma_f32 v228,v208,v226,v228\n"
"s_addk_i32 m0,4096\n"
"s_addk_i32 s74,128\n"
"buffer_load_dwordx4 v250,s[16:19],s74 offen lds\n"
"v_fma_f32 v229,v208,v227,v229\n"
"v_fma_f32 v228,-v209,v227,v228\n"
"v_fmac_f32 v229,v209,v226\n"
"v_fma_f32 v230,v208,v228,v230\n"
"v_fma_f32 v231,v208,v229,v231\n"
"v_fma_f32 v230,-v209,v229,v230\n"
"v_fmac_f32 v231,v209,v228\n"
"s_setprio 3\n"
"ds_write_b64 v253,v[230:231] offset:2048\n"
"s_setprio 0\n"
"s_addk_i32 m0,4096\n"
"s_addk_i32 s74,128\n"
"buffer_load_dwordx4 v250,s[16:19],s74 offen lds\n"
"s_nop 0\n"
"s_waitcnt lgkmcnt(0)\n"
"s_barrier\n"
"s_setprio 3\n"
"ds_read_b128 v[208:211],v248\n"
"ds_read_b128 v[212:215],v249\n"
"ds_read_b128 v[216:219],v248 offset:4096\n"
"ds_read_b128 v[220:223],v249 offset:4096\n"
"ds_read_b128 v[224:227],v248 offset:8192\n"
"ds_read_b128 v[228:231],v249 offset:8192\n"
"s_setprio 1\n"
"s_waitcnt lgkmcnt(4)\n"
"v_mfma_f32_16x16x32_bf16 v[192:195],v[0:3],v[208:211],0\n"
"ds_read_b128 v[232:235],v248 offset:12288\n"
"v_mfma_f32_16x16x32_bf16 v[192:195],v[4:7],v[212:215],v[192:195]\n"
"v_mfma_f32_16x16x32_bf16 v[196:199],v[72:75],v[208:211],0\n"
"ds_read_b128 v[236:239],v249 offset:12288\n"
"v_mfma_f32_16x16x32_bf16 v[196:199],v[76:79],v[212:215],v[196:199]\n"
"s_waitcnt lgkmcnt(4)\n"
"v_mfma_f32_16x16x32_bf16 v[196:199],v[80:83],v[216:219],v[196:199]\n"
"ds_read_b128 v[208:211],v248 offset:16384\n"
"v_mfma_f32_16x16x32_bf16 v[196:199],v[84:87],v[220:223],v[196:199]\n"
"v_mfma_f32_16x16x32_bf16 v[192:195],v[8:11],v[216:219],v[192:195]\n"
"ds_read_b128 v[212:215],v249 offset:16384\n"
"v_mfma_f32_16x16x32_bf16 v[192:195],v[12:15],v[220:223],v[192:195]\n"
"s_waitcnt lgkmcnt(4)\n"
"v_mfma_f32_16x16x32_bf16 v[192:195],v[16:19],v[224:227],v[192:195]\n"
"ds_read_b128 v[216:219],v248 offset:20480\n"
"v_mfma_f32_16x16x32_bf16 v[192:195],v[20:23],v[228:231],v[192:195]\n"
"v_mfma_f32_16x16x32_bf16 v[196:199],v[88:91],v[224:227],v[196:199]\n"
"ds_read_b128 v[220:223],v249 offset:20480\n"
"v_mfma_f32_16x16x32_bf16 v[196:199],v[92:95],v[228:231],v[196:199]\n"
"s_waitcnt lgkmcnt(4)\n"
"v_mfma_f32_16x16x32_bf16 v[196:199],v[96:99],v[232:235],v[196:199]\n"
"ds_read_b128 v[224:227],v248 offset:24576\n"
"v_mfma_f32_16x16x32_bf16 v[196:199],v[100:103],v[236:239],v[196:199]\n"
"v_mfma_f32_16x16x32_bf16 v[192:195],v[24:27],v[232:235],v[192:195]\n"
"ds_read_b128 v[228:231],v249 offset:24576\n"
"v_mfma_f32_16x16x32_bf16 v[192:195],v[28:31],v[236:239],v[192:195]\n"
"s_waitcnt lgkmcnt(4)\n"
"v_mfma_f32_16x16x32_bf16 v[192:195],v[32:35],v[208:211],v[192:195]\n"
"ds_read_b128 v[232:235],v248 offset:28672\n"
"v_mfma_f32_16x16x32_bf16 v[192:195],v[36:39],v[212:215],v[192:195]\n"
"v_mfma_f32_16x16x32_bf16 v[196:199],v[104:107],v[208:211],v[196:199]\n"
"ds_read_b128 v[236:239],v249 offset:28672\n"
"v_mfma_f32_16x16x32_bf16 v[196:199],v[108:111],v[212:215],v[196:199]\n"
"s_waitcnt lgkmcnt(4)\n"
"v_mfma_f32_16x16x32_bf16 v[196:199],v[112:115],v[216:219],v[196:199]\n"
"ds_read_b128 v[208:211],v248 offset:32768\n"
"v_mfma_f32_16x16x32_bf16 v[196:199],v[116:119],v[220:223],v[196:199]\n"
"v_mfma_f32_16x16x32_bf16 v[192:195],v[40:43],v[216:219],v[192:195]\n"
"ds_read_b128 v[212:215],v249 offset:32768\n"
"v_mfma_f32_16x16x32_bf16 v[192:195],v[44:47],v[220:223],v[192:195]\n"
"s_waitcnt lgkmcnt(4)\n"
"v_mfma_f32_16x16x32_bf16 v[192:195],v[48:51],v[224:227],v[192:195]\n"
"ds_read_b128 v[216:219],v248 offset:2048\n"
"v_mfma_f32_16x16x32_bf16 v[192:195],v[52:55],v[228:231],v[192:195]\n"
"v_mfma_f32_16x16x32_bf16 v[196:199],v[120:123],v[224:227],v[196:199]\n"
"ds_read_b128 v[220:223],v249 offset:2048\n"
"v_mfma_f32_16x16x32_bf16 v[196:199],v[124:127],v[228:231],v[196:199]\n"
"s_waitcnt lgkmcnt(4)\n"
"v_mfma_f32_16x16x32_bf16 v[196:199],v[128:131],v[232:235],v[196:199]\n"
"ds_read_b128 v[224:227],v248 offset:6144\n"
"v_mfma_f32_16x16x32_bf16 v[196:199],v[132:135],v[236:239],v[196:199]\n"
"v_mfma_f32_16x16x32_bf16 v[192:195],v[56:59],v[232:235],v[192:195]\n"
"ds_read_b128 v[228:231],v249 offset:6144\n"
"v_mfma_f32_16x16x32_bf16 v[192:195],v[60:63],v[236:239],v[192:195]\n"
"s_waitcnt lgkmcnt(4)\n"
"v_mfma_f32_16x16x32_bf16 v[192:195],v[64:67],v[208:211],v[192:195]\n"
"ds_read_b128 v[232:235],v248 offset:10240\n"
"v_mfma_f32_16x16x32_bf16 v[192:195],v[68:71],v[212:215],v[192:195]\n"
"v_mfma_f32_16x16x32_bf16 v[196:199],v[136:139],v[208:211],v[196:199]\n"
"ds_read_b128 v[236:239],v249 offset:10240\n"
"v_mfma_f32_16x16x32_bf16 v[196:199],v[140:143],v[212:215],v[196:199]\n"
"s_waitcnt lgkmcnt(4)\n"
"v_mfma_f32_16x16x32_bf16 v[200:203],v[0:3],v[216:219],0\n"
"ds_read_b128 v[208:211],v248 offset:14336\n"
"v_mfma_f32_16x16x32_bf16 v[200:203],v[4:7],v[220:223],v[200:203]\n"
"v_mfma_f32_16x16x32_bf16 v[204:207],v[72:75],v[216:219],0\n"
"ds_read_b128 v[212:215],v249 offset:14336\n"
"v_mfma_f32_16x16x32_bf16 v[204:207],v[76:79],v[220:223],v[204:207]\n"
"s_waitcnt lgkmcnt(4)\n"
"v_mfma_f32_16x16x32_bf16 v[204:207],v[80:83],v[224:227],v[204:207]\n"
"ds_read_b128 v[216:219],v248 offset:18432\n"
"v_mfma_f32_16x16x32_bf16 v[204:207],v[84:87],v[228:231],v[204:207]\n"
"v_mfma_f32_16x16x32_bf16 v[200:203],v[8:11],v[224:227],v[200:203]\n"
"ds_read_b128 v[220:223],v249 offset:18432\n"
"v_mfma_f32_16x16x32_bf16 v[200:203],v[12:15],v[228:231],v[200:203]\n"
"s_waitcnt lgkmcnt(4)\n"
"v_mfma_f32_16x16x32_bf16 v[200:203],v[16:19],v[232:235],v[200:203]\n"
"ds_read_b128 v[224:227],v248 offset:22528\n"
"v_mfma_f32_16x16x32_bf16 v[200:203],v[20:23],v[236:239],v[200:203]\n"
"v_mfma_f32_16x16x32_bf16 v[204:207],v[88:91],v[232:235],v[204:207]\n"
"ds_read_b128 v[228:231],v249 offset:22528\n"
"v_mfma_f32_16x16x32_bf16 v[204:207],v[92:95],v[236:239],v[204:207]\n"
"s_waitcnt lgkmcnt(4)\n"
"v_mfma_f32_16x16x32_bf16 v[204:207],v[96:99],v[208:211],v[204:207]\n"
"ds_read_b128 v[232:235],v248 offset:26624\n"
"v_mfma_f32_16x16x32_bf16 v[204:207],v[100:103],v[212:215],v[204:207]\n"
"v_mfma_f32_16x16x32_bf16 v[200:203],v[24:27],v[208:211],v[200:203]\n"
"ds_read_b128 v[236:239],v249 offset:26624\n"
"v_mfma_f32_16x16x32_bf16 v[200:203],v[28:31],v[212:215],v[200:203]\n"
"s_waitcnt lgkmcnt(4)\n"
"v_mfma_f32_16x16x32_bf16 v[200:203],v[32:35],v[216:219],v[200:203]\n"
"ds_read_b128 v[208:211],v248 offset:30720\n"
"v_mfma_f32_16x16x32_bf16 v[200:203],v[36:39],v[220:223],v[200:203]\n"
"v_mfma_f32_16x16x32_bf16 v[204:207],v[104:107],v[216:219],v[204:207]\n"
"ds_read_b128 v[212:215],v249 offset:30720\n"
"v_mfma_f32_16x16x32_bf16 v[204:207],v[108:111],v[220:223],v[204:207]\n"
"s_waitcnt lgkmcnt(4)\n"
"v_mfma_f32_16x16x32_bf16 v[204:207],v[112:115],v[224:227],v[204:207]\n"
"ds_read_b128 v[216:219],v248 offset:34816\n"
"v_mfma_f32_16x16x32_bf16 v[204:207],v[116:119],v[228:231],v[204:207]\n"
"v_mfma_f32_16x16x32_bf16 v[200:203],v[40:43],v[224:227],v[200:203]\n"
"ds_read_b128 v[220:223],v249 offset:34816\n"
"v_mfma_f32_16x16x32_bf16 v[200:203],v[44:47],v[228:231],v[200:203]\n"
"s_waitcnt lgkmcnt(4)\n"
"v_mfma_f32_16x16x32_bf16 v[200:203],v[48:51],v[232:235],v[200:203]\n"
"v_mfma_f32_16x16x32_bf16 v[200:203],v[52:55],v[236:239],v[200:203]\n"
"v_mfma_f32_16x16x32_bf16 v[204:207],v[120:123],v[232:235],v[204:207]\n"
"v_mfma_f32_16x16x32_bf16 v[204:207],v[124:127],v[236:239],v[204:207]\n"
"s_setprio 2\n"
"s_waitcnt lgkmcnt(2)\n"
"v_mfma_f32_16x16x32_bf16 v[204:207],v[128:131],v[208:211],v[204:207]\n"
"s_waitcnt lgkmcnt(0)\n"
"v_mfma_f32_16x16x32_bf16 v[204:207],v[132:135],v[212:215],v[204:207]\n"
"s_waitcnt vmcnt(0)\n"
"s_barrier\n"
"v_mfma_f32_16x16x32_bf16 v[200:203],v[56:59],v[208:211],v[200:203]\n"
"v_mfma_f32_16x16x32_bf16 v[200:203],v[60:63],v[212:215],v[200:203]\n"
"v_mfma_f32_16x16x32_bf16 v[200:203],v[64:67],v[216:219],v[200:203]\n"
"v_mfma_f32_16x16x32_bf16 v[200:203],v[68:71],v[220:223],v[200:203]\n"
"v_mfma_f32_16x16x32_bf16 v[204:207],v[136:139],v[216:219],v[204:207]\n"
"v_mfma_f32_16x16x32_bf16 v[204:207],v[140:143],v[220:223],v[204:207]\n"
"s_setprio 0\n"
"buffer_load_dword v179,v177,s[32:35],0 offen sc1\n"
"v_add_u32 v248,73728,v248\n"
"v_add_u32 v249,73728,v249\n"
"s_setprio 3\n"
"ds_read_b64 v[210:211],v255 offset:384\n"
"ds_read_b64 v[208:209],v255 offset:51584\n"
"s_setprio 0\n"
"s_waitcnt lgkmcnt(0)\n"
"v_mul_f32 v216,v210,v192\n"
"v_mul_f32 v217,v210,v200\n"
"v_fma_f32 v216,-v211,v200,v216\n"
"v_fmac_f32 v217,v211,v192\n"
"s_add_u32 m0,s73,0\n"
"s_add_u32 s74,s80,110592\n"
"buffer_load_dwordx4 v250,s[16:19],s74 offen lds\n"
"v_mul_f32 v218,v210,v193\n"
"v_mul_f32 v219,v210,v201\n"
"v_fma_f32 v218,-v211,v201,v218\n"
"v_fmac_f32 v219,v211,v193\n"
"v_mul_f32 v220,v210,v194\n"
"v_mul_f32 v221,v210,v202\n"
"v_fma_f32 v220,-v211,v202,v220\n"
"v_fmac_f32 v221,v211,v194\n"
"s_addk_i32 m0,4096\n"
"s_addk_i32 s74,128\n"
"buffer_load_dwordx4 v250,s[16:19],s74 offen lds\n"
"v_mul_f32 v222,v210,v195\n"
"v_mul_f32 v223,v210,v203\n"
"v_fma_f32 v222,-v211,v203,v222\n"
"v_fmac_f32 v223,v211,v195\n"
"v_mul_f32 v224,v210,v196\n"
"v_mul_f32 v225,v210,v204\n"
"v_fma_f32 v224,-v211,v204,v224\n"
"v_fmac_f32 v225,v211,v196\n"
"s_addk_i32 m0,4096\n"
"s_addk_i32 s74,128\n"
"buffer_load_dwordx4 v250,s[16:19],s74 offen lds\n"
"v_mul_f32 v226,v210,v197\n"
"v_mul_f32 v227,v210,v205\n"
"v_fma_f32 v226,-v211,v205,v226\n"
"v_fmac_f32 v227,v211,v197\n"
"v_mul_f32 v228,v210,v198\n"
"v_mul_f32 v229,v210,v206\n"
"v_fma_f32 v228,-v211,v206,v228\n"
"v_fmac_f32 v229,v211,v198\n"
"s_addk_i32 m0,4096\n"
"s_addk_i32 s74,128\n"
"buffer_load_dwordx4 v250,s[16:19],s74 offen lds\n"
"v_mul_f32 v230,v210,v199\n"
"v_mul_f32 v231,v210,v207\n"
"v_fma_f32 v230,-v211,v207,v230\n"
"v_fmac_f32 v231,v211,v199\n"
"v_cvt_pk_bf16_f32 v152,v216,v217\n"
"v_cvt_pk_bf16_f32 v153,v218,v219\n"
"v_cvt_pk_bf16_f32 v154,v220,v221\n"
"v_cvt_pk_bf16_f32 v155,v222,v223\n"
"v_cvt_pk_bf16_f32 v156,v224,v225\n"
"s_addk_i32 m0,4096\n"
"s_addk_i32 s74,128\n"
"buffer_load_dwordx4 v250,s[16:19],s74 offen lds\n"
"v_cvt_pk_bf16_f32 v157,v226,v227\n"
"v_cvt_pk_bf16_f32 v158,v228,v229\n"
"v_cvt_pk_bf16_f32 v159,v230,v231\n"
"v_fma_f32 v218,v208,v216,v218\n"
"v_fma_f32 v219,v208,v217,v219\n"
"v_fma_f32 v218,-v209,v217,v218\n"
"v_fmac_f32 v219,v209,v216\n"
"v_fma_f32 v220,v208,v218,v220\n"
"s_addk_i32 m0,4096\n"
"s_addk_i32 s74,128\n"
"buffer_load_dwordx4 v250,s[16:19],s74 offen lds\n"
"v_fma_f32 v221,v208,v219,v221\n"
"v_fma_f32 v220,-v209,v219,v220\n"
"v_fmac_f32 v221,v209,v218\n"
"v_fma_f32 v222,v208,v220,v222\n"
"v_fma_f32 v223,v208,v221,v223\n"
"v_fma_f32 v222,-v209,v221,v222\n"
"v_fmac_f32 v223,v209,v220\n"
"v_fma_f32 v224,v208,v222,v224\n"
"s_addk_i32 m0,4096\n"
"s_addk_i32 s74,128\n"
"buffer_load_dwordx4 v250,s[16:19],s74 offen lds\n"
"v_fma_f32 v225,v208,v223,v225\n"
"v_fma_f32 v224,-v209,v223,v224\n"
"v_fmac_f32 v225,v209,v222\n"
"v_fma_f32 v226,v208,v224,v226\n"
"v_fma_f32 v227,v208,v225,v227\n"
"v_fma_f32 v226,-v209,v225,v226\n"
"v_fmac_f32 v227,v209,v224\n"
"v_fma_f32 v228,v208,v226,v228\n"
"s_addk_i32 m0,4096\n"
"s_addk_i32 s74,128\n"
"buffer_load_dwordx4 v250,s[16:19],s74 offen lds\n"
"v_fma_f32 v229,v208,v227,v229\n"
"v_fma_f32 v228,-v209,v227,v228\n"
"v_fmac_f32 v229,v209,v226\n"
"v_fma_f32 v230,v208,v228,v230\n"
"v_fma_f32 v231,v208,v229,v231\n"
"v_fma_f32 v230,-v209,v229,v230\n"
"v_fmac_f32 v231,v209,v228\n"
"s_setprio 3\n"
"ds_write_b64 v253,v[230:231] offset:6144\n"
"s_setprio 0\n"
"s_addk_i32 m0,4096\n"
"s_addk_i32 s74,128\n"
"buffer_load_dwordx4 v250,s[16:19],s74 offen lds\n"
"s_nop 0\n"
"s_waitcnt lgkmcnt(0)\n"
"s_barrier\n"
"s_setprio 3\n"
"ds_read_b128 v[208:211],v248\n"
"ds_read_b128 v[212:215],v249\n"
"ds_read_b128 v[216:219],v248 offset:4096\n"
"ds_read_b128 v[220:223],v249 offset:4096\n"
"ds_read_b128 v[224:227],v248 offset:8192\n"
"ds_read_b128 v[228:231],v249 offset:8192\n"
"s_setprio 1\n"
"s_waitcnt lgkmcnt(4)\n"
"v_mfma_f32_16x16x32_bf16 v[192:195],v[0:3],v[208:211],0\n"
"ds_read_b128 v[232:235],v248 offset:12288\n"
"v_mfma_f32_16x16x32_bf16 v[192:195],v[4:7],v[212:215],v[192:195]\n"
"v_mfma_f32_16x16x32_bf16 v[196:199],v[72:75],v[208:211],0\n"
"ds_read_b128 v[236:239],v249 offset:12288\n"
"v_mfma_f32_16x16x32_bf16 v[196:199],v[76:79],v[212:215],v[196:199]\n"
"s_waitcnt lgkmcnt(4)\n"
"v_mfma_f32_16x16x32_bf16 v[196:199],v[80:83],v[216:219],v[196:199]\n"
"ds_read_b128 v[208:211],v248 offset:16384\n"
"v_mfma_f32_16x16x32_bf16 v[196:199],v[84:87],v[220:223],v[196:199]\n"
"v_mfma_f32_16x16x32_bf16 v[192:195],v[8:11],v[216:219],v[192:195]\n"
"ds_read_b128 v[212:215],v249 offset:16384\n"
"v_mfma_f32_16x16x32_bf16 v[192:195],v[12:15],v[220:223],v[192:195]\n"
"s_waitcnt lgkmcnt(4)\n"
"v_mfma_f32_16x16x32_bf16 v[192:195],v[16:19],v[224:227],v[192:195]\n"
"ds_read_b128 v[216:219],v248 offset:20480\n"
"v_mfma_f32_16x16x32_bf16 v[192:195],v[20:23],v[228:231],v[192:195]\n"
"v_mfma_f32_16x16x32_bf16 v[196:199],v[88:91],v[224:227],v[196:199]\n"
"ds_read_b128 v[220:223],v249 offset:20480\n"
"v_mfma_f32_16x16x32_bf16 v[196:199],v[92:95],v[228:231],v[196:199]\n"
"s_waitcnt lgkmcnt(4)\n"
"v_mfma_f32_16x16x32_bf16 v[196:199],v[96:99],v[232:235],v[196:199]\n"
"ds_read_b128 v[224:227],v248 offset:24576\n"
"v_mfma_f32_16x16x32_bf16 v[196:199],v[100:103],v[236:239],v[196:199]\n"
"v_mfma_f32_16x16x32_bf16 v[192:195],v[24:27],v[232:235],v[192:195]\n"
"ds_read_b128 v[228:231],v249 offset:24576\n"
"v_mfma_f32_16x16x32_bf16 v[192:195],v[28:31],v[236:239],v[192:195]\n"
"s_waitcnt lgkmcnt(4)\n"
"v_mfma_f32_16x16x32_bf16 v[192:195],v[32:35],v[208:211],v[192:195]\n"
"ds_read_b128 v[232:235],v248 offset:28672\n"
"v_mfma_f32_16x16x32_bf16 v[192:195],v[36:39],v[212:215],v[192:195]\n"
"v_mfma_f32_16x16x32_bf16 v[196:199],v[104:107],v[208:211],v[196:199]\n"
"ds_read_b128 v[236:239],v249 offset:28672\n"
"v_mfma_f32_16x16x32_bf16 v[196:199],v[108:111],v[212:215],v[196:199]\n"
"s_waitcnt lgkmcnt(4)\n"
"v_mfma_f32_16x16x32_bf16 v[196:199],v[112:115],v[216:219],v[196:199]\n"
"ds_read_b128 v[208:211],v248 offset:32768\n"
"v_mfma_f32_16x16x32_bf16 v[196:199],v[116:119],v[220:223],v[196:199]\n"
"v_mfma_f32_16x16x32_bf16 v[192:195],v[40:43],v[216:219],v[192:195]\n"
"ds_read_b128 v[212:215],v249 offset:32768\n"
"v_mfma_f32_16x16x32_bf16 v[192:195],v[44:47],v[220:223],v[192:195]\n"
"s_waitcnt lgkmcnt(4)\n"
"v_mfma_f32_16x16x32_bf16 v[192:195],v[48:51],v[224:227],v[192:195]\n"
"ds_read_b128 v[216:219],v248 offset:2048\n"
"v_mfma_f32_16x16x32_bf16 v[192:195],v[52:55],v[228:231],v[192:195]\n"
"v_mfma_f32_16x16x32_bf16 v[196:199],v[120:123],v[224:227],v[196:199]\n"
"ds_read_b128 v[220:223],v249 offset:2048\n"
"v_mfma_f32_16x16x32_bf16 v[196:199],v[124:127],v[228:231],v[196:199]\n"
"s_waitcnt lgkmcnt(4)\n"
"v_mfma_f32_16x16x32_bf16 v[196:199],v[128:131],v[232:235],v[196:199]\n"
"ds_read_b128 v[224:227],v248 offset:6144\n"
"v_mfma_f32_16x16x32_bf16 v[196:199],v[132:135],v[236:239],v[196:199]\n"
"v_mfma_f32_16x16x32_bf16 v[192:195],v[56:59],v[232:235],v[192:195]\n"
"ds_read_b128 v[228:231],v249 offset:6144\n"
"v_mfma_f32_16x16x32_bf16 v[192:195],v[60:63],v[236:239],v[192:195]\n"
"s_waitcnt lgkmcnt(4)\n"
"v_mfma_f32_16x16x32_bf16 v[192:195],v[64:67],v[208:211],v[192:195]\n"
"ds_read_b128 v[232:235],v248 offset:10240\n"
"v_mfma_f32_16x16x32_bf16 v[192:195],v[68:71],v[212:215],v[192:195]\n"
"v_mfma_f32_16x16x32_bf16 v[196:199],v[136:139],v[208:211],v[196:199]\n"
"ds_read_b128 v[236:239],v249 offset:10240\n"
"v_mfma_f32_16x16x32_bf16 v[196:199],v[140:143],v[212:215],v[196:199]\n"
"s_waitcnt lgkmcnt(4)\n"
"v_mfma_f32_16x16x32_bf16 v[200:203],v[0:3],v[216:219],0\n"
"ds_read_b128 v[208:211],v248 offset:14336\n"
"v_mfma_f32_16x16x32_bf16 v[200:203],v[4:7],v[220:223],v[200:203]\n"
"v_mfma_f32_16x16x32_bf16 v[204:207],v[72:75],v[216:219],0\n"
"ds_read_b128 v[212:215],v249 offset:14336\n"
"v_mfma_f32_16x16x32_bf16 v[204:207],v[76:79],v[220:223],v[204:207]\n"
"s_waitcnt lgkmcnt(4)\n"
"v_mfma_f32_16x16x32_bf16 v[204:207],v[80:83],v[224:227],v[204:207]\n"
"ds_read_b128 v[216:219],v248 offset:18432\n"
"v_mfma_f32_16x16x32_bf16 v[204:207],v[84:87],v[228:231],v[204:207]\n"
"v_mfma_f32_16x16x32_bf16 v[200:203],v[8:11],v[224:227],v[200:203]\n"
"ds_read_b128 v[220:223],v249 offset:18432\n"
"v_mfma_f32_16x16x32_bf16 v[200:203],v[12:15],v[228:231],v[200:203]\n"
"s_waitcnt lgkmcnt(4)\n"
"v_mfma_f32_16x16x32_bf16 v[200:203],v[16:19],v[232:235],v[200:203]\n"
"ds_read_b128 v[224:227],v248 offset:22528\n"
"v_mfma_f32_16x16x32_bf16 v[200:203],v[20:23],v[236:239],v[200:203]\n"
"v_mfma_f32_16x16x32_bf16 v[204:207],v[88:91],v[232:235],v[204:207]\n"
"ds_read_b128 v[228:231],v249 offset:22528\n"
"v_mfma_f32_16x16x32_bf16 v[204:207],v[92:95],v[236:239],v[204:207]\n"
"s_waitcnt lgkmcnt(4)\n"
"v_mfma_f32_16x16x32_bf16 v[204:207],v[96:99],v[208:211],v[204:207]\n"
"ds_read_b128 v[232:235],v248 offset:26624\n"
"v_mfma_f32_16x16x32_bf16 v[204:207],v[100:103],v[212:215],v[204:207]\n"
"v_mfma_f32_16x16x32_bf16 v[200:203],v[24:27],v[208:211],v[200:203]\n"
"ds_read_b128 v[236:239],v249 offset:26624\n"
"v_mfma_f32_16x16x32_bf16 v[200:203],v[28:31],v[212:215],v[200:203]\n"
"s_waitcnt lgkmcnt(4)\n"
"v_mfma_f32_16x16x32_bf16 v[200:203],v[32:35],v[216:219],v[200:203]\n"
"ds_read_b128 v[208:211],v248 offset:30720\n"
"v_mfma_f32_16x16x32_bf16 v[200:203],v[36:39],v[220:223],v[200:203]\n"
"v_mfma_f32_16x16x32_bf16 v[204:207],v[104:107],v[216:219],v[204:207]\n"
"ds_read_b128 v[212:215],v249 offset:30720\n"
"v_mfma_f32_16x16x32_bf16 v[204:207],v[108:111],v[220:223],v[204:207]\n"
"s_waitcnt lgkmcnt(4)\n"
"v_mfma_f32_16x16x32_bf16 v[204:207],v[112:115],v[224:227],v[204:207]\n"
"ds_read_b128 v[216:219],v248 offset:34816\n"
"v_mfma_f32_16x16x32_bf16 v[204:207],v[116:119],v[228:231],v[204:207]\n"
"v_mfma_f32_16x16x32_bf16 v[200:203],v[40:43],v[224:227],v[200:203]\n"
"ds_read_b128 v[220:223],v249 offset:34816\n"
"v_mfma_f32_16x16x32_bf16 v[200:203],v[44:47],v[228:231],v[200:203]\n"
"s_waitcnt lgkmcnt(4)\n"
"v_mfma_f32_16x16x32_bf16 v[200:203],v[48:51],v[232:235],v[200:203]\n"
"v_mfma_f32_16x16x32_bf16 v[200:203],v[52:55],v[236:239],v[200:203]\n"
"v_mfma_f32_16x16x32_bf16 v[204:207],v[120:123],v[232:235],v[204:207]\n"
"v_mfma_f32_16x16x32_bf16 v[204:207],v[124:127],v[236:239],v[204:207]\n"
"s_setprio 2\n"
"s_waitcnt lgkmcnt(2)\n"
"v_mfma_f32_16x16x32_bf16 v[204:207],v[128:131],v[208:211],v[204:207]\n"
"s_waitcnt lgkmcnt(0)\n"
"v_mfma_f32_16x16x32_bf16 v[204:207],v[132:135],v[212:215],v[204:207]\n"
"s_waitcnt vmcnt(0)\n"
"s_barrier\n"
"v_mfma_f32_16x16x32_bf16 v[200:203],v[56:59],v[208:211],v[200:203]\n"
"v_mfma_f32_16x16x32_bf16 v[200:203],v[60:63],v[212:215],v[200:203]\n"
"v_mfma_f32_16x16x32_bf16 v[200:203],v[64:67],v[216:219],v[200:203]\n"
"v_mfma_f32_16x16x32_bf16 v[200:203],v[68:71],v[220:223],v[200:203]\n"
"v_mfma_f32_16x16x32_bf16 v[204:207],v[136:139],v[216:219],v[204:207]\n"
"v_mfma_f32_16x16x32_bf16 v[204:207],v[140:143],v[220:223],v[204:207]\n"
"s_setprio 0\n"
"v_add_u32 v248,4294930432,v248\n"
"v_add_u32 v249,4294930432,v249\n"
"s_setprio 3\n"
"ds_read_b64 v[210:211],v255 offset:640\n"
"ds_read_b64 v[208:209],v255 offset:51840\n"
"s_setprio 0\n"
"s_waitcnt lgkmcnt(0)\n"
"v_mul_f32 v216,v210,v192\n"
"v_mul_f32 v217,v210,v200\n"
"v_fma_f32 v216,-v211,v200,v216\n"
"v_fmac_f32 v217,v211,v192\n"
"s_add_u32 m0,s73,73728\n"
"s_add_u32 s74,s92,0\n"
"buffer_load_dwordx4 v250,s[16:19],s74 offen lds\n"
"v_mul_f32 v218,v210,v193\n"
"v_mul_f32 v219,v210,v201\n"
"v_fma_f32 v218,-v211,v201,v218\n"
"v_fmac_f32 v219,v211,v193\n"
"v_mul_f32 v220,v210,v194\n"
"v_mul_f32 v221,v210,v202\n"
"v_fma_f32 v220,-v211,v202,v220\n"
"v_fmac_f32 v221,v211,v194\n"
"s_addk_i32 m0,4096\n"
"s_addk_i32 s74,128\n"
"buffer_load_dwordx4 v250,s[16:19],s74 offen lds\n"
"v_mul_f32 v222,v210,v195\n"
"v_mul_f32 v223,v210,v203\n"
"v_fma_f32 v222,-v211,v203,v222\n"
"v_fmac_f32 v223,v211,v195\n"
"v_mul_f32 v224,v210,v196\n"
"v_mul_f32 v225,v210,v204\n"
"v_fma_f32 v224,-v211,v204,v224\n"
"v_fmac_f32 v225,v211,v196\n"
"s_addk_i32 m0,4096\n"
"s_addk_i32 s74,128\n"
"buffer_load_dwordx4 v250,s[16:19],s74 offen lds\n"
"v_mul_f32 v226,v210,v197\n"
"v_mul_f32 v227,v210,v205\n"
"v_fma_f32 v226,-v211,v205,v226\n"
"v_fmac_f32 v227,v211,v197\n"
"v_mul_f32 v228,v210,v198\n"
"v_mul_f32 v229,v210,v206\n"
"v_fma_f32 v228,-v211,v206,v228\n"
"v_fmac_f32 v229,v211,v198\n"
"s_addk_i32 m0,4096\n"
"s_addk_i32 s74,128\n"
"buffer_load_dwordx4 v250,s[16:19],s74 offen lds\n"
"v_mul_f32 v230,v210,v199\n"
"v_mul_f32 v231,v210,v207\n"
"v_fma_f32 v230,-v211,v207,v230\n"
"v_fmac_f32 v231,v211,v199\n"
"v_cvt_pk_bf16_f32 v160,v216,v217\n"
"v_cvt_pk_bf16_f32 v161,v218,v219\n"
"v_cvt_pk_bf16_f32 v162,v220,v221\n"
"v_cvt_pk_bf16_f32 v163,v222,v223\n"
"v_cvt_pk_bf16_f32 v164,v224,v225\n"
"s_addk_i32 m0,4096\n"
"s_addk_i32 s74,128\n"
"buffer_load_dwordx4 v250,s[16:19],s74 offen lds\n"
"v_cvt_pk_bf16_f32 v165,v226,v227\n"
"v_cvt_pk_bf16_f32 v166,v228,v229\n"
"v_cvt_pk_bf16_f32 v167,v230,v231\n"
"v_fma_f32 v218,v208,v216,v218\n"
"v_fma_f32 v219,v208,v217,v219\n"
"v_fma_f32 v218,-v209,v217,v218\n"
"v_fmac_f32 v219,v209,v216\n"
"v_fma_f32 v220,v208,v218,v220\n"
"s_addk_i32 m0,4096\n"
"s_addk_i32 s74,128\n"
"buffer_load_dwordx4 v250,s[16:19],s74 offen lds\n"
"v_fma_f32 v221,v208,v219,v221\n"
"v_fma_f32 v220,-v209,v219,v220\n"
"v_fmac_f32 v221,v209,v218\n"
"v_fma_f32 v222,v208,v220,v222\n"
"v_fma_f32 v223,v208,v221,v223\n"
"v_fma_f32 v222,-v209,v221,v222\n"
"v_fmac_f32 v223,v209,v220\n"
"v_fma_f32 v224,v208,v222,v224\n"
"s_addk_i32 m0,4096\n"
"s_addk_i32 s74,128\n"
"buffer_load_dwordx4 v250,s[16:19],s74 offen lds\n"
"v_fma_f32 v225,v208,v223,v225\n"
"v_fma_f32 v224,-v209,v223,v224\n"
"v_fmac_f32 v225,v209,v222\n"
"v_fma_f32 v226,v208,v224,v226\n"
"v_fma_f32 v227,v208,v225,v227\n"
"v_fma_f32 v226,-v209,v225,v226\n"
"v_fmac_f32 v227,v209,v224\n"
"v_fma_f32 v228,v208,v226,v228\n"
"s_addk_i32 m0,4096\n"
"s_addk_i32 s74,128\n"
"buffer_load_dwordx4 v250,s[16:19],s74 offen lds\n"
"v_fma_f32 v229,v208,v227,v229\n"
"v_fma_f32 v228,-v209,v227,v228\n"
"v_fmac_f32 v229,v209,v226\n"
"v_fma_f32 v230,v208,v228,v230\n"
"v_fma_f32 v231,v208,v229,v231\n"
"v_fma_f32 v230,-v209,v229,v230\n"
"v_fmac_f32 v231,v209,v228\n"
"s_setprio 3\n"
"ds_write_b64 v253,v[230:231] offset:10240\n"
"s_setprio 0\n"
"s_addk_i32 m0,4096\n"
"s_addk_i32 s74,128\n"
"buffer_load_dwordx4 v250,s[16:19],s74 offen lds\n"
"s_nop 0\n"
"s_cmp_lg_u32 s11,4\n"
"s_cbranch_scc1 .Lk_he2s\n"
"s_mov_b32 s89,0x100000\n"
".Lk_he_spins:\n"
"v_cmp_ne_u32 vcc,v179,v176\n"
"s_add_u32 s96,s69,12\n"
"s_lshl_b32 s96,1,s96\n"
"s_lshl_b32 s97,1,s68\n"
"s_lshl_b64 s[96:97],s[96:97],16\n"
"s_mov_b32 s77,-1\n"
"s_cmp_eq_u32 vcc_lo,0xffff\n"
"s_cselect_b32 s96,s96,0\n"
"s_min_u32 s77,s77,vcc_lo\n"
"s_cmp_eq_u32 vcc_hi,0xffff\n"
"s_cselect_b32 s97,s97,0\n"
"s_min_u32 s77,s77,vcc_hi\n"
"s_cmp_eq_u64 s[96:97],0\n"
"s_cbranch_scc1 .Lk_he_nss\n"
"s_mov_b64 exec,s[96:97]\n"
"buffer_store_dword v178,v177,s[32:35],0 offen sc1\n"
"s_mov_b64 exec,-1\n"
".Lk_he_nss:\n"
"s_cmp_ge_u32 s77,0xffff\n"
"s_cbranch_scc1 .Lk_he2s\n"
"s_sub_u32 s89,s89,1\n"
"s_cbranch_scc1 .Lk_he2s\n"
"s_sleep 2\n"
"buffer_load_dword v179,v177,s[32:35],0 offen sc1\n"
"s_waitcnt vmcnt(0)\n"
"s_branch .Lk_he_spins\n"
".Lk_he2s:\n"
"s_waitcnt lgkmcnt(0)\n"
"s_barrier\n"
"s_setprio 3\n"
"ds_read_b128 v[208:211],v248\n"
"ds_read_b128 v[212:215],v249\n"
"ds_read_b128 v[216:219],v248 offset:4096\n"
"ds_read_b128 v[220:223],v249 offset:4096\n"
"ds_read_b128 v[224:227],v248 offset:8192\n"
"ds_read_b128 v[228:231],v249 offset:8192\n"
"s_setprio 1\n"
"s_waitcnt lgkmcnt(4)\n"
"v_mfma_f32_16x16x32_bf16 v[192:195],v[0:3],v[208:211],0\n"
"ds_read_b128 v[232:235],v248 offset:12288\n"
"v_mfma_f32_16x16x32_bf16 v[192:195],v[4:7],v[212:215],v[192:195]\n"
"v_mfma_f32_16x16x32_bf16 v[196:199],v[72:75],v[208:211],0\n"
"ds_read_b128 v[236:239],v249 offset:12288\n"
"v_mfma_f32_16x16x32_bf16 v[196:199],v[76:79],v[212:215],v[196:199]\n"
"s_waitcnt lgkmcnt(4)\n"
"v_mfma_f32_16x16x32_bf16 v[196:199],v[80:83],v[216:219],v[196:199]\n"
"ds_read_b128 v[208:211],v248 offset:16384\n"
"v_mfma_f32_16x16x32_bf16 v[196:199],v[84:87],v[220:223],v[196:199]\n"
"v_mfma_f32_16x16x32_bf16 v[192:195],v[8:11],v[216:219],v[192:195]\n"
"ds_read_b128 v[212:215],v249 offset:16384\n"
"v_mfma_f32_16x16x32_bf16 v[192:195],v[12:15],v[220:223],v[192:195]\n"
"s_waitcnt lgkmcnt(4)\n"
"v_mfma_f32_16x16x32_bf16 v[192:195],v[16:19],v[224:227],v[192:195]\n"
"ds_read_b128 v[216:219],v248 offset:20480\n"
"v_mfma_f32_16x16x32_bf16 v[192:195],v[20:23],v[228:231],v[192:195]\n"
"v_mfma_f32_16x16x32_bf16 v[196:199],v[88:91],v[224:227],v[196:199]\n"
"ds_read_b128 v[220:223],v249 offset:20480\n"
"v_mfma_f32_16x16x32_bf16 v[196:199],v[92:95],v[228:231],v[196:199]\n"
"s_waitcnt lgkmcnt(4)\n"
"v_mfma_f32_16x16x32_bf16 v[196:199],v[96:99],v[232:235],v[196:199]\n"
"ds_read_b128 v[224:227],v248 offset:24576\n"
"v_mfma_f32_16x16x32_bf16 v[196:199],v[100:103],v[236:239],v[196:199]\n"
"v_mfma_f32_16x16x32_bf16 v[192:195],v[24:27],v[232:235],v[192:195]\n"
"ds_read_b128 v[228:231],v249 offset:24576\n"
"v_mfma_f32_16x16x32_bf16 v[192:195],v[28:31],v[236:239],v[192:195]\n"
"s_waitcnt lgkmcnt(4)\n"
"v_mfma_f32_16x16x32_bf16 v[192:195],v[32:35],v[208:211],v[192:195]\n"
"ds_read_b128 v[232:235],v248 offset:28672\n"
".Lk_pads5:\n"
"s_cbranch_execz .Lk_hop_rets\n"
"v_mfma_f32_16x16x32_bf16 v[192:195],v[36:39],v[212:215],v[192:195]\n"
"v_mfma_f32_16x16x32_bf16 v[196:199],v[104:107],v[208:211],v[196:199]\n"
"ds_read_b128 v[236:239],v249 offset:28672\n"
"v_mfma_f32_16x16x32_bf16 v[196:199],v[108:111],v[212:215],v[196:199]\n"
"s_waitcnt lgkmcnt(4)\n"
"v_mfma_f32_16x16x32_bf16 v[196:199],v[112:115],v[216:219],v[196:199]\n"
"ds_read_b128 v[208:211],v248 offset:32768\n"
"v_mfma_f32_16x16x32_bf16 v[196:199],v[116:119],v[220:223],v[196:199]\n"
"v_mfma_f32_16x16x32_bf16 v[192:195],v[40:43],v[216:219],v[192:195]\n"
"ds_read_b128 v[212:215],v249 offset:32768\n"
"v_mfma_f32_16x16x32_bf16 v[192:195],v[44:47],v[220:223],v[192:195]\n"
"s_waitcnt lgkmcnt(4)\n"
"v_mfma_f32_16x16x32_bf16 v[192:195],v[48:51],v[224:227],v[192:195]\n"
"ds_read_b128 v[216:219],v248 offset:2048\n"
"v_mfma_f32_16x16x32_bf16 v[192:195],v[52:55],v[228:231],v[192:195]\n"
"v_mfma_f32_16x16x32_bf16 v[196:199],v[120:123],v[224:227],v[196:199]\n"
"ds_read_b128 v[220:223],v249 offset:2048\n"
"v_mfma_f32_16x16x32_bf16 v[196:199],v[124:127],v[228:231],v[196:199]\n"
"s_waitcnt lgkmcnt(4)\n"
"v_mfma_f32_16x16x32_bf16 v[196:199],v[128:131],v[232:235],v[196:199]\n"
"ds_read_b128 v[224:227],v248 offset:6144\n"
"v_mfma_f32_16x16x32_bf16 v[196:199],v[132:135],v[236:239],v[196:199]\n"
"v_mfma_f32_16x16x32_bf16 v[192:195],v[56:59],v[232:235],v[192:195]\n"
"ds_read_b128 v[228:231],v249 offset:6144\n"
"v_mfma_f32_16x16x32_bf16 v[192:195],v[60:63],v[236:239],v[192:195]\n"
"s_waitcnt lgkmcnt(4)\n"
"v_mfma_f32_16x16x32_bf16 v[192:195],v[64:67],v[208:211],v[192:195]\n"
"ds_read_b128 v[232:235],v248 offset:10240\n"
"v_mfma_f32_16x16x32_bf16 v[192:195],v[68:71],v[212:215],v[192:195]\n"
"v_mfma_f32_16x16x32_bf16 v[196:199],v[136:139],v[208:211],v[196:199]\n"
"ds_read_b128 v[236:239],v249 offset:10240\n"
"v_mfma_f32_16x16x32_bf16 v[196:199],v[140:143],v[212:215],v[196:199]\n"
"s_waitcnt lgkmcnt(4)\n"
"v_mfma_f32_16x16x32_bf16 v[200:203],v[0:3],v[216:219],0\n"
"ds_read_b128 v[208:211],v248 offset:14336\n"
"v_mfma_f32_16x16x32_bf16 v[200:203],v[4:7],v[220:223],v[200:203]\n"
"v_mfma_f32_16x16x32_bf16 v[204:207],v[72:75],v[216:219],0\n"
"ds_read_b128 v[212:215],v249 offset:14336\n"
"v_mfma_f32_16x16x32_bf16 v[204:207],v[76:79],v[220:223],v[204:207]\n"
"s_waitcnt lgkmcnt(4)\n"
"v_mfma_f32_16x16x32_bf16 v[204:207],v[80:83],v[224:227],v[204:207]\n"
"ds_read_b128 v[216:219],v248 offset:18432\n"
"v_mfma_f32_16x16x32_bf16 v[204:207],v[84:87],v[228:231],v[204:207]\n"
"v_mfma_f32_16x16x32_bf16 v[200:203],v[8:11],v[224:227],v[200:203]\n"
"ds_read_b128 v[220:223],v249 offset:18432\n"
"v_mfma_f32_16x16x32_bf16 v[200:203],v[12:15],v[228:231],v[200:203]\n"
"s_waitcnt lgkmcnt(4)\n"
"v_mfma_f32_16x16x32_bf16 v[200:203],v[16:19],v[232:235],v[200:203]\n"
"ds_read_b128 v[224:227],v248 offset:22528\n"
"v_mfma_f32_16x16x32_bf16 v[200:203],v[20:23],v[236:239],v[200:203]\n"
"v_mfma_f32_16x16x32_bf16 v[204:207],v[88:91],v[232:235],v[204:207]\n"
"ds_read_b128 v[228:231],v249 offset:22528\n"
"v_mfma_f32_16x16x32_bf16 v[204:207],v[92:95],v[236:239],v[204:207]\n"
"s_waitcnt lgkmcnt(4)\n"
"v_mfma_f32_16x16x32_bf16 v[204:207],v[96:99],v[208:211],v[204:207]\n"
"ds_read_b128 v[232:235],v248 offset:26624\n"
"v_mfma_f32_16x16x32_bf16 v[204:207],v[100:103],v[212:215],v[204:207]\n"
"v_mfma_f32_16x16x32_bf16 v[200:203],v[24:27],v[208:211],v[200:203]\n"
"ds_read_b128 v[236:239],v249 offset:26624\n"
"v_mfma_f32_16x16x32_bf16 v[200:203],v[28:31],v[212:215],v[200:203]\n"
"s_waitcnt lgkmcnt(4)\n"
"v_mfma_f32_16x16x32_bf16 v[200:203],v[32:35],v[216:219],v[200:203]\n"
"ds_read_b128 v[208:211],v248 offset:30720\n"
"v_mfma_f32_16x16x32_bf16 v[200:203],v[36:39],v[220:223],v[200:203]\n"
"v_mfma_f32_16x16x32_bf16 v[204:207],v[104:107],v[216:219],v[204:207]\n"
"ds_read_b128 v[212:215],v249 offset:30720\n"
"v_mfma_f32_16x16x32_bf16 v[204:207],v[108:111],v[220:223],v[204:207]\n"
"s_waitcnt lgkmcnt(4)\n"
"v_mfma_f32_16x16x32_bf16 v[204:207],v[112:115],v[224:227],v[204:207]\n"
"ds_read_b128 v[216:219],v248 offset:34816\n"
"v_mfma_f32_16x16x32_bf16 v[204:207],v[116:119],v[228:231],v[204:207]\n"
"v_mfma_f32_16x16x32_bf16 v[200:203],v[40:43],v[224:227],v[200:203]\n"
"ds_read_b128 v[220:223],v249 offset:34816\n"
"v_mfma_f32_16x16x32_bf16 v[200:203],v[44:47],v[228:231],v[200:203]\n"
"s_waitcnt lgkmcnt(4)\n"
"v_mfma_f32_16x16x32_bf16 v[200:203],v[48:51],v[232:235],v[200:203]\n"
"v_mfma_f32_16x16x32_bf16 v[200:203],v[52:55],v[236:239],v[200:203]\n"
"v_mfma_f32_16x16x32_bf16 v[204:207],v[120:123],v[232:235],v[204:207]\n"
"v_mfma_f32_16x16x32_bf16 v[204:207],v[124:127],v[236:239],v[204:207]\n"
"s_setprio 2\n"
"s_waitcnt lgkmcnt(2)\n"
"v_mfma_f32_16x16x32_bf16 v[204:207],v[128:131],v[208:211],v[204:207]\n"
"s_waitcnt lgkmcnt(0)\n"
"v_mfma_f32_16x16x32_bf16 v[204:207],v[132:135],v[212:215],v[204:207]\n"
"s_waitcnt vmcnt(0)\n"
"s_barrier\n"
"v_mfma_f32_16x16x32_bf16 v[200:203],v[56:59],v[208:211],v[200:203]\n"
"v_mfma_f32_16x16x32_bf16 v[200:203],v[60:63],v[212:215],v[200:203]\n"
"v_mfma_f32_16x16x32_bf16 v[200:203],v[64:67],v[216:219],v[200:203]\n"
"v_mfma_f32_16x16x32_bf16 v[200:203],v[68:71],v[220:223],v[200:203]\n"
"v_mfma_f32_16x16x32_bf16 v[204:207],v[136:139],v[216:219],v[204:207]\n"
"v_mfma_f32_16x16x32_bf16 v[204:207],v[140:143],v[220:223],v[204:207]\n"
"s_setprio 0\n"
"s_setprio 3\n"
"ds_read_b64 v[210:211],v255 offset:896\n"
"ds_read_b64 v[208:209],v255 offset:52096\n"
"s_setprio 0\n"
"s_waitcnt lgkmcnt(0)\n"
"v_mul_f32 v216,v210,v192\n"
"v_mul_f32 v217,v210,v200\n"
"v_fma_f32 v216,-v211,v200,v216\n"
"v_fmac_f32 v217,v211,v192\n"
"v_mul_f32 v218,v210,v193\n"
"v_mul_f32 v219,v210,v201\n"
"v_fma_f32 v218,-v211,v201,v218\n"
"v_fmac_f32 v219,v211,v193\n"
"v_mul_f32 v220,v210,v194\n"
"v_mul_f32 v221,v210,v202\n"
"v_fma_f32 v220,-v211,v202,v220\n"
"v_fmac_f32 v221,v211,v194\n"
"v_mul_f32 v222,v210,v195\n"
"v_mul_f32 v223,v210,v203\n"
"v_fma_f32 v222,-v211,v203,v222\n"
"s_add_u32 m0,s73,40960\n"
"s_add_u32 s74,s81,0\n"
"buffer_load_dwordx4 v251,s[24:27],s74 offen lds\n"
"v_fmac_f32 v223,v211,v195\n"
"v_mul_f32 v224,v210,v196\n"
"v_mul_f32 v225,v210,v204\n"
"v_fma_f32 v224,-v211,v204,v224\n"
"v_fmac_f32 v225,v211,v196\n"
"v_mul_f32 v226,v210,v197\n"
"v_mul_f32 v227,v210,v205\n"
"v_fma_f32 v226,-v211,v205,v226\n"
"v_fmac_f32 v227,v211,v197\n"
"v_mul_f32 v228,v210,v198\n"
"v_mul_f32 v229,v210,v206\n"
"v_fma_f32 v228,-v211,v206,v228\n"
"v_fmac_f32 v229,v211,v198\n"
"v_mul_f32 v230,v210,v199\n"
"v_mul_f32 v231,v210,v207\n"
"v_fma_f32 v230,-v211,v207,v230\n"
"v_fmac_f32 v231,v211,v199\n"
"v_cvt_pk_bf16_f32 v168,v216,v217\n"
"s_addk_i32 m0,8192\n"
"s_addk_i32 s74,128\n"
"buffer_load_dwordx4 v251,s[24:27],s74 offen lds\n"
"v_cvt_pk_bf16_f32 v169,v218,v219\n"
"v_cvt_pk_bf16_f32 v170,v220,v221\n"
"v_cvt_pk_bf16_f32 v171,v222,v223\n"
"v_cvt_pk_bf16_f32 v172,v224,v225\n"
"v_cvt_pk_bf16_f32 v173,v226,v227\n"
"v_cvt_pk_bf16_f32 v174,v228,v229\n"
"v_cvt_pk_bf16_f32 v175,v230,v231\n"
"v_fma_f32 v218,v208,v216,v218\n"
"v_fma_f32 v219,v208,v217,v219\n"
"v_fma_f32 v218,-v209,v217,v218\n"
"v_fmac_f32 v219,v209,v216\n"
"v_fma_f32 v220,v208,v218,v220\n"
"v_fma_f32 v221,v208,v219,v221\n"
"v_fma_f32 v220,-v209,v219,v220\n"
"v_fmac_f32 v221,v209,v218\n"
"v_fma_f32 v222,v208,v220,v222\n"
"v_fma_f32 v223,v208,v221,v223\n"
"v_fma_f32 v222,-v209,v221,v222\n"
"s_addk_i32 m0,8192\n"
"s_addk_i32 s74,-128\n"
"buffer_load_dwordx4 v251,s[28:31],s74 offen lds\n"
"v_fmac_f32 v223,v209,v220\n"
"v_fma_f32 v224,v208,v222,v224\n"
"v_fma_f32 v225,v208,v223,v225\n"
"v_fma_f32 v224,-v209,v223,v224\n"
"v_fmac_f32 v225,v209,v222\n"
"v_fma_f32 v226,v208,v224,v226\n"
"v_fma_f32 v227,v208,v225,v227\n"
"v_fma_f32 v226,-v209,v225,v226\n"
"v_fmac_f32 v227,v209,v224\n"
"v_fma_f32 v228,v208,v226,v228\n"
"v_fma_f32 v229,v208,v227,v229\n"
"v_fma_f32 v228,-v209,v227,v228\n"
"v_fmac_f32 v229,v209,v226\n"
"v_fma_f32 v230,v208,v228,v230\n"
"v_fma_f32 v231,v208,v229,v231\n"
"v_fma_f32 v230,-v209,v229,v230\n"
"v_fmac_f32 v231,v209,v228\n"
"s_setprio 3\n"
"ds_write_b64 v253,v[230:231] offset:14336\n"
"s_setprio 0\n"
"s_addk_i32 m0,8192\n"
"s_addk_i32 s74,128\n"
"buffer_load_dwordx4 v251,s[28:31],s74 offen lds\n"
"s_nop 0\n"
"s_waitcnt lgkmcnt(0)\n"
"s_barrier\n"
"s_setprio 1\n"
"s_barrier\n"
"s_cmp_ge_u32 s71,3\n"
"s_cbranch_scc1 .Lk_cy0\n"
"v_lshrrev_b32 v132,4,v188\n"
"v_and_b32 v133,15,v188\n"
"v_lshlrev_b32 v132,11,v132\n"
"v_lshl_add_u32 v132,v133,3,v132\n"
"v_add_u32 v132,143360,v132\n"
"ds_read_b64 v[72:73],v132\n"
"ds_read_b64 v[74:75],v132 offset:128\n"
"ds_read_b64 v[76:77],v132 offset:256\n"
"ds_read_b64 v[78:79],v132 offset:384\n"
"ds_read_b64 v[80:81],v132 offset:512\n"
"ds_read_b64 v[82:83],v132 offset:640\n"
"ds_read_b64 v[84:85],v132 offset:768\n"
"ds_read_b64 v[86:87],v132 offset:896\n"
"ds_read_b64 v[88:89],v132 offset:1024\n"
"ds_read_b64 v[90:91],v132 offset:1152\n"
"ds_read_b64 v[92:93],v132 offset:1280\n"
"ds_read_b64 v[94:95],v132 offset:1408\n"
"ds_read_b64 v[96:97],v132 offset:1536\n"
"ds_read_b64 v[98:99],v132 offset:1664\n"
"ds_read_b64 v[100:101],v132 offset:1792\n"
"ds_read_b64 v[102:103],v132 offset:1920\n"
"v_lshlrev_b32 v133,4,v188\n"
"s_lshl_b32 s84,s68,2\n"
"s_add_u32 s84,s84,s69\n"
"s_mul_i32 s84,s84,3072\n"
"s_add_u32 s84,s84,270336\n"
"s_lshl_b32 s85,s68,1\n"
"s_waitcnt lgkmcnt(0)\n"
"v_fma_f32 v136,v184,v72,v74\n"
"v_fma_f32 v137,v184,v73,v75\n"
"v_fma_f32 v136,-v185,v73,v136\n"
"v_fmac_f32 v137,v185,v72\n"
"v_fma_f32 v140,v184,v136,v76\n"
"v_fma_f32 v141,v184,v137,v77\n"
"v_fma_f32 v140,-v185,v137,v140\n"
"v_fmac_f32 v141,v185,v136\n"
"v_fma_f32 v136,v184,v140,v78\n"
"v_fma_f32 v137,v184,v141,v79\n"
"v_fma_f32 v136,-v185,v141,v136\n"
"v_fmac_f32 v137,v185,v140\n"
"v_fma_f32 v140,v184,v136,v80\n"
"v_fma_f32 v141,v184,v137,v81\n"
"v_fma_f32 v140,-v185,v137,v140\n"
"v_fmac_f32 v141,v185,v136\n"
"v_fma_f32 v136,v184,v140,v82\n"
"v_fma_f32 v137,v184,v141,v83\n"
"v_fma_f32 v136,-v185,v141,v136\n"
"v_fmac_f32 v137,v185,v140\n"
"v_fma_f32 v140,v184,v136,v84\n"
"v_fma_f32 v141,v184,v137,v85\n"
"v_fma_f32 v140,-v185,v137,v140\n"
"v_fmac_f32 v141,v185,v136\n"
"v_fma_f32 v136,v184,v140,v86\n"
"v_fma_f32 v137,v184,v141,v87\n"
"v_fma_f32 v136,-v185,v141,v136\n"
"v_fmac_f32 v137,v185,v140\n"
"v_fma_f32 v140,v184,v136,v88\n"
"v_fma_f32 v141,v184,v137,v89\n"
"v_fma_f32 v140,-v185,v137,v140\n"
"v_fmac_f32 v141,v185,v136\n"
"v_fma_f32 v136,v184,v140,v90\n"
"v_fma_f32 v137,v184,v141,v91\n"
"v_fma_f32 v136,-v185,v141,v136\n"
"v_fmac_f32 v137,v185,v140\n"
"v_fma_f32 v140,v184,v136,v92\n"
"v_fma_f32 v141,v184,v137,v93\n"
"v_fma_f32 v140,-v185,v137,v140\n"
"v_fmac_f32 v141,v185,v136\n"
"v_fma_f32 v136,v184,v140,v94\n"
"v_fma_f32 v137,v184,v141,v95\n"
"v_fma_f32 v136,-v185,v141,v136\n"
"v_fmac_f32 v137,v185,v140\n"
"v_fma_f32 v140,v184,v136,v96\n"
"v_fma_f32 v141,v184,v137,v97\n"
"v_fma_f32 v140,-v185,v137,v140\n"
"v_fmac_f32 v141,v185,v136\n"
"v_fma_f32 v136,v184,v140,v98\n"
"v_fma_f32 v137,v184,v141,v99\n"
"v_fma_f32 v136,-v185,v141,v136\n"
"v_fmac_f32 v137,v185,v140\n"
"v_fma_f32 v140,v184,v136,v100\n"
"v_fma_f32 v141,v184,v137,v101\n"
"v_fma_f32 v140,-v185,v137,v140\n"
"v_fmac_f32 v141,v185,v136\n"
"v_fma_f32 v136,v184,v140,v102\n"
"v_fma_f32 v137,v184,v141,v103\n"
"v_fma_f32 v136,-v185,v141,v136\n"
"v_fmac_f32 v137,v185,v140\n"
"v_mov_b32 v138,v137\n"
"v_mov_b32 v137,1\n"
"v_mov_b32 v139,1\n"
"buffer_store_dwordx4 v[136:139],v133,s[32:35],s84 offen sc1\n"
".Lk_cy0:\n"
"v_lshlrev_b32 v0,16,v144\n"
"v_and_b32 v1,0xffff0000,v144\n"
"v_lshlrev_b32 v2,16,v145\n"
"v_and_b32 v3,0xffff0000,v145\n"
"v_lshlrev_b32 v4,16,v146\n"
"v_and_b32 v5,0xffff0000,v146\n"
"v_lshlrev_b32 v6,16,v147\n"
"v_and_b32 v7,0xffff0000,v147\n"
"v_lshlrev_b32 v8,16,v148\n"
"v_and_b32 v9,0xffff0000,v148\n"
"v_lshlrev_b32 v10,16,v149\n"
"v_and_b32 v11,0xffff0000,v149\n"
"v_lshlrev_b32 v12,16,v150\n"
"v_and_b32 v13,0xffff0000,v150\n"
"v_lshlrev_b32 v14,16,v151\n"
"v_and_b32 v15,0xffff0000,v151\n"
"v_lshlrev_b32 v16,16,v152\n"
"v_and_b32 v17,0xffff0000,v152\n"
"v_lshlrev_b32 v18,16,v153\n"
"v_and_b32 v19,0xffff0000,v153\n"
"v_lshlrev_b32 v20,16,v154\n"
"v_and_b32 v21,0xffff0000,v154\n"
"v_lshlrev_b32 v22,16,v155\n"
"v_and_b32 v23,0xffff0000,v155\n"
"v_lshlrev_b32 v24,16,v156\n"
"v_and_b32 v25,0xffff0000,v156\n"
"v_lshlrev_b32 v26,16,v157\n"
"v_and_b32 v27,0xffff0000,v157\n"
"v_lshlrev_b32 v28,16,v158\n"
"v_and_b32 v29,0xffff0000,v158\n"
"v_lshlrev_b32 v30,16,v159\n"
"v_and_b32 v31,0xffff0000,v159\n"
"v_lshlrev_b32 v32,16,v160\n"
"v_and_b32 v33,0xffff0000,v160\n"
"v_lshlrev_b32 v34,16,v161\n"
"v_and_b32 v35,0xffff0000,v161\n"
"v_lshlrev_b32 v36,16,v162\n"
"v_and_b32 v37,0xffff0000,v162\n"
"v_lshlrev_b32 v38,16,v163\n"
"v_and_b32 v39,0xffff0000,v163\n"
"v_lshlrev_b32 v40,16,v164\n"
"v_and_b32 v41,0xffff0000,v164\n"
"v_lshlrev_b32 v42,16,v165\n"
"v_and_b32 v43,0xffff0000,v165\n"
"v_lshlrev_b32 v44,16,v166\n"
"v_and_b32 v45,0xffff0000,v166\n"
"v_lshlrev_b32 v46,16,v167\n"
"v_and_b32 v47,0xffff0000,v167\n"
"v_lshlrev_b32 v48,16,v168\n"
"v_and_b32 v49,0xffff0000,v168\n"
"v_lshlrev_b32 v50,16,v169\n"
"v_and_b32 v51,0xffff0000,v169\n"
"v_lshlrev_b32 v52,16,v170\n"
"v_and_b32 v53,0xffff0000,v170\n"
"v_lshlrev_b32 v54,16,v171\n"
"v_and_b32 v55,0xffff0000,v171\n"
"v_lshlrev_b32 v56,16,v172\n"
"v_and_b32 v57,0xffff0000,v172\n"
"v_lshlrev_b32 v58,16,v173\n"
"v_and_b32 v59,0xffff0000,v173\n"
"v_lshlrev_b32 v60,16,v174\n"
"v_and_b32 v61,0xffff0000,v174\n"
"v_lshlrev_b32 v62,16,v175\n"
"v_and_b32 v63,0xffff0000,v175\n"
"s_cmp_ge_u32 s71,3\n"
"s_cbranch_scc1 .Lk_cy1\n"
"v_mov_b32 v154,v184\n"
"v_mov_b32 v155,v185\n"
"v_mov_b32 v156,v186\n"
"v_mov_b32 v157,v187\n"
".Lk_cy1:\n"
"v_and_b32 v242,15,v254\n"
"v_bfe_u32 v241,v254,4,1\n"
"v_lshlrev_b32 v241,3,v241\n"
"v_lshl_add_u32 v240,v242,7,v241\n"
"v_lshl_add_u32 v242,v242,5,v241\n"
"v_add_u32 v240,40960,v240\n"
"v_lshrrev_b32 v241,5,v254\n"
"v_bfe_u32 v243,v254,3,1\n"
"v_xor_b32 v243,v243,v241\n"
"v_lshl_add_u32 v148,v243,4,v242\n"
"v_add_u32 v148,1024,v148\n"
"v_bfe_u32 v243,v254,1,3\n"
"v_xor_b32 v241,v241,v243\n"
"v_lshl_add_u32 v144,v241,4,v240\n"
"v_xor_b32 v145,32,v144\n"
"v_xor_b32 v146,64,v144\n"
"v_xor_b32 v147,96,v144\n"
"v_lshrrev_b32 v241,4,v254\n"
"v_bfe_u32 v242,v254,2,2\n"
"v_xor_b32 v242,v242,v241\n"
"v_and_b32 v240,15,v254\n"
"v_lshl_add_u32 v240,v240,2,v242\n"
"v_lshlrev_b32 v240,3,v240\n"
"s_mul_i32 s75,s71,18432\n"
"s_add_u32 s75,s75,69632\n"
"v_add_u32 v240,s75,v240\n"
"v_xor_b32 v241,v241,v254\n"
"v_lshlrev_b32 v241,3,v241\n"
"v_add_u32 v241,s75,v241\n"
"s_cmp_ge_u32 s71,3\n"
"s_cbranch_scc1 .Lk_cy3\n"
"s_mov_b32 s89,0x80000\n"
".Lk_cy_spin:\n"
"s_sub_u32 s74,s84,184320\n"
"s_cmp_ge_u32 s68,15\n"
"s_cselect_b32 s74,s74,2146435072\n"
"buffer_load_dwordx4 v[72:75],v133,s[32:35],s74 offen sc1\n"
"s_sub_u32 s74,s84,172032\n"
"s_cmp_ge_u32 s68,14\n"
"s_cselect_b32 s74,s74,2146435072\n"
"buffer_load_dwordx4 v[76:79],v133,s[32:35],s74 offen sc1\n"
"s_sub_u32 s74,s84,159744\n"
"s_cmp_ge_u32 s68,13\n"
"s_cselect_b32 s74,s74,2146435072\n"
"buffer_load_dwordx4 v[80:83],v133,s[32:35],s74 offen sc1\n"
"s_sub_u32 s74,s84,147456\n"
"s_cmp_ge_u32 s68,12\n"
"s_cselect_b32 s74,s74,2146435072\n"
"buffer_load_dwordx4 v[84:87],v133,s[32:35],s74 offen sc1\n"
"s_sub_u32 s74,s84,135168\n"
"s_cmp_ge_u32 s68,11\n"
"s_cselect_b32 s74,s74,2146435072\n"
"buffer_load_dwordx4 v[88:91],v133,s[32:35],s74 offen sc1\n"
"s_sub_u32 s74,s84,122880\n"
"s_cmp_ge_u32 s68,10\n"
"s_cselect_b32 s74,s74,2146435072\n"
"buffer_load_dwordx4 v[92:95],v133,s[32:35],s74 offen sc1\n"
"s_sub_u32 s74,s84,110592\n"
"s_cmp_ge_u32 s68,9\n"
"s_cselect_b32 s74,s74,2146435072\n"
"buffer_load_dwordx4 v[96:99],v133,s[32:35],s74 offen sc1\n"
"s_sub_u32 s74,s84,98304\n"
"s_cmp_ge_u32 s68,8\n"
"s_cselect_b32 s74,s74,2146435072\n"
"buffer_load_dwordx4 v[100:103],v133,s[32:35],s74 offen sc1\n"
"s_sub_u32 s74,s84,86016\n"
"s_cmp_ge_u32 s68,7\n"
"s_cselect_b32 s74,s74,2146435072\n"
"buffer_load_dwordx4 v[104:107],v133,s[32:35],s74 offen sc1\n"
"s_sub_u32 s74,s84,73728\n"
"s_cmp_ge_u32 s68,6\n"
"s_cselect_b32 s74,s74,2146435072\n"
"buffer_load_dwordx4 v[108:111],v133,s[32:35],s74 offen sc1\n"
"s_sub_u32 s74,s84,61440\n"
"s_cmp_ge_u32 s68,5\n"
"s_cselect_b32 s74,s74,2146435072\n"
"buffer_load_dwordx4 v[112:115],v133,s[32:35],s74 offen sc1\n"
"s_sub_u32 s74,s84,49152\n"
"s_cmp_ge_u32 s68,4\n"
"s_cselect_b32 s74,s74,2146435072\n"
"buffer_load_dwordx4 v[116:119],v133,s[32:35],s74 offen sc1\n"
"s_sub_u32 s74,s84,36864\n"
"s_cmp_ge_u32 s68,3\n"
"s_cselect_b32 s74,s74,2146435072\n"
"buffer_load_dwordx4 v[120:123],v133,s[32:35],s74 offen sc1\n"
"s_sub_u32 s74,s84,24576\n"
"s_cmp_ge_u32 s68,2\n"
"s_cselect_b32 s74,s74,2146435072\n"
"buffer_load_dwordx4 v[124:127],v133,s[32:35],s74 offen sc1\n"
"s_sub_u32 s74,s84,12288\n"
"s_cmp_ge_u32 s68,1\n"
"s_cselect_b32 s74,s74,2146435072\n"
"buffer_load_dwordx4 v[128:131],v133,s[32:35],s74 offen sc1\n"
"s_waitcnt vmcnt(0)\n"
"v_add_u32 v140,v73,v75\n"
"v_add3_u32 v140,v140,v77,v79\n"
"v_add3_u32 v140,v140,v81,v83\n"
"v_add3_u32 v140,v140,v85,v87\n"
"v_add3_u32 v140,v140,v89,v91\n"
"v_add3_u32 v140,v140,v93,v95\n"
"v_add3_u32 v140,v140,v97,v99\n"
"v_add3_u32 v140,v140,v101,v103\n"
"v_add3_u32 v140,v140,v105,v107\n"
"v_add3_u32 v140,v140,v109,v111\n"
"v_add3_u32 v140,v140,v113,v115\n"
"v_add3_u32 v140,v140,v117,v119\n"
"v_add3_u32 v140,v140,v121,v123\n"
"v_add3_u32 v140,v140,v125,v127\n"
"v_add3_u32 v140,v140,v129,v131\n"
"v_cmp_ne_u32 vcc,s85,v140\n"
"s_sub_u32 s89,s89,1\n"
"s_cbranch_scc1 .Lk_cy_ok\n"
"s_cbranch_vccnz .Lk_cy_spin\n"
".Lk_cy_ok:\n"
"v_fmac_f32 v76,v156,v72\n"
"v_fmac_f32 v78,v156,v74\n"
"v_fma_f32 v76,-v157,v74,v76\n"
"v_fmac_f32 v78,v157,v72\n"
"v_fmac_f32 v80,v156,v76\n"
"v_fmac_f32 v82,v156,v78\n"
"v_fma_f32 v80,-v157,v78,v80\n"
"v_fmac_f32 v82,v157,v76\n"
"v_fmac_f32 v84,v156,v80\n"
"v_fmac_f32 v86,v156,v82\n"
"v_fma_f32 v84,-v157,v82,v84\n"
"v_fmac_f32 v86,v157,v80\n"
"v_fmac_f32 v88,v156,v84\n"
"v_fmac_f32 v90,v156,v86\n"
"v_fma_f32 v88,-v157,v86,v88\n"
"v_fmac_f32 v90,v157,v84\n"
"v_fmac_f32 v92,v156,v88\n"
"v_fmac_f32 v94,v156,v90\n"
"v_fma_f32 v92,-v157,v90,v92\n"
"v_fmac_f32 v94,v157,v88\n"
"v_fmac_f32 v96,v156,v92\n"
"v_fmac_f32 v98,v156,v94\n"
"v_fma_f32 v96,-v157,v94,v96\n"
"v_fmac_f32 v98,v157,v92\n"
"v_fmac_f32 v100,v156,v96\n"
"v_fmac_f32 v102,v156,v98\n"
"v_fma_f32 v100,-v157,v98,v100\n"
"v_fmac_f32 v102,v157,v96\n"
"v_fmac_f32 v104,v156,v100\n"
"v_fmac_f32 v106,v156,v102\n"
"v_fma_f32 v104,-v157,v102,v104\n"
"v_fmac_f32 v106,v157,v100\n"
"v_fmac_f32 v108,v156,v104\n"
"v_fmac_f32 v110,v156,v106\n"
"v_fma_f32 v108,-v157,v106,v108\n"
"v_fmac_f32 v110,v157,v104\n"
"ds_read_b64 v[72:73],v132\n"
"ds_read_b64 v[74:75],v132 offset:128\n"
"ds_read_b64 v[76:77],v132 offset:256\n"
"ds_read_b64 v[78:79],v132 offset:384\n"
"ds_read_b64 v[80:81],v132 offset:512\n"
"ds_read_b64 v[82:83],v132 offset:640\n"
"ds_read_b64 v[84:85],v132 offset:768\n"
"ds_read_b64 v[86:87],v132 offset:896\n"
"ds_read_b64 v[88:89],v132 offset:1024\n"
"ds_read_b64 v[90:91],v132 offset:1152\n"
"ds_read_b64 v[92:93],v132 offset:1280\n"
"ds_read_b64 v[94:95],v132 offset:1408\n"
"ds_read_b64 v[96:97],v132 offset:1536\n"
"ds_read_b64 v[98:99],v132 offset:1664\n"
"ds_read_b64 v[100:101],v132 offset:1792\n"
"ds_read_b64 v[102:103],v132 offset:1920\n"
"v_fmac_f32 v112,v156,v108\n"
"v_fmac_f32 v114,v156,v110\n"
"v_fma_f32 v112,-v157,v110,v112\n"
"v_fmac_f32 v114,v157,v108\n"
"v_fmac_f32 v116,v156,v112\n"
"v_fmac_f32 v118,v156,v114\n"
"v_fma_f32 v116,-v157,v114,v116\n"
"v_fmac_f32 v118,v157,v112\n"
"v_fmac_f32 v120,v156,v116\n"
"v_fmac_f32 v122,v156,v118\n"
"v_fma_f32 v120,-v157,v118,v120\n"
"v_fmac_f32 v122,v157,v116\n"
"v_fmac_f32 v124,v156,v120\n"
"v_fmac_f32 v126,v156,v122\n"
"v_fma_f32 v124,-v157,v122,v124\n"
"v_fmac_f32 v126,v157,v120\n"
"v_fmac_f32 v128,v156,v124\n"
"v_fmac_f32 v130,v156,v126\n"
"v_fma_f32 v128,-v157,v126,v128\n"
"v_fmac_f32 v130,v157,v124\n"
"v_mov_b32 v129,v130\n"
"s_waitcnt lgkmcnt(0)\n"
"ds_write_b64 v132,v[128:129]\n"
"v_fma_f32 v72,v154,v128,v72\n"
"v_fma_f32 v73,v154,v129,v73\n"
"v_fma_f32 v72,-v155,v129,v72\n"
"v_fmac_f32 v73,v155,v128\n"
"ds_write_b64 v132,v[72:73] offset:128\n"
"v_fma_f32 v74,v154,v72,v74\n"
"v_fma_f32 v75,v154,v73,v75\n"
"v_fma_f32 v74,-v155,v73,v74\n"
"v_fmac_f32 v75,v155,v72\n"
"ds_write_b64 v132,v[74:75] offset:256\n"
"v_fma_f32 v76,v154,v74,v76\n"
"v_fma_f32 v77,v154,v75,v77\n"
"v_fma_f32 v76,-v155,v75,v76\n"
"v_fmac_f32 v77,v155,v74\n"
"ds_write_b64 v132,v[76:77] offset:384\n"
"v_fma_f32 v78,v154,v76,v78\n"
"v_fma_f32 v79,v154,v77,v79\n"
"v_fma_f32 v78,-v155,v77,v78\n"
"v_fmac_f32 v79,v155,v76\n"
"ds_write_b64 v132,v[78:79] offset:512\n"
"v_fma_f32 v80,v154,v78,v80\n"
"v_fma_f32 v81,v154,v79,v81\n"
"v_fma_f32 v80,-v155,v79,v80\n"
"v_fmac_f32 v81,v155,v78\n"
"ds_write_b64 v132,v[80:81] offset:640\n"
"v_fma_f32 v82,v154,v80,v82\n"
"v_fma_f32 v83,v154,v81,v83\n"
"v_fma_f32 v82,-v155,v81,v82\n"
"v_fmac_f32 v83,v155,v80\n"
"ds_write_b64 v132,v[82:83] offset:768\n"
"v_fma_f32 v84,v154,v82,v84\n"
"v_fma_f32 v85,v154,v83,v85\n"
"v_fma_f32 v84,-v155,v83,v84\n"
"v_fmac_f32 v85,v155,v82\n"
"ds_write_b64 v132,v[84:85] offset:896\n"
"v_fma_f32 v86,v154,v84,v86\n"
"v_fma_f32 v87,v154,v85,v87\n"
"v_fma_f32 v86,-v155,v85,v86\n"
"v_fmac_f32 v87,v155,v84\n"
"ds_write_b64 v132,v[86:87] offset:1024\n"
"v_fma_f32 v88,v154,v86,v88\n"
"v_fma_f32 v89,v154,v87,v89\n"
"v_fma_f32 v88,-v155,v87,v88\n"
"v_fmac_f32 v89,v155,v86\n"
"ds_write_b64 v132,v[88:89] offset:1152\n"
"v_fma_f32 v90,v154,v88,v90\n"
"v_fma_f32 v91,v154,v89,v91\n"
"v_fma_f32 v90,-v155,v89,v90\n"
"v_fmac_f32 v91,v155,v88\n"
"ds_write_b64 v132,v[90:91] offset:1280\n"
"v_fma_f32 v92,v154,v90,v92\n"
"v_fma_f32 v93,v154,v91,v93\n"
"v_fma_f32 v92,-v155,v91,v92\n"
"v_fmac_f32 v93,v155,v90\n"
"ds_write_b64 v132,v[92:93] offset:1408\n"
"v_fma_f32 v94,v154,v92,v94\n"
"v_fma_f32 v95,v154,v93,v95\n"
"v_fma_f32 v94,-v155,v93,v94\n"
"v_fmac_f32 v95,v155,v92\n"
"ds_write_b64 v132,v[94:95] offset:1536\n"
"v_fma_f32 v96,v154,v94,v96\n"
"v_fma_f32 v97,v154,v95,v97\n"
"v_fma_f32 v96,-v155,v95,v96\n"
"v_fmac_f32 v97,v155,v94\n"
"ds_write_b64 v132,v[96:97] offset:1664\n"
"v_fma_f32 v98,v154,v96,v98\n"
"v_fma_f32 v99,v154,v97,v99\n"
"v_fma_f32 v98,-v155,v97,v98\n"
"v_fmac_f32 v99,v155,v96\n"
"ds_write_b64 v132,v[98:99] offset:1792\n"
"v_fma_f32 v100,v154,v98,v100\n"
"v_fma_f32 v101,v154,v99,v101\n"
"v_fma_f32 v100,-v155,v99,v100\n"
"v_fmac_f32 v101,v155,v98\n"
"ds_write_b64 v132,v[100:101] offset:1920\n"
"s_waitcnt lgkmcnt(0)\n"
".Lk_cy3:\n"
"s_waitcnt lgkmcnt(0)\n"
"s_setprio 0\n"
"s_barrier\n"
"ds_read_b64 v[208:209],v255 offset:51328\n"
"ds_read_b64 v[210:211],v253 offset:2048\n"
"ds_read_b64 v[214:215],v255 offset:51584\n"
"ds_read_b64 v[216:217],v253 offset:6144\n"
"s_waitcnt lgkmcnt(2)\n"
"v_xor_b32 v212,0x80000000,v209\n"
"v_fmac_f32 v0,v208,v210\n"
"v_fmac_f32 v1,v208,v211\n"
"v_fmac_f32 v0,v212,v211\n"
"v_fmac_f32 v1,v209,v210\n"
"v_fmac_f32 v2,v208,v0\n"
"v_fmac_f32 v3,v208,v1\n"
"v_fmac_f32 v2,v212,v1\n"
"v_fmac_f32 v3,v209,v0\n"
"v_fmac_f32 v4,v208,v2\n"
"v_fmac_f32 v5,v208,v3\n"
"v_fmac_f32 v4,v212,v3\n"
".Lk_pads7:\n"
"s_cbranch_execz .Lk_hop_rets\n"
"v_fmac_f32 v5,v209,v2\n"
"v_fmac_f32 v6,v208,v4\n"
"v_fmac_f32 v7,v208,v5\n"
"v_fmac_f32 v6,v212,v5\n"
"v_fmac_f32 v7,v209,v4\n"
"v_cvt_pk_bf16_f32 v64,v0,v2\n"
"v_cvt_pk_bf16_f32 v65,v4,v6\n"
"ds_write_b64 v240,v[64:65] offset:2048\n"
"v_cvt_pk_bf16_f32 v66,-v1,-v3\n"
"v_cvt_pk_bf16_f32 v67,-v5,-v7\n"
"ds_write_b64 v240,v[66:67] offset:2560\n"
"v_fmac_f32 v8,v208,v6\n"
"v_fmac_f32 v9,v208,v7\n"
"v_fmac_f32 v8,v212,v7\n"
"v_fmac_f32 v9,v209,v6\n"
"v_fmac_f32 v10,v208,v8\n"
"v_fmac_f32 v11,v208,v9\n"
"v_fmac_f32 v10,v212,v9\n"
"v_fmac_f32 v11,v209,v8\n"
"v_fmac_f32 v12,v208,v10\n"
"v_fmac_f32 v13,v208,v11\n"
"v_fmac_f32 v12,v212,v11\n"
"v_fmac_f32 v13,v209,v10\n"
"v_fmac_f32 v14,v208,v12\n"
"v_fmac_f32 v15,v208,v13\n"
"v_fmac_f32 v14,v212,v13\n"
"v_fmac_f32 v15,v209,v12\n"
"v_cvt_pk_bf16_f32 v68,v8,v10\n"
"v_cvt_pk_bf16_f32 v69,v12,v14\n"
"ds_write_b64 v240,v[68:69] offset:3072\n"
"v_cvt_pk_bf16_f32 v70,-v9,-v11\n"
"v_cvt_pk_bf16_f32 v71,-v13,-v15\n"
"ds_write_b64 v240,v[70:71] offset:3584\n"
"ds_read_b64_tr_b16 v[80:81],v241 offset:2048\n"
"ds_read_b64_tr_b16 v[82:83],v241 offset:2560\n"
"ds_read_b64_tr_b16 v[84:85],v241 offset:3072\n"
"ds_read_b64_tr_b16 v[86:87],v241 offset:3584\n"
"ds_read_b64 v[208:209],v255 offset:51840\n"
"ds_read_b64 v[210:211],v253 offset:10240\n"
"s_waitcnt lgkmcnt(10)\n"
"v_xor_b32 v218,0x80000000,v215\n"
"v_fmac_f32 v16,v214,v216\n"
"v_fmac_f32 v17,v214,v217\n"
"v_fmac_f32 v16,v218,v217\n"
"v_fmac_f32 v17,v215,v216\n"
"v_fmac_f32 v18,v214,v16\n"
"v_fmac_f32 v19,v214,v17\n"
"v_fmac_f32 v18,v218,v17\n"
"v_fmac_f32 v19,v215,v16\n"
"v_fmac_f32 v20,v214,v18\n"
"v_fmac_f32 v21,v214,v19\n"
"v_fmac_f32 v20,v218,v19\n"
"v_fmac_f32 v21,v215,v18\n"
"v_fmac_f32 v22,v214,v20\n"
"v_fmac_f32 v23,v214,v21\n"
"v_fmac_f32 v22,v218,v21\n"
"v_fmac_f32 v23,v215,v20\n"
"v_cvt_pk_bf16_f32 v64,v16,v18\n"
"v_cvt_pk_bf16_f32 v65,v20,v22\n"
"ds_write_b64 v240,v[64:65] offset:6144\n"
"v_cvt_pk_bf16_f32 v66,-v17,-v19\n"
"v_cvt_pk_bf16_f32 v67,-v21,-v23\n"
"ds_write_b64 v240,v[66:67] offset:6656\n"
"v_fmac_f32 v24,v214,v22\n"
"v_fmac_f32 v25,v214,v23\n"
"v_fmac_f32 v24,v218,v23\n"
"v_fmac_f32 v25,v215,v22\n"
"v_fmac_f32 v26,v214,v24\n"
"v_fmac_f32 v27,v214,v25\n"
"v_fmac_f32 v26,v218,v25\n"
"v_fmac_f32 v27,v215,v24\n"
"v_fmac_f32 v28,v214,v26\n"
"v_fmac_f32 v29,v214,v27\n"
"v_fmac_f32 v28,v218,v27\n"
"v_fmac_f32 v29,v215,v26\n"
"v_fmac_f32 v30,v214,v28\n"
"v_fmac_f32 v31,v214,v29\n"
"v_fmac_f32 v30,v218,v29\n"
"v_fmac_f32 v31,v215,v28\n"
"v_cvt_pk_bf16_f32 v68,v24,v26\n"
"v_cvt_pk_bf16_f32 v69,v28,v30\n"
"ds_write_b64 v240,v[68:69] offset:7168\n"
"v_cvt_pk_bf16_f32 v70,-v25,-v27\n"
"v_cvt_pk_bf16_f32 v71,-v29,-v31\n"
"ds_write_b64 v240,v[70:71] offset:7680\n"
"ds_read_b64_tr_b16 v[96:97],v241 offset:6144\n"
"ds_read_b64_tr_b16 v[98:99],v241 offset:6656\n"
"ds_read_b64_tr_b16 v[100:101],v241 offset:7168\n"
"ds_read_b64_tr_b16 v[102:103],v241 offset:7680\n"
"ds_read_b64 v[214:215],v255 offset:52096\n"
"ds_read_b64 v[216:217],v253 offset:14336\n"
"s_waitcnt lgkmcnt(10)\n"
"v_xor_b32 v212,0x80000000,v209\n"
"v_fmac_f32 v32,v208,v210\n"
"v_fmac_f32 v33,v208,v211\n"
"v_fmac_f32 v32,v212,v211\n"
"v_fmac_f32 v33,v209,v210\n"
"v_fmac_f32 v34,v208,v32\n"
"v_fmac_f32 v35,v208,v33\n"
"v_fmac_f32 v34,v212,v33\n"
"v_fmac_f32 v35,v209,v32\n"
"v_fmac_f32 v36,v208,v34\n"
"v_fmac_f32 v37,v208,v35\n"
"v_fmac_f32 v36,v212,v35\n"
"v_fmac_f32 v37,v209,v34\n"
"v_fmac_f32 v38,v208,v36\n"
"v_fmac_f32 v39,v208,v37\n"
"v_fmac_f32 v38,v212,v37\n"
"v_fmac_f32 v39,v209,v36\n"
"v_cvt_pk_bf16_f32 v64,v32,v34\n"
"v_cvt_pk_bf16_f32 v65,v36,v38\n"
"ds_write_b64 v240,v[64:65] offset:10240\n"
"v_cvt_pk_bf16_f32 v66,-v33,-v35\n"
"v_cvt_pk_bf16_f32 v67,-v37,-v39\n"
"ds_write_b64 v240,v[66:67] offset:10752\n"
"v_fmac_f32 v40,v208,v38\n"
"v_fmac_f32 v41,v208,v39\n"
"v_fmac_f32 v40,v212,v39\n"
"v_fmac_f32 v41,v209,v38\n"
"v_fmac_f32 v42,v208,v40\n"
"v_fmac_f32 v43,v208,v41\n"
"v_fmac_f32 v42,v212,v41\n"
"v_fmac_f32 v43,v209,v40\n"
"v_fmac_f32 v44,v208,v42\n"
"v_fmac_f32 v45,v208,v43\n"
"v_fmac_f32 v44,v212,v43\n"
"v_fmac_f32 v45,v209,v42\n"
"v_fmac_f32 v46,v208,v44\n"
"v_fmac_f32 v47,v208,v45\n"
"v_fmac_f32 v46,v212,v45\n"
"v_fmac_f32 v47,v209,v44\n"
"v_cvt_pk_bf16_f32 v68,v40,v42\n"
"v_cvt_pk_bf16_f32 v69,v44,v46\n"
"ds_write_b64 v240,v[68:69] offset:11264\n"
"v_cvt_pk_bf16_f32 v70,-v41,-v43\n"
"v_cvt_pk_bf16_f32 v71,-v45,-v47\n"
"ds_write_b64 v240,v[70:71] offset:11776\n"
"ds_read_b64_tr_b16 v[112:113],v241 offset:10240\n"
"ds_read_b64_tr_b16 v[114:115],v241 offset:10752\n"
"ds_read_b64_tr_b16 v[116:117],v241 offset:11264\n"
"ds_read_b64_tr_b16 v[118:119],v241 offset:11776\n"
"s_waitcnt lgkmcnt(8)\n"
"v_xor_b32 v218,0x80000000,v215\n"
"v_fmac_f32 v48,v214,v216\n"
"v_fmac_f32 v49,v214,v217\n"
"v_fmac_f32 v48,v218,v217\n"
"v_fmac_f32 v49,v215,v216\n"
"v_fmac_f32 v50,v214,v48\n"
"v_fmac_f32 v51,v214,v49\n"
"v_fmac_f32 v50,v218,v49\n"
"v_fmac_f32 v51,v215,v48\n"
"v_fmac_f32 v52,v214,v50\n"
"v_fmac_f32 v53,v214,v51\n"
"v_fmac_f32 v52,v218,v51\n"
"v_fmac_f32 v53,v215,v50\n"
"v_fmac_f32 v54,v214,v52\n"
"v_fmac_f32 v55,v214,v53\n"
"v_fmac_f32 v54,v218,v53\n"
"v_fmac_f32 v55,v215,v52\n"
"v_cvt_pk_bf16_f32 v64,v48,v50\n"
"v_cvt_pk_bf16_f32 v65,v52,v54\n"
"ds_write_b64 v240,v[64:65] offset:14336\n"
"v_cvt_pk_bf16_f32 v66,-v49,-v51\n"
"v_cvt_pk_bf16_f32 v67,-v53,-v55\n"
"ds_write_b64 v240,v[66:67] offset:14848\n"
"v_fmac_f32 v56,v214,v54\n"
"v_fmac_f32 v57,v214,v55\n"
"v_fmac_f32 v56,v218,v55\n"
"v_fmac_f32 v57,v215,v54\n"
"v_fmac_f32 v58,v214,v56\n"
"v_fmac_f32 v59,v214,v57\n"
"v_fmac_f32 v58,v218,v57\n"
"v_fmac_f32 v59,v215,v56\n"
"v_fmac_f32 v60,v214,v58\n"
"v_fmac_f32 v61,v214,v59\n"
"v_fmac_f32 v60,v218,v59\n"
"v_fmac_f32 v61,v215,v58\n"
"v_fmac_f32 v62,v214,v60\n"
"v_fmac_f32 v63,v214,v61\n"
"v_fmac_f32 v62,v218,v61\n"
"v_fmac_f32 v63,v215,v60\n"
"v_cvt_pk_bf16_f32 v68,v56,v58\n"
"v_cvt_pk_bf16_f32 v69,v60,v62\n"
"ds_write_b64 v240,v[68:69] offset:15360\n"
"v_cvt_pk_bf16_f32 v70,-v57,-v59\n"
"v_cvt_pk_bf16_f32 v71,-v61,-v63\n"
"ds_write_b64 v240,v[70:71] offset:15872\n"
"ds_read_b64_tr_b16 v[128:129],v241 offset:14336\n"
"ds_read_b64_tr_b16 v[130:131],v241 offset:14848\n"
"ds_read_b64_tr_b16 v[132:133],v241 offset:15360\n"
"ds_read_b64_tr_b16 v[134:135],v241 offset:15872\n"
"s_waitcnt lgkmcnt(0)\n"
"s_waitcnt vmcnt(0)\n"
"s_barrier\n"
"ds_read_b64_tr_b16 v[72:73],v241\n"
"ds_read_b64_tr_b16 v[74:75],v241 offset:512\n"
"ds_read_b64_tr_b16 v[76:77],v241 offset:1024\n"
"ds_read_b64_tr_b16 v[78:79],v241 offset:1536\n"
"ds_read_b64_tr_b16 v[88:89],v241 offset:4096\n"
"ds_read_b64_tr_b16 v[90:91],v241 offset:4608\n"
"ds_read_b64_tr_b16 v[92:93],v241 offset:5120\n"
"ds_read_b64_tr_b16 v[94:95],v241 offset:5632\n"
"ds_read_b64_tr_b16 v[104:105],v241 offset:8192\n"
"ds_read_b64_tr_b16 v[106:107],v241 offset:8704\n"
"ds_read_b64_tr_b16 v[108:109],v241 offset:9216\n"
"ds_read_b64_tr_b16 v[110:111],v241 offset:9728\n"
"ds_read_b64_tr_b16 v[120:121],v241 offset:12288\n"
"ds_read_b64_tr_b16 v[122:123],v241 offset:12800\n"
"ds_read_b64_tr_b16 v[124:125],v241 offset:13312\n"
"ds_read_b64_tr_b16 v[126:127],v241 offset:13824\n"
"ds_read_b64_tr_b16 v[136:137],v241 offset:16384\n"
"ds_read_b64_tr_b16 v[138:139],v241 offset:16896\n"
"ds_read_b64_tr_b16 v[140:141],v241 offset:17408\n"
"ds_read_b64_tr_b16 v[142:143],v241 offset:17920\n"
"s_waitcnt lgkmcnt(0)\n"
"s_barrier\n"
"s_add_u32 m0,s73,69632\n"
"s_add_u32 s74,s81,184320\n"
"buffer_load_dwordx4 v251,s[24:27],s74 offen lds\n"
"s_addk_i32 m0,8192\n"
"s_addk_i32 s74,128\n"
"buffer_load_dwordx4 v251,s[24:27],s74 offen lds\n"
"s_addk_i32 m0,8192\n"
"s_addk_i32 s74,-128\n"
"buffer_load_dwordx4 v251,s[28:31],s74 offen lds\n"
"s_addk_i32 m0,8192\n"
"s_addk_i32 s74,128\n"
"buffer_load_dwordx4 v251,s[28:31],s74 offen lds\n"
"s_addk_i32 m0,-20480\n"
"s_add_u32 s74,s81,221184\n"
"buffer_load_dwordx4 v251,s[24:27],s74 offen lds\n"
"s_addk_i32 m0,8192\n"
"s_addk_i32 s74,128\n"
"buffer_load_dwordx4 v251,s[24:27],s74 offen lds\n"
"s_addk_i32 m0,8192\n"
"s_addk_i32 s74,-128\n"
"buffer_load_dwordx4 v251,s[28:31],s74 offen lds\n"
"s_addk_i32 m0,8192\n"
"s_addk_i32 s74,128\n"
"buffer_load_dwordx4 v251,s[28:31],s74 offen lds\n"
"s_setprio 3\n"
"s_barrier\n"
"ds_read_b64 v[208:209],v144\n"
"ds_read_b64 v[210:211],v144 offset:16384\n"
"ds_read_b64 v[212:213],v145\n"
"ds_read_b64 v[214:215],v145 offset:16384\n"
"ds_read_b64 v[216:217],v146\n"
"ds_read_b64 v[218:219],v146 offset:16384\n"
"ds_read_b64 v[220:221],v147\n"
"ds_read_b64 v[222:223],v147 offset:16384\n"
"ds_read_b64 v[224:225],v144 offset:8192\n"
"ds_read_b64 v[226:227],v144 offset:24576\n"
"ds_read_b64 v[228:229],v145 offset:8192\n"
"ds_read_b64 v[230:231],v145 offset:24576\n"
"s_setprio 1\n"
"s_waitcnt lgkmcnt(8)\n"
"v_mfma_f32_16x16x32_bf16 v[0:3],v[208:211],v[72:75],0\n"
"ds_read_b64 v[232:233],v146 offset:8192\n"
"v_mfma_f32_16x16x32_bf16 v[0:3],v[212:215],v[80:83],v[0:3]\n"
"ds_read_b64 v[234:235],v146 offset:24576\n"
"v_mfma_f32_16x16x32_bf16 v[8:11],v[208:211],v[76:79],0\n"
"ds_read_b64 v[236:237],v147 offset:8192\n"
"v_mfma_f32_16x16x32_bf16 v[8:11],v[212:215],v[84:87],v[8:11]\n"
"ds_read_b64 v[238:239],v147 offset:24576\n"
"s_waitcnt lgkmcnt(8)\n"
"v_mfma_f32_16x16x32_bf16 v[8:11],v[216:219],v[92:95],v[8:11]\n"
"ds_read_b64 v[208:209],v148\n"
"v_mfma_f32_16x16x32_bf16 v[8:11],v[220:223],v[100:103],v[8:11]\n"
"ds_read_b64 v[210:211],v148 offset:18432\n"
"s_setprio 2\n"
"v_mfma_f32_16x16x32_bf16 v[0:3],v[216:219],v[88:91],v[0:3]\n"
"ds_read_b64 v[212:213],v144 offset:2048\n"
"v_mfma_f32_16x16x32_bf16 v[0:3],v[220:223],v[96:99],v[0:3]\n"
"ds_read_b64 v[214:215],v144 offset:18432\n"
"s_waitcnt lgkmcnt(8)\n"
"v_mfma_f32_16x16x32_bf16 v[0:3],v[224:227],v[104:107],v[0:3]\n"
"ds_read_b64 v[216:217],v145 offset:2048\n"
"v_mfma_f32_16x16x32_bf16 v[0:3],v[228:231],v[112:115],v[0:3]\n"
"ds_read_b64 v[218:219],v145 offset:18432\n"
"v_mfma_f32_16x16x32_bf16 v[8:11],v[224:227],v[108:111],v[8:11]\n"
"ds_read_b64 v[220:221],v146 offset:2048\n"
"v_mfma_f32_16x16x32_bf16 v[8:11],v[228:231],v[116:119],v[8:11]\n"
"ds_read_b64 v[222:223],v146 offset:18432\n"
"s_waitcnt lgkmcnt(8)\n"
"v_mfma_f32_16x16x32_bf16 v[8:11],v[232:235],v[124:127],v[8:11]\n"
"ds_read_b64 v[224:225],v147 offset:2048\n"
"v_mfma_f32_16x16x32_bf16 v[8:11],v[236:239],v[132:135],v[8:11]\n"
"ds_read_b64 v[226:227],v147 offset:18432\n"
"v_mfma_f32_16x16x32_bf16 v[0:3],v[232:235],v[120:123],v[0:3]\n"
"ds_read_b64 v[228:229],v144 offset:10240\n"
"v_mfma_f32_16x16x32_bf16 v[0:3],v[236:239],v[128:131],v[0:3]\n"
"ds_read_b64 v[230:231],v144 offset:26624\n"
"s_waitcnt lgkmcnt(8)\n"
"v_mfma_f32_16x16x32_bf16 v[0:3],v[208:211],v[136:139],v[0:3]\n"
"ds_read_b64 v[232:233],v145 offset:10240\n"
"v_mfma_f32_16x16x32_bf16 v[8:11],v[208:211],v[140:143],v[8:11]\n"
"ds_read_b64 v[234:235],v145 offset:26624\n"
"v_mfma_f32_16x16x32_bf16 v[4:7],v[212:215],v[72:75],0\n"
"ds_read_b64 v[236:237],v146 offset:10240\n"
"s_waitcnt lgkmcnt(7)\n"
"v_mfma_f32_16x16x32_bf16 v[4:7],v[216:219],v[80:83],v[4:7]\n"
"ds_read_b64 v[238:239],v146 offset:26624\n"
"v_mfma_f32_16x16x32_bf16 v[12:15],v[212:215],v[76:79],0\n"
"ds_read_b64 v[208:209],v147 offset:10240\n"
"v_mfma_f32_16x16x32_bf16 v[12:15],v[216:219],v[84:87],v[12:15]\n"
"ds_read_b64 v[210:211],v147 offset:26624\n"
"v_mfma_f32_16x16x32_bf16 v[12:15],v[220:223],v[92:95],v[12:15]\n"
"ds_read_b64 v[212:213],v148 offset:512\n"
"s_waitcnt vmcnt(0)\n"
"s_waitcnt lgkmcnt(7)\n"
"v_mfma_f32_16x16x32_bf16 v[12:15],v[224:227],v[100:103],v[12:15]\n"
"ds_read_b64 v[214:215],v148 offset:18944\n"
"v_mfma_f32_16x16x32_bf16 v[4:7],v[220:223],v[88:91],v[4:7]\n"
"s_barrier\n"
"v_mfma_f32_16x16x32_bf16 v[4:7],v[224:227],v[96:99],v[4:7]\n"
"v_mfma_f32_16x16x32_bf16 v[4:7],v[228:231],v[104:107],v[4:7]\n"
"s_waitcnt lgkmcnt(4)\n"
"v_mfma_f32_16x16x32_bf16 v[4:7],v[232:235],v[112:115],v[4:7]\n"
"v_mfma_f32_16x16x32_bf16 v[12:15],v[228:231],v[108:111],v[12:15]\n"
"v_mfma_f32_16x16x32_bf16 v[12:15],v[232:235],v[116:119],v[12:15]\n"
"v_mfma_f32_16x16x32_bf16 v[12:15],v[236:239],v[124:127],v[12:15]\n"
"s_waitcnt lgkmcnt(0)\n"
"v_mfma_f32_16x16x32_bf16 v[12:15],v[208:211],v[132:135],v[12:15]\n"
"v_mfma_f32_16x16x32_bf16 v[4:7],v[236:239],v[120:123],v[4:7]\n"
"v_mfma_f32_16x16x32_bf16 v[4:7],v[208:211],v[128:131],v[4:7]\n"
"v_mfma_f32_16x16x32_bf16 v[4:7],v[212:215],v[136:139],v[4:7]\n"
"v_mfma_f32_16x16x32_bf16 v[12:15],v[212:215],v[140:143],v[12:15]\n"
"s_setprio 0\n"
"v_add_u32 v144,32768,v144\n"
"v_add_u32 v145,32768,v145\n"
"v_add_u32 v146,32768,v146\n"
"v_add_u32 v147,32768,v147\n"
"s_addk_i32 m0,8192\n"
"s_add_u32 s74,s81,442368\n"
"buffer_load_dwordx4 v251,s[24:27],s74 offen lds\n"
"s_addk_i32 m0,8192\n"
"s_addk_i32 s74,128\n"
"buffer_load_dwordx4 v251,s[24:27],s74 offen lds\n"
"s_addk_i32 m0,8192\n"
"s_addk_i32 s74,-128\n"
"buffer_load_dwordx4 v251,s[28:31],s74 offen lds\n"
"s_addk_i32 m0,8192\n"
"s_addk_i32 s74,128\n"
"buffer_load_dwordx4 v251,s[28:31],s74 offen lds\n"
"v_cvt_pk_bf16_f32 v16,v0,v1\n"
"v_cvt_pk_bf16_f32 v17,v2,v3\n"
"v_cvt_pk_bf16_f32 v18,v4,v5\n"
"v_cvt_pk_bf16_f32 v19,v6,v7\n"
"s_add_u32 s78,s87,0\n"
"buffer_store_dwordx4 v[16:19],v252,s[32:35],s78 offen sc1\n"
"v_cvt_pk_bf16_f32 v20,v8,v9\n"
"v_cvt_pk_bf16_f32 v21,v10,v11\n"
"v_cvt_pk_bf16_f32 v22,v12,v13\n"
"v_cvt_pk_bf16_f32 v23,v14,v15\n"
"s_add_u32 s78,s87,18432\n"
"buffer_store_dwordx4 v[20:23],v252,s[32:35],s78 offen sc1\n"
"s_setprio 3\n"
"s_barrier\n"
"ds_read_b64 v[208:209],v144\n"
"ds_read_b64 v[210:211],v144 offset:16384\n"
"ds_read_b64 v[212:213],v145\n"
"ds_read_b64 v[214:215],v145 offset:16384\n"
"ds_read_b64 v[216:217],v146\n"
"ds_read_b64 v[218:219],v146 offset:16384\n"
"ds_read_b64 v[220:221],v147\n"
"ds_read_b64 v[222:223],v147 offset:16384\n"
"ds_read_b64 v[224:225],v144 offset:8192\n"
"ds_read_b64 v[226:227],v144 offset:24576\n"
"ds_read_b64 v[228:229],v145 offset:8192\n"
"ds_read_b64 v[230:231],v145 offset:24576\n"
"s_setprio 1\n"
"s_waitcnt lgkmcnt(8)\n"
"v_mfma_f32_16x16x32_bf16 v[0:3],v[208:211],v[72:75],0\n"
"ds_read_b64 v[232:233],v146 offset:8192\n"
"v_mfma_f32_16x16x32_bf16 v[0:3],v[212:215],v[80:83],v[0:3]\n"
"ds_read_b64 v[234:235],v146 offset:24576\n"
"v_mfma_f32_16x16x32_bf16 v[8:11],v[208:211],v[76:79],0\n"
"ds_read_b64 v[236:237],v147 offset:8192\n"
"v_mfma_f32_16x16x32_bf16 v[8:11],v[212:215],v[84:87],v[8:11]\n"
"ds_read_b64 v[238:239],v147 offset:24576\n"
"s_waitcnt lgkmcnt(8)\n"
"v_mfma_f32_16x16x32_bf16 v[8:11],v[216:219],v[92:95],v[8:11]\n"
"ds_read_b64 v[208:209],v148 offset:6144\n"
"v_mfma_f32_16x16x32_bf16 v[8:11],v[220:223],v[100:103],v[8:11]\n"
"ds_read_b64 v[210:211],v148 offset:24576\n"
"s_setprio 2\n"
"v_mfma_f32_16x16x32_bf16 v[0:3],v[216:219],v[88:91],v[0:3]\n"
"ds_read_b64 v[212:213],v144 offset:2048\n"
"v_mfma_f32_16x16x32_bf16 v[0:3],v[220:223],v[96:99],v[0:3]\n"
"ds_read_b64 v[214:215],v144 offset:18432\n"
"s_waitcnt lgkmcnt(8)\n"
"v_mfma_f32_16x16x32_bf16 v[0:3],v[224:227],v[104:107],v[0:3]\n"
"ds_read_b64 v[216:217],v145 offset:2048\n"
"v_mfma_f32_16x16x32_bf16 v[0:3],v[228:231],v[112:115],v[0:3]\n"
"ds_read_b64 v[218:219],v145 offset:18432\n"
"v_mfma_f32_16x16x32_bf16 v[8:11],v[224:227],v[108:111],v[8:11]\n"
"ds_read_b64 v[220:221],v146 offset:2048\n"
"v_mfma_f32_16x16x32_bf16 v[8:11],v[228:231],v[116:119],v[8:11]\n"
"ds_read_b64 v[222:223],v146 offset:18432\n"
"s_waitcnt lgkmcnt(8)\n"
"v_mfma_f32_16x16x32_bf16 v[8:11],v[232:235],v[124:127],v[8:11]\n"
"ds_read_b64 v[224:225],v147 offset:2048\n"
"v_mfma_f32_16x16x32_bf16 v[8:11],v[236:239],v[132:135],v[8:11]\n"
"ds_read_b64 v[226:227],v147 offset:18432\n"
"v_mfma_f32_16x16x32_bf16 v[0:3],v[232:235],v[120:123],v[0:3]\n"
"ds_read_b64 v[228:229],v144 offset:10240\n"
"v_mfma_f32_16x16x32_bf16 v[0:3],v[236:239],v[128:131],v[0:3]\n"
"ds_read_b64 v[230:231],v144 offset:26624\n"
"s_waitcnt lgkmcnt(8)\n"
"v_mfma_f32_16x16x32_bf16 v[0:3],v[208:211],v[136:139],v[0:3]\n"
"ds_read_b64 v[232:233],v145 offset:10240\n"
"v_mfma_f32_16x16x32_bf16 v[8:11],v[208:211],v[140:143],v[8:11]\n"
"ds_read_b64 v[234:235],v145 offset:26624\n"
"v_mfma_f32_16x16x32_bf16 v[4:7],v[212:215],v[72:75],0\n"
"ds_read_b64 v[236:237],v146 offset:10240\n"
"s_waitcnt lgkmcnt(7)\n"
"v_mfma_f32_16x16x32_bf16 v[4:7],v[216:219],v[80:83],v[4:7]\n"
"ds_read_b64 v[238:239],v146 offset:26624\n"
"v_mfma_f32_16x16x32_bf16 v[12:15],v[212:215],v[76:79],0\n"
"ds_read_b64 v[208:209],v147 offset:10240\n"
"v_mfma_f32_16x16x32_bf16 v[12:15],v[216:219],v[84:87],v[12:15]\n"
"ds_read_b64 v[210:211],v147 offset:26624\n"
"v_mfma_f32_16x16x32_bf16 v[12:15],v[220:223],v[92:95],v[12:15]\n"
"ds_read_b64 v[212:213],v148 offset:6656\n"
"s_waitcnt lgkmcnt(7)\n"
"v_mfma_f32_16x16x32_bf16 v[12:15],v[224:227],v[100:103],v[12:15]\n"
"ds_read_b64 v[214:215],v148 offset:25088\n"
"v_mfma_f32_16x16x32_bf16 v[4:7],v[220:223],v[88:91],v[4:7]\n"
"s_barrier\n"
"v_mfma_f32_16x16x32_bf16 v[4:7],v[224:227],v[96:99],v[4:7]\n"
"v_mfma_f32_16x16x32_bf16 v[4:7],v[228:231],v[104:107],v[4:7]\n"
"s_waitcnt lgkmcnt(4)\n"
"v_mfma_f32_16x16x32_bf16 v[4:7],v[232:235],v[112:115],v[4:7]\n"
"v_mfma_f32_16x16x32_bf16 v[12:15],v[228:231],v[108:111],v[12:15]\n"
"v_mfma_f32_16x16x32_bf16 v[12:15],v[232:235],v[116:119],v[12:15]\n"
"v_mfma_f32_16x16x32_bf16 v[12:15],v[236:239],v[124:127],v[12:15]\n"
"s_waitcnt lgkmcnt(0)\n"
"v_mfma_f32_16x16x32_bf16 v[12:15],v[208:211],v[132:135],v[12:15]\n"
"v_mfma_f32_16x16x32_bf16 v[4:7],v[236:239],v[120:123],v[4:7]\n"
"v_mfma_f32_16x16x32_bf16 v[4:7],v[208:211],v[128:131],v[4:7]\n"
"v_mfma_f32_16x16x32_bf16 v[4:7],v[212:215],v[136:139],v[4:7]\n"
"v_mfma_f32_16x16x32_bf16 v[12:15],v[212:215],v[140:143],v[12:15]\n"
"s_setprio 0\n"
"v_add_u32 v144,32768,v144\n"
"v_add_u32 v145,32768,v145\n"
"v_add_u32 v146,32768,v146\n"
"v_add_u32 v147,32768,v147\n"
"s_add_u32 m0,s73,40960\n"
"s_add_u32 s74,s81,73728\n"
"buffer_load_dwordx4 v251,s[24:27],s74 offen lds\n"
"s_addk_i32 m0,8192\n"
"s_addk_i32 s74,128\n"
"buffer_load_dwordx4 v251,s[24:27],s74 offen lds\n"
"s_addk_i32 m0,8192\n"
"s_addk_i32 s74,-128\n"
"buffer_load_dwordx4 v251,s[28:31],s74 offen lds\n"
"s_addk_i32 m0,8192\n"
"s_addk_i32 s74,128\n"
"buffer_load_dwordx4 v251,s[28:31],s74 offen lds\n"
"v_cvt_pk_bf16_f32 v16,v0,v1\n"
"v_cvt_pk_bf16_f32 v17,v2,v3\n"
"v_cvt_pk_bf16_f32 v18,v4,v5\n"
"v_cvt_pk_bf16_f32 v19,v6,v7\n"
"s_add_u32 s78,s87,6144\n"
"buffer_store_dwordx4 v[16:19],v252,s[32:35],s78 offen sc1\n"
"v_cvt_pk_bf16_f32 v20,v8,v9\n"
"v_cvt_pk_bf16_f32 v21,v10,v11\n"
"v_cvt_pk_bf16_f32 v22,v12,v13\n"
"v_cvt_pk_bf16_f32 v23,v14,v15\n"
"s_add_u32 s78,s87,24576\n"
"buffer_store_dwordx4 v[20:23],v252,s[32:35],s78 offen sc1\n"
"s_waitcnt vmcnt(8)\n"
"s_setprio 3\n"
"s_barrier\n"
"ds_read_b64 v[208:209],v144\n"
"ds_read_b64 v[210:211],v144 offset:16384\n"
"ds_read_b64 v[212:213],v145\n"
"ds_read_b64 v[214:215],v145 offset:16384\n"
"ds_read_b64 v[216:217],v146\n"
"ds_read_b64 v[218:219],v146 offset:16384\n"
"ds_read_b64 v[220:221],v147\n"
"ds_read_b64 v[222:223],v147 offset:16384\n"
"ds_read_b64 v[224:225],v144 offset:8192\n"
"ds_read_b64 v[226:227],v144 offset:24576\n"
"ds_read_b64 v[228:229],v145 offset:8192\n"
"ds_read_b64 v[230:231],v145 offset:24576\n"
"s_setprio 1\n"
"s_waitcnt lgkmcnt(8)\n"
"v_mfma_f32_16x16x32_bf16 v[0:3],v[208:211],v[72:75],0\n"
"ds_read_b64 v[232:233],v146 offset:8192\n"
"v_mfma_f32_16x16x32_bf16 v[0:3],v[212:215],v[80:83],v[0:3]\n"
"ds_read_b64 v[234:235],v146 offset:24576\n"
"v_mfma_f32_16x16x32_bf16 v[8:11],v[208:211],v[76:79],0\n"
"ds_read_b64 v[236:237],v147 offset:8192\n"
"v_mfma_f32_16x16x32_bf16 v[8:11],v[212:215],v[84:87],v[8:11]\n"
"ds_read_b64 v[238:239],v147 offset:24576\n"
"s_waitcnt lgkmcnt(8)\n"
"v_mfma_f32_16x16x32_bf16 v[8:11],v[216:219],v[92:95],v[8:11]\n"
"ds_read_b64 v[208:209],v148 offset:12288\n"
"v_mfma_f32_16x16x32_bf16 v[8:11],v[220:223],v[100:103],v[8:11]\n"
"ds_read_b64 v[210:211],v148 offset:30720\n"
"s_setprio 2\n"
"v_mfma_f32_16x16x32_bf16 v[0:3],v[216:219],v[88:91],v[0:3]\n"
"ds_read_b64 v[212:213],v144 offset:2048\n"
"v_mfma_f32_16x16x32_bf16 v[0:3],v[220:223],v[96:99],v[0:3]\n"
"ds_read_b64 v[214:215],v144 offset:18432\n"
"s_waitcnt lgkmcnt(8)\n"
"v_mfma_f32_16x16x32_bf16 v[0:3],v[224:227],v[104:107],v[0:3]\n"
"ds_read_b64 v[216:217],v145 offset:2048\n"
"v_mfma_f32_16x16x32_bf16 v[0:3],v[228:231],v[112:115],v[0:3]\n"
"ds_read_b64 v[218:219],v145 offset:18432\n"
"v_mfma_f32_16x16x32_bf16 v[8:11],v[224:227],v[108:111],v[8:11]\n"
"ds_read_b64 v[220:221],v146 offset:2048\n"
"v_mfma_f32_16x16x32_bf16 v[8:11],v[228:231],v[116:119],v[8:11]\n"
"ds_read_b64 v[222:223],v146 offset:18432\n"
"s_waitcnt lgkmcnt(8)\n"
"v_mfma_f32_16x16x32_bf16 v[8:11],v[232:235],v[124:127],v[8:11]\n"
"ds_read_b64 v[224:225],v147 offset:2048\n"
"v_mfma_f32_16x16x32_bf16 v[8:11],v[236:239],v[132:135],v[8:11]\n"
"ds_read_b64 v[226:227],v147 offset:18432\n"
"v_mfma_f32_16x16x32_bf16 v[0:3],v[232:235],v[120:123],v[0:3]\n"
"ds_read_b64 v[228:229],v144 offset:10240\n"
"v_mfma_f32_16x16x32_bf16 v[0:3],v[236:239],v[128:131],v[0:3]\n"
"ds_read_b64 v[230:231],v144 offset:26624\n"
"s_waitcnt lgkmcnt(8)\n"
"v_mfma_f32_16x16x32_bf16 v[0:3],v[208:211],v[136:139],v[0:3]\n"
"ds_read_b64 v[232:233],v145 offset:10240\n"
"v_mfma_f32_16x16x32_bf16 v[8:11],v[208:211],v[140:143],v[8:11]\n"
"ds_read_b64 v[234:235],v145 offset:26624\n"
"v_mfma_f32_16x16x32_bf16 v[4:7],v[212:215],v[72:75],0\n"
"ds_read_b64 v[236:237],v146 offset:10240\n"
"s_waitcnt lgkmcnt(7)\n"
"v_mfma_f32_16x16x32_bf16 v[4:7],v[216:219],v[80:83],v[4:7]\n"
"ds_read_b64 v[238:239],v146 offset:26624\n"
"v_mfma_f32_16x16x32_bf16 v[12:15],v[212:215],v[76:79],0\n"
"ds_read_b64 v[208:209],v147 offset:10240\n"
"v_mfma_f32_16x16x32_bf16 v[12:15],v[216:219],v[84:87],v[12:15]\n"
"ds_read_b64 v[210:211],v147 offset:26624\n"
"v_mfma_f32_16x16x32_bf16 v[12:15],v[220:223],v[92:95],v[12:15]\n"
"ds_read_b64 v[212:213],v148 offset:12800\n"
"s_waitcnt lgkmcnt(7)\n"
"v_mfma_f32_16x16x32_bf16 v[12:15],v[224:227],v[100:103],v[12:15]\n"
"ds_read_b64 v[214:215],v148 offset:31232\n"
"v_mfma_f32_16x16x32_bf16 v[4:7],v[220:223],v[88:91],v[4:7]\n"
"s_barrier\n"
"v_mfma_f32_16x16x32_bf16 v[4:7],v[224:227],v[96:99],v[4:7]\n"
"v_mfma_f32_16x16x32_bf16 v[4:7],v[228:231],v[104:107],v[4:7]\n"
"s_waitcnt lgkmcnt(4)\n"
"v_mfma_f32_16x16x32_bf16 v[4:7],v[232:235],v[112:115],v[4:7]\n"
"v_mfma_f32_16x16x32_bf16 v[12:15],v[228:231],v[108:111],v[12:15]\n"
"v_mfma_f32_16x16x32_bf16 v[12:15],v[232:235],v[116:119],v[12:15]\n"
"v_mfma_f32_16x16x32_bf16 v[12:15],v[236:239],v[124:127],v[12:15]\n"
"s_waitcnt lgkmcnt(0)\n"
"v_mfma_f32_16x16x32_bf16 v[12:15],v[208:211],v[132:135],v[12:15]\n"
"v_mfma_f32_16x16x32_bf16 v[4:7],v[236:239],v[120:123],v[4:7]\n"
"v_mfma_f32_16x16x32_bf16 v[4:7],v[208:211],v[128:131],v[4:7]\n"
"v_mfma_f32_16x16x32_bf16 v[4:7],v[212:215],v[136:139],v[4:7]\n"
"v_mfma_f32_16x16x32_bf16 v[12:15],v[212:215],v[140:143],v[12:15]\n"
"s_setprio 0\n"
"v_add_u32 v144,4294901760,v144\n"
"v_add_u32 v145,4294901760,v145\n"
"v_add_u32 v146,4294901760,v146\n"
"v_add_u32 v147,4294901760,v147\n"
"s_addk_i32 m0,8192\n"
"s_add_u32 s74,s81,294912\n"
"buffer_load_dwordx4 v251,s[24:27],s74 offen lds\n"
"s_addk_i32 m0,8192\n"
"s_addk_i32 s74,128\n"
"buffer_load_dwordx4 v251,s[24:27],s74 offen lds\n"
"s_addk_i32 m0,8192\n"
"s_addk_i32 s74,-128\n"
"buffer_load_dwordx4 v251,s[28:31],s74 offen lds\n"
"s_addk_i32 m0,8192\n"
"s_addk_i32 s74,128\n"
"buffer_load_dwordx4 v251,s[28:31],s74 offen lds\n"
"v_cvt_pk_bf16_f32 v16,v0,v1\n"
"v_cvt_pk_bf16_f32 v17,v2,v3\n"
"v_cvt_pk_bf16_f32 v18,v4,v5\n"
"v_cvt_pk_bf16_f32 v19,v6,v7\n"
"s_add_u32 s78,s87,12288\n"
"buffer_store_dwordx4 v[16:19],v252,s[32:35],s78 offen sc1\n"
"v_cvt_pk_bf16_f32 v20,v8,v9\n"
"v_cvt_pk_bf16_f32 v21,v10,v11\n"
"v_cvt_pk_bf16_f32 v22,v12,v13\n"
"v_cvt_pk_bf16_f32 v23,v14,v15\n"
"s_add_u32 s78,s87,30720\n"
"buffer_store_dwordx4 v[20:23],v252,s[32:35],s78 offen sc1\n"
"s_waitcnt vmcnt(8)\n"
"s_setprio 3\n"
"s_barrier\n"
"ds_read_b64 v[208:209],v144\n"
"ds_read_b64 v[210:211],v144 offset:16384\n"
"ds_read_b64 v[212:213],v145\n"
"ds_read_b64 v[214:215],v145 offset:16384\n"
"ds_read_b64 v[216:217],v146\n"
"ds_read_b64 v[218:219],v146 offset:16384\n"
"ds_read_b64 v[220:221],v147\n"
"ds_read_b64 v[222:223],v147 offset:16384\n"
"ds_read_b64 v[224:225],v144 offset:8192\n"
"ds_read_b64 v[226:227],v144 offset:24576\n"
"ds_read_b64 v[228:229],v145 offset:8192\n"
"ds_read_b64 v[230:231],v145 offset:24576\n"
"s_setprio 1\n"
"s_waitcnt lgkmcnt(8)\n"
"v_mfma_f32_16x16x32_bf16 v[0:3],v[208:211],v[72:75],0\n"
"ds_read_b64 v[232:233],v146 offset:8192\n"
"v_mfma_f32_16x16x32_bf16 v[0:3],v[212:215],v[80:83],v[0:3]\n"
"ds_read_b64 v[234:235],v146 offset:24576\n"
"v_mfma_f32_16x16x32_bf16 v[8:11],v[208:211],v[76:79],0\n"
"ds_read_b64 v[236:237],v147 offset:8192\n"
"v_mfma_f32_16x16x32_bf16 v[8:11],v[212:215],v[84:87],v[8:11]\n"
"ds_read_b64 v[238:239],v147 offset:24576\n"
"s_waitcnt lgkmcnt(8)\n"
"v_mfma_f32_16x16x32_bf16 v[8:11],v[216:219],v[92:95],v[8:11]\n"
"ds_read_b64 v[208:209],v148 offset:2048\n"
"v_mfma_f32_16x16x32_bf16 v[8:11],v[220:223],v[100:103],v[8:11]\n"
"ds_read_b64 v[210:211],v148 offset:20480\n"
"s_setprio 2\n"
".Lk_pads9:\n"
"s_cbranch_execz .Lk_hop_rets\n"
"v_mfma_f32_16x16x32_bf16 v[0:3],v[216:219],v[88:91],v[0:3]\n"
"ds_read_b64 v[212:213],v144 offset:2048\n"
"v_mfma_f32_16x16x32_bf16 v[0:3],v[220:223],v[96:99],v[0:3]\n"
"ds_read_b64 v[214:215],v144 offset:18432\n"
"s_waitcnt lgkmcnt(8)\n"
"v_mfma_f32_16x16x32_bf16 v[0:3],v[224:227],v[104:107],v[0:3]\n"
"ds_read_b64 v[216:217],v145 offset:2048\n"
"v_mfma_f32_16x16x32_bf16 v[0:3],v[228:231],v[112:115],v[0:3]\n"
"ds_read_b64 v[218:219],v145 offset:18432\n"
"v_mfma_f32_16x16x32_bf16 v[8:11],v[224:227],v[108:111],v[8:11]\n"
"ds_read_b64 v[220:221],v146 offset:2048\n"
"v_mfma_f32_16x16x32_bf16 v[8:11],v[228:231],v[116:119],v[8:11]\n"
"ds_read_b64 v[222:223],v146 offset:18432\n"
"s_waitcnt lgkmcnt(8)\n"
"v_mfma_f32_16x16x32_bf16 v[8:11],v[232:235],v[124:127],v[8:11]\n"
"ds_read_b64 v[224:225],v147 offset:2048\n"
"v_mfma_f32_16x16x32_bf16 v[8:11],v[236:239],v[132:135],v[8:11]\n"
"ds_read_b64 v[226:227],v147 offset:18432\n"
"v_mfma_f32_16x16x32_bf16 v[0:3],v[232:235],v[120:123],v[0:3]\n"
"ds_read_b64 v[228:229],v144 offset:10240\n"
"v_mfma_f32_16x16x32_bf16 v[0:3],v[236:239],v[128:131],v[0:3]\n"
"ds_read_b64 v[230:231],v144 offset:26624\n"
"s_waitcnt lgkmcnt(8)\n"
"v_mfma_f32_16x16x32_bf16 v[0:3],v[208:211],v[136:139],v[0:3]\n"
"ds_read_b64 v[232:233],v145 offset:10240\n"
"v_mfma_f32_16x16x32_bf16 v[8:11],v[208:211],v[140:143],v[8:11]\n"
"ds_read_b64 v[234:235],v145 offset:26624\n"
"v_mfma_f32_16x16x32_bf16 v[4:7],v[212:215],v[72:75],0\n"
"ds_read_b64 v[236:237],v146 offset:10240\n"
"s_waitcnt lgkmcnt(7)\n"
"v_mfma_f32_16x16x32_bf16 v[4:7],v[216:219],v[80:83],v[4:7]\n"
"ds_read_b64 v[238:239],v146 offset:26624\n"
"v_mfma_f32_16x16x32_bf16 v[12:15],v[212:215],v[76:79],0\n"
"ds_read_b64 v[208:209],v147 offset:10240\n"
"v_mfma_f32_16x16x32_bf16 v[12:15],v[216:219],v[84:87],v[12:15]\n"
"ds_read_b64 v[210:211],v147 offset:26624\n"
"v_mfma_f32_16x16x32_bf16 v[12:15],v[220:223],v[92:95],v[12:15]\n"
"ds_read_b64 v[212:213],v148 offset:2560\n"
"s_waitcnt lgkmcnt(7)\n"
"v_mfma_f32_16x16x32_bf16 v[12:15],v[224:227],v[100:103],v[12:15]\n"
"ds_read_b64 v[214:215],v148 offset:20992\n"
"v_mfma_f32_16x16x32_bf16 v[4:7],v[220:223],v[88:91],v[4:7]\n"
"s_barrier\n"
"v_mfma_f32_16x16x32_bf16 v[4:7],v[224:227],v[96:99],v[4:7]\n"
"v_mfma_f32_16x16x32_bf16 v[4:7],v[228:231],v[104:107],v[4:7]\n"
"s_waitcnt lgkmcnt(4)\n"
"v_mfma_f32_16x16x32_bf16 v[4:7],v[232:235],v[112:115],v[4:7]\n"
"v_mfma_f32_16x16x32_bf16 v[12:15],v[228:231],v[108:111],v[12:15]\n"
"v_mfma_f32_16x16x32_bf16 v[12:15],v[232:235],v[116:119],v[12:15]\n"
"v_mfma_f32_16x16x32_bf16 v[12:15],v[236:239],v[124:127],v[12:15]\n"
"s_waitcnt lgkmcnt(0)\n"
"v_mfma_f32_16x16x32_bf16 v[12:15],v[208:211],v[132:135],v[12:15]\n"
"v_mfma_f32_16x16x32_bf16 v[4:7],v[236:239],v[120:123],v[4:7]\n"
"v_mfma_f32_16x16x32_bf16 v[4:7],v[208:211],v[128:131],v[4:7]\n"
"v_mfma_f32_16x16x32_bf16 v[4:7],v[212:215],v[136:139],v[4:7]\n"
"v_mfma_f32_16x16x32_bf16 v[12:15],v[212:215],v[140:143],v[12:15]\n"
"s_setprio 0\n"
"v_add_u32 v144,32768,v144\n"
"v_add_u32 v145,32768,v145\n"
"v_add_u32 v146,32768,v146\n"
"v_add_u32 v147,32768,v147\n"
"s_addk_i32 m0,8192\n"
"s_add_u32 s74,s81,516096\n"
"buffer_load_dwordx4 v251,s[24:27],s74 offen lds\n"
"s_addk_i32 m0,8192\n"
"s_addk_i32 s74,128\n"
"buffer_load_dwordx4 v251,s[24:27],s74 offen lds\n"
"s_addk_i32 m0,8192\n"
"s_addk_i32 s74,-128\n"
"buffer_load_dwordx4 v251,s[28:31],s74 offen lds\n"
"s_addk_i32 m0,8192\n"
"s_addk_i32 s74,128\n"
"buffer_load_dwordx4 v251,s[28:31],s74 offen lds\n"
"v_cvt_pk_bf16_f32 v16,v0,v1\n"
"v_cvt_pk_bf16_f32 v17,v2,v3\n"
"v_cvt_pk_bf16_f32 v18,v4,v5\n"
"v_cvt_pk_bf16_f32 v19,v6,v7\n"
"s_add_u32 s78,s87,2048\n"
"buffer_store_dwordx4 v[16:19],v252,s[32:35],s78 offen sc1\n"
"v_cvt_pk_bf16_f32 v20,v8,v9\n"
"v_cvt_pk_bf16_f32 v21,v10,v11\n"
"v_cvt_pk_bf16_f32 v22,v12,v13\n"
"v_cvt_pk_bf16_f32 v23,v14,v15\n"
"s_add_u32 s78,s87,20480\n"
"buffer_store_dwordx4 v[20:23],v252,s[32:35],s78 offen sc1\n"
"s_waitcnt vmcnt(8)\n"
"s_setprio 3\n"
"s_barrier\n"
"ds_read_b64 v[208:209],v144\n"
"ds_read_b64 v[210:211],v144 offset:16384\n"
"ds_read_b64 v[212:213],v145\n"
"ds_read_b64 v[214:215],v145 offset:16384\n"
"ds_read_b64 v[216:217],v146\n"
"ds_read_b64 v[218:219],v146 offset:16384\n"
"ds_read_b64 v[220:221],v147\n"
"ds_read_b64 v[222:223],v147 offset:16384\n"
"ds_read_b64 v[224:225],v144 offset:8192\n"
"ds_read_b64 v[226:227],v144 offset:24576\n"
"ds_read_b64 v[228:229],v145 offset:8192\n"
"ds_read_b64 v[230:231],v145 offset:24576\n"
"s_waitcnt vmcnt(6)\n"
"v_mov_b32 v240,1\n"
"s_mov_b64 exec,1\n"
"buffer_store_dword v240,off,s[32:35],s88 sc1\n"
"s_mov_b64 exec,-1\n"
"s_setprio 1\n"
"s_waitcnt lgkmcnt(8)\n"
"v_mfma_f32_16x16x32_bf16 v[0:3],v[208:211],v[72:75],0\n"
"ds_read_b64 v[232:233],v146 offset:8192\n"
"v_mfma_f32_16x16x32_bf16 v[0:3],v[212:215],v[80:83],v[0:3]\n"
"ds_read_b64 v[234:235],v146 offset:24576\n"
"v_mfma_f32_16x16x32_bf16 v[8:11],v[208:211],v[76:79],0\n"
"ds_read_b64 v[236:237],v147 offset:8192\n"
"v_mfma_f32_16x16x32_bf16 v[8:11],v[212:215],v[84:87],v[8:11]\n"
"ds_read_b64 v[238:239],v147 offset:24576\n"
"s_waitcnt lgkmcnt(8)\n"
"v_mfma_f32_16x16x32_bf16 v[8:11],v[216:219],v[92:95],v[8:11]\n"
"ds_read_b64 v[208:209],v148 offset:8192\n"
"v_mfma_f32_16x16x32_bf16 v[8:11],v[220:223],v[100:103],v[8:11]\n"
"ds_read_b64 v[210:211],v148 offset:26624\n"
"s_setprio 2\n"
"v_mfma_f32_16x16x32_bf16 v[0:3],v[216:219],v[88:91],v[0:3]\n"
"ds_read_b64 v[212:213],v144 offset:2048\n"
"v_mfma_f32_16x16x32_bf16 v[0:3],v[220:223],v[96:99],v[0:3]\n"
"ds_read_b64 v[214:215],v144 offset:18432\n"
"s_waitcnt lgkmcnt(8)\n"
"v_mfma_f32_16x16x32_bf16 v[0:3],v[224:227],v[104:107],v[0:3]\n"
"ds_read_b64 v[216:217],v145 offset:2048\n"
"v_mfma_f32_16x16x32_bf16 v[0:3],v[228:231],v[112:115],v[0:3]\n"
"ds_read_b64 v[218:219],v145 offset:18432\n"
"v_mfma_f32_16x16x32_bf16 v[8:11],v[224:227],v[108:111],v[8:11]\n"
"ds_read_b64 v[220:221],v146 offset:2048\n"
"v_mfma_f32_16x16x32_bf16 v[8:11],v[228:231],v[116:119],v[8:11]\n"
"ds_read_b64 v[222:223],v146 offset:18432\n"
"s_waitcnt lgkmcnt(8)\n"
"v_mfma_f32_16x16x32_bf16 v[8:11],v[232:235],v[124:127],v[8:11]\n"
"ds_read_b64 v[224:225],v147 offset:2048\n"
"v_mfma_f32_16x16x32_bf16 v[8:11],v[236:239],v[132:135],v[8:11]\n"
"ds_read_b64 v[226:227],v147 offset:18432\n"
"v_mfma_f32_16x16x32_bf16 v[0:3],v[232:235],v[120:123],v[0:3]\n"
"ds_read_b64 v[228:229],v144 offset:10240\n"
"v_mfma_f32_16x16x32_bf16 v[0:3],v[236:239],v[128:131],v[0:3]\n"
"ds_read_b64 v[230:231],v144 offset:26624\n"
"s_waitcnt lgkmcnt(8)\n"
"v_mfma_f32_16x16x32_bf16 v[0:3],v[208:211],v[136:139],v[0:3]\n"
"ds_read_b64 v[232:233],v145 offset:10240\n"
"v_mfma_f32_16x16x32_bf16 v[8:11],v[208:211],v[140:143],v[8:11]\n"
"ds_read_b64 v[234:235],v145 offset:26624\n"
"v_mfma_f32_16x16x32_bf16 v[4:7],v[212:215],v[72:75],0\n"
"ds_read_b64 v[236:237],v146 offset:10240\n"
"s_waitcnt lgkmcnt(7)\n"
"v_mfma_f32_16x16x32_bf16 v[4:7],v[216:219],v[80:83],v[4:7]\n"
"ds_read_b64 v[238:239],v146 offset:26624\n"
"v_mfma_f32_16x16x32_bf16 v[12:15],v[212:215],v[76:79],0\n"
"ds_read_b64 v[208:209],v147 offset:10240\n"
"v_mfma_f32_16x16x32_bf16 v[12:15],v[216:219],v[84:87],v[12:15]\n"
"ds_read_b64 v[210:211],v147 offset:26624\n"
"v_mfma_f32_16x16x32_bf16 v[12:15],v[220:223],v[92:95],v[12:15]\n"
"ds_read_b64 v[212:213],v148 offset:8704\n"
"s_waitcnt lgkmcnt(7)\n"
"v_mfma_f32_16x16x32_bf16 v[12:15],v[224:227],v[100:103],v[12:15]\n"
"ds_read_b64 v[214:215],v148 offset:27136\n"
"v_mfma_f32_16x16x32_bf16 v[4:7],v[220:223],v[88:91],v[4:7]\n"
"s_barrier\n"
"v_mfma_f32_16x16x32_bf16 v[4:7],v[224:227],v[96:99],v[4:7]\n"
"v_mfma_f32_16x16x32_bf16 v[4:7],v[228:231],v[104:107],v[4:7]\n"
"s_waitcnt lgkmcnt(4)\n"
"v_mfma_f32_16x16x32_bf16 v[4:7],v[232:235],v[112:115],v[4:7]\n"
"v_mfma_f32_16x16x32_bf16 v[12:15],v[228:231],v[108:111],v[12:15]\n"
"v_mfma_f32_16x16x32_bf16 v[12:15],v[232:235],v[116:119],v[12:15]\n"
"v_mfma_f32_16x16x32_bf16 v[12:15],v[236:239],v[124:127],v[12:15]\n"
"s_waitcnt lgkmcnt(0)\n"
"v_mfma_f32_16x16x32_bf16 v[12:15],v[208:211],v[132:135],v[12:15]\n"
"v_mfma_f32_16x16x32_bf16 v[4:7],v[236:239],v[120:123],v[4:7]\n"
"v_mfma_f32_16x16x32_bf16 v[4:7],v[208:211],v[128:131],v[4:7]\n"
"v_mfma_f32_16x16x32_bf16 v[4:7],v[212:215],v[136:139],v[4:7]\n"
"v_mfma_f32_16x16x32_bf16 v[12:15],v[212:215],v[140:143],v[12:15]\n"
"s_setprio 0\n"
"v_add_u32 v144,32768,v144\n"
"v_add_u32 v145,32768,v145\n"
"v_add_u32 v146,32768,v146\n"
"v_add_u32 v147,32768,v147\n"
"s_add_u32 m0,s73,40960\n"
"s_add_u32 s74,s81,147456\n"
"buffer_load_dwordx4 v251,s[24:27],s74 offen lds\n"
"s_addk_i32 m0,8192\n"
"s_addk_i32 s74,128\n"
"buffer_load_dwordx4 v251,s[24:27],s74 offen lds\n"
"s_addk_i32 m0,8192\n"
"s_addk_i32 s74,-128\n"
"buffer_load_dwordx4 v251,s[28:31],s74 offen lds\n"
"s_addk_i32 m0,8192\n"
"s_addk_i32 s74,128\n"
"buffer_load_dwordx4 v251,s[28:31],s74 offen lds\n"
"v_cvt_pk_bf16_f32 v16,v0,v1\n"
"v_cvt_pk_bf16_f32 v17,v2,v3\n"
"v_cvt_pk_bf16_f32 v18,v4,v5\n"
"v_cvt_pk_bf16_f32 v19,v6,v7\n"
"s_add_u32 s78,s87,8192\n"
"buffer_store_dwordx4 v[16:19],v252,s[32:35],s78 offen sc1\n"
"v_cvt_pk_bf16_f32 v20,v8,v9\n"
"v_cvt_pk_bf16_f32 v21,v10,v11\n"
"v_cvt_pk_bf16_f32 v22,v12,v13\n"
"v_cvt_pk_bf16_f32 v23,v14,v15\n"
"s_add_u32 s78,s87,26624\n"
"buffer_store_dwordx4 v[20:23],v252,s[32:35],s78 offen sc1\n"
"s_waitcnt vmcnt(9)\n"
"s_setprio 3\n"
"s_barrier\n"
"ds_read_b64 v[208:209],v144\n"
"ds_read_b64 v[210:211],v144 offset:16384\n"
"ds_read_b64 v[212:213],v145\n"
"ds_read_b64 v[214:215],v145 offset:16384\n"
"ds_read_b64 v[216:217],v146\n"
"ds_read_b64 v[218:219],v146 offset:16384\n"
"ds_read_b64 v[220:221],v147\n"
"ds_read_b64 v[222:223],v147 offset:16384\n"
"ds_read_b64 v[224:225],v144 offset:8192\n"
"ds_read_b64 v[226:227],v144 offset:24576\n"
"ds_read_b64 v[228:229],v145 offset:8192\n"
"ds_read_b64 v[230:231],v145 offset:24576\n"
"s_setprio 1\n"
"s_waitcnt lgkmcnt(8)\n"
"v_mfma_f32_16x16x32_bf16 v[0:3],v[208:211],v[72:75],0\n"
"ds_read_b64 v[232:233],v146 offset:8192\n"
"v_mfma_f32_16x16x32_bf16 v[0:3],v[212:215],v[80:83],v[0:3]\n"
"ds_read_b64 v[234:235],v146 offset:24576\n"
"v_mfma_f32_16x16x32_bf16 v[8:11],v[208:211],v[76:79],0\n"
"ds_read_b64 v[236:237],v147 offset:8192\n"
"v_mfma_f32_16x16x32_bf16 v[8:11],v[212:215],v[84:87],v[8:11]\n"
"ds_read_b64 v[238:239],v147 offset:24576\n"
"s_waitcnt lgkmcnt(8)\n"
"v_mfma_f32_16x16x32_bf16 v[8:11],v[216:219],v[92:95],v[8:11]\n"
"ds_read_b64 v[208:209],v148 offset:14336\n"
"v_mfma_f32_16x16x32_bf16 v[8:11],v[220:223],v[100:103],v[8:11]\n"
"ds_read_b64 v[210:211],v148 offset:32768\n"
"s_setprio 2\n"
"v_mfma_f32_16x16x32_bf16 v[0:3],v[216:219],v[88:91],v[0:3]\n"
"ds_read_b64 v[212:213],v144 offset:2048\n"
"v_mfma_f32_16x16x32_bf16 v[0:3],v[220:223],v[96:99],v[0:3]\n"
"ds_read_b64 v[214:215],v144 offset:18432\n"
"s_waitcnt lgkmcnt(8)\n"
"v_mfma_f32_16x16x32_bf16 v[0:3],v[224:227],v[104:107],v[0:3]\n"
"ds_read_b64 v[216:217],v145 offset:2048\n"
"v_mfma_f32_16x16x32_bf16 v[0:3],v[228:231],v[112:115],v[0:3]\n"
"ds_read_b64 v[218:219],v145 offset:18432\n"
"v_mfma_f32_16x16x32_bf16 v[8:11],v[224:227],v[108:111],v[8:11]\n"
"ds_read_b64 v[220:221],v146 offset:2048\n"
"v_mfma_f32_16x16x32_bf16 v[8:11],v[228:231],v[116:119],v[8:11]\n"
"ds_read_b64 v[222:223],v146 offset:18432\n"
"s_waitcnt lgkmcnt(8)\n"
"v_mfma_f32_16x16x32_bf16 v[8:11],v[232:235],v[124:127],v[8:11]\n"
"ds_read_b64 v[224:225],v147 offset:2048\n"
"v_mfma_f32_16x16x32_bf16 v[8:11],v[236:239],v[132:135],v[8:11]\n"
"ds_read_b64 v[226:227],v147 offset:18432\n"
"v_mfma_f32_16x16x32_bf16 v[0:3],v[232:235],v[120:123],v[0:3]\n"
"ds_read_b64 v[228:229],v144 offset:10240\n"
"v_mfma_f32_16x16x32_bf16 v[0:3],v[236:239],v[128:131],v[0:3]\n"
"ds_read_b64 v[230:231],v144 offset:26624\n"
"s_waitcnt lgkmcnt(8)\n"
"v_mfma_f32_16x16x32_bf16 v[0:3],v[208:211],v[136:139],v[0:3]\n"
"ds_read_b64 v[232:233],v145 offset:10240\n"
"v_mfma_f32_16x16x32_bf16 v[8:11],v[208:211],v[140:143],v[8:11]\n"
"ds_read_b64 v[234:235],v145 offset:26624\n"
"v_mfma_f32_16x16x32_bf16 v[4:7],v[212:215],v[72:75],0\n"
"ds_read_b64 v[236:237],v146 offset:10240\n"
"s_waitcnt lgkmcnt(7)\n"
"v_mfma_f32_16x16x32_bf16 v[4:7],v[216:219],v[80:83],v[4:7]\n"
"ds_read_b64 v[238:239],v146 offset:26624\n"
"v_mfma_f32_16x16x32_bf16 v[12:15],v[212:215],v[76:79],0\n"
"ds_read_b64 v[208:209],v147 offset:10240\n"
"v_mfma_f32_16x16x32_bf16 v[12:15],v[216:219],v[84:87],v[12:15]\n"
"ds_read_b64 v[210:211],v147 offset:26624\n"
"v_mfma_f32_16x16x32_bf16 v[12:15],v[220:223],v[92:95],v[12:15]\n"
"ds_read_b64 v[212:213],v148 offset:14848\n"
"s_waitcnt lgkmcnt(7)\n"
"v_mfma_f32_16x16x32_bf16 v[12:15],v[224:227],v[100:103],v[12:15]\n"
"ds_read_b64 v[214:215],v148 offset:33280\n"
"v_mfma_f32_16x16x32_bf16 v[4:7],v[220:223],v[88:91],v[4:7]\n"
"s_barrier\n"
"v_mfma_f32_16x16x32_bf16 v[4:7],v[224:227],v[96:99],v[4:7]\n"
"v_mfma_f32_16x16x32_bf16 v[4:7],v[228:231],v[104:107],v[4:7]\n"
"s_waitcnt lgkmcnt(4)\n"
"v_mfma_f32_16x16x32_bf16 v[4:7],v[232:235],v[112:115],v[4:7]\n"
"v_mfma_f32_16x16x32_bf16 v[12:15],v[228:231],v[108:111],v[12:15]\n"
"v_mfma_f32_16x16x32_bf16 v[12:15],v[232:235],v[116:119],v[12:15]\n"
"v_mfma_f32_16x16x32_bf16 v[12:15],v[236:239],v[124:127],v[12:15]\n"
"s_waitcnt lgkmcnt(0)\n"
"v_mfma_f32_16x16x32_bf16 v[12:15],v[208:211],v[132:135],v[12:15]\n"
"v_mfma_f32_16x16x32_bf16 v[4:7],v[236:239],v[120:123],v[4:7]\n"
"v_mfma_f32_16x16x32_bf16 v[4:7],v[208:211],v[128:131],v[4:7]\n"
"v_mfma_f32_16x16x32_bf16 v[4:7],v[212:215],v[136:139],v[4:7]\n"
"v_mfma_f32_16x16x32_bf16 v[12:15],v[212:215],v[140:143],v[12:15]\n"
"s_setprio 0\n"
"v_add_u32 v144,4294901760,v144\n"
"v_add_u32 v145,4294901760,v145\n"
"v_add_u32 v146,4294901760,v146\n"
"v_add_u32 v147,4294901760,v147\n"
"s_addk_i32 m0,8192\n"
"s_add_u32 s74,s81,368640\n"
"buffer_load_dwordx4 v251,s[24:27],s74 offen lds\n"
"s_addk_i32 m0,8192\n"
"s_addk_i32 s74,128\n"
"buffer_load_dwordx4 v251,s[24:27],s74 offen lds\n"
"s_addk_i32 m0,8192\n"
"s_addk_i32 s74,-128\n"
"buffer_load_dwordx4 v251,s[28:31],s74 offen lds\n"
"s_addk_i32 m0,8192\n"
"s_addk_i32 s74,128\n"
"buffer_load_dwordx4 v251,s[28:31],s74 offen lds\n"
"v_cvt_pk_bf16_f32 v16,v0,v1\n"
"v_cvt_pk_bf16_f32 v17,v2,v3\n"
"v_cvt_pk_bf16_f32 v18,v4,v5\n"
"v_cvt_pk_bf16_f32 v19,v6,v7\n"
"s_add_u32 s78,s87,14336\n"
"buffer_store_dwordx4 v[16:19],v252,s[32:35],s78 offen sc1\n"
"v_cvt_pk_bf16_f32 v20,v8,v9\n"
"v_cvt_pk_bf16_f32 v21,v10,v11\n"
"v_cvt_pk_bf16_f32 v22,v12,v13\n"
"v_cvt_pk_bf16_f32 v23,v14,v15\n"
"s_add_u32 s78,s87,32768\n"
"buffer_store_dwordx4 v[20:23],v252,s[32:35],s78 offen sc1\n"
"s_waitcnt vmcnt(8)\n"
"s_setprio 3\n"
"s_barrier\n"
"ds_read_b64 v[208:209],v144\n"
"ds_read_b64 v[210:211],v144 offset:16384\n"
"ds_read_b64 v[212:213],v145\n"
"ds_read_b64 v[214:215],v145 offset:16384\n"
"ds_read_b64 v[216:217],v146\n"
"ds_read_b64 v[218:219],v146 offset:16384\n"
"ds_read_b64 v[220:221],v147\n"
"ds_read_b64 v[222:223],v147 offset:16384\n"
"ds_read_b64 v[224:225],v144 offset:8192\n"
"ds_read_b64 v[226:227],v144 offset:24576\n"
"ds_read_b64 v[228:229],v145 offset:8192\n"
"ds_read_b64 v[230:231],v145 offset:24576\n"
"s_setprio 1\n"
"s_waitcnt lgkmcnt(8)\n"
"v_mfma_f32_16x16x32_bf16 v[0:3],v[208:211],v[72:75],0\n"
"ds_read_b64 v[232:233],v146 offset:8192\n"
"v_mfma_f32_16x16x32_bf16 v[0:3],v[212:215],v[80:83],v[0:3]\n"
"ds_read_b64 v[234:235],v146 offset:24576\n"
"v_mfma_f32_16x16x32_bf16 v[8:11],v[208:211],v[76:79],0\n"
"ds_read_b64 v[236:237],v147 offset:8192\n"
"v_mfma_f32_16x16x32_bf16 v[8:11],v[212:215],v[84:87],v[8:11]\n"
"ds_read_b64 v[238:239],v147 offset:24576\n"
"s_waitcnt lgkmcnt(8)\n"
"v_mfma_f32_16x16x32_bf16 v[8:11],v[216:219],v[92:95],v[8:11]\n"
"ds_read_b64 v[208:209],v148 offset:4096\n"
"v_mfma_f32_16x16x32_bf16 v[8:11],v[220:223],v[100:103],v[8:11]\n"
"ds_read_b64 v[210:211],v148 offset:22528\n"
"s_setprio 2\n"
"v_mfma_f32_16x16x32_bf16 v[0:3],v[216:219],v[88:91],v[0:3]\n"
"ds_read_b64 v[212:213],v144 offset:2048\n"
"v_mfma_f32_16x16x32_bf16 v[0:3],v[220:223],v[96:99],v[0:3]\n"
"ds_read_b64 v[214:215],v144 offset:18432\n"
"s_waitcnt lgkmcnt(8)\n"
"v_mfma_f32_16x16x32_bf16 v[0:3],v[224:227],v[104:107],v[0:3]\n"
"ds_read_b64 v[216:217],v145 offset:2048\n"
"v_mfma_f32_16x16x32_bf16 v[0:3],v[228:231],v[112:115],v[0:3]\n"
"ds_read_b64 v[218:219],v145 offset:18432\n"
"v_mfma_f32_16x16x32_bf16 v[8:11],v[224:227],v[108:111],v[8:11]\n"
"ds_read_b64 v[220:221],v146 offset:2048\n"
"v_mfma_f32_16x16x32_bf16 v[8:11],v[228:231],v[116:119],v[8:11]\n"
"ds_read_b64 v[222:223],v146 offset:18432\n"
"s_waitcnt lgkmcnt(8)\n"
"v_mfma_f32_16x16x32_bf16 v[8:11],v[232:235],v[124:127],v[8:11]\n"
"ds_read_b64 v[224:225],v147 offset:2048\n"
"v_mfma_f32_16x16x32_bf16 v[8:11],v[236:239],v[132:135],v[8:11]\n"
"ds_read_b64 v[226:227],v147 offset:18432\n"
"v_mfma_f32_16x16x32_bf16 v[0:3],v[232:235],v[120:123],v[0:3]\n"
"ds_read_b64 v[228:229],v144 offset:10240\n"
"v_mfma_f32_16x16x32_bf16 v[0:3],v[236:239],v[128:131],v[0:3]\n"
"ds_read_b64 v[230:231],v144 offset:26624\n"
"s_waitcnt lgkmcnt(8)\n"
"v_mfma_f32_16x16x32_bf16 v[0:3],v[208:211],v[136:139],v[0:3]\n"
"ds_read_b64 v[232:233],v145 offset:10240\n"
"v_mfma_f32_16x16x32_bf16 v[8:11],v[208:211],v[140:143],v[8:11]\n"
"ds_read_b64 v[234:235],v145 offset:26624\n"
"v_mfma_f32_16x16x32_bf16 v[4:7],v[212:215],v[72:75],0\n"
"ds_read_b64 v[236:237],v146 offset:10240\n"
"s_waitcnt lgkmcnt(7)\n"
"v_mfma_f32_16x16x32_bf16 v[4:7],v[216:219],v[80:83],v[4:7]\n"
"ds_read_b64 v[238:239],v146 offset:26624\n"
"v_mfma_f32_16x16x32_bf16 v[12:15],v[212:215],v[76:79],0\n"
"ds_read_b64 v[208:209],v147 offset:10240\n"
"v_mfma_f32_16x16x32_bf16 v[12:15],v[216:219],v[84:87],v[12:15]\n"
"ds_read_b64 v[210:211],v147 offset:26624\n"
"v_mfma_f32_16x16x32_bf16 v[12:15],v[220:223],v[92:95],v[12:15]\n"
"ds_read_b64 v[212:213],v148 offset:4608\n"
"s_waitcnt lgkmcnt(7)\n"
"v_mfma_f32_16x16x32_bf16 v[12:15],v[224:227],v[100:103],v[12:15]\n"
"ds_read_b64 v[214:215],v148 offset:23040\n"
"v_mfma_f32_16x16x32_bf16 v[4:7],v[220:223],v[88:91],v[4:7]\n"
"s_barrier\n"
"v_mfma_f32_16x16x32_bf16 v[4:7],v[224:227],v[96:99],v[4:7]\n"
"v_mfma_f32_16x16x32_bf16 v[4:7],v[228:231],v[104:107],v[4:7]\n"
"s_waitcnt lgkmcnt(4)\n"
"v_mfma_f32_16x16x32_bf16 v[4:7],v[232:235],v[112:115],v[4:7]\n"
"v_mfma_f32_16x16x32_bf16 v[12:15],v[228:231],v[108:111],v[12:15]\n"
"v_mfma_f32_16x16x32_bf16 v[12:15],v[232:235],v[116:119],v[12:15]\n"
"v_mfma_f32_16x16x32_bf16 v[12:15],v[236:239],v[124:127],v[12:15]\n"
"s_waitcnt lgkmcnt(0)\n"
"v_mfma_f32_16x16x32_bf16 v[12:15],v[208:211],v[132:135],v[12:15]\n"
"v_mfma_f32_16x16x32_bf16 v[4:7],v[236:239],v[120:123],v[4:7]\n"
"v_mfma_f32_16x16x32_bf16 v[4:7],v[208:211],v[128:131],v[4:7]\n"
"v_mfma_f32_16x16x32_bf16 v[4:7],v[212:215],v[136:139],v[4:7]\n"
"v_mfma_f32_16x16x32_bf16 v[12:15],v[212:215],v[140:143],v[12:15]\n"
"s_setprio 0\n"
"v_add_u32 v144,32768,v144\n"
"v_add_u32 v145,32768,v145\n"
"v_add_u32 v146,32768,v146\n"
"v_add_u32 v147,32768,v147\n"
"s_addk_i32 m0,8192\n"
"s_add_u32 s74,s81,589824\n"
"buffer_load_dwordx4 v251,s[24:27],s74 offen lds\n"
"s_addk_i32 m0,8192\n"
"s_addk_i32 s74,128\n"
"buffer_load_dwordx4 v251,s[24:27],s74 offen lds\n"
"s_addk_i32 m0,8192\n"
"s_addk_i32 s74,-128\n"
"buffer_load_dwordx4 v251,s[28:31],s74 offen lds\n"
"s_addk_i32 m0,8192\n"
"s_addk_i32 s74,128\n"
"buffer_load_dwordx4 v251,s[28:31],s74 offen lds\n"
"v_cvt_pk_bf16_f32 v16,v0,v1\n"
"v_cvt_pk_bf16_f32 v17,v2,v3\n"
"v_cvt_pk_bf16_f32 v18,v4,v5\n"
"v_cvt_pk_bf16_f32 v19,v6,v7\n"
"s_add_u32 s78,s87,4096\n"
"buffer_store_dwordx4 v[16:19],v252,s[32:35],s78 offen sc1\n"
"v_cvt_pk_bf16_f32 v20,v8,v9\n"
"v_cvt_pk_bf16_f32 v21,v10,v11\n"
"v_cvt_pk_bf16_f32 v22,v12,v13\n"
"v_cvt_pk_bf16_f32 v23,v14,v15\n"
"s_add_u32 s78,s87,22528\n"
"buffer_store_dwordx4 v[20:23],v252,s[32:35],s78 offen sc1\n"
"s_waitcnt vmcnt(8)\n"
"s_setprio 3\n"
"s_barrier\n"
"ds_read_b64 v[208:209],v144\n"
"ds_read_b64 v[210:211],v144 offset:16384\n"
"ds_read_b64 v[212:213],v145\n"
"ds_read_b64 v[214:215],v145 offset:16384\n"
"ds_read_b64 v[216:217],v146\n"
"ds_read_b64 v[218:219],v146 offset:16384\n"
"ds_read_b64 v[220:221],v147\n"
"ds_read_b64 v[222:223],v147 offset:16384\n"
"ds_read_b64 v[224:225],v144 offset:8192\n"
"ds_read_b64 v[226:227],v144 offset:24576\n"
"ds_read_b64 v[228:229],v145 offset:8192\n"
"ds_read_b64 v[230:231],v145 offset:24576\n"
"s_waitcnt vmcnt(6)\n"
"v_mov_b32 v240,2\n"
"s_mov_b64 exec,1\n"
"buffer_store_dword v240,off,s[32:35],s88 sc1\n"
"s_mov_b64 exec,-1\n"
"s_setprio 1\n"
"s_waitcnt lgkmcnt(8)\n"
"v_mfma_f32_16x16x32_bf16 v[0:3],v[208:211],v[72:75],0\n"
"ds_read_b64 v[232:233],v146 offset:8192\n"
"v_mfma_f32_16x16x32_bf16 v[0:3],v[212:215],v[80:83],v[0:3]\n"
"ds_read_b64 v[234:235],v146 offset:24576\n"
"v_mfma_f32_16x16x32_bf16 v[8:11],v[208:211],v[76:79],0\n"
"ds_read_b64 v[236:237],v147 offset:8192\n"
"v_mfma_f32_16x16x32_bf16 v[8:11],v[212:215],v[84:87],v[8:11]\n"
"ds_read_b64 v[238:239],v147 offset:24576\n"
"s_waitcnt lgkmcnt(8)\n"
"v_mfma_f32_16x16x32_bf16 v[8:11],v[216:219],v[92:95],v[8:11]\n"
"ds_read_b64 v[208:209],v148 offset:10240\n"
"v_mfma_f32_16x16x32_bf16 v[8:11],v[220:223],v[100:103],v[8:11]\n"
"ds_read_b64 v[210:211],v148 offset:28672\n"
"s_setprio 2\n"
"v_mfma_f32_16x16x32_bf16 v[0:3],v[216:219],v[88:91],v[0:3]\n"
"ds_read_b64 v[212:213],v144 offset:2048\n"
"v_mfma_f32_16x16x32_bf16 v[0:3],v[220:223],v[96:99],v[0:3]\n"
"ds_read_b64 v[214:215],v144 offset:18432\n"
"s_waitcnt lgkmcnt(8)\n"
"v_mfma_f32_16x16x32_bf16 v[0:3],v[224:227],v[104:107],v[0:3]\n"
"ds_read_b64 v[216:217],v145 offset:2048\n"
"v_mfma_f32_16x16x32_bf16 v[0:3],v[228:231],v[112:115],v[0:3]\n"
"ds_read_b64 v[218:219],v145 offset:18432\n"
"v_mfma_f32_16x16x32_bf16 v[8:11],v[224:227],v[108:111],v[8:11]\n"
"ds_read_b64 v[220:221],v146 offset:2048\n"
"v_mfma_f32_16x16x32_bf16 v[8:11],v[228:231],v[116:119],v[8:11]\n"
"ds_read_b64 v[222:223],v146 offset:18432\n"
"s_waitcnt lgkmcnt(8)\n"
"v_mfma_f32_16x16x32_bf16 v[8:11],v[232:235],v[124:127],v[8:11]\n"
"ds_read_b64 v[224:225],v147 offset:2048\n"
"v_mfma_f32_16x16x32_bf16 v[8:11],v[236:239],v[132:135],v[8:11]\n"
"ds_read_b64 v[226:227],v147 offset:18432\n"
"v_mfma_f32_16x16x32_bf16 v[0:3],v[232:235],v[120:123],v[0:3]\n"
"ds_read_b64 v[228:229],v144 offset:10240\n"
"v_mfma_f32_16x16x32_bf16 v[0:3],v[236:239],v[128:131],v[0:3]\n"
"ds_read_b64 v[230:231],v144 offset:26624\n"
"s_waitcnt lgkmcnt(8)\n"
"v_mfma_f32_16x16x32_bf16 v[0:3],v[208:211],v[136:139],v[0:3]\n"
"ds_read_b64 v[232:233],v145 offset:10240\n"
"v_mfma_f32_16x16x32_bf16 v[8:11],v[208:211],v[140:143],v[8:11]\n"
"ds_read_b64 v[234:235],v145 offset:26624\n"
"v_mfma_f32_16x16x32_bf16 v[4:7],v[212:215],v[72:75],0\n"
"ds_read_b64 v[236:237],v146 offset:10240\n"
"s_waitcnt lgkmcnt(7)\n"
"v_mfma_f32_16x16x32_bf16 v[4:7],v[216:219],v[80:83],v[4:7]\n"
"ds_read_b64 v[238:239],v146 offset:26624\n"
"v_mfma_f32_16x16x32_bf16 v[12:15],v[212:215],v[76:79],0\n"
"ds_read_b64 v[208:209],v147 offset:10240\n"
"v_mfma_f32_16x16x32_bf16 v[12:15],v[216:219],v[84:87],v[12:15]\n"
"ds_read_b64 v[210:211],v147 offset:26624\n"
"v_mfma_f32_16x16x32_bf16 v[12:15],v[220:223],v[92:95],v[12:15]\n"
"ds_read_b64 v[212:213],v148 offset:10752\n"
"s_waitcnt lgkmcnt(7)\n"
"v_mfma_f32_16x16x32_bf16 v[12:15],v[224:227],v[100:103],v[12:15]\n"
"ds_read_b64 v[214:215],v148 offset:29184\n"
"v_mfma_f32_16x16x32_bf16 v[4:7],v[220:223],v[88:91],v[4:7]\n"
"s_barrier\n"
"v_mfma_f32_16x16x32_bf16 v[4:7],v[224:227],v[96:99],v[4:7]\n"
"v_mfma_f32_16x16x32_bf16 v[4:7],v[228:231],v[104:107],v[4:7]\n"
"s_waitcnt lgkmcnt(4)\n"
"v_mfma_f32_16x16x32_bf16 v[4:7],v[232:235],v[112:115],v[4:7]\n"
"v_mfma_f32_16x16x32_bf16 v[12:15],v[228:231],v[108:111],v[12:15]\n"
"v_mfma_f32_16x16x32_bf16 v[12:15],v[232:235],v[116:119],v[12:15]\n"
"v_mfma_f32_16x16x32_bf16 v[12:15],v[236:239],v[124:127],v[12:15]\n"
"s_waitcnt lgkmcnt(0)\n"
"v_mfma_f32_16x16x32_bf16 v[12:15],v[208:211],v[132:135],v[12:15]\n"
"v_mfma_f32_16x16x32_bf16 v[4:7],v[236:239],v[120:123],v[4:7]\n"
"v_mfma_f32_16x16x32_bf16 v[4:7],v[208:211],v[128:131],v[4:7]\n"
"v_mfma_f32_16x16x32_bf16 v[4:7],v[212:215],v[136:139],v[4:7]\n"
"v_mfma_f32_16x16x32_bf16 v[12:15],v[212:215],v[140:143],v[12:15]\n"
"s_setprio 0\n"
"v_add_u32 v144,32768,v144\n"
"v_add_u32 v145,32768,v145\n"
"v_add_u32 v146,32768,v146\n"
"v_add_u32 v147,32768,v147\n"
"v_cvt_pk_bf16_f32 v16,v0,v1\n"
"v_cvt_pk_bf16_f32 v17,v2,v3\n"
"v_cvt_pk_bf16_f32 v18,v4,v5\n"
"v_cvt_pk_bf16_f32 v19,v6,v7\n"
"s_add_u32 s78,s87,10240\n"
"buffer_store_dwordx4 v[16:19],v252,s[32:35],s78 offen sc1\n"
"v_cvt_pk_bf16_f32 v20,v8,v9\n"
"v_cvt_pk_bf16_f32 v21,v10,v11\n"
"v_cvt_pk_bf16_f32 v22,v12,v13\n"
"v_cvt_pk_bf16_f32 v23,v14,v15\n"
"s_add_u32 s78,s87,28672\n"
"buffer_store_dwordx4 v[20:23],v252,s[32:35],s78 offen sc1\n"
"s_waitcnt vmcnt(5)\n"
"s_setprio 3\n"
"s_barrier\n"
"ds_read_b64 v[208:209],v144\n"
"ds_read_b64 v[210:211],v144 offset:16384\n"
"ds_read_b64 v[212:213],v145\n"
"ds_read_b64 v[214:215],v145 offset:16384\n"
"ds_read_b64 v[216:217],v146\n"
"ds_read_b64 v[218:219],v146 offset:16384\n"
"ds_read_b64 v[220:221],v147\n"
"ds_read_b64 v[222:223],v147 offset:16384\n"
"ds_read_b64 v[224:225],v144 offset:8192\n"
"ds_read_b64 v[226:227],v144 offset:24576\n"
"ds_read_b64 v[228:229],v145 offset:8192\n"
"ds_read_b64 v[230:231],v145 offset:24576\n"
"s_setprio 1\n"
"s_waitcnt lgkmcnt(8)\n"
"v_mfma_f32_16x16x32_bf16 v[0:3],v[208:211],v[72:75],0\n"
"ds_read_b64 v[232:233],v146 offset:8192\n"
"v_mfma_f32_16x16x32_bf16 v[0:3],v[212:215],v[80:83],v[0:3]\n"
"ds_read_b64 v[234:235],v146 offset:24576\n"
".Lk_pads11:\n"
"s_cbranch_execz .Lk_hop_rets\n"
"v_mfma_f32_16x16x32_bf16 v[8:11],v[208:211],v[76:79],0\n"
"ds_read_b64 v[236:237],v147 offset:8192\n"
"v_mfma_f32_16x16x32_bf16 v[8:11],v[212:215],v[84:87],v[8:11]\n"
"ds_read_b64 v[238:239],v147 offset:24576\n"
"s_waitcnt lgkmcnt(8)\n"
"v_mfma_f32_16x16x32_bf16 v[8:11],v[216:219],v[92:95],v[8:11]\n"
"ds_read_b64 v[208:209],v148 offset:16384\n"
"v_mfma_f32_16x16x32_bf16 v[8:11],v[220:223],v[100:103],v[8:11]\n"
"ds_read_b64 v[210:211],v148 offset:34816\n"
"v_mfma_f32_16x16x32_bf16 v[0:3],v[216:219],v[88:91],v[0:3]\n"
"ds_read_b64 v[212:213],v144 offset:2048\n"
"v_mfma_f32_16x16x32_bf16 v[0:3],v[220:223],v[96:99],v[0:3]\n"
"ds_read_b64 v[214:215],v144 offset:18432\n"
"s_waitcnt lgkmcnt(8)\n"
"v_mfma_f32_16x16x32_bf16 v[0:3],v[224:227],v[104:107],v[0:3]\n"
"ds_read_b64 v[216:217],v145 offset:2048\n"
"v_mfma_f32_16x16x32_bf16 v[0:3],v[228:231],v[112:115],v[0:3]\n"
"ds_read_b64 v[218:219],v145 offset:18432\n"
"v_mfma_f32_16x16x32_bf16 v[8:11],v[224:227],v[108:111],v[8:11]\n"
"ds_read_b64 v[220:221],v146 offset:2048\n"
"v_mfma_f32_16x16x32_bf16 v[8:11],v[228:231],v[116:119],v[8:11]\n"
"ds_read_b64 v[222:223],v146 offset:18432\n"
"s_waitcnt lgkmcnt(8)\n"
"v_mfma_f32_16x16x32_bf16 v[8:11],v[232:235],v[124:127],v[8:11]\n"
"ds_read_b64 v[224:225],v147 offset:2048\n"
"v_mfma_f32_16x16x32_bf16 v[8:11],v[236:239],v[132:135],v[8:11]\n"
"ds_read_b64 v[226:227],v147 offset:18432\n"
"v_mfma_f32_16x16x32_bf16 v[0:3],v[232:235],v[120:123],v[0:3]\n"
"ds_read_b64 v[228:229],v144 offset:10240\n"
"v_mfma_f32_16x16x32_bf16 v[0:3],v[236:239],v[128:131],v[0:3]\n"
"ds_read_b64 v[230:231],v144 offset:26624\n"
"s_waitcnt lgkmcnt(8)\n"
"v_mfma_f32_16x16x32_bf16 v[0:3],v[208:211],v[136:139],v[0:3]\n"
"ds_read_b64 v[232:233],v145 offset:10240\n"
"v_mfma_f32_16x16x32_bf16 v[8:11],v[208:211],v[140:143],v[8:11]\n"
"ds_read_b64 v[234:235],v145 offset:26624\n"
"v_mfma_f32_16x16x32_bf16 v[4:7],v[212:215],v[72:75],0\n"
"ds_read_b64 v[236:237],v146 offset:10240\n"
"s_waitcnt lgkmcnt(7)\n"
"v_mfma_f32_16x16x32_bf16 v[4:7],v[216:219],v[80:83],v[4:7]\n"
"ds_read_b64 v[238:239],v146 offset:26624\n"
"v_mfma_f32_16x16x32_bf16 v[12:15],v[212:215],v[76:79],0\n"
"ds_read_b64 v[208:209],v147 offset:10240\n"
"v_mfma_f32_16x16x32_bf16 v[12:15],v[216:219],v[84:87],v[12:15]\n"
"ds_read_b64 v[210:211],v147 offset:26624\n"
"v_mfma_f32_16x16x32_bf16 v[12:15],v[220:223],v[92:95],v[12:15]\n"
"ds_read_b64 v[212:213],v148 offset:16896\n"
"s_waitcnt lgkmcnt(7)\n"
"v_mfma_f32_16x16x32_bf16 v[12:15],v[224:227],v[100:103],v[12:15]\n"
"ds_read_b64 v[214:215],v148 offset:35328\n"
"v_mfma_f32_16x16x32_bf16 v[4:7],v[220:223],v[88:91],v[4:7]\n"
"v_mfma_f32_16x16x32_bf16 v[4:7],v[224:227],v[96:99],v[4:7]\n"
"v_mfma_f32_16x16x32_bf16 v[4:7],v[228:231],v[104:107],v[4:7]\n"
"s_waitcnt lgkmcnt(4)\n"
"v_mfma_f32_16x16x32_bf16 v[4:7],v[232:235],v[112:115],v[4:7]\n"
"v_mfma_f32_16x16x32_bf16 v[12:15],v[228:231],v[108:111],v[12:15]\n"
"v_mfma_f32_16x16x32_bf16 v[12:15],v[232:235],v[116:119],v[12:15]\n"
"v_mfma_f32_16x16x32_bf16 v[12:15],v[236:239],v[124:127],v[12:15]\n"
"s_waitcnt lgkmcnt(0)\n"
"v_mfma_f32_16x16x32_bf16 v[12:15],v[208:211],v[132:135],v[12:15]\n"
"v_mfma_f32_16x16x32_bf16 v[4:7],v[236:239],v[120:123],v[4:7]\n"
"v_mfma_f32_16x16x32_bf16 v[4:7],v[208:211],v[128:131],v[4:7]\n"
"v_mfma_f32_16x16x32_bf16 v[4:7],v[212:215],v[136:139],v[4:7]\n"
"v_mfma_f32_16x16x32_bf16 v[12:15],v[212:215],v[140:143],v[12:15]\n"
"s_setprio 0\n"
"v_cvt_pk_bf16_f32 v16,v0,v1\n"
"v_cvt_pk_bf16_f32 v17,v2,v3\n"
"s_nop 3\n"
"v_cvt_pk_bf16_f32 v18,v4,v5\n"
"v_cvt_pk_bf16_f32 v19,v6,v7\n"
"s_add_u32 s78,s87,16384\n"
"buffer_store_dwordx4 v[16:19],v252,s[32:35],s78 offen sc1\n"
"v_cvt_pk_bf16_f32 v20,v8,v9\n"
"v_cvt_pk_bf16_f32 v21,v10,v11\n"
"v_cvt_pk_bf16_f32 v22,v12,v13\n"
"v_cvt_pk_bf16_f32 v23,v14,v15\n"
"s_add_u32 s78,s87,34816\n"
"buffer_store_dwordx4 v[20:23],v252,s[32:35],s78 offen sc1\n"
"s_waitcnt vmcnt(0)\n"
"v_mov_b32 v240,3\n"
"s_mov_b64 exec,1\n"
"buffer_store_dword v240,off,s[32:35],s88 sc1\n"
"s_mov_b64 exec,-1\n"
"s_endpgm\n"
".Lk_ssm_end:\n"
    : "+{v0}"(tid)
    : [ka] "s"(__builtin_amdgcn_kernarg_segment_ptr()), [bid] "s"(blockIdx.x), [lds] "s"((unsigned)(size_t)smem)
    : "v1", "v2", "v3", "v4", "v5", "v6", "v7", "v8", "v9", "v10", "v11", "v12", "v13", "v14", "v15", "v16", "v17", "v18", "v19", "v20", "v21", "v22", "v23", "v24", "v25", "v26", "v27", "v28", "v29", "v30", "v31", "v32", "v33", "v34", "v35", "v36", "v37", "v38", "v39", "v40", "v41", "v42", "v43", "v44", "v45", "v46", "v47", "v48", "v49", "v50", "v51", "v52", "v53", "v54", "v55", "v56", "v57", "v58", "v59", "v60", "v61", "v62", "v63", "v64", "v65", "v66", "v67", "v68", "v69", "v70", "v71", "v72", "v73", "v74", "v75", "v76", "v77", "v78", "v79", "v80", "v81", "v82", "v83", "v84", "v85", "v86", "v87", "v88", "v89", "v90", "v91", "v92", "v93", "v94", "v95", "v96", "v97", "v98", "v99", "v100", "v101", "v102", "v103", "v104", "v105", "v106", "v107", "v108", "v109", "v110", "v111", "v112", "v113", "v114", "v115", "v116", "v117", "v118", "v119", "v120", "v121", "v122", "v123", "v124", "v125", "v126", "v127", "v128", "v129", "v130", "v131", "v132", "v133", "v134", "v135", "v136", "v137", "v138", "v139", "v140", "v141", "v142", "v143", "v144", "v145", "v146", "v147", "v148", "v149", "v150", "v151", "v152", "v153", "v154", "v155", "v156", "v157", "v158", "v159", "v160", "v161", "v162", "v163", "v164", "v165", "v166", "v167", "v168", "v169", "v170", "v171", "v172", "v173", "v174", "v175", "v176", "v177", "v178", "v179", "v180", "v181", "v182", "v183", "v184", "v185", "v186", "v187", "v188", "v189", "v190", "v191", "v192", "v193", "v194", "v195", "v196", "v197", "v198", "v199", "v200", "v201", "v202", "v203", "v204", "v205", "v206", "v207", "v208", "v209", "v210", "v211", "v212", "v213", "v214", "v215", "v216", "v217", "v218", "v219", "v220", "v221", "v222", "v223", "v224", "v225", "v226", "v227", "v228", "v229", "v230", "v231", "v232", "v233", "v234", "v235", "v236", "v237", "v238", "v239", "v240", "v241", "v242", "v243", "v244", "v245", "v246", "v247", "v248", "v249", "v250", "v251", "v252", "v253", "v254", "v255", "s4", "s5", "s6", "s7", "s8", "s9", "s10", "s11", "s12", "s13", "s14", "s15", "s16", "s17", "s18", "s19", "s20", "s21", "s22", "s23", "s24", "s25", "s26", "s27", "s28", "s29", "s30", "s31", "s33", "s34", "s35", "s36", "s37", "s38", "s39", "s40", "s41", "s42", "s43", "s44", "s45", "s46", "s47", "s48", "s49", "s50", "s51", "s52", "s53", "s54", "s55", "s56", "s57", "s58", "s59", "s60", "s61", "s62", "s63", "s64", "s65", "s66", "s67", "s68", "s69", "s70", "s71", "s72", "s73", "s74", "s75", "s76", "s77", "s78", "s79", "s80", "s81", "s82", "s83", "s84", "s85", "s86", "s87", "s88", "s89", "s90", "s91", "s92", "s93", "s94", "s95", "s96", "s97", "s98", "s99", "vcc", "scc", "memory");
}

extern "C" void kernel_launch(void* const* d_in, const int* in_sizes, int n_in, void* d_out, int out_size, void* d_ws, size_t ws_size,
                              hipStream_t stream) {
  const u16* const* in = (const u16* const*)d_in;
  fused_main<<<256, 512, 0, stream>>>(in[0], in[1], in[2], in[3], in[4], in[5], in[6], in[7], in[8], in[10], in[11], in[12], in[13], (float*)d_out, (char*)d_ws);
}
```
